```python
import torch

def _softmax(x, axis=-1):
    return torch.softmax(x, dim=axis)
def _gelu(x):
    return torch.nn.functional.gelu(x, approximate="tanh")
def _silu(x):
    return torch.nn.functional.silu(x)
def _sigmoid(x):
    return torch.sigmoid(x)
def _layernorm(x, axis=-1, eps=1e-5):
    m = x.mean(dim=axis, keepdim=True)
    v = x.var(dim=axis, keepdim=True, unbiased=False)
    return (x - m) / torch.sqrt(v + eps)
def _scatter_add(tgt, idx, src, axis=0):
    return tgt.clone().index_add_(axis, idx, src)
def _segment_sum(src, seg, n):
    out = torch.zeros((n,) + src.shape[1:], dtype=src.dtype)
    return out.index_add_(0, seg, src)
def _pad(x, pw):
    flat = tuple(v for pair in reversed(pw) for v in pair)
    return torch.nn.functional.pad(x, flat)

kernel_name = 'gated_mix__mha_block__sliding_block__prompt'

def setup_inputs():

    _w = (lambda a: (lambda t: t.to(torch.bfloat16) if t.is_floating_point() else t)(torch.as_tensor(a)))
    def _A_setup():
        B, S, D, H = 1, 2048, 512, 32
        return {
            "x": _w((lambda *s: torch.randn(*s, dtype=torch.bfloat16) * 0.05)(B, S, D)),
            "wqkv": _w((lambda *s: torch.randn(*s, dtype=torch.bfloat16) * 1.6)(D, 3 * D)),
            "wo": _w((lambda *s: torch.randn(*s, dtype=torch.bfloat16) * 0.1)(D, D)),
        }
    _A_in = _A_setup()

    def _B_setup():
        B, S, D = 1, 2048, 512
        return {
            "x": _w((lambda *s: torch.randn(*s, dtype=torch.bfloat16) * 0.05)(B, S, D)),
            "wqkv": _w((lambda *s: torch.randn(*s, dtype=torch.bfloat16) * 1.6)(D, 3 * D)),
            "wo": _w((lambda *s: torch.randn(*s, dtype=torch.bfloat16) * 0.05)(D, D)),
        }
    _B_in = _B_setup()

    return {
        "A_x": _A_in["x"],
        "A_wqkv": _A_in["wqkv"],
        "A_wo": _A_in["wo"],
        "B_x": _B_in["x"],
        "B_wqkv": _B_in["wqkv"],
        "B_wo": _B_in["wo"],
        "gate_w": _w((lambda *s: torch.randn(*s, dtype=torch.bfloat16) * 0.05)(512) * 16.0),
    }

def reference(A_x, A_wqkv, A_wo, B_x, B_wqkv, B_wo, gate_w):
    def _A_ref(x, wqkv, wo):
        B, S, D = x.shape
        H = 32
        Dh = D // H
        qkv = (lambda x, *d: x.permute(*d))((x @ wqkv).reshape(B, S, 3, H, Dh), 2, 0, 3, 1, 4)
        q, k, v = qkv[0], qkv[1], qkv[2]
        scores = torch.einsum("bhqd,bhkd->bhqk", q, k) * (Dh ** -0.5)
        p = _softmax(scores, axis=-1)
        o = torch.einsum("bhqk,bhkd->bhqd", p, v)
        return (lambda x, *d: x.permute(*d))(o, 0, 2, 1, 3).reshape(B, S, D) @ wo

    def _B_ref(x, wqkv, wo):
        B, S, D = x.shape
        H = 32; Dh = D // H; W = 256
        qkv = (lambda x, *d: x.permute(*d))((x @ wqkv).reshape(B, S, 3, H, Dh), 2, 0, 3, 1, 4)
        q, k, v = qkv[0], qkv[1], qkv[2]
        scores = torch.einsum("bhqd,bhkd->bhqk", q, k) * (Dh ** -0.5)
        idx = torch.arange(S)
        mask = (idx[:, None] - idx[None, :] >= 0) & (idx[:, None] - idx[None, :] < W)
        scores = torch.where(mask[None, None], scores, -1e9)
        p = _softmax(scores, axis=-1)
        o = torch.einsum("bhqk,bhkd->bhqd", p, v)
        return (lambda x, *d: x.permute(*d))(o, 0, 2, 1, 3).reshape(B, S, D) @ wo

    x = A_x
    g = _sigmoid((x * gate_w).sum(-1, keepdims=True))
    a = _A_ref(x, A_wqkv, A_wo)
    b = _B_ref(x, B_wqkv, B_wo)
    return g * a + (1 - g) * b
```

```cpp
#include <hip/hip_runtime.h>
#include <hip/hip_bf16.h>

typedef __attribute__((ext_vector_type(8))) __bf16 bf16x8;
typedef __attribute__((ext_vector_type(16))) __bf16 bf16x16;
typedef __attribute__((ext_vector_type(16))) float f32x16;
typedef __attribute__((ext_vector_type(8))) float f32x8;
typedef __attribute__((ext_vector_type(4))) float f32x4;
typedef __attribute__((ext_vector_type(4))) unsigned u32x4;
typedef __attribute__((ext_vector_type(2))) unsigned u32x2;
typedef short s16x4 __attribute__((ext_vector_type(4)));
typedef unsigned long long u64;
#define GAS __attribute__((address_space(1)))
#define LAS __attribute__((address_space(3)))
typedef LAS const char* lds_cptr;
typedef LAS char* lds_ptr;

constexpr unsigned WS_EPOCH = 0;
constexpr unsigned WS_KVFLAG = 4096;
constexpr unsigned WS_OFLAG = 65536;
constexpr unsigned WS_G = 131072;
constexpr unsigned WS_KV = 1u << 20;
constexpr unsigned TS = 2176;
constexpr unsigned KV_BYTES = 2u * 32u * 64u * TS;
constexpr unsigned WS_O = 10u << 20;
constexpr unsigned O_BYTES = 4u << 20;
constexpr unsigned WS_STAMP = 16u << 20;
constexpr unsigned OOB = 0x7ff00000u;
constexpr unsigned SLOT = 32768;
constexpr unsigned L_PART = 0, L_FIN = 49152, L_WO = 65536, L_MDIAG = 131072, L_MFAR = 135168, L_Q = 139264, L_M = 155648, L_PL = 157696, L_END = 160768;
constexpr float QSCALE = 0.25f * 1.4426950408889634f;
constexpr float LMAX = 1.8446744073709552e19f;
constexpr unsigned SPIN_CAP = 1u << 18;

#define SBAR() __builtin_amdgcn_sched_barrier(0)
#define PIN(x) asm volatile("" : "+v"(x))
#define MFMA(a, b, c) __builtin_amdgcn_mfma_f32_32x32x16_bf16(a, b, c, 0, 0, 0)
#define WAIT_BAR(N) asm volatile("s_waitcnt vmcnt(" #N ") lgkmcnt(0)\n\ts_barrier" ::: "memory")
#define RLX_AGENT __ATOMIC_RELAXED, __HIP_MEMORY_SCOPE_AGENT
#define STAMP(i)
#define CSTAMP(i)

__device__ __forceinline__ void glds16(unsigned voff, const void* sbase, unsigned ldsaddr) {
    asm volatile("s_mov_b32 m0, %2\n\ts_nop 0\n\tglobal_load_lds_dwordx4 %0, %1" ::"v"(voff), "s"(sbase), "s"(ldsaddr) : "memory");
}
__device__ __forceinline__ unsigned cvtpk(float lo, float hi) {
    typedef __attribute__((ext_vector_type(2))) __bf16 bf16x2;
    typedef __attribute__((ext_vector_type(2))) float f32x2;
    return __builtin_bit_cast(unsigned, __builtin_convertvector((f32x2){lo, hi}, bf16x2));
}
__device__ __forceinline__ bf16x8 as_frag(u32x4 v) { return __builtin_bit_cast(bf16x8, v); }
__device__ __forceinline__ bf16x8 lds_frag(lds_cptr p) { return *(const LAS bf16x8*)p; }
__device__ __forceinline__ bf16x8 tr_frag(lds_cptr p0, lds_cptr p1) {
    s16x4 a = __builtin_amdgcn_ds_read_tr16_b64_v4i16((LAS s16x4*)p0);
    s16x4 b = __builtin_amdgcn_ds_read_tr16_b64_v4i16((LAS s16x4*)p1);
    return __builtin_bit_cast(bf16x8, __builtin_shufflevector(a, b, 0, 1, 2, 3, 4, 5, 6, 7));
}
__device__ __forceinline__ f32x16 pv_lo(bf16x8 v, const f32x16& p, f32x16 acc, int idx) {
    f32x8 h = __builtin_shufflevector(p, p, 0, 1, 2, 3, 4, 5, 6, 7);
    return __builtin_amdgcn_smfmac_f32_32x32x32_bf16(v, __builtin_bit_cast(bf16x16, h), acc, idx, 0, 0);
}
__device__ __forceinline__ f32x16 pv_hi(bf16x8 v, const f32x16& p, f32x16 acc, int idx) {
    f32x8 h = __builtin_shufflevector(p, p, 8, 9, 10, 11, 12, 13, 14, 15);
    return __builtin_amdgcn_smfmac_f32_32x32x32_bf16(v, __builtin_bit_cast(bf16x16, h), acc, idx, 0, 0);
}
#define MX3(a, b, c) __builtin_fmaxf(__builtin_fmaxf((a), (b)), (c))
__device__ __forceinline__ float max16(const f32x16& s) {
    float a = MX3(s[0], s[1], s[2]), b = MX3(s[3], s[4], s[5]);
    a = MX3(a, s[6], s[7]); b = MX3(b, s[8], s[9]); a = MX3(a, s[10], s[11]); b = MX3(b, s[12], s[13]);
    return MX3(a, b, __builtin_fmaxf(s[14], s[15]));
}
__device__ __forceinline__ float halfmax(float m) {
    auto r = __builtin_amdgcn_permlane32_swap(__float_as_uint(m), __float_as_uint(m), false, false);
    return __builtin_fmaxf(__uint_as_float(r[0]), __uint_as_float(r[1]));
}
__device__ __forceinline__ f32x16 lds_f32x16(lds_cptr p) {
    f32x4 a = *(const LAS f32x4*)p, b = *(const LAS f32x4*)(p + 1024), c = *(const LAS f32x4*)(p + 2048), d = *(const LAS f32x4*)(p + 3072);
    f32x8 lo = __builtin_shufflevector(a, b, 0, 1, 2, 3, 4, 5, 6, 7), hi = __builtin_shufflevector(c, d, 0, 1, 2, 3, 4, 5, 6, 7);
    return __builtin_shufflevector(lo, hi, 0, 1, 2, 3, 4, 5, 6, 7, 8, 9, 10, 11, 12, 13, 14, 15);
}
#define BLOAD(rs, vo, so) as_frag(__builtin_amdgcn_raw_buffer_load_b128(rs, vo, so, 16))
#define BSTORE(v, rs, vo, so) __builtin_amdgcn_raw_buffer_store_b128(v, rs, (vo) + (so), 0, 16)

__device__ __forceinline__ void wait_tags(const GAS u64* p, u64 tag) {
    for (unsigned spins = 0; spins < SPIN_CAP; ++spins) {
        const u64 v = __hip_atomic_load(p, RLX_AGENT);
        if (__all(v == tag)) break;
        __builtin_amdgcn_s_sleep(1);
    }
    asm volatile("" ::: "memory");
}

__device__ __forceinline__ void slow_rows(__amdgpu_buffer_rsrc_t rs, unsigned sbase, int lo, int hi, int g, bool band, bf16x8 q,
                                          unsigned vk, unsigned vv0, unsigned vv1, lds_cptr lds, int lane, int idx, f32x16& acc) {
    float m = -__builtin_inff();
    for (int pass = 0; pass < 2; ++pass) {
        if (pass) { m = halfmax(m); acc = f32x16{}; }
        for (int kt = lo; kt <= hi; ++kt) {
            const unsigned so = sbase + (unsigned)kt * TS;
            const bf16x8 k = BLOAD(rs, vk, so), v0 = BLOAD(rs, vv0, so), v1 = BLOAD(rs, vv1, so);
            f32x16 c = f32x16{};
            if (band && kt == g) c = lds_f32x16(lds + L_MDIAG + lane * 16);
            if (band && kt == g - 8) c = lds_f32x16(lds + L_MFAR + lane * 16);
            f32x16 s = MFMA(k, q, c);
            if (!pass) m = __builtin_fmaxf(m, max16(s));
            else {
#pragma unroll
                for (int r = 0; r < 16; ++r) s[r] = __builtin_amdgcn_exp2f(s[r] - m);
                acc = pv_lo(v0, s, acc, idx); acc = pv_hi(v1, s, acc, idx);
            }
        }
    }
}

__global__ void __launch_bounds__(512, 2)
gmix_kernel(const char* __restrict__ x, const char* __restrict__ wA, const char* __restrict__ woA, const char* __restrict__ wB,
            const char* __restrict__ woB, const char* __restrict__ gw, char* __restrict__ out, char* ws) {
    extern __shared__ __attribute__((aligned(16))) char smem[];
    const lds_ptr lds = (lds_ptr)smem;
    const unsigned lds0 = (unsigned)(uintptr_t)lds;
    const int tid = threadIdx.x, lane = tid & 63, r32 = lane & 31, hh = lane >> 5;
    const int w = __builtin_amdgcn_readfirstlane(tid >> 6);
    const int bid = blockIdx.x, xcd = bid & 7, rr = bid >> 3;
    const int p = 4 * (xcd & 3) + (rr & 3), j = 8 * (xcd >> 2) + (rr >> 2);
    const int jo = 2 * xcd + (rr >> 4), po = rr & 15;
    const int t = w & 3, c = w >> 2;
    STAMP(0);
    u32x4 gx, gg; u32x2 ep;
    {
        const unsigned vo = lane * 16;
        const char* xr = x + (size_t)(128 * j + 8 * p + w) * 1024;
        const unsigned z = 0;
        asm volatile("global_load_dwordx4 %0, %3, %4\n\tglobal_load_dwordx4 %1, %3, %5\n\tglobal_load_dwordx2 %2, %6, %7 sc1"
                     : "=&v"(gx), "=&v"(gg), "=&v"(ep) : "v"(vo), "s"(xr), "s"(gw), "v"(z), "s"(ws) : "memory");
    }
    const int xrow = 16 * w + (lane >> 3);
    const unsigned vx0 = xrow * 1024 + (((lane & 7) ^ ((xrow >> 1) & 7)) * 16), vx1 = (vx0 ^ 64) + 8192;
    const unsigned vw = (lane >> 2) * 3072 + (lane & 3) * 16;
    const char* xs = x + (size_t)j * 131072;
    const char* wkv = ((w >> 2) ? wB : wA) + (((w >> 1) & 1) ? 2048 : 1024) + 64 * p + (size_t)(32 * (w & 1)) * 3072;
    const char* wq = (c ? wB : wA) + 64 * p + (size_t)(64 * t) * 3072;
#define DMA_KV(s) do { const unsigned sl_ = lds0 + ((s) & 3) * SLOT; \
        glds16(vx0, xs + (s) * 128, sl_ + w * 2048); glds16(vx1, xs + (s) * 128, sl_ + w * 2048 + 1024); \
        glds16(vw, wkv + (size_t)(s) * 64 * 3072, sl_ + 16384 + w * 2048); glds16(vw, wkv + (size_t)((s) * 64 + 16) * 3072, sl_ + 16384 + w * 2048 + 1024); } while (0)
#define DMA_Q(s) do { const unsigned sl_ = lds0 + ((s) & 3) * SLOT + w * 4096; const char* b_ = wq + (size_t)((s) - 8) * 256 * 3072; \
        glds16(vw, b_, sl_); glds16(vw, b_ + 16 * 3072, sl_ + 1024); glds16(vw, b_ + 32 * 3072, sl_ + 2048); glds16(vw, b_ + 48 * 3072, sl_ + 3072); } while (0)
    DMA_KV(0); DMA_KV(1); DMA_KV(2); DMA_KV(3);
    {
        f32x4 mk;
#pragma unroll
        for (int i = 0; i < 4; ++i) { const bool le = (i + 8 * t + 4 * hh) <= r32; mk[i] = (le != (c != 0)) ? 0.f : -__builtin_inff(); }
        *(LAS f32x4*)(lds + L_MDIAG + w * 1024 + lane * 16) = mk;
    }
    asm volatile("s_waitcnt vmcnt(16)" : "+v"(gx), "+v"(gg), "+v"(ep)::"memory");
    u64 tag;
    {
        const u64 e = ((u64)(unsigned)__builtin_amdgcn_readfirstlane(ep[1]) << 32) | (unsigned)__builtin_amdgcn_readfirstlane(ep[0]);
        tag = e * 0x9E3779B97F4A7C15ull + 0x632BE59BD9B4E019ull;
    }
    float gate;
    {
        float z = 0.f;
#pragma unroll
        for (int i = 0; i < 4; ++i) {
            z = __builtin_fmaf(__uint_as_float(gx[i] << 16), __uint_as_float(gg[i] << 16), z);
            z = __builtin_fmaf(__uint_as_float(gx[i] & 0xffff0000u), __uint_as_float(gg[i] & 0xffff0000u), z);
        }
#pragma unroll
        for (int o = 32; o >= 1; o >>= 1) z += __shfl_xor(z, o);
        gate = 1.f / (1.f + __expf(-z));
        PIN(gate);
    }
    const __amdgpu_buffer_rsrc_t rkv = __builtin_amdgcn_make_buffer_rsrc(ws + WS_KV, 0, KV_BYTES, 0x00020000);
    bf16x8 xf[32];
    f32x16 aK = f32x16{}, aV = f32x16{};
    const lds_cptr xa = lds + (32 * t + r32) * 128;
    const unsigned xsw = (hh ^ ((r32 >> 1) & 7)) * 16;
    const unsigned wlane = (8 * hh + ((lane & 15) >> 2)) * 64 + ((lane >> 4) & 1) * 32 + (lane & 3) * 8;
    const lds_cptr wa = lds + 16384 + c * 8192 + wlane;
    bf16x8 wk[2][4], wv[2][4];
#define RD_STAGE(s) do { _Pragma("unroll") for (int ks = 0; ks < 4; ++ks) { \
        xf[4 * (s) + ks] = lds_frag(xa + ((s) & 3) * SLOT + (xsw ^ (ks * 32))); \
        wk[(s) & 1][ks] = tr_frag(wa + ((s) & 3) * SLOT + ks * 1024, wa + ((s) & 3) * SLOT + ks * 1024 + 256); \
        wv[(s) & 1][ks] = tr_frag(wa + ((s) & 3) * SLOT + 4096 + ks * 1024, wa + ((s) & 3) * SLOT + 4096 + ks * 1024 + 256); } } while (0)
#define MM_STAGE(s) do { _Pragma("unroll") for (int ks = 0; ks < 4; ++ks) { \
        aK = MFMA(wk[(s) & 1][ks], xf[4 * (s) + ks], aK); aV = MFMA(xf[4 * (s) + ks], wv[(s) & 1][ks], aV); } } while (0)
    WAIT_BAR(12); STAMP(1);        RD_STAGE(0);
    WAIT_BAR(8); DMA_KV(4); RD_STAGE(1); MM_STAGE(0);
    WAIT_BAR(8); DMA_KV(5); RD_STAGE(2); MM_STAGE(1);
    WAIT_BAR(8); DMA_KV(6); RD_STAGE(3); MM_STAGE(2);
    WAIT_BAR(8); DMA_KV(7); RD_STAGE(4); MM_STAGE(3);
    WAIT_BAR(8); DMA_Q(8);  RD_STAGE(5); MM_STAGE(4);
    WAIT_BAR(8); DMA_Q(9);  RD_STAGE(6); MM_STAGE(5);
    WAIT_BAR(8);            RD_STAGE(7); MM_STAGE(6);
    WAIT_BAR(4);                         MM_STAGE(7);
    u32x4 k0, k1;
    {
        k0 = (u32x4){cvtpk(aK[0], aK[1]), cvtpk(aK[2], aK[3]), cvtpk(aK[4], aK[5]), cvtpk(aK[6], aK[7])};
        k1 = (u32x4){cvtpk(aK[8], aK[9]), cvtpk(aK[10], aK[11]), cvtpk(aK[12], aK[13]), cvtpk(aK[14], aK[15])};
        const unsigned so = (unsigned)((c * 32 + 2 * p) * 64 + 4 * j + t) * TS;
        BSTORE(k0, rkv, lane * 16, so); BSTORE(k1, rkv, lane * 16, so + 64 * TS);
        const unsigned vo = (r32 >> 4) * (64 * TS) + 1024 + hh * 256 + (r32 & 15) * 16;
#pragma unroll
        for (int s = 0; s < 2; ++s) {
            auto a = __builtin_amdgcn_permlane32_swap(cvtpk(aV[8 * s], aV[8 * s + 1]), cvtpk(aV[8 * s + 4], aV[8 * s + 5]), false, false);
            auto b = __builtin_amdgcn_permlane32_swap(cvtpk(aV[8 * s + 2], aV[8 * s + 3]), cvtpk(aV[8 * s + 6], aV[8 * s + 7]), false, false);
            BSTORE(((u32x4){a[0], b[0], a[1], b[1]}), rkv, vo + s * 512, so);
        }
        BSTORE(((u32x4){0x3f803f80u, 0x3f803f80u, 0x3f803f80u, 0x3f803f80u}), rkv, lane < 2 ? lane * (64 * TS) + 2048 : OOB, so);
        if (lane == 0) __hip_atomic_store((GAS float*)(ws + WS_G) + 128 * j + 8 * p + w, gate, RLX_AGENT);
    }
    STAMP(2);
    f32x16 aQ = f32x16{};
    const lds_cptr wqa = lds + c * 16384 + wlane;
#pragma unroll
    for (int ks = 0; ks < 16; ++ks) aQ = MFMA(tr_frag(wqa + ks * 1024, wqa + ks * 1024 + 256), xf[ks], aQ);
    WAIT_BAR(0);
    if (tid == 0) __hip_atomic_store((GAS u64*)(ws + WS_KVFLAG + (p * 16 + j) * 128), tag, RLX_AGENT);
    STAMP(3);
    {
        const char* wo = (c ? woB : woA) + 64 * po + (size_t)(128 * t) * 1024;
        const unsigned vo = (lane >> 2) * 1024 + (lane & 3) * 16;
#pragma unroll
        for (int i = 0; i < 8; ++i) glds16(vo, wo + i * 16384, lds0 + L_WO + w * 8192 + i * 1024);
    }
#pragma unroll
    for (int ks = 0; ks < 16; ++ks) aQ = MFMA(tr_frag(wqa + SLOT + ks * 1024, wqa + SLOT + ks * 1024 + 256), xf[16 + ks], aQ);
    {
#pragma unroll
        for (int r = 0; r < 16; ++r) aQ[r] *= QSCALE;
        const u32x4 q0 = (u32x4){cvtpk(aQ[0], aQ[1]), cvtpk(aQ[2], aQ[3]), cvtpk(aQ[4], aQ[5]), cvtpk(aQ[6], aQ[7])};
        const u32x4 q1 = (u32x4){cvtpk(aQ[8], aQ[9]), cvtpk(aQ[10], aQ[11]), cvtpk(aQ[12], aQ[13]), cvtpk(aQ[14], aQ[15])};
        *(LAS u32x4*)(lds + L_Q + c * 8192 + t * 1024 + lane * 16) = q0;
        *(LAS u32x4*)(lds + L_Q + c * 8192 + 4096 + t * 1024 + lane * 16) = q1;
        const f32x16 md = lds_f32x16(lds + L_MDIAG + lane * 16);
        const float m0 = max16(MFMA(as_frag(k0), as_frag(q0), md)), m1 = max16(MFMA(as_frag(k1), as_frag(q1), md));
        auto sw = __builtin_amdgcn_permlane32_swap(__float_as_uint(m0), __float_as_uint(m1), false, false);
        *(LAS float*)(lds + L_M + c * 1024 + hh * 512 + t * 128 + r32 * 4) = __builtin_fmaxf(__uint_as_float(sw[0]), __uint_as_float(sw[1]));
    }
    asm volatile("s_waitcnt lgkmcnt(0)\n\ts_barrier" ::: "memory");
    STAMP(4);
    const int head = 2 * p + c;
    const int idx = 0xdddddddd;
    const unsigned vk = lane * 16;
    const unsigned vv0 = r32 < 16 ? 1024 + hh * 256 + r32 * 16 : (r32 == 16 ? 2048 : OOB);
    const unsigned vv1 = r32 < 16 ? vv0 + 512 : vv0;
    bf16x8 q0, q1, q2, q3; f32x16 nm;
    {
        const lds_cptr qp = lds + L_Q + c * 4096 + lane * 16;
        q0 = lds_frag(qp); q1 = lds_frag(qp + 1024); q2 = lds_frag(qp + 2048); q3 = lds_frag(qp + 3072);
        const LAS float* mp = (const LAS float*)(lds + L_M + c * 512) + r32;
        const float m = -__builtin_fminf(__builtin_fminf(mp[0], mp[32]), __builtin_fminf(mp[64], mp[96]));
#pragma unroll
        for (int r = 0; r < 16; ++r) nm[r] = m;
        PIN(nm);
    }
    wait_tags((const GAS u64*)(ws + WS_KVFLAG + (p * 16 + (lane & 15)) * 128), tag);
    STAMP(5); CSTAMP(14);
    f32x16 o0 = f32x16{}, o1 = f32x16{}, o2 = f32x16{}, o3 = f32x16{}, S0, S1;
    bf16x8 ka, va0, va1, kb, vb0, vb1, kc, vc0, vc1, kd, vd0, vd1;
    const unsigned sA = (unsigned)(head * 64 + t) * TS;
    const int g = 4 * j + t;
    const unsigned sB = (unsigned)((32 + head) * 64) * TS;
#define SOFF_A(i) (sA + (unsigned)((j + (i)) & 15) * (4 * TS))
#define LD3(K, V0, V1, rs, so) do { K = BLOAD(rs, vk, so); V0 = BLOAD(rs, vv0, so); V1 = BLOAD(rs, vv1, so); } while (0)
#define LDB(K, V0, V1, i) do { const int kt_ = g - (i); \
        const __amdgpu_buffer_rsrc_t rb_ = __builtin_amdgcn_make_buffer_rsrc(ws + WS_KV, 0, kt_ >= 0 ? KV_BYTES : 0u, 0x00020000); \
        LD3(K, V0, V1, rb_, sB + (unsigned)(kt_ >= 0 ? kt_ : 0) * TS); } while (0)
#define EX(X, r) X[r] = __builtin_amdgcn_exp2f(X[r])
#define STEP(X, Y, OP, VP, KN, QN, CN, O, V0) do { \
        EX(X, 0); EX(X, 1); SBAR(); OP = pv_hi(VP, Y, OP, idx); SBAR(); EX(X, 2); EX(X, 3); EX(X, 4); SBAR(); Y = MFMA(KN, QN, CN); SBAR(); \
        EX(X, 5); EX(X, 6); EX(X, 7); EX(X, 8); EX(X, 9); SBAR(); O = pv_lo(V0, X, O, idx); SBAR(); \
        EX(X, 10); EX(X, 11); EX(X, 12); EX(X, 13); EX(X, 14); EX(X, 15); SBAR(); } while (0)
#define TILE(K, V0, V1, VPREV, KN, LOADS) do { \
        STEP(S0, S1, o3, VPREV, K, q1, nm, o0, V0); LOADS; SBAR(); STEP(S1, S0, o0, V1, K, q2, nm, o1, V0); \
        STEP(S0, S1, o1, V1, K, q3, nm, o2, V0); STEP(S1, S0, o2, V1, KN, q0, nm, o3, V0); } while (0)
    LD3(ka, va0, va1, rkv, SOFF_A(0)); LD3(kb, vb0, vb1, rkv, SOFF_A(1)); LD3(kc, vc0, vc1, rkv, SOFF_A(2));
    vd1 = bf16x8{}; S1 = f32x16{};
    S0 = MFMA(ka, q0, nm);
    for (int it = 0; it < 12; it += 4) {
        TILE(ka, va0, va1, vd1, kb, LD3(kd, vd0, vd1, rkv, SOFF_A(it + 3)));
        TILE(kb, vb0, vb1, va1, kc, LD3(ka, va0, va1, rkv, SOFF_A(it + 4)));
        TILE(kc, vc0, vc1, vb1, kd, LD3(kb, vb0, vb1, rkv, SOFF_A(it + 5)));
        TILE(kd, vd0, vd1, vc1, ka, LD3(kc, vc0, vc1, rkv, SOFF_A(it + 6)));
    }
    TILE(ka, va0, va1, vd1, kb, LD3(kd, vd0, vd1, rkv, SOFF_A(15)));
    TILE(kb, vb0, vb1, va1, kc, LDB(ka, va0, va1, 0));
    TILE(kc, vc0, vc1, vb1, kd, LDB(kb, vb0, vb1, 1));
    TILE(kd, vd0, vd1, vc1, kd, LDB(kc, vc0, vc1, 2));
    o3 = pv_hi(vd1, S1, o3, idx);
    LDB(kd, vd0, vd1, 3);
    STAMP(6); CSTAMP(15);
    {
#define PUT(O, a) do { if (t != (a)) { const int sl_ = ((a) - t - 1) & 3; lds_ptr d_ = lds + L_PART + (w * 3 + sl_) * 2048 + lane * 16; \
        *(LAS f32x4*)d_ = __builtin_shufflevector(O, O, 0, 1, 2, 3); *(LAS f32x4*)(d_ + 1024) = __builtin_shufflevector(O, O, 4, 5, 6, 7); \
        if (hh == 0) *(LAS float*)(lds + L_PL + (w * 3 + sl_) * 128 + r32 * 4) = O[8]; } } while (0)
        PUT(o0, 0); PUT(o1, 1); PUT(o2, 2); PUT(o3, 3);
    }
    f32x16 oo = t == 0 ? o0 : (t == 1 ? o1 : (t == 2 ? o2 : o3));
    const bf16x8 qa = t == 0 ? q0 : (t == 1 ? q1 : (t == 2 ? q2 : q3));
    asm volatile("s_waitcnt lgkmcnt(0)\n\ts_barrier" ::: "memory");
    const __amdgpu_buffer_rsrc_t ro = __builtin_amdgcn_make_buffer_rsrc(ws + WS_O, 0, O_BYTES, 0x00020000);
#define FINISH(BR, LO, HI, Q, SB) do { \
        auto lsw_ = __builtin_amdgcn_permlane32_swap(__float_as_uint(oo[8]), __float_as_uint(oo[8]), false, false); \
        float l_ = __uint_as_float(lsw_[0]) + lrest; float z_ = 0.f; \
        _Pragma("unroll") for (int r = 0; r < 8; ++r) z_ = __builtin_fmaf(oo[r], 0.f, z_); \
        if (__builtin_expect(__any(!(l_ + z_ < LMAX)), 0)) { \
            slow_rows(rkv, SB, LO, HI, g, BR, Q, vk, vv0, vv1, lds, lane, idx, oo); \
            auto l2_ = __builtin_amdgcn_permlane32_swap(__float_as_uint(oo[8]), __float_as_uint(oo[8]), false, false); l_ = __uint_as_float(l2_[0]); } \
        const float il_ = 1.f / l_; \
        BSTORE(((u32x4){cvtpk(oo[0] * il_, oo[1] * il_), cvtpk(oo[2] * il_, oo[3] * il_), cvtpk(oo[4] * il_, oo[5] * il_), cvtpk(oo[6] * il_, oo[7] * il_)}), \
               ro, vk, (unsigned)(((BR) * 64 + g) * 32 + head) * 1024); } while (0)
    {
        float lrest = 0.f;
#pragma unroll
        for (int s = 0; s < 3; ++s) {
            const int src = (c * 4 + ((t - 1 - s) & 3)) * 3 + s;
            const lds_cptr d = lds + L_PART + src * 2048 + lane * 16;
            const f32x4 a = *(const LAS f32x4*)d, b = *(const LAS f32x4*)(d + 1024);
#pragma unroll
            for (int r = 0; r < 4; ++r) { oo[r] += a[r]; oo[4 + r] += b[r]; }
            lrest += *(const LAS float*)(lds + L_PL + src * 128 + r32 * 4);
        }
        FINISH(0, 0, 63, qa, (unsigned)(head * 64) * TS);
    }
    STAMP(7);
    {
        const bf16x8 qb = lds_frag(lds + L_Q + 8192 + c * 4096 + t * 1024 + lane * 16);
        const float m = -*(const LAS float*)(lds + L_M + 1024 + c * 512 + t * 128 + r32 * 4);
        f32x16 cd = lds_f32x16(lds + L_MDIAG + lane * 16), cf = lds_f32x16(lds + L_MFAR + lane * 16);
#pragma unroll
        for (int r = 0; r < 16; ++r) { nm[r] = m; cd[r] += m; cf[r] += m; }
        PIN(nm); PIN(cf);
        oo = f32x16{}; S1 = f32x16{};
        S0 = MFMA(ka, qb, cd);
        bf16x8 vz = bf16x8{};
        STEP(S0, S1, oo, vz, kb, qb, nm, oo, va0);
        STEP(S1, S0, oo, va1, kc, qb, nm, oo, vb0); LDB(ka, va0, va1, 4); SBAR();
        STEP(S0, S1, oo, vb1, kd, qb, nm, oo, vc0); LDB(kb, vb0, vb1, 5); SBAR();
        STEP(S1, S0, oo, vc1, ka, qb, nm, oo, vd0); LDB(kc, vc0, vc1, 6); SBAR();
        STEP(S0, S1, oo, vd1, kb, qb, nm, oo, va0); LDB(kd, vd0, vd1, 7); SBAR();
        STEP(S1, S0, oo, va1, kc, qb, nm, oo, vb0); LDB(ka, va0, va1, 8); SBAR();
        STEP(S0, S1, oo, vb1, kd, qb, nm, oo, vc0);
        STEP(S1, S0, oo, vc1, ka, qb, cf, oo, vd0);
        asm volatile("s_waitcnt vmcnt(0)" ::: "memory");
        if (lane == 0) __hip_atomic_store((GAS u64*)(ws + WS_OFLAG) + g * 32 + head, tag, RLX_AGENT);
        SBAR();
        STEP(S0, S1, oo, vd1, ka, qb, cf, oo, va0);
        oo = pv_hi(va1, S0, oo, idx);
        const float lrest = 0.f;
        FINISH(1, (g >= 8 ? g - 8 : 0), g, qb, sB);
        asm volatile("s_waitcnt vmcnt(0)" ::: "memory");
        if (lane == 0) __hip_atomic_store((GAS u64*)(ws + WS_OFLAG) + (64 + g) * 32 + head, tag, RLX_AGENT);
    }
    STAMP(8);
    {
        const int go = 4 * jo + t;
        const lds_cptr wo = lds + L_WO + c * 16384 + (4 * hh + ((lane & 15) >> 2)) * 64 + ((lane >> 4) & 1) * 32 + (lane & 3) * 8;
        f32x16 ya = f32x16{}, yb = f32x16{};
        bf16x8 of[16];
        wait_tags(lane < 32 ? (const GAS u64*)(ws + WS_OFLAG) + go * 32 + 16 * c + (lane & 15)
                            : (const GAS u64*)(ws + WS_KVFLAG + ((4 * t + (lane & 3)) * 16 + jo) * 128), tag);
        STAMP(9);
        const float gt = __hip_atomic_load((const GAS float*)(ws + WS_G) + 32 * go + r32, RLX_AGENT);
#pragma unroll
        for (int i = 0; i < 16; ++i) of[i] = BLOAD(ro, vk, (unsigned)(go * 32 + 16 * c + i) * 1024);
        SBAR();
#pragma unroll
        for (int i = 0; i < 16; ++i) ya = MFMA(tr_frag(wo + i * 1024, wo + i * 1024 + 512), of[i], ya);
        STAMP(10);
        wait_tags((const GAS u64*)(ws + WS_OFLAG) + (64 + go) * 32 + 16 * c + (lane & 15), tag);
        STAMP(11);
#pragma unroll
        for (int i = 0; i < 16; ++i) of[i] = BLOAD(ro, vk, (unsigned)((64 + go) * 32 + 16 * c + i) * 1024);
        SBAR();
#pragma unroll
        for (int i = 0; i < 16; ++i) yb = MFMA(tr_frag(wo + 32768 + i * 1024, wo + 32768 + i * 1024 + 512), of[i], yb);
        if (bid == 0 && tid == 0) __hip_atomic_store((GAS u64*)(ws + WS_EPOCH), tag, RLX_AGENT);
        const float g1 = 1.f - gt;
        float keep[8]; f32x4 s0, s1;
#pragma unroll
        for (int r = 0; r < 8; ++r) {
            const float lo = gt * ya[r] + g1 * yb[r], hi = gt * ya[8 + r] + g1 * yb[8 + r];
            keep[r] = c ? hi : lo;
            const float snd = c ? lo : hi;
            if (r < 4) s0[r] = snd; else s1[r - 4] = snd;
        }
        *(LAS f32x4*)(lds + L_FIN + w * 2048 + lane * 16) = s0;
        *(LAS f32x4*)(lds + L_FIN + w * 2048 + 1024 + lane * 16) = s1;
        asm volatile("s_waitcnt lgkmcnt(0)\n\ts_barrier" ::: "memory");
        s0 = *(const LAS f32x4*)(lds + L_FIN + (w ^ 4) * 2048 + lane * 16);
        s1 = *(const LAS f32x4*)(lds + L_FIN + (w ^ 4) * 2048 + 1024 + lane * 16);
        auto a = __builtin_amdgcn_permlane32_swap(cvtpk(keep[0] + s0[0], keep[1] + s0[1]), cvtpk(keep[4] + s1[0], keep[5] + s1[1]), false, false);
        auto b = __builtin_amdgcn_permlane32_swap(cvtpk(keep[2] + s0[2], keep[3] + s0[3]), cvtpk(keep[6] + s1[2], keep[7] + s1[3]), false, false);
        *(GAS u32x4*)(out + (size_t)(32 * go + r32) * 1024 + 64 * po + 32 * c + 16 * hh) = (u32x4){a[0], b[0], a[1], b[1]};
    }
    STAMP(12);
}

extern "C" void kernel_launch(void* const* d_in, const int* in_sizes, int n_in, void* d_out, int out_size, void* d_ws, size_t ws_size,
                              hipStream_t stream) {
    (void)in_sizes; (void)n_in; (void)out_size; (void)ws_size;
    (void)hipFuncSetAttribute((const void*)gmix_kernel, hipFuncAttributeMaxDynamicSharedMemorySize, L_END);
    gmix_kernel<<<256, 512, L_END, stream>>>((const char*)d_in[0], (const char*)d_in[1], (const char*)d_in[2], (const char*)d_in[4],
                                             (const char*)d_in[5], (const char*)d_in[6], (char*)d_out, (char*)d_ws);
}
```

```cpp
#include <hip/hip_runtime.h>
#include <hip/hip_bf16.h>

typedef __attribute__((ext_vector_type(8))) __bf16 bf16x8;
typedef __attribute__((ext_vector_type(16))) __bf16 bf16x16;
typedef __attribute__((ext_vector_type(16))) float f32x16;
typedef __attribute__((ext_vector_type(8))) float f32x8;
typedef __attribute__((ext_vector_type(4))) float f32x4;
typedef __attribute__((ext_vector_type(4))) unsigned u32x4;
typedef __attribute__((ext_vector_type(2))) unsigned u32x2;
typedef __attribute__((ext_vector_type(2))) __bf16 bf16x2;
typedef short s16x4 __attribute__((ext_vector_type(4)));
typedef unsigned long long u64;
#define GAS __attribute__((address_space(1)))
#define LAS __attribute__((address_space(3)))
typedef LAS const char* lds_cptr;
typedef LAS char* lds_ptr;

constexpr unsigned WS_EPOCH = 0;
#define FSTR 8
constexpr unsigned WS_OFLAG = 65536;
constexpr unsigned WS_KVFLAG = 131072;
constexpr unsigned WS_KV = 1u << 20;
constexpr unsigned TS = 2176;
constexpr unsigned KV_BYTES = 2u * 32u * 64u * TS;
constexpr unsigned WS_O = 10u << 20;
constexpr unsigned O_BYTES = 4u << 20;
constexpr unsigned WS_STAMP = 16u << 20;
constexpr unsigned OOB = 0x7ff00000u;
constexpr unsigned L_W = 65536;
constexpr unsigned L_WO = 0, L_PART = 65536, L_PL = 114688, L_CNT = 117760, L_MDIAG = 118784, L_MFAR = 122880, L_Q = 126976, L_GW = 143360, L_Z = 147456, L_G = 148480, L_Q14 = 106496, L_Q15 = 148992, L_END = 157184;
constexpr float QSCALE = 0.25f * 1.4426950408889634f;
constexpr float LMAX = 1.2676506e30f, LMIN = 9.5367432e-7f;
constexpr unsigned SPIN_CAP = 1u << 17;

#define SBAR() __builtin_amdgcn_sched_barrier(0)
#define PIN(x) asm volatile("" : "+v"(x))
#define MFMA(a, b, c) __builtin_amdgcn_mfma_f32_32x32x16_bf16(a, b, c, 0, 0, 0)
#define BAR() asm volatile("s_barrier" ::: "memory")
#define CBAR() asm volatile("s_waitcnt lgkmcnt(0)\n\ts_barrier" ::: "memory")
#define DRAIN() asm volatile("s_waitcnt vmcnt(0)" ::: "memory")
#define PACEN 3
#define PACE() asm volatile("s_nop %0" :: "i"(PACEN) : "memory")
#define RLX_AGENT __ATOMIC_RELAXED, __HIP_MEMORY_SCOPE_AGENT
#define STAMP(i)

__device__ __forceinline__ void glds16(unsigned voff, const void* sbase, unsigned ldsaddr) {
    asm volatile("s_mov_b32 m0, %2\n\ts_nop 0\n\tglobal_load_lds_dwordx4 %0, %1" ::"v"(voff), "s"(sbase), "s"(ldsaddr) : "memory");
}
__device__ __forceinline__ void glds16x2(unsigned v0, unsigned v1, const void* sbase, unsigned ldsaddr) {
    asm volatile("s_mov_b32 m0, %3\n\ts_nop 0\n\tglobal_load_lds_dwordx4 %0, %2\n\tglobal_load_lds_dwordx4 %1, %2 offset:1024"
                 ::"v"(v0), "v"(v1), "s"(sbase), "s"(ldsaddr) : "memory");
}
__device__ __forceinline__ void glds16x4(unsigned v0, unsigned v1, unsigned v2, unsigned v3, const void* sbase, unsigned ldsaddr) {
    asm volatile("s_mov_b32 m0, %5\n\ts_nop 0\n\tglobal_load_lds_dwordx4 %0, %4\n\tglobal_load_lds_dwordx4 %1, %4 offset:1024\n\t"
                 "global_load_lds_dwordx4 %2, %4 offset:2048\n\tglobal_load_lds_dwordx4 %3, %4 offset:3072"
                 ::"v"(v0), "v"(v1), "v"(v2), "v"(v3), "s"(sbase), "s"(ldsaddr) : "memory");
}
__device__ __forceinline__ unsigned cvtpk(float lo, float hi) {
    typedef __attribute__((ext_vector_type(2))) float f32x2;
    return __builtin_bit_cast(unsigned, __builtin_convertvector((f32x2){lo, hi}, bf16x2));
}
__device__ __forceinline__ bf16x8 as_frag(u32x4 v) { return __builtin_bit_cast(bf16x8, v); }
__device__ __forceinline__ bf16x8 lds_frag(lds_cptr p) { return *(const LAS bf16x8*)p; }
__device__ __forceinline__ bf16x8 tr_frag(lds_cptr p0, lds_cptr p1) {
    s16x4 a = __builtin_amdgcn_ds_read_tr16_b64_v4i16((LAS s16x4*)p0);
    s16x4 b = __builtin_amdgcn_ds_read_tr16_b64_v4i16((LAS s16x4*)p1);
    return __builtin_bit_cast(bf16x8, __builtin_shufflevector(a, b, 0, 1, 2, 3, 4, 5, 6, 7));
}
__device__ __forceinline__ f32x16 pv_lo(bf16x8 v, const f32x16& p, f32x16 acc, int idx) {
    f32x8 h = __builtin_shufflevector(p, p, 0, 1, 2, 3, 4, 5, 6, 7);
    return __builtin_amdgcn_smfmac_f32_32x32x32_bf16(v, __builtin_bit_cast(bf16x16, h), acc, idx, 0, 0);
}
__device__ __forceinline__ f32x16 pv_hi(bf16x8 v, const f32x16& p, f32x16 acc, int idx) {
    f32x8 h = __builtin_shufflevector(p, p, 8, 9, 10, 11, 12, 13, 14, 15);
    return __builtin_amdgcn_smfmac_f32_32x32x32_bf16(v, __builtin_bit_cast(bf16x16, h), acc, idx, 0, 0);
}
#define MX3(a, b, c) __builtin_fmaxf(__builtin_fmaxf((a), (b)), (c))
__device__ __forceinline__ float max16(const f32x16& s) {
    float a = MX3(s[0], s[1], s[2]), b = MX3(s[3], s[4], s[5]);
    a = MX3(a, s[6], s[7]); b = MX3(b, s[8], s[9]); a = MX3(a, s[10], s[11]); b = MX3(b, s[12], s[13]);
    return MX3(a, b, __builtin_fmaxf(s[14], s[15]));
}
__device__ __forceinline__ float halfmax(float m) {
    auto r = __builtin_amdgcn_permlane32_swap(__float_as_uint(m), __float_as_uint(m), false, false);
    return __builtin_fmaxf(__uint_as_float(r[0]), __uint_as_float(r[1]));
}
__device__ __forceinline__ f32x16 lds_f32x16(lds_cptr p) {
    f32x4 a = *(const LAS f32x4*)p, b = *(const LAS f32x4*)(p + 1024), c = *(const LAS f32x4*)(p + 2048), d = *(const LAS f32x4*)(p + 3072);
    f32x8 lo = __builtin_shufflevector(a, b, 0, 1, 2, 3, 4, 5, 6, 7), hi = __builtin_shufflevector(c, d, 0, 1, 2, 3, 4, 5, 6, 7);
    return __builtin_shufflevector(lo, hi, 0, 1, 2, 3, 4, 5, 6, 7, 8, 9, 10, 11, 12, 13, 14, 15);
}
#define BLOAD(rs, vo, so) as_frag(__builtin_amdgcn_raw_buffer_load_b128(rs, vo, so, 16))
#define BSTORE(v, rs, vo, so) __builtin_amdgcn_raw_buffer_store_b128(v, rs, (vo) + (so), 0, 16)

#define POLLSLP 8
#define BSLP 44
__device__ __forceinline__ void wait_tags_end(u64 v, const GAS u64* p, u64 tag, GAS u64* pub, int lane) {
    if (pub) { DRAIN(); if (lane == 0) __hip_atomic_store(pub, tag, RLX_AGENT); }
#pragma nounroll
    for (unsigned spins = 0; spins < SPIN_CAP && !__all(v == tag); ++spins) {
        __builtin_amdgcn_s_sleep(POLLSLP);
        v = __hip_atomic_load(p, RLX_AGENT);
    }
    asm volatile("" ::: "memory");
}

__device__ __forceinline__ void wait_tags(const GAS u64* p, u64 tag, GAS u64* pub = nullptr, int lane = 0) {
    wait_tags_end(__hip_atomic_load(p, RLX_AGENT), p, tag, pub, lane);
}

__device__ __forceinline__ void cnt_arrive(lds_ptr cnt, int lane) {
    asm volatile("s_waitcnt lgkmcnt(0)" ::: "memory");
    if (lane == 0) __hip_atomic_fetch_add((LAS unsigned*)cnt, 1u, __ATOMIC_RELAXED, __HIP_MEMORY_SCOPE_WORKGROUP);
}
__device__ __forceinline__ void cnt_wait(lds_ptr cnt) {
#pragma nounroll
    for (unsigned spins = 0; spins < (1u << 22); ++spins) {
        if (__builtin_amdgcn_readfirstlane(__hip_atomic_load((LAS unsigned*)cnt, __ATOMIC_RELAXED, __HIP_MEMORY_SCOPE_WORKGROUP)) == 4) break;
        __builtin_amdgcn_s_sleep(1);
    }
    asm volatile("" ::: "memory");
}
__device__ __forceinline__ unsigned kvflag(int br, int quad, int tile, int type) {
    return WS_KVFLAG + (unsigned)((((br * 8 + quad) * 4 + (tile & 3)) * 2 + type) * 16 + (tile >> 2)) * FSTR;
}

__device__ __forceinline__ void slow_rows(__amdgpu_buffer_rsrc_t rs, unsigned sbase, int lo, int hi, int g, bool band, bf16x8 q,
                                          unsigned vk, unsigned vv0, unsigned vv1, lds_cptr lds, int lane, int idx, f32x16& acc) {
    float m = -__builtin_inff();
    for (int pass = 0; pass < 2; ++pass) {
        if (pass) { m = halfmax(m); acc = f32x16{}; }
        for (int kt = lo; kt <= hi; ++kt) {
            const unsigned so = sbase + (unsigned)kt * TS;
            const bf16x8 k = BLOAD(rs, vk, so), v0 = BLOAD(rs, vv0, so), v1 = BLOAD(rs, vv1, so);
            f32x16 c = f32x16{};
            if (band && kt == g) c = lds_f32x16(lds + L_MDIAG + lane * 16);
            if (band && kt == g - 8) c = lds_f32x16(lds + L_MFAR + lane * 16);
            f32x16 s = MFMA(k, q, c);
            if (!pass) m = __builtin_fmaxf(m, max16(s));
            else {
#pragma unroll
                for (int r = 0; r < 16; ++r) s[r] = __builtin_amdgcn_exp2f(s[r] - m);
                acc = pv_lo(v0, s, acc, idx); acc = pv_hi(v1, s, acc, idx);
            }
        }
    }
}

#define PK4(A, o) ((u32x4){cvtpk(A[o], A[o + 1]), cvtpk(A[o + 2], A[o + 3]), cvtpk(A[o + 4], A[o + 5]), cvtpk(A[o + 6], A[o + 7])})
#define PROF_ARG
#define PROF_PASS
__device__ __forceinline__ u64 load_tag(const char* ws) {
    const u64 e = *(const __attribute__((address_space(4))) u64*)(ws + WS_EPOCH);
    return e * 0x9E3779B97F4A7C15ull + 0x632BE59BD9B4E019ull;
}
template <int E>
__device__ __forceinline__ u32x4 pack_kv(int i, const f32x16& a) {
    const int s = i & 1;
    if (E == 0) return PK4(a, 8 * s);
    auto x = __builtin_amdgcn_permlane32_swap(cvtpk(a[8 * s], a[8 * s + 1]), cvtpk(a[8 * s + 4], a[8 * s + 5]), false, false);
    auto y = __builtin_amdgcn_permlane32_swap(cvtpk(a[8 * s + 2], a[8 * s + 3]), cvtpk(a[8 * s + 6], a[8 * s + 7]), false, false);
    return (u32x4){x[0], y[0], x[1], y[1]};
}
#define PUT_KV(i, v, vo) BSTORE(v, rkv, vo, E ? ((i) & 1) * 512 + ((i) >> 1) * (128 * TS) : (i) * (64 * TS))
template <int E>
__device__ __forceinline__ void project(lds_ptr lds, char* ws, __amdgpu_buffer_rsrc_t rkv, __amdgpu_buffer_rsrc_t ro, int lane, int t, int p, int j, u64 tag PROF_ARG) {
    const int r32 = lane & 31, hh = lane >> 5;
    const unsigned q4 = (lane & 15) >> 2, wcol = ((lane >> 4) & 1) * 32 + (lane & 3) * 8;
    lds_cptr xa[4];
#pragma unroll
    for (int ks = 0; ks < 4; ++ks) xa[ks] = lds + (32 * t + r32) * 128 + (((hh ^ ((r32 >> 1) & 7)) * 16) ^ (ks * 32));
    unsigned w0o = L_W + (8 * hh + q4) * 128 + (q4 >> 1) * 64 + wcol, w1o = w0o ^ 64;
    PIN(w0o); PIN(w1o);
    const lds_cptr w0 = lds + w0o, w1 = lds + w1o, wq = lds + (8 * hh + q4) * 64 + wcol;
#define XRD(n) lds_frag(xa[(n) & 3] + (((n) >> 2) & 3) * 16384)
#define WRD(b, n) tr_frag(b + ((n) & 15) * 2048, b + ((n) & 15) * 2048 + 512)
#define YOF(n) ((n) >= 28 ? 32768 + ((n) & 3) * 2048 : ((n) & 15) * 2048)
#define YRD(b, n) tr_frag(b + YOF(n), b + YOF(n) + 512)
#define QOF(br, n) (((n) >= 28 ? L_Q15 - L_Q14 : ((n) >= 24 ? 0 : (((n) >> 2) & 3) * 16384)) + (br) * 4096 + ((n) & 3) * 1024)
#define QRD(br, n) tr_frag(((n) >= 24 ? wqh : wq) + QOF(br, n), ((n) >= 24 ? wqh : wq) + QOF(br, n) + 256)
#define MM(W, X, A) (E ? MFMA(X, W, A) : MFMA(W, X, A))
    bf16x8 xf[32], f0[4], f1[4];
    f32x16 a0 = f32x16{}, a1 = f32x16{};
    unsigned gwo = (lane & 15) == ((lane >> 4) & 1) ? L_GW + t * 1024 + hh * 16 : L_Z;
    PIN(gwo);
    f32x4 zg = f32x4{};
    bf16x8 gf[4];
#define RD1(n) do { xf[n] = XRD(n); f0[(n) & 3] = WRD(w0, n); f1[(n) & 3] = WRD(w1, n); SBAR(); } while (0)
#define GRD(n) gf[(n) & 3] = lds_frag(lds + gwo + (n) * 32)
#define GMM(n) zg = __builtin_amdgcn_mfma_f32_16x16x32_bf16(gf[(n) & 3], xf[n], zg, 0, 0, 0)
#define MM1(n) do { a0 = MM(f0[(n) & 3], xf[n], a0); a1 = MM(f1[(n) & 3], xf[n], a1); SBAR(); } while (0)
#pragma unroll
    for (int s = 0; s < 8; ++s) {
        if (s == 0) CBAR(); else BAR();
        if (s == 3) { if (E) BSTORE(((u32x4){0x3f803f80u, 0x3f803f80u, 0x3f803f80u, 0x3f803f80u}), rkv,
                                  (unsigned)((hh * 32 + 4 * (p >> 1) + (r32 >> 3)) * 64 + 4 * j + t) * TS + 2048 + (lane & 7) * 16, 0); }
        if (s == 5) BSTORE(((u32x4){~0u, ~0u, ~0u, ~0u}), ro, lane * 16, (unsigned)((4 * j + t) * 32 + 2 * p) * 1024);
        SBAR();
        RD1(4 * s); RD1(4 * s + 1);
        if (s) { GMM(4 * s - 4); GMM(4 * s - 3); GMM(4 * s - 2); GMM(4 * s - 1); SBAR(); }
        MM1(4 * s); RD1(4 * s + 2); MM1(4 * s + 1); RD1(4 * s + 3); MM1(4 * s + 2); MM1(4 * s + 3);
        if (s == 0) DRAIN();
        GRD(4 * s); GRD(4 * s + 1); GRD(4 * s + 2); GRD(4 * s + 3); SBAR();
    }
    GMM(28); GMM(29); GMM(30); GMM(31); SBAR();
    const unsigned tile = 4 * j + t;
    const unsigned vo = (unsigned)(4 * (p >> 1) * 64 + tile) * TS + (E ? (r32 >> 4) * (64 * TS) + 1024 + hh * 256 + (r32 & 15) * 16 : lane * 16);
    u32x4 pk[4] = {pack_kv<E>(0, a0), pack_kv<E>(1, a0), pack_kv<E>(2, a1), pack_kv<E>(3, a1)};
    PIN(pk[0]); PIN(pk[1]); PIN(pk[2]); PIN(pk[3]);
#define PRE8 2
#define XBAR
#define STPB 1
#define PUBN 15
#pragma unroll
    for (int i = 0; i < PRE8; ++i) PUT_KV(i, pk[i], vo);
    SBAR();
    BAR();
    if (lane < 16) {
        LAS float* gp = (LAS float*)(lds + L_G + t * 128) + lane;
        gp[0] = __builtin_amdgcn_rcpf(1.f + __expf(-zg[0])); gp[16] = __builtin_amdgcn_rcpf(1.f + __expf(-zg[1]));
    }
    SBAR();
    BAR(); SBAR();
    unsigned wqo = L_Q14 + (8 * hh + q4) * 64 + wcol;
    PIN(wqo);
    const lds_cptr wqh = lds + wqo;
    bf16x8 u0 = QRD(0, 0), u1 = QRD(1, 0), y0 = WRD(w0, 0), y1 = WRD(w1, 0);
    f32x16 b0 = f32x16{}, b1 = f32x16{}, aQ0 = f32x16{}, aQ1 = f32x16{};
    SBAR();
#pragma unroll
    for (int n = 0; n < 24; ++n) {
        aQ0 = MFMA(u0, xf[n], aQ0); SBAR();
        if (n == PUBN) { DRAIN(); if (lane == 0) __hip_atomic_store((GAS u64*)(ws + kvflag(0, p >> 1, tile, E)), tag, RLX_AGENT); SBAR(); }
        if ((n & 3) == 3) {
#pragma unroll
            for (int i = PRE8 + (n >> 2) * STPB; i < PRE8 + (n >> 2) * STPB + STPB; ++i) if (i < 4) PUT_KV(i, pk[i], vo);
            SBAR(); BAR(); SBAR();
        }
        u0 = QRD(0, n + 1); SBAR();
        aQ1 = MFMA(u1, xf[n], aQ1); SBAR();
        u1 = n < 23 ? QRD(1, n + 1) : QRD(0, 25); SBAR();
        b0 = MM(y0, xf[n], b0); SBAR(); y0 = n < 23 ? YRD(w0, n + 1) : QRD(0, 26); SBAR();
        b1 = MM(y1, xf[n], b1); SBAR(); y1 = n < 23 ? YRD(w1, n + 1) : QRD(0, 27); SBAR();
    }
    aQ0 = MFMA(u0, xf[24], aQ0); SBAR(); u0 = QRD(0, 28); SBAR(); aQ0 = MFMA(u1, xf[25], aQ0); SBAR(); u1 = QRD(0, 29); SBAR();
    aQ0 = MFMA(y0, xf[26], aQ0); SBAR(); y0 = QRD(0, 30); SBAR(); aQ0 = MFMA(y1, xf[27], aQ0); SBAR(); y1 = QRD(0, 31); SBAR();
    aQ0 = MFMA(u0, xf[28], aQ0); SBAR(); aQ0 = MFMA(u1, xf[29], aQ0); SBAR(); u1 = QRD(1, 24); SBAR();
    aQ0 = MFMA(y0, xf[30], aQ0); SBAR(); y0 = YRD(w0, 24); SBAR(); aQ0 = MFMA(y1, xf[31], aQ0); SBAR(); y1 = YRD(w1, 24); SBAR();
    const lds_ptr qd = lds + L_Q + t * 1024 + lane * 16;
#pragma unroll
    for (int n = 24; n < 32; ++n) {
        aQ1 = MFMA(u1, xf[n], aQ1); SBAR(); if (n < 31) u1 = QRD(1, n + 1); if (n > 24) PACE(); SBAR();
        b0 = MM(y0, xf[n], b0); SBAR(); if (n < 31) y0 = YRD(w0, n + 1); if (n > 24) PACE(); SBAR();
        if (n == 24) {
#pragma unroll
            for (int r = 8; r < 16; ++r) aQ0[r] *= QSCALE;
            *(LAS u32x4*)(qd + 4096) = PK4(aQ0, 8);
            SBAR();
        }
        b1 = MM(y1, xf[n], b1); SBAR();
        if (n == 24) { __builtin_amdgcn_s_setprio(2); cnt_arrive(lds + L_CNT + 12, lane); STAMP(4); SBAR(); }
        if (n < 31) y1 = YRD(w1, n + 1); if (n > 24) PACE(); SBAR();
    }
#pragma unroll
    for (int r = 0; r < 8; ++r) aQ0[r] *= QSCALE;
#pragma unroll
    for (int r = 0; r < 16; ++r) aQ1[r] *= QSCALE;
    *(LAS u32x4*)qd = PK4(aQ0, 0); *(LAS u32x4*)(qd + 8192) = PK4(aQ1, 0); *(LAS u32x4*)(qd + 12288) = PK4(aQ1, 8);
    cnt_arrive(lds + L_CNT + 8, lane);
    const unsigned vo2 = vo + 32 * 64 * TS;
#define YSLP 12
    PUT_KV(0, pack_kv<E>(0, b0), vo2); if (YSLP) __builtin_amdgcn_s_sleep(YSLP); PUT_KV(1, pack_kv<E>(1, b0), vo2); if (YSLP) __builtin_amdgcn_s_sleep(YSLP);
    PUT_KV(2, pack_kv<E>(2, b1), vo2); if (YSLP) __builtin_amdgcn_s_sleep(YSLP); PUT_KV(3, pack_kv<E>(3, b1), vo2);
    DRAIN();
    if (lane == 0) __hip_atomic_store((GAS u64*)(ws + kvflag(1, p >> 1, tile, E)), tag, RLX_AGENT);
    STAMP(19);
}

#define WOIN 2
#define CODE_LINES 256

struct Att {
    lds_ptr lds; char* ws; __amdgpu_buffer_rsrc_t rkv, ro;
    int lane, t, p, g, head; u64 tag; unsigned vk, vv0, vv1;
};
#define IDX 0xdddddddd
#define LD3(K, V0, V1, rs, so) do { K = BLOAD(rs, a.vk, so); V0 = BLOAD(rs, a.vv0, so); V1 = BLOAD(rs, a.vv1, so); } while (0)
#define EX(X, r) X[r] = __builtin_amdgcn_exp2f(X[r])
#define STEP(X, Y, OP, VP, KN, QN, CN, O, V0, F1, F2) do { \
        EX(X, 0); EX(X, 1); SBAR(); OP = pv_hi(VP, Y, OP, IDX); SBAR(); F1; SBAR(); EX(X, 2); EX(X, 3); EX(X, 4); SBAR(); Y = MFMA(KN, QN, CN); SBAR(); \
        EX(X, 5); EX(X, 6); EX(X, 7); SBAR(); EX(X, 8); EX(X, 9); SBAR(); O = pv_lo(V0, X, O, IDX); SBAR(); F2; SBAR(); \
        EX(X, 10); EX(X, 11); EX(X, 12); EX(X, 13); EX(X, 14); EX(X, 15); SBAR(); } while (0)

__device__ __forceinline__ f32x16 lds_zero16(lds_cptr lds, int lane) {
    const volatile LAS f32x4* z = (const volatile LAS f32x4*)(lds + L_Z) + lane;
    const f32x4 a = *z, b = *z, c = *z, d = *z;
    f32x8 lo = __builtin_shufflevector(a, b, 0, 1, 2, 3, 4, 5, 6, 7), hi = __builtin_shufflevector(c, d, 0, 1, 2, 3, 4, 5, 6, 7);
    return __builtin_shufflevector(lo, hi, 0, 1, 2, 3, 4, 5, 6, 7, 8, 9, 10, 11, 12, 13, 14, 15);
}

template <int C, int BR>
__device__ __forceinline__ GAS u64* finish(const Att& a, f32x16 oo, float lrest, bf16x8 qa) {
    const float gt = *(const LAS float*)(a.lds + L_G + a.t * 128 + (a.lane & 31) * 4);
    auto lsw = __builtin_amdgcn_permlane32_swap(__float_as_uint(oo[8]), __float_as_uint(oo[8]), false, false);
    float l = __uint_as_float(lsw[0]) + lrest, z = 0.f;
#pragma unroll
    for (int r = 0; r < 8; ++r) z = __builtin_fmaf(oo[r], 0.f, z);
    if (__builtin_expect(__any(!(l + z < LMAX && l > LMIN)), 0)) {
        if (!BR) { wait_tags((const GAS u64*)(a.ws + kvflag(0, a.p >> 1, 0, 0) + a.lane * FSTR), a.tag); wait_tags((const GAS u64*)(a.ws + kvflag(0, a.p >> 1, 2, 0) + a.lane * FSTR), a.tag); }
        slow_rows(a.rkv, (unsigned)((BR * 32 + a.head) * 64) * TS, BR ? (a.g >= 8 ? a.g - 8 : 0) : 0, BR ? a.g : 63, a.g, BR, qa, a.vk, a.vv0, a.vv1, a.lds, a.lane, IDX, oo);
        auto l2 = __builtin_amdgcn_permlane32_swap(__float_as_uint(oo[8]), __float_as_uint(oo[8]), false, false);
        l = __uint_as_float(l2[0]);
    }
    const float il = __builtin_amdgcn_rcpf(l) * (BR ? 1.f - gt : gt);
    u32x4 v = (u32x4){cvtpk(oo[0] * il, oo[1] * il), cvtpk(oo[2] * il, oo[3] * il), cvtpk(oo[4] * il, oo[5] * il), cvtpk(oo[6] * il, oo[7] * il)};
    if (!C && !BR) v = __builtin_elementwise_min(v, (u32x4){~1u, ~1u, ~1u, ~1u});
    BSTORE(v, a.ro, a.vk, (unsigned)((BR * 64 + a.g) * 32 + a.head) * 1024);
    return (GAS u64*)(a.ws + WS_OFLAG) + ((BR * 64 + a.g) * 2 + C) * 16 + a.p;
}

template <int C>
__device__ __forceinline__ GAS u64* attn_full(const Att& a, f32x16 oo, f32x16 o1, f32x16 o2, f32x16 o3, f32x16 S1, __amdgpu_buffer_rsrc_t rwo, unsigned vwo, unsigned wi PROF_ARG) {
    const lds_ptr lds = a.lds; const int t = a.t, lane = a.lane, w = 4 * C + t;
    const f32x16 zc = f32x16{};
    bf16x8 ka, va0, va1, kb, vb0, vb1, kc, vc0, vc1, kd, vd0, vd1;
    const unsigned sBeg = (unsigned)(a.head * 64 + t) * TS, sEnd = sBeg + 64 * TS;
    unsigned so = (unsigned)(a.head * 64 + a.g) * TS;
#define NEXT_A() asm volatile("s_addk_i32 %0, %1" : "+s"(so) : "i"(4 * TS) : "scc")
#define WRAP_A() asm volatile("s_cmp_eq_u32 %0, %1\n\ts_cselect_b32 %0, %2, %0" : "+s"(so) : "s"(sEnd), "s"(sBeg) : "scc")
    LD3(ka, va0, va1, a.rkv, so); NEXT_A(); WRAP_A(); SBAR(); LD3(kb, vb0, vb1, a.rkv, so); NEXT_A(); WRAP_A(); SBAR();
    LD3(kc, vc0, vc1, a.rkv, so); NEXT_A(); WRAP_A(); SBAR();
    if (C) { cnt_wait(lds + L_CNT + 12); STAMP(4); }
    const lds_cptr qp = lds + L_Q + C * 4096 + lane * 16;
    bf16x8 qa = lds_frag(qp + t * 1024), q1 = lds_frag(qp + ((t + 1) & 3) * 1024), q2 = lds_frag(qp + ((t + 2) & 3) * 1024), q3 = lds_frag(qp + ((t + 3) & 3) * 1024);
    vd1 = lds_frag(lds + L_Z + lane * 16);
    PIN(q1); PIN(q2); PIN(q3); PIN(vd1); PIN(oo); PIN(o1); PIN(o2); PIN(o3); PIN(S1);
    __builtin_amdgcn_s_waitcnt(0x0F76);
    f32x16 S0 = MFMA(ka, qa, zc);
    PIN(S0);
#pragma unroll
    for (int i = 0; i < 12; ++i) __builtin_amdgcn_s_setprio(C);
    STAMP(3);
    SBAR();
    unsigned wso = 0;
#define NEXT_AW() do { if (C) asm volatile("s_addk_i32 %0, %3\n\ts_lshl_b32 %1, %2, 4" : "+s"(so), "=s"(wso) : "s"(wi), "i"(4 * TS) : "scc"); else NEXT_A(); } while (0)
#define WO_DMA() do { if (C) __builtin_amdgcn_raw_ptr_buffer_load_lds(rwo, (LAS void*)(lds + L_WO + t * 16384 + wi), 16, vwo, wso, 0, 0); } while (0)
#define WO_NEXT() do { if (C) asm volatile("s_addk_i32 %0, 0x400\n\ts_and_b32 %0, %0, 0x3fff" : "+s"(wi) :: "scc"); } while (0)
#define TILE(K, V0, V1, KN, LK, LV0, LV1, NW) do { \
        STEP(S0, S1, o3, LV1, K, q1, zc, oo, V0, LK = BLOAD(a.rkv, a.vk, so), LV0 = BLOAD(a.rkv, a.vv0, so)); \
        STEP(S1, S0, oo, V1, K, q2, zc, o1, V0, LV1 = BLOAD(a.rkv, a.vv1, so), NEXT_AW()); \
        STEP(S0, S1, o1, V1, K, q3, zc, o2, V0, WRAP_A(), WO_DMA()); \
        STEP(S1, S0, o2, V1, KN, qa, zc, o3, V0, __builtin_amdgcn_s_waitcnt(0x0F70 | (C ? NW : 6)), WO_NEXT()); } while (0)
#pragma nounroll
    for (int it = 0; it < 4; ++it) {
        TILE(ka, va0, va1, kb, kd, vd0, vd1, 7);
        TILE(kb, vb0, vb1, kc, ka, va0, va1, 8);
        TILE(kc, vc0, vc1, kd, kb, vb0, vb1, 9);
        TILE(kd, vd0, vd1, ka, kc, vc0, vc1, 9);
    }
    o3 = pv_hi(vd1, S1, o3, IDX);
    STAMP(6);
    if (C) cnt_wait(lds + L_CNT + 8); else __builtin_amdgcn_s_setprio(2);
#define PUT(O, sl) do { lds_ptr d_ = lds + L_PART + (w * 3 + sl) * 2048 + lane * 16; \
        *(LAS f32x4*)d_ = __builtin_shufflevector(O, O, 0, 1, 2, 3); *(LAS f32x4*)(d_ + 1024) = __builtin_shufflevector(O, O, 4, 5, 6, 7); } while (0)
    PUT(o1, 0); PUT(o2, 1); PUT(o3, 2);
    if (lane < 32) { LAS float* lp = (LAS float*)(lds + L_PL + w * 384) + lane; lp[0] = o1[8]; lp[32] = o2[8]; lp[64] = o3[8]; }
    if (C) { DRAIN(); cnt_arrive(lds + L_CNT, lane); cnt_wait(lds + L_CNT); } else CBAR();
    float lrest = 0.f;
#pragma unroll
    for (int s = 0; s < 3; ++s) {
        const int src = (C * 4 + ((t - 1 - s) & 3)) * 3 + s;
        const lds_cptr d = lds + L_PART + src * 2048 + lane * 16;
        const f32x4 x = *(const LAS f32x4*)d, y = *(const LAS f32x4*)(d + 1024);
#pragma unroll
        for (int r = 0; r < 4; ++r) { oo[r] += x[r]; oo[4 + r] += y[r]; }
        const float lp = *(const LAS float*)(lds + L_PL + src * 128 + (lane & 31) * 4);
        lrest = s ? lrest + lp : lp;
    }
    return finish<C, 0>(a, oo, lrest, qa);
}

template <int C>
__device__ __forceinline__ GAS u64* attn_window(const Att& a, GAS u64* pub PROF_ARG) {
    const lds_ptr lds = a.lds; const int t = a.t, lane = a.lane, g = a.g;
    const f32x16 zc = f32x16{};
    { const int i = lane & 15, kt = g - (i < 9 ? i : 0);
      wait_tags((const GAS u64*)(a.ws + kvflag(1, a.p >> 1, kt >= 0 ? kt : 0, (lane >> 4) & 1)), a.tag, pub, lane); }
    STAMP(25);
    bf16x8 k[9], v0[9], v1[9];
    const unsigned sB = (unsigned)((32 + a.head) * 64) * TS;
#define LDB(i) do { const int kt_ = g - (i); \
        const __amdgpu_buffer_rsrc_t rb_ = __builtin_amdgcn_make_buffer_rsrc(a.ws + WS_KV, 0, kt_ >= 0 ? KV_BYTES : 0u, 0x00020000); \
        LD3(k[i], v0[i], v1[i], rb_, sB + (unsigned)(kt_ >= 0 ? kt_ : 0) * TS); } while (0)
    LDB(0); SBAR(); LDB(1); SBAR(); LDB(2); SBAR();
    const bf16x8 qa = lds_frag(lds + L_Q + 8192 + C * 4096 + t * 1024 + lane * 16);
    const f32x16 cd = lds_f32x16(lds + L_MDIAG + lane * 16), cf = lds_f32x16(lds + L_MFAR + lane * 16);
    f32x16 oo = lds_zero16(lds, lane);
    f32x16 S0 = MFMA(k[0], qa, cd), S1 = MFMA(k[1], qa, zc);
    SBAR(); LDB(3); SBAR();
#pragma unroll
    for (int r = 0; r < 10; ++r) { EX(S0, r); SBAR(); }
    oo = pv_lo(v0[0], S0, oo, IDX); SBAR();
#pragma unroll
    for (int r = 10; r < 16; ++r) EX(S0, r);
    SBAR();
    STEP(S1, S0, oo, v1[0], k[2], qa, zc, oo, v0[1], LDB(4), );
    STEP(S0, S1, oo, v1[1], k[3], qa, zc, oo, v0[2], LDB(5), );
    STEP(S1, S0, oo, v1[2], k[4], qa, zc, oo, v0[3], LDB(6), );
    STEP(S0, S1, oo, v1[3], k[5], qa, zc, oo, v0[4], LDB(7), );
    STEP(S1, S0, oo, v1[4], k[6], qa, zc, oo, v0[5], LDB(8), );
    STEP(S0, S1, oo, v1[5], k[7], qa, zc, oo, v0[6], , );
    STEP(S1, S0, oo, v1[6], k[8], qa, cf, oo, v0[7], , );
    STEP(S0, S1, oo, v1[7], k[8], qa, cf, oo, v0[8], , );
    oo = pv_hi(v1[8], S0, oo, IDX);
    STAMP(7);
    return finish<C, 1>(a, oo, 0.f, qa);
}

__global__ void __launch_bounds__(512, 2)
gmix_kernel(const char* x, const char* wA, const char* woA, const char* wB, const char* woB, const char* gw, char* out, char* ws) {
    extern __shared__ __attribute__((aligned(16))) char smem[];
    const lds_ptr lds = (lds_ptr)smem;
    const unsigned lds0 = (unsigned)(uintptr_t)lds;
    const int tid = threadIdx.x, lane = tid & 63, r32 = lane & 31, hh = lane >> 5;
    const int w = __builtin_amdgcn_readfirstlane(tid >> 6);
    const int bid = blockIdx.x, xcd = bid & 7, rr = bid >> 3;
    const int p = 4 * (xcd & 3) + (rr & 3), j = 8 * (xcd >> 2) + (rr >> 2);
    const int jo = 2 * xcd + (rr >> 4), po = rr & 15;
    const int t = w & 3, c = w >> 2;
    const int e = p & 1;
    STAMP(0);
    u64 pc; asm volatile("s_getpc_b64 %0" : "=s"(pc));
    unsigned rot = (j + 2 * (p & 3)) & 7;
    unsigned vx0 = (128 * j + 32 * t + (lane >> 3)) * 1024 + (((lane & 7) ^ (lane >> 4)) * 16), vx1 = (vx0 ^ 64) + 7168, vx2 = vx0 + 14336, vx3 = vx1 + 14336;
    unsigned vw0 = (16 * t + (lane >> 3)) * 3072 + 1024 + 1024 * e + 128 * (p >> 1) + (((lane & 7) ^ (((lane >> 4) & 1) << 2)) * 16), vw1 = vw0 + 8 * 3072 - 1024;
    unsigned mx = lds0 + t * 4096, mw = lds0 + L_W + t * 2048, ow = rot * (64 * 3072);
    asm volatile("" : "+v"(vx0), "+v"(vx1), "+v"(vx2), "+v"(vx3), "+v"(vw0), "+v"(vw1));
    asm volatile("" : "+s"(mx), "+s"(mw), "+s"(rot), "+s"(ow));
    SBAR();
    asm volatile("" : "+s"(x), "+s"(wA), "+s"(woA), "+s"(wB), "+s"(woB), "+s"(gw), "+s"(out), "+s"(ws));
#define MK_ATT(C) Att a; a.lds = lds; a.ws = ws; a.lane = lane; a.t = t; a.p = p; a.g = 4 * j + t; a.head = 2 * p + C; \
    a.rkv = __builtin_amdgcn_make_buffer_rsrc(ws + WS_KV, 0, KV_BYTES, 0x00020000); a.ro = __builtin_amdgcn_make_buffer_rsrc(ws + WS_O, 0, O_BYTES, 0x00020000); \
    a.vk = lane * 16; a.vv0 = r32 < 16 ? 1024 + hh * 256 + r32 * 16 : (r32 == 16 ? 2048 : OOB); a.vv1 = r32 < 16 ? a.vv0 + 512 : a.vv0
    if (c) {
#define DMA1(s) do { const unsigned ks_ = ((s) + rot) & 7; \
        glds16x4(vx0, vx1, vx2, vx3, x + ks_ * 128, mx + ((s) & 3) * 16384); glds16x2(vw0, vw1, wA + ks_ * (64 * 3072), mw + ((s) & 3) * 8192); } while (0)
#define DMA2(s) do { const unsigned ks_ = (((s) + rot) & 7) * (64 * 3072); \
        glds16x2(vq0, vq1, wq + ks_, mq + ((s) == 14 ? L_Q14 : ((s) & 3) * 16384)); glds16x2(vw0, vw1, wB + ks_, mw + ((s) & 3) * 8192); SBAR(); } while (0)
#define LBAR(N) asm volatile("s_waitcnt vmcnt(" #N ")\n\ts_barrier" ::: "memory")
        STAMP(20);
        glds16x4(vx0, vx1, vx2, vx3, x + rot * 128, mx); glds16x2(vw0, vw1, wA + ow, mw);
        SBAR();
        DMA1(1);
        STAMP(22);
        SBAR();
#pragma nounroll
        for (int s = 2; s < 8; ++s) { LBAR(6); DMA1(s);
        }
        const unsigned vq0 = (32 * (t & 1) + (lane >> 2)) * 3072 + 64 * p + (lane & 3) * 16, vq1 = vq0 + 16 * 3072 - 1024, mq = lds0 + t * 2048;
        const char* wq = (t >> 1) ? wB : wA;
        LBAR(6); STAMP(14); DMA2(8);
        LBAR(4); STAMP(15);
        BAR();
        DMA2(9);
        MK_ATT(1);
        a.tag = load_tag(ws);
        asm volatile("" : "+s"(a.tag)); PIN(a.vk); PIN(a.vv0); PIN(a.vv1);
        const GAS u64* fp = (const GAS u64*)(ws + kvflag(0, p >> 1, t, 0) + r32 * FSTR);
        PIN(fp);
        LBAR(4); STAMP(16); DMA2(10);
        const unsigned k7 = ((7 + rot) & 7) * (64 * 3072), sx = mq + L_Q15, sw = mw + 32768;
        LBAR(4); STAMP(17); glds16(vq0, wq + k7, sx); DMA2(11);
        f32x16 oo = f32x16{}, o1 = f32x16{}; PIN(oo); PIN(o1);
        LBAR(5); STAMP(18); glds16(vw0, wB + k7, sw); DMA2(12);
        f32x16 o2 = f32x16{}, o3 = f32x16{}; PIN(o2); PIN(o3);
        LBAR(5); STAMP(19); glds16(vq1, wq + k7 + 1024, sx + 1024); DMA2(13);
        f32x16 S1 = f32x16{}; PIN(S1);
        const __amdgpu_buffer_rsrc_t rwo = __builtin_amdgcn_make_buffer_rsrc((void*)((t >> 1) ? woB : woA), 0, 512 * 1024, 0x00020000);
        unsigned vwo = ((t & 1) * 256 + (lane >> 2)) * 1024 + 64 * po + (lane & 3) * 16; PIN(vwo);
        LBAR(5); STAMP(21); glds16(vw1, wB + k7 + 1024, sw + 1024); DMA2(14);
        __builtin_amdgcn_s_setprio(1);
        LBAR(5); STAMP(24);
        LBAR(0); STAMP(29);
        wait_tags(fp, a.tag);
        STAMP(5);
        GAS u64* pub = attn_full<1>(a, oo, o1, o2, o3, S1, rwo, vwo, 4096 * (((rr >> 4) << 1) | (po & 1)) PROF_PASS);
        STAMP(23);
        pub = attn_window<1>(a, pub PROF_PASS);
        STAMP(8);
        const int go = 4 * jo + t;
        const lds_cptr wo = lds + L_WO + (4 * hh + ((lane & 15) >> 2)) * 64 + ((lane >> 4) & 1) * 32 + (lane & 3) * 8;
        const GAS u64* of_ = (const GAS u64*)(ws + WS_OFLAG);
        wait_tags(of_ + ((lane & 16) ? go * 32 + 16 : (64 + go) * 32) + (lane & 15), a.tag, pub, lane);
        STAMP(9);
#define OBR(n) ((n) >> 4 == 1 ? 0 : 1)
#define OHD(n) (2 * ((n) & 15) + ((n) >= 16))
#define OLD(n) BLOAD(a.ro, a.vk, (unsigned)((OBR(n) * 64 + go) * 32 + OHD(n)) * 1024)
#define OWF(n) tr_frag(wo + OBR(n) * 32768 + OHD(n) * 1024, wo + OBR(n) * 32768 + OHD(n) * 1024 + 512)
#define OIN 7
        bf16x8 oe[48];
#pragma unroll
        for (int n = 0; n < 48; ++n) {
            if (n == 32) wait_tags(of_ + (64 + go) * 32 + 16 + (lane & 15), a.tag);
            oe[n] = OLD(n); SBAR();
            if (n >= OIN - 1) __builtin_amdgcn_s_waitcnt(0x0F70 | (OIN - 1));
            SBAR();
        }
        bf16x8 wn[4];
#pragma unroll
        for (int n = 0; n < 4; ++n) { wn[n] = OWF(n); PIN(wn[n]); }
        __builtin_amdgcn_s_waitcnt(0x0F70);
#define NPRE 0
#define NMID 24
        f32x16 ya = f32x16{};
        bf16x8 wf[16], of[16];
#define OMM(n) do { ya = MFMA(wn[(n) & 3], oe[n], ya); SBAR(); if ((n) + 4 < 48) wn[(n) & 3] = OWF((n) + 4); \
            if ((n) >= 32) wf[(n) - 32] = tr_frag(wo + 2 * ((n) - 32) * 1024, wo + 2 * ((n) - 32) * 1024 + 512); SBAR(); } while (0)
#pragma unroll
        for (int n = 0; n < NPRE; ++n) { OMM(n); PACE(); }
        STAMP(10);
        BAR();
        BAR();
#pragma unroll
        for (int n = NPRE; n < NMID; ++n) OMM(n);
        STAMP(30);
        BAR();
        STAMP(11);
#define GRP_BAD(q) ({ unsigned mx_ = 0; _Pragma("unroll") for (int i = 4 * (q); i < 4 * (q) + 4; ++i) { const u32x4 u_ = __builtin_bit_cast(u32x4, of[i]); \
            mx_ = __builtin_elementwise_max(__builtin_elementwise_max(mx_, u_[0]), u_[1]); mx_ = __builtin_elementwise_max(__builtin_elementwise_max(mx_, u_[2]), u_[3]); } \
            __any(mx_ == ~0u); })
#define GRP_MM(q) do { _Pragma("unroll") for (int i = 4 * (q); i < 4 * (q) + 4; ++i) ya = MFMA(wf[i], of[i], ya); } while (0)
#define GRP_LD(q) do { _Pragma("unroll") for (int i = 4 * (q); i < 4 * (q) + 4; ++i) of[i] = BLOAD(a.ro, a.vk, (unsigned)(go * 32 + 2 * i) * 1024); } while (0)
#pragma unroll
        for (int n = NMID; n < 48; ++n) { if (n - NMID < 16) { of[n - NMID] = BLOAD(a.ro, a.vk, (unsigned)(go * 32 + 2 * (n - NMID)) * 1024); SBAR(); } OMM(n); }
        STAMP(31);
        int done = 0;
        if (!GRP_BAD(0)) { GRP_MM(0); done = 1; if (!GRP_BAD(1)) { GRP_MM(1); done = 2; if (!GRP_BAD(2)) { GRP_MM(2); done = 3; if (!GRP_BAD(3)) { GRP_MM(3); done = 4; } } } }
        if (done < 4) {
#pragma nounroll
            for (unsigned spins = 0; spins < SPIN_CAP; ++spins) {
                const bool b0 = GRP_BAD(0), b1 = GRP_BAD(1), b2 = GRP_BAD(2), b3 = GRP_BAD(3);
                if (!(b0 || b1 || b2 || b3)) break;
                asm volatile("" ::: "memory");
                if (b0) GRP_LD(0); if (b1) GRP_LD(1); if (b2) GRP_LD(2); if (b3) GRP_LD(3);
            }
            if (done < 1) GRP_MM(0); if (done < 2) GRP_MM(1); if (done < 3) GRP_MM(2); GRP_MM(3);
        }
#pragma unroll
        for (int s = 0; s < 2; ++s) {
            auto u = __builtin_amdgcn_permlane32_swap(cvtpk(ya[8 * s], ya[8 * s + 1]), cvtpk(ya[8 * s + 4], ya[8 * s + 5]), false, false);
            auto v = __builtin_amdgcn_permlane32_swap(cvtpk(ya[8 * s + 2], ya[8 * s + 3]), cvtpk(ya[8 * s + 6], ya[8 * s + 7]), false, false);
            *(GAS u32x4*)(out + (size_t)(32 * go + r32) * 1024 + 64 * po + 32 * s + 16 * hh) = (u32x4){u[0], v[0], u[1], v[1]};
        }
        if (bid == 0 && tid == 256) __hip_atomic_store((GAS u64*)(ws + WS_EPOCH), a.tag, RLX_AGENT);
        STAMP(12);
    } else {
        unsigned pf0 = 0;
        {
            const unsigned ln = rr * 4 + t;
            asm volatile("s_load_dword %0, %1, %2\n\ts_load_dword %0, %1, %3" : "+s"(pf0) : "s"(pc & ~127ull), "s"(ln * 128), "s"((ln + CODE_LINES - 128) * 128) : "memory");
        }
        glds16((((lane >> 3) + rot) & 7) * 128 + (lane & 7) * 16, gw, lds0 + L_GW + t * 1024);
#pragma unroll
        for (int m = 0; m < 2; ++m) {
            f32x4 mk;
#pragma unroll
            for (int i = 0; i < 4; ++i) { const bool le = (i + 8 * t + 4 * hh) <= r32; mk[i] = (le != (m != 0)) ? 0.f : -__builtin_inff(); }
            *(LAS f32x4*)(lds + L_MDIAG + m * 4096 + t * 1024 + lane * 16) = mk;
        }
        if (tid < 4) *(LAS unsigned*)(lds + L_CNT + tid * 4) = 0;
        *(LAS unsigned*)(lds + L_Z + tid * 4) = 0;
        u64 tag = load_tag(ws);
        asm volatile("s_waitcnt lgkmcnt(0)" : "+s"(pf0), "+s"(tag)::"memory");
        STAMP(21);
        {
            const __amdgpu_buffer_rsrc_t rkv = __builtin_amdgcn_make_buffer_rsrc(ws + WS_KV, 0, KV_BYTES, 0x00020000);
            const __amdgpu_buffer_rsrc_t ro = __builtin_amdgcn_make_buffer_rsrc(ws + WS_O, 0, O_BYTES, 0x00020000);
            if (e) project<1>(lds, ws, rkv, ro, lane, t, p, j, tag PROF_PASS); else project<0>(lds, ws, rkv, ro, lane, t, p, j, tag PROF_PASS);
        }
        cnt_wait(lds + L_CNT + 8);
        __builtin_amdgcn_s_sleep(BSLP);
        STAMP(24);
        MK_ATT(0);
        a.tag = tag;
        GAS u64* pub = attn_window<0>(a, nullptr PROF_PASS);
        STAMP(23);
        wait_tags((const GAS u64*)(ws + kvflag(0, p >> 1, t, 0) + r32 * FSTR), tag, pub, lane);
        STAMP(5);
        attn_full<0>(a, lds_zero16(lds, lane), lds_zero16(lds, lane), lds_zero16(lds, lane), lds_zero16(lds, lane), lds_zero16(lds, lane), a.rkv, 0, 0 PROF_PASS);
        BAR();
        STAMP(8);
        DRAIN(); BAR();
    }
}

extern "C" void kernel_launch(void* const* d_in, const int* in_sizes, int n_in, void* d_out, int out_size, void* d_ws, size_t ws_size,
                              hipStream_t stream) {
    (void)in_sizes; (void)n_in; (void)out_size; (void)ws_size;
    (void)hipFuncSetAttribute((const void*)gmix_kernel, hipFuncAttributeMaxDynamicSharedMemorySize, L_END);
    gmix_kernel<<<256, 512, L_END, stream>>>((const char*)d_in[0], (const char*)d_in[1], (const char*)d_in[2], (const char*)d_in[4],
                                             (const char*)d_in[5], (const char*)d_in[6], (char*)d_out, (char*)d_ws);
}
```

```cpp
#include <hip/hip_runtime.h>
#include <hip/hip_bf16.h>

typedef __attribute__((ext_vector_type(8))) __bf16 bf16x8;
typedef __attribute__((ext_vector_type(16))) __bf16 bf16x16;
typedef __attribute__((ext_vector_type(16))) float f32x16;
typedef __attribute__((ext_vector_type(8))) float f32x8;
typedef __attribute__((ext_vector_type(4))) float f32x4;
typedef __attribute__((ext_vector_type(4))) unsigned u32x4;
typedef __attribute__((ext_vector_type(2))) unsigned u32x2;
typedef short s16x4 __attribute__((ext_vector_type(4)));
typedef unsigned long long u64;
#define GAS __attribute__((address_space(1)))
#define LAS __attribute__((address_space(3)))
typedef LAS const char* lds_cptr;
typedef LAS char* lds_ptr;

constexpr unsigned WS_EPOCH = 0;
#define FSTR 8
constexpr unsigned WS_G = 4096;
constexpr unsigned WS_OFLAG = 65536;
constexpr unsigned WS_KVFLAG = 131072;
constexpr unsigned WS_KV = 1u << 20;
constexpr unsigned TS = 2176;
constexpr unsigned KV_BYTES = 2u * 32u * 64u * TS;
constexpr unsigned WS_O = 10u << 20;
constexpr unsigned O_BYTES = 4u << 20;
constexpr unsigned WS_STAMP = 16u << 20;
constexpr unsigned OOB = 0x7ff00000u;
constexpr unsigned L_W = 65536;
constexpr unsigned L_WO = 0, L_PART = 65536, L_PL = 114688, L_CNT = 117760, L_MDIAG = 118784, L_MFAR = 122880, L_Q = 126976, L_END = 143360;
constexpr float QSCALE = 0.25f * 1.4426950408889634f;
constexpr float LMAX = 1.2676506e30f, LMIN = 9.5367432e-7f;
constexpr unsigned SPIN_CAP = 1u << 18;

#define SBAR() __builtin_amdgcn_sched_barrier(0)
#define PIN(x) asm volatile("" : "+v"(x))
#define MFMA(a, b, c) __builtin_amdgcn_mfma_f32_32x32x16_bf16(a, b, c, 0, 0, 0)
#define BAR() asm volatile("s_barrier" ::: "memory")
#define CBAR() asm volatile("s_waitcnt lgkmcnt(0)\n\ts_barrier" ::: "memory")
#define DRAIN() asm volatile("s_waitcnt vmcnt(0)" ::: "memory")
#define RLX_AGENT __ATOMIC_RELAXED, __HIP_MEMORY_SCOPE_AGENT
#define STAMP(i)

__device__ __forceinline__ void glds16(unsigned voff, const void* sbase, unsigned ldsaddr) {
    asm volatile("s_mov_b32 m0, %2\n\ts_nop 0\n\tglobal_load_lds_dwordx4 %0, %1" ::"v"(voff), "s"(sbase), "s"(ldsaddr) : "memory");
}
__device__ __forceinline__ void glds16x2(unsigned v0, unsigned v1, const void* sbase, unsigned ldsaddr) {
    asm volatile("s_mov_b32 m0, %3\n\ts_nop 0\n\tglobal_load_lds_dwordx4 %0, %2\n\tglobal_load_lds_dwordx4 %1, %2 offset:1024"
                 ::"v"(v0), "v"(v1), "s"(sbase), "s"(ldsaddr) : "memory");
}
__device__ __forceinline__ void glds16x4(unsigned v0, unsigned v1, unsigned v2, unsigned v3, const void* sbase, unsigned ldsaddr) {
    asm volatile("s_mov_b32 m0, %5\n\ts_nop 0\n\tglobal_load_lds_dwordx4 %0, %4\n\tglobal_load_lds_dwordx4 %1, %4 offset:1024\n\t"
                 "global_load_lds_dwordx4 %2, %4 offset:2048\n\tglobal_load_lds_dwordx4 %3, %4 offset:3072"
                 ::"v"(v0), "v"(v1), "v"(v2), "v"(v3), "s"(sbase), "s"(ldsaddr) : "memory");
}
__device__ __forceinline__ unsigned cvtpk(float lo, float hi) {
    typedef __attribute__((ext_vector_type(2))) __bf16 bf16x2;
    typedef __attribute__((ext_vector_type(2))) float f32x2;
    return __builtin_bit_cast(unsigned, __builtin_convertvector((f32x2){lo, hi}, bf16x2));
}
__device__ __forceinline__ bf16x8 as_frag(u32x4 v) { return __builtin_bit_cast(bf16x8, v); }
__device__ __forceinline__ bf16x8 lds_frag(lds_cptr p) { return *(const LAS bf16x8*)p; }
__device__ __forceinline__ bf16x8 tr_frag(lds_cptr p0, lds_cptr p1) {
    s16x4 a = __builtin_amdgcn_ds_read_tr16_b64_v4i16((LAS s16x4*)p0);
    s16x4 b = __builtin_amdgcn_ds_read_tr16_b64_v4i16((LAS s16x4*)p1);
    return __builtin_bit_cast(bf16x8, __builtin_shufflevector(a, b, 0, 1, 2, 3, 4, 5, 6, 7));
}
__device__ __forceinline__ f32x16 pv_lo(bf16x8 v, const f32x16& p, f32x16 acc, int idx) {
    f32x8 h = __builtin_shufflevector(p, p, 0, 1, 2, 3, 4, 5, 6, 7);
    return __builtin_amdgcn_smfmac_f32_32x32x32_bf16(v, __builtin_bit_cast(bf16x16, h), acc, idx, 0, 0);
}
__device__ __forceinline__ f32x16 pv_hi(bf16x8 v, const f32x16& p, f32x16 acc, int idx) {
    f32x8 h = __builtin_shufflevector(p, p, 8, 9, 10, 11, 12, 13, 14, 15);
    return __builtin_amdgcn_smfmac_f32_32x32x32_bf16(v, __builtin_bit_cast(bf16x16, h), acc, idx, 0, 0);
}
#define MX3(a, b, c) __builtin_fmaxf(__builtin_fmaxf((a), (b)), (c))
__device__ __forceinline__ float max16(const f32x16& s) {
    float a = MX3(s[0], s[1], s[2]), b = MX3(s[3], s[4], s[5]);
    a = MX3(a, s[6], s[7]); b = MX3(b, s[8], s[9]); a = MX3(a, s[10], s[11]); b = MX3(b, s[12], s[13]);
    return MX3(a, b, __builtin_fmaxf(s[14], s[15]));
}
__device__ __forceinline__ float halfmax(float m) {
    auto r = __builtin_amdgcn_permlane32_swap(__float_as_uint(m), __float_as_uint(m), false, false);
    return __builtin_fmaxf(__uint_as_float(r[0]), __uint_as_float(r[1]));
}
__device__ __forceinline__ f32x16 lds_f32x16(lds_cptr p) {
    f32x4 a = *(const LAS f32x4*)p, b = *(const LAS f32x4*)(p + 1024), c = *(const LAS f32x4*)(p + 2048), d = *(const LAS f32x4*)(p + 3072);
    f32x8 lo = __builtin_shufflevector(a, b, 0, 1, 2, 3, 4, 5, 6, 7), hi = __builtin_shufflevector(c, d, 0, 1, 2, 3, 4, 5, 6, 7);
    return __builtin_shufflevector(lo, hi, 0, 1, 2, 3, 4, 5, 6, 7, 8, 9, 10, 11, 12, 13, 14, 15);
}
#define BLOAD(rs, vo, so) as_frag(__builtin_amdgcn_raw_buffer_load_b128(rs, vo, so, 16))
#define BSTORE(v, rs, vo, so) __builtin_amdgcn_raw_buffer_store_b128(v, rs, (vo) + (so), 0, 16)

__device__ __forceinline__ void wait_tags_end(u64 v, const GAS u64* p, u64 tag, GAS u64* pub, int lane) {
    if (pub) { DRAIN(); if (lane == 0) __hip_atomic_store(pub, tag, RLX_AGENT); }
#pragma nounroll
    for (unsigned spins = 0; spins < SPIN_CAP && !__all(v == tag); ++spins) {
        __builtin_amdgcn_s_sleep(4);
        v = __hip_atomic_load(p, RLX_AGENT);
    }
    asm volatile("" ::: "memory");
}

__device__ __forceinline__ void wait_tags(const GAS u64* p, u64 tag, GAS u64* pub = nullptr, int lane = 0) {
    wait_tags_end(__hip_atomic_load(p, RLX_AGENT), p, tag, pub, lane);
}

__device__ __forceinline__ void cnt_arrive(lds_ptr cnt, int lane) {
    asm volatile("s_waitcnt lgkmcnt(0)" ::: "memory");
    if (lane == 0) __hip_atomic_fetch_add((LAS unsigned*)cnt, 1u, __ATOMIC_RELAXED, __HIP_MEMORY_SCOPE_WORKGROUP);
}
__device__ __forceinline__ void cnt_wait(lds_ptr cnt) {
#pragma nounroll
    for (unsigned spins = 0; spins < (1u << 22); ++spins) {
        if (__builtin_amdgcn_readfirstlane(__hip_atomic_load((LAS unsigned*)cnt, __ATOMIC_RELAXED, __HIP_MEMORY_SCOPE_WORKGROUP)) == 4) break;
        __builtin_amdgcn_s_sleep(2);
    }
    asm volatile("" ::: "memory");
}
__device__ __forceinline__ unsigned kvflag(int br, int quad, int tile, int type) {
    return WS_KVFLAG + (unsigned)((((br * 8 + quad) * 4 + (tile & 3)) * 2 + type) * 16 + (tile >> 2)) * FSTR;
}

__device__ __forceinline__ void slow_rows(__amdgpu_buffer_rsrc_t rs, unsigned sbase, int lo, int hi, int g, bool band, bf16x8 q,
                                          unsigned vk, unsigned vv0, unsigned vv1, lds_cptr lds, int lane, int idx, f32x16& acc) {
    float m = -__builtin_inff();
    for (int pass = 0; pass < 2; ++pass) {
        if (pass) { m = halfmax(m); acc = f32x16{}; }
        for (int kt = lo; kt <= hi; ++kt) {
            const unsigned so = sbase + (unsigned)kt * TS;
            const bf16x8 k = BLOAD(rs, vk, so), v0 = BLOAD(rs, vv0, so), v1 = BLOAD(rs, vv1, so);
            f32x16 c = f32x16{};
            if (band && kt == g) c = lds_f32x16(lds + L_MDIAG + lane * 16);
            if (band && kt == g - 8) c = lds_f32x16(lds + L_MFAR + lane * 16);
            f32x16 s = MFMA(k, q, c);
            if (!pass) m = __builtin_fmaxf(m, max16(s));
            else {
#pragma unroll
                for (int r = 0; r < 16; ++r) s[r] = __builtin_amdgcn_exp2f(s[r] - m);
                acc = pv_lo(v0, s, acc, idx); acc = pv_hi(v1, s, acc, idx);
            }
        }
    }
}

#define PK4(A, o) ((u32x4){cvtpk(A[o], A[o + 1]), cvtpk(A[o + 2], A[o + 3]), cvtpk(A[o + 4], A[o + 5]), cvtpk(A[o + 6], A[o + 7])})
#define PROF_ARG
#define PROF_PASS
__device__ __forceinline__ u64 make_tag(u32x2 ep) {
    const u64 e = ((u64)(unsigned)__builtin_amdgcn_readfirstlane(ep[1]) << 32) | (unsigned)__builtin_amdgcn_readfirstlane(ep[0]);
    return e * 0x9E3779B97F4A7C15ull + 0x632BE59BD9B4E019ull;
}
template <int E>
__device__ __forceinline__ u32x4 pack_kv(int i, const f32x16& a) {
    const int s = i & 1;
    if (E == 0) return PK4(a, 8 * s);
    auto x = __builtin_amdgcn_permlane32_swap(cvtpk(a[8 * s], a[8 * s + 1]), cvtpk(a[8 * s + 4], a[8 * s + 5]), false, false);
    auto y = __builtin_amdgcn_permlane32_swap(cvtpk(a[8 * s + 2], a[8 * s + 3]), cvtpk(a[8 * s + 6], a[8 * s + 7]), false, false);
    return (u32x4){x[0], y[0], x[1], y[1]};
}
#define PUT_KV(i, v, vo) BSTORE(v, rkv, vo, E ? ((i) & 1) * 512 + ((i) >> 1) * (128 * TS) : (i) * (64 * TS))
template <int E>
__device__ __forceinline__ void project(lds_ptr lds, char* ws, __amdgpu_buffer_rsrc_t rkv, __amdgpu_buffer_rsrc_t ro, int lane, int t, int p, int j, u32x2 ep, u64& tag PROF_ARG) {
    const int r32 = lane & 31, hh = lane >> 5;
    const unsigned q4 = (lane & 15) >> 2, wcol = ((lane >> 4) & 1) * 32 + (lane & 3) * 8;
    lds_cptr xa[4];
#pragma unroll
    for (int ks = 0; ks < 4; ++ks) xa[ks] = lds + (32 * t + r32) * 128 + (((hh ^ ((r32 >> 1) & 7)) * 16) ^ (ks * 32));
    unsigned w0o = L_W + (8 * hh + q4) * 128 + (q4 >> 1) * 64 + wcol, w1o = w0o ^ 64;
    PIN(w0o); PIN(w1o);
    const lds_cptr w0 = lds + w0o, w1 = lds + w1o, wq = lds + (8 * hh + q4) * 64 + wcol;
#define XRD(n) lds_frag(xa[(n) & 3] + (((n) >> 2) & 3) * 16384)
#define WRD(b, n) tr_frag(b + ((n) & 15) * 2048, b + ((n) & 15) * 2048 + 512)
#define YOF(n) ((n) >= 28 ? 32768 + ((n) & 3) * 2048 : ((n) & 15) * 2048)
#define YRD(b, n) tr_frag(b + YOF(n), b + YOF(n) + 512)
#define QOF(br, n) ((((n) >> 2) & 3) * 16384 + ((n) >= 28 ? 8192 : 0) + (br) * 4096 + ((n) & 3) * 1024)
#define QRD(br, n) tr_frag(wq + QOF(br, n), wq + QOF(br, n) + 256)
#define MM(W, X, A) (E ? MFMA(X, W, A) : MFMA(W, X, A))
    bf16x8 xf[32], f0[4], f1[4];
    f32x16 a0 = f32x16{}, a1 = f32x16{};
#define RD1(n) do { xf[n] = XRD(n); f0[(n) & 3] = WRD(w0, n); f1[(n) & 3] = WRD(w1, n); SBAR(); } while (0)
#define MM1(n) do { a0 = MM(f0[(n) & 3], xf[n], a0); a1 = MM(f1[(n) & 3], xf[n], a1); SBAR(); } while (0)
#pragma unroll
    for (int s = 0; s < 8; ++s) {
        if (s == 0) CBAR(); else BAR();
        if (s == 3) { if (E) BSTORE(((u32x4){0x3f803f80u, 0x3f803f80u, 0x3f803f80u, 0x3f803f80u}), rkv,
                                  (unsigned)((hh * 32 + 4 * (p >> 1) + (r32 >> 3)) * 64 + 4 * j + t) * TS + 2048 + (lane & 7) * 16, 0); }
        if (s == 5) BSTORE(((u32x4){~0u, ~0u, ~0u, ~0u}), ro, lane * 16, (unsigned)((4 * j + t) * 32 + 2 * p) * 1024);
        SBAR();
        RD1(4 * s); RD1(4 * s + 1); MM1(4 * s); RD1(4 * s + 2); MM1(4 * s + 1); RD1(4 * s + 3); MM1(4 * s + 2);
        if (s < 7) MM1(4 * s + 3);
    }
    const unsigned tile = 4 * j + t;
    const unsigned vo = (unsigned)(4 * (p >> 1) * 64 + tile) * TS + (E ? (r32 >> 4) * (64 * TS) + 1024 + hh * 256 + (r32 & 15) * 16 : lane * 16);
    MM1(31);
    BAR(); SBAR();
    bf16x8 u0 = QRD(0, 0), u1 = QRD(1, 0), y0 = WRD(w0, 0), y1 = WRD(w1, 0);
    SBAR();
    u32x4 pk[4] = {pack_kv<E>(0, a0), pack_kv<E>(1, a0), pack_kv<E>(2, a1), pack_kv<E>(3, a1)};
    PIN(pk[0]); PIN(pk[1]); PIN(pk[2]); PIN(pk[3]);
    f32x16 b0 = f32x16{}, b1 = f32x16{}, aQ0 = f32x16{}, aQ1 = f32x16{};
    SBAR();
#define STSP 3
#define PUBN 16
#pragma unroll
    for (int n = 0; n < 32; ++n) {
        aQ0 = MFMA(u0, xf[n], aQ0); SBAR();
        if (n == PUBN) { asm volatile("s_waitcnt vmcnt(0)" : "+v"(ep)::"memory"); tag = make_tag(ep); if (lane == 0) __hip_atomic_store((GAS u64*)(ws + kvflag(0, p >> 1, tile, E)), tag, RLX_AGENT); SBAR(); }
        if ((n & 3) == 3 && n < 27) { BAR(); SBAR(); }
        if (n < 31) u0 = QRD(0, n + 1); SBAR();
        aQ1 = MFMA(u1, xf[n], aQ1); SBAR();
        if (n % STSP == 0 && n / STSP < 4) { PUT_KV(n / STSP, pk[n / STSP], vo); SBAR(); }
        if (n < 31) u1 = QRD(1, n + 1); SBAR();
        b0 = MM(y0, xf[n], b0); SBAR(); if (n < 31) y0 = YRD(w0, n + 1); SBAR();
        b1 = MM(y1, xf[n], b1); SBAR(); if (n < 31) y1 = YRD(w1, n + 1); SBAR();
    }
#pragma unroll
    for (int r = 0; r < 16; ++r) { aQ0[r] *= QSCALE; aQ1[r] *= QSCALE; }
    const lds_ptr qd = lds + L_Q + t * 1024 + lane * 16;
    *(LAS u32x4*)(qd + 4096) = PK4(aQ0, 8); *(LAS u32x4*)qd = PK4(aQ0, 0);
    *(LAS u32x4*)(qd + 8192) = PK4(aQ1, 0); *(LAS u32x4*)(qd + 12288) = PK4(aQ1, 8);
    __builtin_amdgcn_s_setprio(2);
    STAMP(3);
    CBAR();
    STAMP(4);
    SBAR();
    const unsigned vo2 = vo + 32 * 64 * TS;
#define YSLP 0
    PUT_KV(0, pack_kv<E>(0, b0), vo2); if (YSLP) __builtin_amdgcn_s_sleep(YSLP); PUT_KV(1, pack_kv<E>(1, b0), vo2); if (YSLP) __builtin_amdgcn_s_sleep(YSLP);
    PUT_KV(2, pack_kv<E>(2, b1), vo2); if (YSLP) __builtin_amdgcn_s_sleep(YSLP); PUT_KV(3, pack_kv<E>(3, b1), vo2);
    DRAIN();
    if (lane == 0) __hip_atomic_store((GAS u64*)(ws + kvflag(1, p >> 1, tile, E)), tag, RLX_AGENT);
    STAMP(19);
}

#define WOIN 2
#define NWO 16
#define NOP 4
#define CODE_LINES 212
__global__ void __launch_bounds__(512, 2)
gmix_kernel(const char* x, const char* wA, const char* woA, const char* wB, const char* woB, const char* gw, char* out, char* ws) {
    extern __shared__ __attribute__((aligned(16))) char smem[];
    const lds_ptr lds = (lds_ptr)smem;
    const unsigned lds0 = (unsigned)(uintptr_t)lds;
    const int tid = threadIdx.x, lane = tid & 63, r32 = lane & 31, hh = lane >> 5;
    const int w = __builtin_amdgcn_readfirstlane(tid >> 6);
    const int bid = blockIdx.x, xcd = bid & 7, rr = bid >> 3;
    const int p = 4 * (xcd & 3) + (rr & 3), j = 8 * (xcd >> 2) + (rr >> 2);
    const int jo = 2 * xcd + (rr >> 4), po = rr & 15;
    const int t = w & 3, c = w >> 2;
    const int e = p & 1;
    STAMP(0);
    u64 pc; asm volatile("s_getpc_b64 %0" : "=s"(pc));
    unsigned rot = (j + 2 * (p & 3)) & 7;
    unsigned vx0 = (128 * j + 32 * t + (lane >> 3)) * 1024 + (((lane & 7) ^ (lane >> 4)) * 16), vx1 = (vx0 ^ 64) + 7168, vx2 = vx0 + 14336, vx3 = vx1 + 14336;
    unsigned vw0 = (16 * t + (lane >> 3)) * 3072 + 1024 + 1024 * e + 128 * (p >> 1) + (((lane & 7) ^ (((lane >> 4) & 1) << 2)) * 16), vw1 = vw0 + 8 * 3072 - 1024;
    unsigned mx = lds0 + t * 4096, mw = lds0 + L_W + t * 2048, ow = rot * (64 * 3072);
    asm volatile("" : "+v"(vx0), "+v"(vx1), "+v"(vx2), "+v"(vx3), "+v"(vw0), "+v"(vw1));
    asm volatile("" : "+s"(mx), "+s"(mw), "+s"(rot), "+s"(ow));
    SBAR();
    asm volatile("" : "+s"(x), "+s"(wA), "+s"(woA), "+s"(wB), "+s"(woB), "+s"(gw), "+s"(out), "+s"(ws));
    u32x2 ep;
    u64 tag;
#define LD_EPOCH() do { const unsigned z_ = 0; asm volatile("global_load_dwordx2 %0, %1, %2 sc1" : "=v"(ep) : "v"(z_), "s"(ws) : "memory"); } while (0)
#define MK_RSRC() const __amdgpu_buffer_rsrc_t rkv = __builtin_amdgcn_make_buffer_rsrc(ws + WS_KV, 0, KV_BYTES, 0x00020000); \
                  const __amdgpu_buffer_rsrc_t ro = __builtin_amdgcn_make_buffer_rsrc(ws + WS_O, 0, O_BYTES, 0x00020000)
    if (c) {
#define DMA1(s) do { const unsigned ks_ = ((s) + rot) & 7; \
        glds16x4(vx0, vx1, vx2, vx3, x + ks_ * 128, mx + ((s) & 3) * 16384); glds16x2(vw0, vw1, wA + ks_ * (64 * 3072), mw + ((s) & 3) * 8192); } while (0)
#define DMA2(s) do { const unsigned ks_ = (((s) + rot) & 7) * (64 * 3072); \
        glds16x2(vq0, vq1, wq + ks_, mq + ((s) & 3) * 16384); glds16x2(vw0, vw1, wB + ks_, mw + ((s) & 3) * 8192); } while (0)
#define LBAR(N) asm volatile("s_waitcnt vmcnt(" #N ")\n\ts_barrier" ::: "memory")
        STAMP(20);
        glds16x4(vx0, vx1, vx2, vx3, x + rot * 128, mx); glds16x2(vw0, vw1, wA + ow, mw);
        SBAR();
        DMA1(1);
        STAMP(22);
        SBAR();
#pragma nounroll
        for (int s = 2; s < 8; ++s) { LBAR(6); DMA1(s);
        }
        const unsigned vq0 = (32 * (t & 1) + (lane >> 2)) * 3072 + 64 * p + (lane & 3) * 16, vq1 = vq0 + 16 * 3072 - 1024, mq = lds0 + t * 2048;
        const char* wq = (t >> 1) ? wB : wA;
        LBAR(6); STAMP(14); DMA2(8);
        LBAR(4); STAMP(15); DMA2(9);
        u32x4 x0, x1, g0, g1;
        LBAR(4); STAMP(16);
        { const unsigned vo_ = r32 * 16, vr_ = vo_ + (128 * j + 32 * t + 2 * p + hh) * 1024;
          asm volatile("global_load_dwordx4 %0, %4, %6\n\tglobal_load_dwordx4 %1, %4, %6 offset:512\n\tglobal_load_dwordx4 %2, %5, %7\n\tglobal_load_dwordx4 %3, %5, %7 offset:512"
                       : "=&v"(x0), "=&v"(x1), "=&v"(g0), "=&v"(g1) : "v"(vr_), "v"(vo_), "s"(x), "s"(gw) : "memory"); }
        LD_EPOCH();
        DMA2(10);
        const unsigned k7 = ((7 + rot) & 7) * (64 * 3072), sx = mq + 3 * 16384 + 8192, sw = mw + 32768;
        LBAR(4); STAMP(17); glds16(vq0, wq + k7, sx); DMA2(11);
        asm volatile("" : "+v"(x0), "+v"(x1), "+v"(g0), "+v"(g1), "+v"(ep));
        tag = make_tag(ep);
        float z = 0.f;
#pragma unroll
        for (int i = 0; i < 4; ++i) {
            z = __builtin_fmaf(__uint_as_float(x0[i] << 16), __uint_as_float(g0[i] << 16), z);
            z = __builtin_fmaf(__uint_as_float(x0[i] & 0xffff0000u), __uint_as_float(g0[i] & 0xffff0000u), z);
            z = __builtin_fmaf(__uint_as_float(x1[i] << 16), __uint_as_float(g1[i] << 16), z);
            z = __builtin_fmaf(__uint_as_float(x1[i] & 0xffff0000u), __uint_as_float(g1[i] & 0xffff0000u), z);
        }
        PIN(z);
        LBAR(5); STAMP(18); glds16(vw0, wB + k7, sw); DMA2(12);
#pragma unroll
        for (int o = 16; o >= 4; o >>= 1) z += __shfl_xor(z, o);
        PIN(z);
        LBAR(5); STAMP(19); glds16(vq1, wq + k7 + 1024, sx + 1024); DMA2(13);
        z += __shfl_xor(z, 2); z += __shfl_xor(z, 1);
        float gate = __builtin_amdgcn_rcpf(1.f + __expf(-z));
        PIN(gate);
        LBAR(5); STAMP(21); glds16(vw1, wB + k7 + 1024, sw + 1024); DMA2(14);
        LBAR(5); STAMP(24);
        if (r32 == 0) __hip_atomic_store((GAS float*)(ws + WS_G) + 128 * j + 32 * t + 2 * p + hh, gate, RLX_AGENT);
        LBAR(1); STAMP(29);
        __builtin_amdgcn_s_setprio(1);
    } else {
        unsigned pf0 = 0;
        if (CODE_LINES) {
            const unsigned ln = rr * 4 + t;
            asm volatile("s_load_dword %0, %1, %2\n\ts_load_dword %0, %1, %3" : "+s"(pf0) : "s"(pc & ~127ull), "s"(ln * 128), "s"((ln + 128 < CODE_LINES ? ln + 128 : 0) * 128) : "memory");
        }
        LD_EPOCH();
        MK_RSRC();
        unsigned gtouch;
        { const unsigned vo_ = (lane & 7) * 128; asm volatile("global_load_dword %0, %1, %2" : "=v"(gtouch) : "v"(vo_), "s"(gw) : "memory"); }
#pragma unroll
        for (int m = 0; m < 2; ++m) {
            f32x4 mk;
#pragma unroll
            for (int i = 0; i < 4; ++i) { const bool le = (i + 8 * t + 4 * hh) <= r32; mk[i] = (le != (m != 0)) ? 0.f : -__builtin_inff(); }
            *(LAS f32x4*)(lds + L_MDIAG + m * 4096 + t * 1024 + lane * 16) = mk;
        }
        if (tid < 4) *(LAS unsigned*)(lds + L_CNT + tid * 4) = 0;
        asm volatile("s_waitcnt lgkmcnt(0)" : "+s"(pf0)::"memory");
        STAMP(21);
        if (e) project<1>(lds, ws, rkv, ro, lane, t, p, j, ep, tag PROF_PASS); else project<0>(lds, ws, rkv, ro, lane, t, p, j, ep, tag PROF_PASS);
        asm volatile("" :: "v"(gtouch));
        const unsigned vo = (lane >> 2) * 1024 + (lane & 3) * 16;
        const char* wo = ((t >> 1) ? woB : woA) + 64 * po + (t & 1) * (16 * 16384);
        const int r0 = 4 * (((rr >> 4) << 1) | (po & 1));
#pragma nounroll
        for (int i = 0; i < NWO; ++i) {
            const int ii = (i + r0) & 15;
            glds16(vo, wo + ii * 16384, lds0 + L_WO + (t * 16 + ii) * 1024);
            asm volatile("s_waitcnt vmcnt(%0)" :: "i"(WOIN - 1) : "memory");
        }
        DRAIN();
        cnt_arrive(lds + L_CNT + 4, lane);
        STAMP(24);
    }
    MK_RSRC();
    const int head = 2 * p + c;
    const int idx = 0xdddddddd;
    const unsigned vk = lane * 16;
    const unsigned vv0 = r32 < 16 ? 1024 + hh * 256 + r32 * 16 : (r32 == 16 ? 2048 : OOB);
    const unsigned vv1 = r32 < 16 ? vv0 + 512 : vv0;
#define LD3(K, V0, V1, rs, so) do { K = BLOAD(rs, vk, so); V0 = BLOAD(rs, vv0, so); V1 = BLOAD(rs, vv1, so); } while (0)
#define EX(X, r) X[r] = __builtin_amdgcn_exp2f(X[r])
#define STEP(X, Y, OP, VP, KN, QN, CN, O, V0, F1, F2) do { \
        EX(X, 0); EX(X, 1); SBAR(); OP = pv_hi(VP, Y, OP, idx); SBAR(); F1; SBAR(); EX(X, 2); EX(X, 3); EX(X, 4); SBAR(); Y = MFMA(KN, QN, CN); SBAR(); \
        EX(X, 5); EX(X, 6); EX(X, 7); SBAR(); EX(X, 8); EX(X, 9); SBAR(); O = pv_lo(V0, X, O, idx); SBAR(); F2; SBAR(); \
        EX(X, 10); EX(X, 11); EX(X, 12); EX(X, 13); EX(X, 14); EX(X, 15); SBAR(); } while (0)
    GAS u64* pub = nullptr;
#pragma nounroll
    for (int ph = 0; ph < 2; ++ph) {
        const int br = ph ^ c ^ 1;
        int g = 4 * j + t;
        asm volatile("" : "+s"(g));
        f32x16 oo; bf16x8 qa; float lrest = 0.f;
        const f32x16 zc = f32x16{};
        const int fi = lane & 15, fkt = g - (fi < 9 ? fi : 0);
        const GAS u64* fp = (const GAS u64*)(ws + kvflag(br, p >> 1, br ? (fkt >= 0 ? fkt : 0) : 4 * fi + t, (lane >> 4) & 1));
        const u64 fv = __hip_atomic_load(fp, RLX_AGENT);
        SBAR();
        if (!br) {
            f32x16 o1 = f32x16{}, o2, o3 = f32x16{}, S0, S1 = f32x16{};
            bf16x8 vd1 = bf16x8{};
            oo = f32x16{};
            PIN(oo); PIN(o1); PIN(o3); PIN(S1); PIN(vd1);
            wait_tags_end(fv, fp, tag, pub, lane);
            STAMP(5);
            bf16x8 ka, va0, va1, kb, vb0, vb1, kc, vc0, vc1, kd, vd0;
            const unsigned sBeg = (unsigned)(head * 64 + t) * TS, sEnd = sBeg + 64 * TS;
            unsigned so = (unsigned)(head * 64 + g) * TS;
#define NEXT_A() asm volatile("s_addk_i32 %0, %1" : "+s"(so) : "i"(4 * TS) : "scc")
#define WRAP_A() asm volatile("s_cmp_eq_u32 %0, %1\n\ts_cselect_b32 %0, %2, %0" : "+s"(so) : "s"(sEnd), "s"(sBeg) : "scc")
            LD3(ka, va0, va1, rkv, so); NEXT_A(); WRAP_A(); SBAR(); LD3(kb, vb0, vb1, rkv, so); NEXT_A(); WRAP_A(); SBAR();
            LD3(kc, vc0, vc1, rkv, so); NEXT_A(); WRAP_A(); SBAR();
            if (c) { BAR(); STAMP(4); } else __builtin_amdgcn_s_setprio(0);
            const lds_cptr qp = lds + L_Q + c * 4096 + lane * 16;
            qa = lds_frag(qp + t * 1024);
            bf16x8 q1 = lds_frag(qp + ((t + 1) & 3) * 1024), q2 = lds_frag(qp + ((t + 2) & 3) * 1024), q3 = lds_frag(qp + ((t + 3) & 3) * 1024);
            __builtin_amdgcn_s_waitcnt(0x0F76);
            S0 = MFMA(ka, qa, zc);
            SBAR();
            o2 = f32x16{};
            PIN(o2); PIN(q1); PIN(q2); PIN(q3);
#define TILE(K, V0, V1, KN, LK, LV0, LV1) do { \
        STEP(S0, S1, o3, LV1, K, q1, zc, oo, V0, LK = BLOAD(rkv, vk, so), LV0 = BLOAD(rkv, vv0, so)); \
        STEP(S1, S0, oo, V1, K, q2, zc, o1, V0, LV1 = BLOAD(rkv, vv1, so), NEXT_A()); \
        STEP(S0, S1, o1, V1, K, q3, zc, o2, V0, WRAP_A(), ); \
        STEP(S1, S0, o2, V1, KN, qa, zc, o3, V0, __builtin_amdgcn_s_waitcnt(0x0F76), ); } while (0)
#pragma nounroll
            for (int it = 0; it < 4; ++it) {
                TILE(ka, va0, va1, kb, kd, vd0, vd1);
                TILE(kb, vb0, vb1, kc, ka, va0, va1);
                TILE(kc, vc0, vc1, kd, kb, vb0, vb1);
                TILE(kd, vd0, vd1, ka, kc, vc0, vc1);
            }
            o3 = pv_hi(vd1, S1, o3, idx);
            STAMP(6);
#define PUT(O, sl) do { lds_ptr d_ = lds + L_PART + (w * 3 + sl) * 2048 + lane * 16; \
        *(LAS f32x4*)d_ = __builtin_shufflevector(O, O, 0, 1, 2, 3); *(LAS f32x4*)(d_ + 1024) = __builtin_shufflevector(O, O, 4, 5, 6, 7); } while (0)
            PUT(o1, 0); PUT(o2, 1); PUT(o3, 2);
            if (hh == 0) { LAS float* lp = (LAS float*)(lds + L_PL + w * 384) + r32; lp[0] = o1[8]; lp[32] = o2[8]; lp[64] = o3[8]; }
            if (c) { cnt_arrive(lds + L_CNT, lane); cnt_wait(lds + L_CNT); } else CBAR();
#pragma unroll
            for (int s = 0; s < 3; ++s) {
                const int src = (c * 4 + ((t - 1 - s) & 3)) * 3 + s;
                const lds_cptr d = lds + L_PART + src * 2048 + lane * 16;
                const f32x4 a = *(const LAS f32x4*)d, b = *(const LAS f32x4*)(d + 1024);
#pragma unroll
                for (int r = 0; r < 4; ++r) { oo[r] += a[r]; oo[4 + r] += b[r]; }
                lrest += *(const LAS float*)(lds + L_PL + src * 128 + r32 * 4);
            }
        } else {
            wait_tags_end(fv, fp, tag, pub, lane);
            STAMP(25);
            bf16x8 k[9], v0[9], v1[9];
            const unsigned sB = (unsigned)((32 + head) * 64) * TS;
#define LDB(i) do { const int kt_ = g - (i); \
            const __amdgpu_buffer_rsrc_t rb_ = __builtin_amdgcn_make_buffer_rsrc(ws + WS_KV, 0, kt_ >= 0 ? KV_BYTES : 0u, 0x00020000); \
            LD3(k[i], v0[i], v1[i], rb_, sB + (unsigned)(kt_ >= 0 ? kt_ : 0) * TS); } while (0)
            LDB(0); SBAR(); LDB(1); SBAR(); LDB(2); SBAR();
            qa = lds_frag(lds + L_Q + 8192 + c * 4096 + t * 1024 + lane * 16);
            const f32x16 cd = lds_f32x16(lds + L_MDIAG + lane * 16), cf = lds_f32x16(lds + L_MFAR + lane * 16);
            oo = f32x16{};
            f32x16 S0 = MFMA(k[0], qa, cd), S1 = f32x16{};
            const bf16x8 vz = bf16x8{};
            STEP(S0, S1, oo, vz, k[1], qa, zc, oo, v0[0], LDB(3), );
            STEP(S1, S0, oo, v1[0], k[2], qa, zc, oo, v0[1], LDB(4), );
            STEP(S0, S1, oo, v1[1], k[3], qa, zc, oo, v0[2], LDB(5), );
            STEP(S1, S0, oo, v1[2], k[4], qa, zc, oo, v0[3], LDB(6), );
            STEP(S0, S1, oo, v1[3], k[5], qa, zc, oo, v0[4], LDB(7), );
            STEP(S1, S0, oo, v1[4], k[6], qa, zc, oo, v0[5], LDB(8), );
            STEP(S0, S1, oo, v1[5], k[7], qa, zc, oo, v0[6], , );
            STEP(S1, S0, oo, v1[6], k[8], qa, cf, oo, v0[7], , );
            STEP(S0, S1, oo, v1[7], k[8], qa, cf, oo, v0[8], , );
            oo = pv_hi(v1[8], S0, oo, idx);
            STAMP(7);
        }
        auto lsw = __builtin_amdgcn_permlane32_swap(__float_as_uint(oo[8]), __float_as_uint(oo[8]), false, false);
        float l = __uint_as_float(lsw[0]) + lrest, z = 0.f;
#pragma unroll
        for (int r = 0; r < 8; ++r) z = __builtin_fmaf(oo[r], 0.f, z);
        if (__builtin_expect(__any(!(l + z < LMAX && l > LMIN)), 0)) {
            if (!br) { wait_tags((const GAS u64*)(ws + kvflag(0, p >> 1, 0, 0) + lane * FSTR), tag); wait_tags((const GAS u64*)(ws + kvflag(0, p >> 1, 2, 0) + lane * FSTR), tag); }
            slow_rows(rkv, (unsigned)((br * 32 + head) * 64) * TS, br ? (g >= 8 ? g - 8 : 0) : 0, br ? g : 63, g, br, qa, vk, vv0, vv1, lds, lane, idx, oo);
            auto l2 = __builtin_amdgcn_permlane32_swap(__float_as_uint(oo[8]), __float_as_uint(oo[8]), false, false);
            l = __uint_as_float(l2[0]);
        }
        const float il = __builtin_amdgcn_rcpf(l);
#define NOMARK(v) __builtin_elementwise_min(v, 0xfffffffeu)
        BSTORE(((u32x4){NOMARK(cvtpk(oo[0] * il, oo[1] * il)), NOMARK(cvtpk(oo[2] * il, oo[3] * il)), NOMARK(cvtpk(oo[4] * il, oo[5] * il)), NOMARK(cvtpk(oo[6] * il, oo[7] * il))}),
               ro, vk, (unsigned)((br * 64 + g) * 32 + head) * 1024);
        pub = (GAS u64*)(ws + WS_OFLAG) + ((br * 64 + g) * 2 + c) * 16 + p;
        if (ph == 0) STAMP(23);
    }
    STAMP(8);
    if (c) {
        const int go = 4 * jo + t;
        const lds_cptr wo = lds + L_WO + (4 * hh + ((lane & 15) >> 2)) * 64 + ((lane >> 4) & 1) * 32 + (lane & 3) * 8;
        const GAS u64* of_ = (const GAS u64*)(ws + WS_OFLAG);
        f32x16 ya = f32x16{}, yb = f32x16{};
        wait_tags(of_ + ((lane & 16) ? go * 32 + 16 : (64 + go) * 32) + (lane & 15), tag, pub, lane);
        cnt_wait(lds + L_CNT + 4);
        STAMP(9);
        const float gt = __hip_atomic_load((const GAS float*)(ws + WS_G) + 32 * go + r32, RLX_AGENT);
#define OBR(n) ((n) >> 4 == 1 ? 0 : 1)
#define OHD(n) (2 * ((n) & 15) + ((n) >= 16))
#define OLD(n) BLOAD(ro, vk, (unsigned)((OBR(n) * 64 + go) * 32 + OHD(n)) * 1024)
#define OWF(n) tr_frag(wo + OBR(n) * 32768 + OHD(n) * 1024, wo + OBR(n) * 32768 + OHD(n) * 1024 + 512)
        bf16x8 wf[16], of[16];
#pragma unroll
        for (int n = 0; n < 6; ++n) of[n] = OLD(n);
        SBAR();
#pragma unroll
        for (int n = 0; n < 48; ++n) {
            if (n == 26) wait_tags(of_ + (64 + go) * 32 + 16 + (lane & 15), tag);
            const bf16x8 wv = OWF(n);
            if (OBR(n)) yb = MFMA(wv, of[n % 6], yb); else ya = MFMA(wv, of[n % 6], ya);
            SBAR();
            if (n + 6 < 48) of[n % 6] = OLD(n + 6);
            SBAR();
        }
#pragma unroll
        for (int i = 0; i < 16; ++i) { wf[i] = tr_frag(wo + 2 * i * 1024, wo + 2 * i * 1024 + 512); PIN(wf[i]); }
        const float g1 = 1.f - gt;
#pragma unroll
        for (int r = 0; r < 16; ++r) yb[r] *= g1;
        PIN(yb); PIN(ya);
        STAMP(10);
        BAR();
        BAR();
        STAMP(11);
#pragma unroll
        for (int i = 0; i < 16; ++i) of[i] = BLOAD(ro, vk, (unsigned)(go * 32 + 2 * i) * 1024);
        SBAR();
#define GRP_BAD(q) ({ unsigned mx_ = 0; _Pragma("unroll") for (int i = 4 * (q); i < 4 * (q) + 4; ++i) { const u32x4 u_ = __builtin_bit_cast(u32x4, of[i]); \
            mx_ = __builtin_elementwise_max(__builtin_elementwise_max(mx_, u_[0]), u_[1]); mx_ = __builtin_elementwise_max(__builtin_elementwise_max(mx_, u_[2]), u_[3]); } \
            __any(mx_ == ~0u); })
#define GRP_MM(q) do { _Pragma("unroll") for (int i = 4 * (q); i < 4 * (q) + 4; ++i) ya = MFMA(wf[i], of[i], ya); } while (0)
#define GRP_LD(q) do { _Pragma("unroll") for (int i = 4 * (q); i < 4 * (q) + 4; ++i) of[i] = BLOAD(ro, vk, (unsigned)(go * 32 + 2 * i) * 1024); } while (0)
        int done = 0;
        if (!GRP_BAD(0)) { GRP_MM(0); done = 1; if (!GRP_BAD(1)) { GRP_MM(1); done = 2; if (!GRP_BAD(2)) { GRP_MM(2); done = 3; if (!GRP_BAD(3)) { GRP_MM(3); done = 4; } } } }
        if (done < 4) {
#pragma nounroll
            for (unsigned spins = 0; spins < SPIN_CAP; ++spins) {
                const bool b0 = GRP_BAD(0), b1 = GRP_BAD(1), b2 = GRP_BAD(2), b3 = GRP_BAD(3);
                if (!(b0 || b1 || b2 || b3)) break;
                asm volatile("" ::: "memory");
                if (b0) GRP_LD(0); if (b1) GRP_LD(1); if (b2) GRP_LD(2); if (b3) GRP_LD(3);
            }
            if (done < 1) GRP_MM(0); if (done < 2) GRP_MM(1); if (done < 3) GRP_MM(2); GRP_MM(3);
        }
        if (bid == 0 && tid == 256) __hip_atomic_store((GAS u64*)(ws + WS_EPOCH), tag, RLX_AGENT);
#pragma unroll
        for (int s = 0; s < 2; ++s) {
            float y[8];
#pragma unroll
            for (int r = 0; r < 8; ++r) y[r] = __builtin_fmaf(gt, ya[8 * s + r], yb[8 * s + r]);
            auto a = __builtin_amdgcn_permlane32_swap(cvtpk(y[0], y[1]), cvtpk(y[4], y[5]), false, false);
            auto b = __builtin_amdgcn_permlane32_swap(cvtpk(y[2], y[3]), cvtpk(y[6], y[7]), false, false);
            *(GAS u32x4*)(out + (size_t)(32 * go + r32) * 1024 + 64 * po + 32 * s + 16 * hh) = (u32x4){a[0], b[0], a[1], b[1]};
        }
        STAMP(12);
    } else {
        DRAIN(); BAR();
    }
}

extern "C" void kernel_launch(void* const* d_in, const int* in_sizes, int n_in, void* d_out, int out_size, void* d_ws, size_t ws_size,
                              hipStream_t stream) {
    (void)in_sizes; (void)n_in; (void)out_size; (void)ws_size;
    (void)hipFuncSetAttribute((const void*)gmix_kernel, hipFuncAttributeMaxDynamicSharedMemorySize, L_END);
    gmix_kernel<<<256, 512, L_END, stream>>>((const char*)d_in[0], (const char*)d_in[1], (const char*)d_in[2], (const char*)d_in[4],
                                             (const char*)d_in[5], (const char*)d_in[6], (char*)d_out, (char*)d_ws);
}
```

```cpp
#include <hip/hip_runtime.h>
#include <hip/hip_bf16.h>

typedef __attribute__((ext_vector_type(8))) __bf16 bf16x8;
typedef __attribute__((ext_vector_type(16))) __bf16 bf16x16;
typedef __attribute__((ext_vector_type(16))) float f32x16;
typedef __attribute__((ext_vector_type(8))) float f32x8;
typedef __attribute__((ext_vector_type(4))) float f32x4;
typedef __attribute__((ext_vector_type(4))) unsigned u32x4;
typedef __attribute__((ext_vector_type(2))) unsigned u32x2;
typedef __attribute__((ext_vector_type(2))) __bf16 bf16x2;
typedef short s16x4 __attribute__((ext_vector_type(4)));
typedef unsigned long long u64;
#define GAS __attribute__((address_space(1)))
#define LAS __attribute__((address_space(3)))
typedef LAS const char* lds_cptr;
typedef LAS char* lds_ptr;

constexpr unsigned WS_EPOCH = 0;
#define FSTR 8
constexpr unsigned WS_OFLAG = 65536;
constexpr unsigned WS_KVFLAG = 131072;
constexpr unsigned WS_KV = 1u << 20;
constexpr unsigned TS = 2176;
constexpr unsigned KV_BYTES = 2u * 32u * 64u * TS;
constexpr unsigned WS_O = 10u << 20;
constexpr unsigned O_BYTES = 4u << 20;
constexpr unsigned WS_STAMP = 16u << 20;
constexpr unsigned OOB = 0x7ff00000u;
constexpr unsigned L_W = 65536;
constexpr unsigned L_WO = 0, L_PART = 65536, L_PL = 114688, L_CNT = 117760, L_MDIAG = 118784, L_MFAR = 122880, L_Q = 126976, L_GW = 143360, L_Z = 147456, L_G = 148480, L_Q14 = 106496, L_Q15 = 148992, L_END = 157184;
constexpr float QSCALE = 0.25f * 1.4426950408889634f;
constexpr float LMAX = 1.2676506e30f, LMIN = 9.5367432e-7f;
constexpr unsigned SPIN_CAP = 1u << 17;

#define SBAR() __builtin_amdgcn_sched_barrier(0)
#define PIN(x) asm volatile("" : "+v"(x))
#define MFMA(a, b, c) __builtin_amdgcn_mfma_f32_32x32x16_bf16(a, b, c, 0, 0, 0)
#define BAR() asm volatile("s_barrier" ::: "memory")
#define CBAR() asm volatile("s_waitcnt lgkmcnt(0)\n\ts_barrier" ::: "memory")
#define DRAIN() asm volatile("s_waitcnt vmcnt(0)" ::: "memory")
#define PACEN 3
#define PACE() asm volatile("s_nop %0" :: "i"(PACEN) : "memory")
#define RLX_AGENT __ATOMIC_RELAXED, __HIP_MEMORY_SCOPE_AGENT
#define STAMP(i)

__device__ __forceinline__ void glds16(unsigned voff, const void* sbase, unsigned ldsaddr) {
    asm volatile("s_mov_b32 m0, %2\n\ts_nop 0\n\tglobal_load_lds_dwordx4 %0, %1" ::"v"(voff), "s"(sbase), "s"(ldsaddr) : "memory");
}
__device__ __forceinline__ void glds16x2(unsigned v0, unsigned v1, const void* sbase, unsigned ldsaddr) {
    asm volatile("s_mov_b32 m0, %3\n\ts_nop 0\n\tglobal_load_lds_dwordx4 %0, %2\n\tglobal_load_lds_dwordx4 %1, %2 offset:1024"
                 ::"v"(v0), "v"(v1), "s"(sbase), "s"(ldsaddr) : "memory");
}
__device__ __forceinline__ void glds16x4(unsigned v0, unsigned v1, unsigned v2, unsigned v3, const void* sbase, unsigned ldsaddr) {
    asm volatile("s_mov_b32 m0, %5\n\ts_nop 0\n\tglobal_load_lds_dwordx4 %0, %4\n\tglobal_load_lds_dwordx4 %1, %4 offset:1024\n\t"
                 "global_load_lds_dwordx4 %2, %4 offset:2048\n\tglobal_load_lds_dwordx4 %3, %4 offset:3072"
                 ::"v"(v0), "v"(v1), "v"(v2), "v"(v3), "s"(sbase), "s"(ldsaddr) : "memory");
}
__device__ __forceinline__ unsigned cvtpk(float lo, float hi) {
    typedef __attribute__((ext_vector_type(2))) float f32x2;
    return __builtin_bit_cast(unsigned, __builtin_convertvector((f32x2){lo, hi}, bf16x2));
}
__device__ __forceinline__ bf16x8 as_frag(u32x4 v) { return __builtin_bit_cast(bf16x8, v); }
__device__ __forceinline__ bf16x8 lds_frag(lds_cptr p) { return *(const LAS bf16x8*)p; }
__device__ __forceinline__ bf16x8 tr_frag(lds_cptr p0, lds_cptr p1) {
    s16x4 a = __builtin_amdgcn_ds_read_tr16_b64_v4i16((LAS s16x4*)p0);
    s16x4 b = __builtin_amdgcn_ds_read_tr16_b64_v4i16((LAS s16x4*)p1);
    return __builtin_bit_cast(bf16x8, __builtin_shufflevector(a, b, 0, 1, 2, 3, 4, 5, 6, 7));
}
__device__ __forceinline__ f32x16 pv_lo(bf16x8 v, const f32x16& p, f32x16 acc, int idx) {
    f32x8 h = __builtin_shufflevector(p, p, 0, 1, 2, 3, 4, 5, 6, 7);
    return __builtin_amdgcn_smfmac_f32_32x32x32_bf16(v, __builtin_bit_cast(bf16x16, h), acc, idx, 0, 0);
}
__device__ __forceinline__ f32x16 pv_hi(bf16x8 v, const f32x16& p, f32x16 acc, int idx) {
    f32x8 h = __builtin_shufflevector(p, p, 8, 9, 10, 11, 12, 13, 14, 15);
    return __builtin_amdgcn_smfmac_f32_32x32x32_bf16(v, __builtin_bit_cast(bf16x16, h), acc, idx, 0, 0);
}
#define MX3(a, b, c) __builtin_fmaxf(__builtin_fmaxf((a), (b)), (c))
__device__ __forceinline__ float max16(const f32x16& s) {
    float a = MX3(s[0], s[1], s[2]), b = MX3(s[3], s[4], s[5]);
    a = MX3(a, s[6], s[7]); b = MX3(b, s[8], s[9]); a = MX3(a, s[10], s[11]); b = MX3(b, s[12], s[13]);
    return MX3(a, b, __builtin_fmaxf(s[14], s[15]));
}
__device__ __forceinline__ float halfmax(float m) {
    auto r = __builtin_amdgcn_permlane32_swap(__float_as_uint(m), __float_as_uint(m), false, false);
    return __builtin_fmaxf(__uint_as_float(r[0]), __uint_as_float(r[1]));
}
__device__ __forceinline__ f32x16 lds_f32x16(lds_cptr p) {
    f32x4 a = *(const LAS f32x4*)p, b = *(const LAS f32x4*)(p + 1024), c = *(const LAS f32x4*)(p + 2048), d = *(const LAS f32x4*)(p + 3072);
    f32x8 lo = __builtin_shufflevector(a, b, 0, 1, 2, 3, 4, 5, 6, 7), hi = __builtin_shufflevector(c, d, 0, 1, 2, 3, 4, 5, 6, 7);
    return __builtin_shufflevector(lo, hi, 0, 1, 2, 3, 4, 5, 6, 7, 8, 9, 10, 11, 12, 13, 14, 15);
}
#define BLOAD(rs, vo, so) as_frag(__builtin_amdgcn_raw_buffer_load_b128(rs, vo, so, 16))
#define BSTORE(v, rs, vo, so) __builtin_amdgcn_raw_buffer_store_b128(v, rs, (vo) + (so), 0, 16)

#define POLLSLP 8
#define BSLP 44
__device__ __forceinline__ void wait_tags_end(u64 v, const GAS u64* p, u64 tag, GAS u64* pub, int lane) {
    if (pub) { DRAIN(); if (lane == 0) __hip_atomic_store(pub, tag, RLX_AGENT); }
#pragma nounroll
    for (unsigned spins = 0; spins < SPIN_CAP && !__all(v == tag); ++spins) {
        __builtin_amdgcn_s_sleep(POLLSLP);
        v = __hip_atomic_load(p, RLX_AGENT);
    }
    asm volatile("" ::: "memory");
}

__device__ __forceinline__ void wait_tags(const GAS u64* p, u64 tag, GAS u64* pub = nullptr, int lane = 0) {
    wait_tags_end(__hip_atomic_load(p, RLX_AGENT), p, tag, pub, lane);
}

__device__ __forceinline__ void cnt_arrive(lds_ptr cnt, int lane) {
    asm volatile("s_waitcnt lgkmcnt(0)" ::: "memory");
    if (lane == 0) __hip_atomic_fetch_add((LAS unsigned*)cnt, 1u, __ATOMIC_RELAXED, __HIP_MEMORY_SCOPE_WORKGROUP);
}
__device__ __forceinline__ void cnt_wait(lds_ptr cnt) {
#pragma nounroll
    for (unsigned spins = 0; spins < (1u << 22); ++spins) {
        if (__builtin_amdgcn_readfirstlane(__hip_atomic_load((LAS unsigned*)cnt, __ATOMIC_RELAXED, __HIP_MEMORY_SCOPE_WORKGROUP)) == 4) break;
        __builtin_amdgcn_s_sleep(1);
    }
    asm volatile("" ::: "memory");
}
__device__ __forceinline__ unsigned kvflag(int br, int quad, int tile, int type) {
    return WS_KVFLAG + (unsigned)((((br * 8 + quad) * 4 + (tile & 3)) * 2 + type) * 16 + (tile >> 2)) * FSTR;
}

__device__ __forceinline__ void slow_rows(__amdgpu_buffer_rsrc_t rs, unsigned sbase, int lo, int hi, int g, bool band, bf16x8 q,
                                          unsigned vk, unsigned vv0, unsigned vv1, lds_cptr lds, int lane, int idx, f32x16& acc) {
    float m = -__builtin_inff();
    for (int pass = 0; pass < 2; ++pass) {
        if (pass) { m = halfmax(m); acc = f32x16{}; }
        for (int kt = lo; kt <= hi; ++kt) {
            const unsigned so = sbase + (unsigned)kt * TS;
            const bf16x8 k = BLOAD(rs, vk, so), v0 = BLOAD(rs, vv0, so), v1 = BLOAD(rs, vv1, so);
            f32x16 c = f32x16{};
            if (band && kt == g) c = lds_f32x16(lds + L_MDIAG + lane * 16);
            if (band && kt == g - 8) c = lds_f32x16(lds + L_MFAR + lane * 16);
            f32x16 s = MFMA(k, q, c);
            if (!pass) m = __builtin_fmaxf(m, max16(s));
            else {
#pragma unroll
                for (int r = 0; r < 16; ++r) s[r] = __builtin_amdgcn_exp2f(s[r] - m);
                acc = pv_lo(v0, s, acc, idx); acc = pv_hi(v1, s, acc, idx);
            }
        }
    }
}

#define PK4(A, o) ((u32x4){cvtpk(A[o], A[o + 1]), cvtpk(A[o + 2], A[o + 3]), cvtpk(A[o + 4], A[o + 5]), cvtpk(A[o + 6], A[o + 7])})
#define PROF_ARG
#define PROF_PASS
__device__ __forceinline__ u64 load_tag(const char* ws) {
    const u64 e = *(const __attribute__((address_space(4))) u64*)(ws + WS_EPOCH);
    return e * 0x9E3779B97F4A7C15ull + 0x632BE59BD9B4E019ull;
}
template <int E>
__device__ __forceinline__ u32x4 pack_kv(int i, const f32x16& a) {
    const int s = i & 1;
    if (E == 0) return PK4(a, 8 * s);
    auto x = __builtin_amdgcn_permlane32_swap(cvtpk(a[8 * s], a[8 * s + 1]), cvtpk(a[8 * s + 4], a[8 * s + 5]), false, false);
    auto y = __builtin_amdgcn_permlane32_swap(cvtpk(a[8 * s + 2], a[8 * s + 3]), cvtpk(a[8 * s + 6], a[8 * s + 7]), false, false);
    return (u32x4){x[0], y[0], x[1], y[1]};
}
#define PUT_KV(i, v, vo) BSTORE(v, rkv, vo, E ? ((i) & 1) * 512 + ((i) >> 1) * (128 * TS) : (i) * (64 * TS))
template <int E>
__device__ __forceinline__ void project(lds_ptr lds, char* ws, __amdgpu_buffer_rsrc_t rkv, __amdgpu_buffer_rsrc_t ro, int lane, int t, int p, int j, u64 tag PROF_ARG) {
    const int r32 = lane & 31, hh = lane >> 5;
    const unsigned q4 = (lane & 15) >> 2, wcol = ((lane >> 4) & 1) * 32 + (lane & 3) * 8;
    lds_cptr xa[4];
#pragma unroll
    for (int ks = 0; ks < 4; ++ks) xa[ks] = lds + (32 * t + r32) * 128 + (((hh ^ ((r32 >> 1) & 7)) * 16) ^ (ks * 32));
    unsigned w0o = L_W + (8 * hh + q4) * 128 + (q4 >> 1) * 64 + wcol, w1o = w0o ^ 64;
    PIN(w0o); PIN(w1o);
    const lds_cptr w0 = lds + w0o, w1 = lds + w1o, wq = lds + (8 * hh + q4) * 64 + wcol;
#define XRD(n) lds_frag(xa[(n) & 3] + (((n) >> 2) & 3) * 16384)
#define WRD(b, n) tr_frag(b + ((n) & 15) * 2048, b + ((n) & 15) * 2048 + 512)
#define YOF(n) ((n) >= 28 ? 32768 + ((n) & 3) * 2048 : ((n) & 15) * 2048)
#define YRD(b, n) tr_frag(b + YOF(n), b + YOF(n) + 512)
#define QOF(br, n) (((n) >= 28 ? L_Q15 - L_Q14 : ((n) >= 24 ? 0 : (((n) >> 2) & 3) * 16384)) + (br) * 4096 + ((n) & 3) * 1024)
#define QRD(br, n) tr_frag(((n) >= 24 ? wqh : wq) + QOF(br, n), ((n) >= 24 ? wqh : wq) + QOF(br, n) + 256)
#define MM(W, X, A) (E ? MFMA(X, W, A) : MFMA(W, X, A))
    bf16x8 xf[32], f0[4], f1[4];
    f32x16 a0 = f32x16{}, a1 = f32x16{};
    unsigned gwo = (lane & 15) == ((lane >> 4) & 1) ? L_GW + t * 1024 + hh * 16 : L_Z;
    PIN(gwo);
    f32x4 zg = f32x4{};
    bf16x8 gf[4];
#define RD1(n) do { xf[n] = XRD(n); f0[(n) & 3] = WRD(w0, n); f1[(n) & 3] = WRD(w1, n); SBAR(); } while (0)
#define GRD(n) gf[(n) & 3] = lds_frag(lds + gwo + (n) * 32)
#define GMM(n) zg = __builtin_amdgcn_mfma_f32_16x16x32_bf16(gf[(n) & 3], xf[n], zg, 0, 0, 0)
#define MM1(n) do { a0 = MM(f0[(n) & 3], xf[n], a0); a1 = MM(f1[(n) & 3], xf[n], a1); SBAR(); } while (0)
#pragma unroll
    for (int s = 0; s < 8; ++s) {
        if (s == 0) CBAR(); else BAR();
        if (s == 3) { if (E) BSTORE(((u32x4){0x3f803f80u, 0x3f803f80u, 0x3f803f80u, 0x3f803f80u}), rkv,
                                  (unsigned)((hh * 32 + 4 * (p >> 1) + (r32 >> 3)) * 64 + 4 * j + t) * TS + 2048 + (lane & 7) * 16, 0); }
        if (s == 5) BSTORE(((u32x4){~0u, ~0u, ~0u, ~0u}), ro, lane * 16, (unsigned)((4 * j + t) * 32 + 2 * p) * 1024);
        SBAR();
        RD1(4 * s); RD1(4 * s + 1);
        if (s) { GMM(4 * s - 4); GMM(4 * s - 3); GMM(4 * s - 2); GMM(4 * s - 1); SBAR(); }
        MM1(4 * s); RD1(4 * s + 2); MM1(4 * s + 1); RD1(4 * s + 3); MM1(4 * s + 2); MM1(4 * s + 3);
        if (s == 0) DRAIN();
        GRD(4 * s); GRD(4 * s + 1); GRD(4 * s + 2); GRD(4 * s + 3); SBAR();
    }
    GMM(28); GMM(29); GMM(30); GMM(31); SBAR();
    const unsigned tile = 4 * j + t;
    const unsigned vo = (unsigned)(4 * (p >> 1) * 64 + tile) * TS + (E ? (r32 >> 4) * (64 * TS) + 1024 + hh * 256 + (r32 & 15) * 16 : lane * 16);
    u32x4 pk[4] = {pack_kv<E>(0, a0), pack_kv<E>(1, a0), pack_kv<E>(2, a1), pack_kv<E>(3, a1)};
    PIN(pk[0]); PIN(pk[1]); PIN(pk[2]); PIN(pk[3]);
#define PRE8 2
#define XBAR
#define STPB 1
#define PUBN 15
#pragma unroll
    for (int i = 0; i < PRE8; ++i) PUT_KV(i, pk[i], vo);
    SBAR();
    BAR();
    if (lane < 16) {
        LAS float* gp = (LAS float*)(lds + L_G + t * 128) + lane;
        gp[0] = __builtin_amdgcn_rcpf(1.f + __expf(-zg[0])); gp[16] = __builtin_amdgcn_rcpf(1.f + __expf(-zg[1]));
    }
    SBAR();
    BAR(); SBAR();
    unsigned wqo = L_Q14 + (8 * hh + q4) * 64 + wcol;
    PIN(wqo);
    const lds_cptr wqh = lds + wqo;
    bf16x8 u0 = QRD(0, 0), u1 = QRD(1, 0), y0 = WRD(w0, 0), y1 = WRD(w1, 0);
    f32x16 b0 = f32x16{}, b1 = f32x16{}, aQ0 = f32x16{}, aQ1 = f32x16{};
    SBAR();
#pragma unroll
    for (int n = 0; n < 24; ++n) {
        aQ0 = MFMA(u0, xf[n], aQ0); SBAR();
        if (n == PUBN) { DRAIN(); if (lane == 0) __hip_atomic_store((GAS u64*)(ws + kvflag(0, p >> 1, tile, E)), tag, RLX_AGENT); SBAR(); }
        if ((n & 3) == 3) {
#pragma unroll
            for (int i = PRE8 + (n >> 2) * STPB; i < PRE8 + (n >> 2) * STPB + STPB; ++i) if (i < 4) PUT_KV(i, pk[i], vo);
            SBAR(); BAR(); SBAR();
        }
        u0 = QRD(0, n + 1); SBAR();
        aQ1 = MFMA(u1, xf[n], aQ1); SBAR();
        u1 = n < 23 ? QRD(1, n + 1) : QRD(0, 25); SBAR();
        b0 = MM(y0, xf[n], b0); SBAR(); y0 = n < 23 ? YRD(w0, n + 1) : QRD(0, 26); SBAR();
        b1 = MM(y1, xf[n], b1); SBAR(); y1 = n < 23 ? YRD(w1, n + 1) : QRD(0, 27); SBAR();
    }
    aQ0 = MFMA(u0, xf[24], aQ0); SBAR(); u0 = QRD(0, 28); SBAR(); aQ0 = MFMA(u1, xf[25], aQ0); SBAR(); u1 = QRD(0, 29); SBAR();
    aQ0 = MFMA(y0, xf[26], aQ0); SBAR(); y0 = QRD(0, 30); SBAR(); aQ0 = MFMA(y1, xf[27], aQ0); SBAR(); y1 = QRD(0, 31); SBAR();
    aQ0 = MFMA(u0, xf[28], aQ0); SBAR(); aQ0 = MFMA(u1, xf[29], aQ0); SBAR(); u1 = QRD(1, 24); SBAR();
    aQ0 = MFMA(y0, xf[30], aQ0); SBAR(); y0 = YRD(w0, 24); SBAR(); aQ0 = MFMA(y1, xf[31], aQ0); SBAR(); y1 = YRD(w1, 24); SBAR();
    const lds_ptr qd = lds + L_Q + t * 1024 + lane * 16;
#pragma unroll
    for (int n = 24; n < 32; ++n) {
        aQ1 = MFMA(u1, xf[n], aQ1); SBAR(); if (n < 31) u1 = QRD(1, n + 1); if (n > 24) PACE(); SBAR();
        b0 = MM(y0, xf[n], b0); SBAR(); if (n < 31) y0 = YRD(w0, n + 1); if (n > 24) PACE(); SBAR();
        if (n == 24) {
#pragma unroll
            for (int r = 8; r < 16; ++r) aQ0[r] *= QSCALE;
            *(LAS u32x4*)(qd + 4096) = PK4(aQ0, 8);
            SBAR();
        }
        b1 = MM(y1, xf[n], b1); SBAR();
        if (n == 24) { __builtin_amdgcn_s_setprio(2); cnt_arrive(lds + L_CNT + 12, lane); STAMP(4); SBAR(); }
        if (n < 31) y1 = YRD(w1, n + 1); if (n > 24) PACE(); SBAR();
    }
#pragma unroll
    for (int r = 0; r < 8; ++r) aQ0[r] *= QSCALE;
#pragma unroll
    for (int r = 0; r < 16; ++r) aQ1[r] *= QSCALE;
    *(LAS u32x4*)qd = PK4(aQ0, 0); *(LAS u32x4*)(qd + 8192) = PK4(aQ1, 0); *(LAS u32x4*)(qd + 12288) = PK4(aQ1, 8);
    cnt_arrive(lds + L_CNT + 8, lane);
    const unsigned vo2 = vo + 32 * 64 * TS;
#define YSLP 12
    PUT_KV(0, pack_kv<E>(0, b0), vo2); if (YSLP) __builtin_amdgcn_s_sleep(YSLP); PUT_KV(1, pack_kv<E>(1, b0), vo2); if (YSLP) __builtin_amdgcn_s_sleep(YSLP);
    PUT_KV(2, pack_kv<E>(2, b1), vo2); if (YSLP) __builtin_amdgcn_s_sleep(YSLP); PUT_KV(3, pack_kv<E>(3, b1), vo2);
    DRAIN();
    if (lane == 0) __hip_atomic_store((GAS u64*)(ws + kvflag(1, p >> 1, tile, E)), tag, RLX_AGENT);
    STAMP(19);
}

#define AB_WO (512 * 1024)
#define WOIN 2
#define CODE_LINES 256

struct Att {
    lds_ptr lds; char* ws; __amdgpu_buffer_rsrc_t rkv, ro;
    int lane, t, p, g, head; u64 tag; unsigned vk, vv0, vv1;
};
#define IDX 0xdddddddd
#define BLOADL(old, rs, vo, so) BLOAD(rs, vo, so)
#define LD3(K, V0, V1, rs, so) do { K = BLOAD(rs, a.vk, so); V0 = BLOAD(rs, a.vv0, so); V1 = BLOAD(rs, a.vv1, so); } while (0)
#define EX(X, r) X[r] = __builtin_amdgcn_exp2f(X[r])
#define STEP(X, Y, OP, VP, KN, QN, CN, O, V0, F1, F2) do { \
        EX(X, 0); EX(X, 1); SBAR(); OP = pv_hi(VP, Y, OP, IDX); SBAR(); F1; SBAR(); EX(X, 2); EX(X, 3); EX(X, 4); SBAR(); Y = MFMA(KN, QN, CN); SBAR(); \
        EX(X, 5); EX(X, 6); EX(X, 7); SBAR(); EX(X, 8); EX(X, 9); SBAR(); O = pv_lo(V0, X, O, IDX); SBAR(); F2; SBAR(); \
        EX(X, 10); EX(X, 11); EX(X, 12); EX(X, 13); EX(X, 14); EX(X, 15); SBAR(); } while (0)

__device__ __forceinline__ f32x16 lds_zero16(lds_cptr lds, int lane) {
    const volatile LAS f32x4* z = (const volatile LAS f32x4*)(lds + L_Z) + lane;
    const f32x4 a = *z, b = *z, c = *z, d = *z;
    f32x8 lo = __builtin_shufflevector(a, b, 0, 1, 2, 3, 4, 5, 6, 7), hi = __builtin_shufflevector(c, d, 0, 1, 2, 3, 4, 5, 6, 7);
    return __builtin_shufflevector(lo, hi, 0, 1, 2, 3, 4, 5, 6, 7, 8, 9, 10, 11, 12, 13, 14, 15);
}

template <int C, int BR>
__device__ __forceinline__ GAS u64* finish(const Att& a, f32x16 oo, float lrest, bf16x8 qa) {
    const float gt = *(const LAS float*)(a.lds + L_G + a.t * 128 + (a.lane & 31) * 4);
    auto lsw = __builtin_amdgcn_permlane32_swap(__float_as_uint(oo[8]), __float_as_uint(oo[8]), false, false);
    float l = __uint_as_float(lsw[0]) + lrest, z = 0.f;
#pragma unroll
    for (int r = 0; r < 8; ++r) z = __builtin_fmaf(oo[r], 0.f, z);
    if (__builtin_expect(__any(!(l + z < LMAX && l > LMIN)), 0)) {
        if (!BR) { wait_tags((const GAS u64*)(a.ws + kvflag(0, a.p >> 1, 0, 0) + a.lane * FSTR), a.tag); wait_tags((const GAS u64*)(a.ws + kvflag(0, a.p >> 1, 2, 0) + a.lane * FSTR), a.tag); }
        slow_rows(a.rkv, (unsigned)((BR * 32 + a.head) * 64) * TS, BR ? (a.g >= 8 ? a.g - 8 : 0) : 0, BR ? a.g : 63, a.g, BR, qa, a.vk, a.vv0, a.vv1, a.lds, a.lane, IDX, oo);
        auto l2 = __builtin_amdgcn_permlane32_swap(__float_as_uint(oo[8]), __float_as_uint(oo[8]), false, false);
        l = __uint_as_float(l2[0]);
    }
    const float il = __builtin_amdgcn_rcpf(l) * (BR ? 1.f - gt : gt);
    u32x4 v = (u32x4){cvtpk(oo[0] * il, oo[1] * il), cvtpk(oo[2] * il, oo[3] * il), cvtpk(oo[4] * il, oo[5] * il), cvtpk(oo[6] * il, oo[7] * il)};
    if (!C && !BR) v = __builtin_elementwise_min(v, (u32x4){~1u, ~1u, ~1u, ~1u});
    BSTORE(v, a.ro, a.vk, (unsigned)((BR * 64 + a.g) * 32 + a.head) * 1024);
    return (GAS u64*)(a.ws + WS_OFLAG) + ((BR * 64 + a.g) * 2 + C) * 16 + a.p;
}

template <int C>
__device__ __forceinline__ GAS u64* attn_full(const Att& a, f32x16 oo, f32x16 o1, f32x16 o2, f32x16 o3, f32x16 S1, __amdgpu_buffer_rsrc_t rwo, unsigned vwo, unsigned wi PROF_ARG) {
    const lds_ptr lds = a.lds; const int t = a.t, lane = a.lane, w = 4 * C + t;
    const f32x16 zc = f32x16{};
    bf16x8 ka, va0, va1, kb, vb0, vb1, kc, vc0, vc1, kd, vd0, vd1;
    const unsigned sBeg = (unsigned)(a.head * 64 + t) * TS, sEnd = sBeg + 64 * TS;
    unsigned so = (unsigned)(a.head * 64 + a.g) * TS;
#define NEXT_A() asm volatile("s_addk_i32 %0, %1" : "+s"(so) : "i"(4 * TS) : "scc")
#define WRAP_A() asm volatile("s_cmp_eq_u32 %0, %1\n\ts_cselect_b32 %0, %2, %0" : "+s"(so) : "s"(sEnd), "s"(sBeg) : "scc")
    LD3(ka, va0, va1, a.rkv, so); NEXT_A(); WRAP_A(); SBAR(); LD3(kb, vb0, vb1, a.rkv, so); NEXT_A(); WRAP_A(); SBAR();
    LD3(kc, vc0, vc1, a.rkv, so); NEXT_A(); WRAP_A(); SBAR();
    if (C) { cnt_wait(lds + L_CNT + 12); STAMP(4); }
    const lds_cptr qp = lds + L_Q + C * 4096 + lane * 16;
    bf16x8 qa = lds_frag(qp + t * 1024), q1 = lds_frag(qp + ((t + 1) & 3) * 1024), q2 = lds_frag(qp + ((t + 2) & 3) * 1024), q3 = lds_frag(qp + ((t + 3) & 3) * 1024);
    vd1 = lds_frag(lds + L_Z + lane * 16);
    PIN(q1); PIN(q2); PIN(q3); PIN(vd1); PIN(oo); PIN(o1); PIN(o2); PIN(o3); PIN(S1);
    __builtin_amdgcn_s_waitcnt(0x0F76);
    f32x16 S0 = MFMA(ka, qa, zc);
    PIN(S0);
#pragma unroll
    for (int i = 0; i < 12; ++i) __builtin_amdgcn_s_setprio(C);
    STAMP(3);
    SBAR();
    unsigned wso = 0;
#define NEXT_AW() do { if (C) asm volatile("s_addk_i32 %0, %3\n\ts_lshl_b32 %1, %2, 4" : "+s"(so), "=s"(wso) : "s"(wi), "i"(4 * TS) : "scc"); else NEXT_A(); } while (0)
#define WO_DMA() do { if (C) __builtin_amdgcn_raw_ptr_buffer_load_lds(rwo, (LAS void*)(lds + L_WO + t * 16384 + wi), 16, vwo, wso, 0, 0); } while (0)
#define WO_NEXT() do { if (C) asm volatile("s_addk_i32 %0, 0x400\n\ts_and_b32 %0, %0, 0x3fff" : "+s"(wi) :: "scc"); } while (0)
#define TILE(K, V0, V1, KN, LK, LV0, LV1, NW) do { \
        STEP(S0, S1, o3, LV1, K, q1, zc, oo, V0, LK = BLOADL(K, a.rkv, a.vk, so), LV0 = BLOADL(V0, a.rkv, a.vv0, so)); \
        STEP(S1, S0, oo, V1, K, q2, zc, o1, V0, LV1 = BLOADL(V1, a.rkv, a.vv1, so), NEXT_AW()); \
        STEP(S0, S1, o1, V1, K, q3, zc, o2, V0, WRAP_A(), WO_DMA()); \
        STEP(S1, S0, o2, V1, KN, qa, zc, o3, V0, __builtin_amdgcn_s_waitcnt(0x0F70 | (C ? NW : 6)), WO_NEXT()); } while (0)
#pragma nounroll
    for (int it = 0; it < 4; ++it) {
        TILE(ka, va0, va1, kb, kd, vd0, vd1, 7);
        TILE(kb, vb0, vb1, kc, ka, va0, va1, 8);
        TILE(kc, vc0, vc1, kd, kb, vb0, vb1, 9);
        TILE(kd, vd0, vd1, ka, kc, vc0, vc1, 9);
    }
    o3 = pv_hi(vd1, S1, o3, IDX);
    STAMP(6);
    if (C) cnt_wait(lds + L_CNT + 8); else __builtin_amdgcn_s_setprio(2);
#define PUT(O, sl) do { lds_ptr d_ = lds + L_PART + (w * 3 + sl) * 2048 + lane * 16; \
        *(LAS f32x4*)d_ = __builtin_shufflevector(O, O, 0, 1, 2, 3); *(LAS f32x4*)(d_ + 1024) = __builtin_shufflevector(O, O, 4, 5, 6, 7); } while (0)
    PUT(o1, 0); PUT(o2, 1); PUT(o3, 2);
    if (lane < 32) { LAS float* lp = (LAS float*)(lds + L_PL + w * 384) + lane; lp[0] = o1[8]; lp[32] = o2[8]; lp[64] = o3[8]; }
    if (C) { DRAIN(); cnt_arrive(lds + L_CNT, lane); cnt_wait(lds + L_CNT); } else CBAR();
    float lrest = 0.f;
#pragma unroll
    for (int s = 0; s < 3; ++s) {
        const int src = (C * 4 + ((t - 1 - s) & 3)) * 3 + s;
        const lds_cptr d = lds + L_PART + src * 2048 + lane * 16;
        const f32x4 x = *(const LAS f32x4*)d, y = *(const LAS f32x4*)(d + 1024);
#pragma unroll
        for (int r = 0; r < 4; ++r) { oo[r] += x[r]; oo[4 + r] += y[r]; }
        const float lp = *(const LAS float*)(lds + L_PL + src * 128 + (lane & 31) * 4);
        lrest = s ? lrest + lp : lp;
    }
    return finish<C, 0>(a, oo, lrest, qa);
}

template <int C>
__device__ __forceinline__ GAS u64* attn_window(const Att& a, GAS u64* pub PROF_ARG) {
    const lds_ptr lds = a.lds; const int t = a.t, lane = a.lane, g = a.g;
    const f32x16 zc = f32x16{};
    { const int i = lane & 15, kt = g - (i < 9 ? i : 0);
      wait_tags((const GAS u64*)(a.ws + kvflag(1, a.p >> 1, kt >= 0 ? kt : 0, (lane >> 4) & 1)), a.tag, pub, lane); }
    STAMP(25);
    bf16x8 k[9], v0[9], v1[9];
    const unsigned sB = (unsigned)((32 + a.head) * 64) * TS;
#define LDB(i) do { const int kt_ = g - (i); \
        const __amdgpu_buffer_rsrc_t rb_ = __builtin_amdgcn_make_buffer_rsrc(a.ws + WS_KV, 0, kt_ >= 0 ? KV_BYTES : 0u, 0x00020000); \
        LD3(k[i], v0[i], v1[i], rb_, sB + (unsigned)(kt_ >= 0 ? kt_ : 0) * TS); } while (0)
    LDB(0); SBAR(); LDB(1); SBAR(); LDB(2); SBAR();
    const bf16x8 qa = lds_frag(lds + L_Q + 8192 + C * 4096 + t * 1024 + lane * 16);
    const f32x16 cd = lds_f32x16(lds + L_MDIAG + lane * 16), cf = lds_f32x16(lds + L_MFAR + lane * 16);
    f32x16 oo = lds_zero16(lds, lane);
    f32x16 S0 = MFMA(k[0], qa, cd), S1 = MFMA(k[1], qa, zc);
    SBAR(); LDB(3); SBAR();
#pragma unroll
    for (int r = 0; r < 10; ++r) { EX(S0, r); SBAR(); }
    oo = pv_lo(v0[0], S0, oo, IDX); SBAR();
#pragma unroll
    for (int r = 10; r < 16; ++r) EX(S0, r);
    SBAR();
    STEP(S1, S0, oo, v1[0], k[2], qa, zc, oo, v0[1], LDB(4), );
    STEP(S0, S1, oo, v1[1], k[3], qa, zc, oo, v0[2], LDB(5), );
    STEP(S1, S0, oo, v1[2], k[4], qa, zc, oo, v0[3], LDB(6), );
    STEP(S0, S1, oo, v1[3], k[5], qa, zc, oo, v0[4], LDB(7), );
    STEP(S1, S0, oo, v1[4], k[6], qa, zc, oo, v0[5], LDB(8), );
    STEP(S0, S1, oo, v1[5], k[7], qa, zc, oo, v0[6], , );
    STEP(S1, S0, oo, v1[6], k[8], qa, cf, oo, v0[7], , );
    STEP(S0, S1, oo, v1[7], k[8], qa, cf, oo, v0[8], , );
    oo = pv_hi(v1[8], S0, oo, IDX);
    STAMP(7);
    return finish<C, 1>(a, oo, 0.f, qa);
}

__global__ void __launch_bounds__(512, 2)
gmix_kernel(const char* x, const char* wA, const char* woA, const char* wB, const char* woB, const char* gw, char* out, char* ws) {
    extern __shared__ __attribute__((aligned(16))) char smem[];
    const lds_ptr lds = (lds_ptr)smem;
    const unsigned lds0 = (unsigned)(uintptr_t)lds;
    const int tid = threadIdx.x, lane = tid & 63, r32 = lane & 31, hh = lane >> 5;
    const int w = __builtin_amdgcn_readfirstlane(tid >> 6);
    const int bid = blockIdx.x, xcd = bid & 7, rr = bid >> 3;
    const int p = 4 * (xcd & 3) + (rr & 3), j = 8 * (xcd >> 2) + (rr >> 2);
    const int jo = 2 * xcd + (rr >> 4), po = rr & 15;
    const int t = w & 3, c = w >> 2;
    const int e = p & 1;
    STAMP(0);
    u64 pc; asm volatile("s_getpc_b64 %0" : "=s"(pc));
    unsigned rot = (j + 2 * (p & 3)) & 7;
    unsigned vx0 = (128 * j + 32 * t + (lane >> 3)) * 1024 + (((lane & 7) ^ (lane >> 4)) * 16), vx1 = (vx0 ^ 64) + 7168, vx2 = vx0 + 14336, vx3 = vx1 + 14336;
    unsigned vw0 = (16 * t + (lane >> 3)) * 3072 + 1024 + 1024 * e + 128 * (p >> 1) + (((lane & 7) ^ (((lane >> 4) & 1) << 2)) * 16), vw1 = vw0 + 8 * 3072 - 1024;
    unsigned mx = lds0 + t * 4096, mw = lds0 + L_W + t * 2048, ow = rot * (64 * 3072);
    asm volatile("" : "+v"(vx0), "+v"(vx1), "+v"(vx2), "+v"(vx3), "+v"(vw0), "+v"(vw1));
    asm volatile("" : "+s"(mx), "+s"(mw), "+s"(rot), "+s"(ow));
    SBAR();
    asm volatile("" : "+s"(x), "+s"(wA), "+s"(woA), "+s"(wB), "+s"(woB), "+s"(gw), "+s"(out), "+s"(ws));
#define MK_ATT(C) Att a; a.lds = lds; a.ws = ws; a.lane = lane; a.t = t; a.p = p; a.g = 4 * j + t; a.head = 2 * p + C; \
    a.rkv = __builtin_amdgcn_make_buffer_rsrc(ws + WS_KV, 0, KV_BYTES, 0x00020000); a.ro = __builtin_amdgcn_make_buffer_rsrc(ws + WS_O, 0, O_BYTES, 0x00020000); \
    a.vk = lane * 16; a.vv0 = r32 < 16 ? 1024 + hh * 256 + r32 * 16 : (r32 == 16 ? 2048 : OOB); a.vv1 = r32 < 16 ? a.vv0 + 512 : a.vv0
    if (c) {
#define DMA1(s) do { const unsigned ks_ = ((s) + rot) & 7; \
        glds16x4(vx0, vx1, vx2, vx3, x + ks_ * 128, mx + ((s) & 3) * 16384); glds16x2(vw0, vw1, wA + ks_ * (64 * 3072), mw + ((s) & 3) * 8192); } while (0)
#define DMA2(s) do { const unsigned ks_ = (((s) + rot) & 7) * (64 * 3072); \
        glds16x2(vq0, vq1, wq + ks_, mq + ((s) == 14 ? L_Q14 : ((s) & 3) * 16384)); glds16x2(vw0, vw1, wB + ks_, mw + ((s) & 3) * 8192); SBAR(); } while (0)
#define LBAR(N) asm volatile("s_waitcnt vmcnt(" #N ")\n\ts_barrier" ::: "memory")
        STAMP(20);
        glds16x4(vx0, vx1, vx2, vx3, x + rot * 128, mx); glds16x2(vw0, vw1, wA + ow, mw);
        SBAR();
        DMA1(1);
        STAMP(22);
        SBAR();
#pragma nounroll
        for (int s = 2; s < 8; ++s) { LBAR(6); DMA1(s);
        }
        const unsigned vq0 = (32 * (t & 1) + (lane >> 2)) * 3072 + 64 * p + (lane & 3) * 16, vq1 = vq0 + 16 * 3072 - 1024, mq = lds0 + t * 2048;
        const char* wq = (t >> 1) ? wB : wA;
        LBAR(6); STAMP(14); DMA2(8);
        LBAR(4); STAMP(15);
        BAR();
        DMA2(9);
        MK_ATT(1);
        a.tag = load_tag(ws);
        asm volatile("" : "+s"(a.tag)); PIN(a.vk); PIN(a.vv0); PIN(a.vv1);
        const GAS u64* fp = (const GAS u64*)(ws + kvflag(0, p >> 1, t, 0) + r32 * FSTR);
        PIN(fp);
        LBAR(4); STAMP(16); DMA2(10);
        const unsigned k7 = ((7 + rot) & 7) * (64 * 3072), sx = mq + L_Q15, sw = mw + 32768;
        LBAR(4); STAMP(17); glds16(vq0, wq + k7, sx); DMA2(11);
        f32x16 oo = f32x16{}, o1 = f32x16{}; PIN(oo); PIN(o1);
        LBAR(5); STAMP(18); glds16(vw0, wB + k7, sw); DMA2(12);
        f32x16 o2 = f32x16{}, o3 = f32x16{}; PIN(o2); PIN(o3);
        LBAR(5); STAMP(19); glds16(vq1, wq + k7 + 1024, sx + 1024); DMA2(13);
        f32x16 S1 = f32x16{}; PIN(S1);
        const __amdgpu_buffer_rsrc_t rwo = __builtin_amdgcn_make_buffer_rsrc((void*)((t >> 1) ? woB : woA), 0, AB_WO, 0x00020000);
        unsigned vwo = ((t & 1) * 256 + (lane >> 2)) * 1024 + 64 * po + (lane & 3) * 16; PIN(vwo);
        LBAR(5); STAMP(21); glds16(vw1, wB + k7 + 1024, sw + 1024); DMA2(14);
        __builtin_amdgcn_s_setprio(1);
        LBAR(5); STAMP(24);
        LBAR(0); STAMP(29);
        wait_tags(fp, a.tag);
        STAMP(5);
        GAS u64* pub = attn_full<1>(a, oo, o1, o2, o3, S1, rwo, vwo, 4096 * (((rr >> 4) << 1) | (po & 1)) PROF_PASS);
        STAMP(23);
        pub = attn_window<1>(a, pub PROF_PASS);
        STAMP(8);
        const int go = 4 * jo + t;
        const lds_cptr wo = lds + L_WO + (4 * hh + ((lane & 15) >> 2)) * 64 + ((lane >> 4) & 1) * 32 + (lane & 3) * 8;
        const GAS u64* of_ = (const GAS u64*)(ws + WS_OFLAG);
        wait_tags(of_ + ((lane & 16) ? go * 32 + 16 : (64 + go) * 32) + (lane & 15), a.tag, pub, lane);
        STAMP(9);
#define OBR(n) ((n) >> 4 == 1 ? 0 : 1)
#define OHD(n) (2 * ((n) & 15) + ((n) >= 16))
#define OLD(n) BLOAD(a.ro, a.vk, (unsigned)((OBR(n) * 64 + go) * 32 + OHD(n)) * 1024)
#define OWF(n) tr_frag(wo + OBR(n) * 32768 + OHD(n) * 1024, wo + OBR(n) * 32768 + OHD(n) * 1024 + 512)
#define OIN 7
        bf16x8 oe[48];
#pragma unroll
        for (int n = 0; n < 48; ++n) {
            if (n == 32) wait_tags(of_ + (64 + go) * 32 + 16 + (lane & 15), a.tag);
            oe[n] = OLD(n); SBAR();
            if (n >= OIN - 1) __builtin_amdgcn_s_waitcnt(0x0F70 | (OIN - 1));
            SBAR();
        }
        bf16x8 wn[4];
#pragma unroll
        for (int n = 0; n < 4; ++n) { wn[n] = OWF(n); PIN(wn[n]); }
        __builtin_amdgcn_s_waitcnt(0x0F70);
#define NPRE 0
#define NMID 24
        f32x16 ya = f32x16{};
        bf16x8 wf[16], of[16];
#define OMM(n) do { ya = MFMA(wn[(n) & 3], oe[n], ya); SBAR(); if ((n) + 4 < 48) wn[(n) & 3] = OWF((n) + 4); \
            if ((n) >= 32) wf[(n) - 32] = tr_frag(wo + 2 * ((n) - 32) * 1024, wo + 2 * ((n) - 32) * 1024 + 512); SBAR(); } while (0)
#pragma unroll
        for (int n = 0; n < NPRE; ++n) { OMM(n); PACE(); }
        STAMP(10);
        BAR();
        BAR();
#pragma unroll
        for (int n = NPRE; n < NMID; ++n) OMM(n);
        STAMP(30);
        BAR();
        STAMP(11);
#define GRP_BAD(q) ({ unsigned mx_ = 0; _Pragma("unroll") for (int i = 4 * (q); i < 4 * (q) + 4; ++i) { const u32x4 u_ = __builtin_bit_cast(u32x4, of[i]); \
            mx_ = __builtin_elementwise_max(__builtin_elementwise_max(mx_, u_[0]), u_[1]); mx_ = __builtin_elementwise_max(__builtin_elementwise_max(mx_, u_[2]), u_[3]); } \
            __any(mx_ == ~0u); })
#define GRP_MM(q) do { _Pragma("unroll") for (int i = 4 * (q); i < 4 * (q) + 4; ++i) ya = MFMA(wf[i], of[i], ya); } while (0)
#define GRP_LD(q) do { _Pragma("unroll") for (int i = 4 * (q); i < 4 * (q) + 4; ++i) of[i] = BLOAD(a.ro, a.vk, (unsigned)(go * 32 + 2 * i) * 1024); } while (0)
#pragma unroll
        for (int n = NMID; n < 48; ++n) { if (n - NMID < 16) { of[n - NMID] = BLOAD(a.ro, a.vk, (unsigned)(go * 32 + 2 * (n - NMID)) * 1024); SBAR(); } OMM(n); }
        STAMP(31);
        int done = 0;
        if (!GRP_BAD(0)) { GRP_MM(0); done = 1; if (!GRP_BAD(1)) { GRP_MM(1); done = 2; if (!GRP_BAD(2)) { GRP_MM(2); done = 3; if (!GRP_BAD(3)) { GRP_MM(3); done = 4; } } } }
        if (done < 4) {
#pragma nounroll
            for (unsigned spins = 0; spins < SPIN_CAP; ++spins) {
                const bool b0 = GRP_BAD(0), b1 = GRP_BAD(1), b2 = GRP_BAD(2), b3 = GRP_BAD(3);
                if (!(b0 || b1 || b2 || b3)) break;
                asm volatile("" ::: "memory");
                if (b0) GRP_LD(0); if (b1) GRP_LD(1); if (b2) GRP_LD(2); if (b3) GRP_LD(3);
            }
            if (done < 1) GRP_MM(0); if (done < 2) GRP_MM(1); if (done < 3) GRP_MM(2); GRP_MM(3);
        }
#pragma unroll
        for (int s = 0; s < 2; ++s) {
            auto u = __builtin_amdgcn_permlane32_swap(cvtpk(ya[8 * s], ya[8 * s + 1]), cvtpk(ya[8 * s + 4], ya[8 * s + 5]), false, false);
            auto v = __builtin_amdgcn_permlane32_swap(cvtpk(ya[8 * s + 2], ya[8 * s + 3]), cvtpk(ya[8 * s + 6], ya[8 * s + 7]), false, false);
            *(GAS u32x4*)(out + (size_t)(32 * go + r32) * 1024 + 64 * po + 32 * s + 16 * hh) = (u32x4){u[0], v[0], u[1], v[1]};
        }
        if (bid == 0 && tid == 256) __hip_atomic_store((GAS u64*)(ws + WS_EPOCH), a.tag, RLX_AGENT);
        STAMP(12);
    } else {
        unsigned pf0 = 0;
        {
            const unsigned ln = rr * 4 + t;
            asm volatile("s_load_dword %0, %1, %2\n\ts_load_dword %0, %1, %3" : "+s"(pf0) : "s"(pc & ~127ull), "s"(ln * 128), "s"((ln + CODE_LINES - 128) * 128) : "memory");
        }
        glds16((((lane >> 3) + rot) & 7) * 128 + (lane & 7) * 16, gw, lds0 + L_GW + t * 1024);
#pragma unroll
        for (int m = 0; m < 2; ++m) {
            f32x4 mk;
#pragma unroll
            for (int i = 0; i < 4; ++i) { const bool le = (i + 8 * t + 4 * hh) <= r32; mk[i] = (le != (m != 0)) ? 0.f : -__builtin_inff(); }
            *(LAS f32x4*)(lds + L_MDIAG + m * 4096 + t * 1024 + lane * 16) = mk;
        }
        if (tid < 4) *(LAS unsigned*)(lds + L_CNT + tid * 4) = 0;
        *(LAS unsigned*)(lds + L_Z + tid * 4) = 0;
        u64 tag = load_tag(ws);
        asm volatile("s_waitcnt lgkmcnt(0)" : "+s"(pf0), "+s"(tag)::"memory");
        STAMP(21);
        {
            const __amdgpu_buffer_rsrc_t rkv = __builtin_amdgcn_make_buffer_rsrc(ws + WS_KV, 0, KV_BYTES, 0x00020000);
            const __amdgpu_buffer_rsrc_t ro = __builtin_amdgcn_make_buffer_rsrc(ws + WS_O, 0, O_BYTES, 0x00020000);
            if (e) project<1>(lds, ws, rkv, ro, lane, t, p, j, tag PROF_PASS); else project<0>(lds, ws, rkv, ro, lane, t, p, j, tag PROF_PASS);
        }
        cnt_wait(lds + L_CNT + 8);
        __builtin_amdgcn_s_sleep(BSLP);
        STAMP(24);
        MK_ATT(0);
        a.tag = tag;
        GAS u64* pub = attn_window<0>(a, nullptr PROF_PASS);
        STAMP(23);
        wait_tags((const GAS u64*)(ws + kvflag(0, p >> 1, t, 0) + r32 * FSTR), tag, pub, lane);
        STAMP(5);
        attn_full<0>(a, lds_zero16(lds, lane), lds_zero16(lds, lane), lds_zero16(lds, lane), lds_zero16(lds, lane), lds_zero16(lds, lane), a.rkv, 0, 0 PROF_PASS);
        BAR();
        STAMP(8);
        DRAIN(); BAR();
    }
}

extern "C" void kernel_launch(void* const* d_in, const int* in_sizes, int n_in, void* d_out, int out_size, void* d_ws, size_t ws_size,
                              hipStream_t stream) {
    (void)in_sizes; (void)n_in; (void)out_size; (void)ws_size;
    (void)hipFuncSetAttribute((const void*)gmix_kernel, hipFuncAttributeMaxDynamicSharedMemorySize, L_END);
    gmix_kernel<<<256, 512, L_END, stream>>>((const char*)d_in[0], (const char*)d_in[1], (const char*)d_in[2], (const char*)d_in[4],
                                             (const char*)d_in[5], (const char*)d_in[6], (char*)d_out, (char*)d_ws);
}
```

```cpp
#define REVEAL 99
#define RROLE 1
#define RCYC 0
#include <hip/hip_runtime.h>
#include <hip/hip_bf16.h>

typedef __attribute__((ext_vector_type(8))) __bf16 bf16x8;
typedef __attribute__((ext_vector_type(16))) __bf16 bf16x16;
typedef __attribute__((ext_vector_type(16))) float f32x16;
typedef __attribute__((ext_vector_type(8))) float f32x8;
typedef __attribute__((ext_vector_type(4))) float f32x4;
typedef __attribute__((ext_vector_type(4))) unsigned u32x4;
typedef __attribute__((ext_vector_type(2))) unsigned u32x2;
typedef __attribute__((ext_vector_type(2))) __bf16 bf16x2;
typedef short s16x4 __attribute__((ext_vector_type(4)));
typedef unsigned long long u64;
#define GAS __attribute__((address_space(1)))
#define LAS __attribute__((address_space(3)))
typedef LAS const char* lds_cptr;
typedef LAS char* lds_ptr;

constexpr unsigned WS_EPOCH = 0;
#define FSTR 8
constexpr unsigned WS_OFLAG = 65536;
constexpr unsigned WS_KVFLAG = 131072;
constexpr unsigned WS_KV = 1u << 20;
constexpr unsigned TS = 2176;
constexpr unsigned KV_BYTES = 2u * 32u * 64u * TS;
constexpr unsigned WS_O = 10u << 20;
constexpr unsigned O_BYTES = 4u << 20;
constexpr unsigned WS_STAMP = 16u << 20;
constexpr unsigned OOB = 0x7ff00000u;
constexpr unsigned L_W = 65536;
constexpr unsigned L_WO = 0, L_PART = 65536, L_PL = 114688, L_CNT = 117760, L_MDIAG = 118784, L_MFAR = 122880, L_Q = 126976, L_GW = 143360, L_Z = 147456, L_G = 148480, L_Q14 = 106496, L_Q15 = 148992, L_END = 157184;
constexpr float QSCALE = 0.25f * 1.4426950408889634f;
constexpr float LMAX = 1.2676506e30f, LMIN = 9.5367432e-7f;
constexpr unsigned SPIN_CAP = 1u << 17;

#define SBAR() __builtin_amdgcn_sched_barrier(0)
#define PIN(x) asm volatile("" : "+v"(x))
#define MFMA(a, b, c) __builtin_amdgcn_mfma_f32_32x32x16_bf16(a, b, c, 0, 0, 0)
#define BAR() asm volatile("s_barrier" ::: "memory")
#define CBAR() asm volatile("s_waitcnt lgkmcnt(0)\n\ts_barrier" ::: "memory")
#define DRAIN() asm volatile("s_waitcnt vmcnt(0)" ::: "memory")
#define PACEN 3
#define PACE() asm volatile("s_nop %0" :: "i"(PACEN) : "memory")
#define RLX_AGENT __ATOMIC_RELAXED, __HIP_MEMORY_SCOPE_AGENT
#define STAMP(i) do { if ((i) == 0 || (i) == REVEAL) { u64 a_, b_; SBAR(); asm volatile("s_memrealtime %0\n\ts_memtime %1\n\ts_waitcnt lgkmcnt(0)" : "=s"(a_), "=s"(b_) :: "memory"); \
    if ((i) == 0) { stv.r0 = a_; stv.c0 = b_; stv.d = 0; } else stv.d = RCYC ? (unsigned)(b_ - stv.c0) / 20u : (unsigned)(a_ - stv.r0); SBAR(); } } while (0)
struct Rev { u64 r0, c0; unsigned d; };

__device__ __forceinline__ void glds16(unsigned voff, const void* sbase, unsigned ldsaddr) {
    asm volatile("s_mov_b32 m0, %2\n\ts_nop 0\n\tglobal_load_lds_dwordx4 %0, %1" ::"v"(voff), "s"(sbase), "s"(ldsaddr) : "memory");
}
__device__ __forceinline__ void glds16x2(unsigned v0, unsigned v1, const void* sbase, unsigned ldsaddr) {
    asm volatile("s_mov_b32 m0, %3\n\ts_nop 0\n\tglobal_load_lds_dwordx4 %0, %2\n\tglobal_load_lds_dwordx4 %1, %2 offset:1024"
                 ::"v"(v0), "v"(v1), "s"(sbase), "s"(ldsaddr) : "memory");
}
__device__ __forceinline__ void glds16x4(unsigned v0, unsigned v1, unsigned v2, unsigned v3, const void* sbase, unsigned ldsaddr) {
    asm volatile("s_mov_b32 m0, %5\n\ts_nop 0\n\tglobal_load_lds_dwordx4 %0, %4\n\tglobal_load_lds_dwordx4 %1, %4 offset:1024\n\t"
                 "global_load_lds_dwordx4 %2, %4 offset:2048\n\tglobal_load_lds_dwordx4 %3, %4 offset:3072"
                 ::"v"(v0), "v"(v1), "v"(v2), "v"(v3), "s"(sbase), "s"(ldsaddr) : "memory");
}
__device__ __forceinline__ unsigned cvtpk(float lo, float hi) {
    typedef __attribute__((ext_vector_type(2))) float f32x2;
    return __builtin_bit_cast(unsigned, __builtin_convertvector((f32x2){lo, hi}, bf16x2));
}
__device__ __forceinline__ bf16x8 as_frag(u32x4 v) { return __builtin_bit_cast(bf16x8, v); }
__device__ __forceinline__ bf16x8 lds_frag(lds_cptr p) { return *(const LAS bf16x8*)p; }
__device__ __forceinline__ bf16x8 tr_frag(lds_cptr p0, lds_cptr p1) {
    s16x4 a = __builtin_amdgcn_ds_read_tr16_b64_v4i16((LAS s16x4*)p0);
    s16x4 b = __builtin_amdgcn_ds_read_tr16_b64_v4i16((LAS s16x4*)p1);
    return __builtin_bit_cast(bf16x8, __builtin_shufflevector(a, b, 0, 1, 2, 3, 4, 5, 6, 7));
}
__device__ __forceinline__ f32x16 pv_lo(bf16x8 v, const f32x16& p, f32x16 acc, int idx) {
    f32x8 h = __builtin_shufflevector(p, p, 0, 1, 2, 3, 4, 5, 6, 7);
    return __builtin_amdgcn_smfmac_f32_32x32x32_bf16(v, __builtin_bit_cast(bf16x16, h), acc, idx, 0, 0);
}
__device__ __forceinline__ f32x16 pv_hi(bf16x8 v, const f32x16& p, f32x16 acc, int idx) {
    f32x8 h = __builtin_shufflevector(p, p, 8, 9, 10, 11, 12, 13, 14, 15);
    return __builtin_amdgcn_smfmac_f32_32x32x32_bf16(v, __builtin_bit_cast(bf16x16, h), acc, idx, 0, 0);
}
#define MX3(a, b, c) __builtin_fmaxf(__builtin_fmaxf((a), (b)), (c))
__device__ __forceinline__ float max16(const f32x16& s) {
    float a = MX3(s[0], s[1], s[2]), b = MX3(s[3], s[4], s[5]);
    a = MX3(a, s[6], s[7]); b = MX3(b, s[8], s[9]); a = MX3(a, s[10], s[11]); b = MX3(b, s[12], s[13]);
    return MX3(a, b, __builtin_fmaxf(s[14], s[15]));
}
__device__ __forceinline__ float halfmax(float m) {
    auto r = __builtin_amdgcn_permlane32_swap(__float_as_uint(m), __float_as_uint(m), false, false);
    return __builtin_fmaxf(__uint_as_float(r[0]), __uint_as_float(r[1]));
}
__device__ __forceinline__ f32x16 lds_f32x16(lds_cptr p) {
    f32x4 a = *(const LAS f32x4*)p, b = *(const LAS f32x4*)(p + 1024), c = *(const LAS f32x4*)(p + 2048), d = *(const LAS f32x4*)(p + 3072);
    f32x8 lo = __builtin_shufflevector(a, b, 0, 1, 2, 3, 4, 5, 6, 7), hi = __builtin_shufflevector(c, d, 0, 1, 2, 3, 4, 5, 6, 7);
    return __builtin_shufflevector(lo, hi, 0, 1, 2, 3, 4, 5, 6, 7, 8, 9, 10, 11, 12, 13, 14, 15);
}
#define BLOAD(rs, vo, so) as_frag(__builtin_amdgcn_raw_buffer_load_b128(rs, vo, so, 16))
#define BSTORE(v, rs, vo, so) __builtin_amdgcn_raw_buffer_store_b128(v, rs, (vo) + (so), 0, 16)

#define POLLSLP 8
#define BSLP 44
__device__ __forceinline__ void wait_tags_end(u64 v, const GAS u64* p, u64 tag, GAS u64* pub, int lane) {
    if (pub) { DRAIN(); if (lane == 0) __hip_atomic_store(pub, tag, RLX_AGENT); }
#pragma nounroll
    for (unsigned spins = 0; spins < SPIN_CAP && !__all(v == tag); ++spins) {
        __builtin_amdgcn_s_sleep(POLLSLP);
        v = __hip_atomic_load(p, RLX_AGENT);
    }
    asm volatile("" ::: "memory");
}

__device__ __forceinline__ void wait_tags(const GAS u64* p, u64 tag, GAS u64* pub = nullptr, int lane = 0) {
    wait_tags_end(__hip_atomic_load(p, RLX_AGENT), p, tag, pub, lane);
}

__device__ __forceinline__ void cnt_arrive(lds_ptr cnt, int lane) {
    asm volatile("s_waitcnt lgkmcnt(0)" ::: "memory");
    if (lane == 0) __hip_atomic_fetch_add((LAS unsigned*)cnt, 1u, __ATOMIC_RELAXED, __HIP_MEMORY_SCOPE_WORKGROUP);
}
__device__ __forceinline__ void cnt_wait(lds_ptr cnt) {
#pragma nounroll
    for (unsigned spins = 0; spins < (1u << 22); ++spins) {
        if (__builtin_amdgcn_readfirstlane(__hip_atomic_load((LAS unsigned*)cnt, __ATOMIC_RELAXED, __HIP_MEMORY_SCOPE_WORKGROUP)) == 4) break;
        __builtin_amdgcn_s_sleep(1);
    }
    asm volatile("" ::: "memory");
}
__device__ __forceinline__ unsigned kvflag(int br, int quad, int tile, int type) {
    return WS_KVFLAG + (unsigned)((((br * 8 + quad) * 4 + (tile & 3)) * 2 + type) * 16 + (tile >> 2)) * FSTR;
}

__device__ __forceinline__ void slow_rows(__amdgpu_buffer_rsrc_t rs, unsigned sbase, int lo, int hi, int g, bool band, bf16x8 q,
                                          unsigned vk, unsigned vv0, unsigned vv1, lds_cptr lds, int lane, int idx, f32x16& acc) {
    float m = -__builtin_inff();
    for (int pass = 0; pass < 2; ++pass) {
        if (pass) { m = halfmax(m); acc = f32x16{}; }
        for (int kt = lo; kt <= hi; ++kt) {
            const unsigned so = sbase + (unsigned)kt * TS;
            const bf16x8 k = BLOAD(rs, vk, so), v0 = BLOAD(rs, vv0, so), v1 = BLOAD(rs, vv1, so);
            f32x16 c = f32x16{};
            if (band && kt == g) c = lds_f32x16(lds + L_MDIAG + lane * 16);
            if (band && kt == g - 8) c = lds_f32x16(lds + L_MFAR + lane * 16);
            f32x16 s = MFMA(k, q, c);
            if (!pass) m = __builtin_fmaxf(m, max16(s));
            else {
#pragma unroll
                for (int r = 0; r < 16; ++r) s[r] = __builtin_amdgcn_exp2f(s[r] - m);
                acc = pv_lo(v0, s, acc, idx); acc = pv_hi(v1, s, acc, idx);
            }
        }
    }
}

#define PK4(A, o) ((u32x4){cvtpk(A[o], A[o + 1]), cvtpk(A[o + 2], A[o + 3]), cvtpk(A[o + 4], A[o + 5]), cvtpk(A[o + 6], A[o + 7])})
#define PROF_ARG , Rev& stv
#define PROF_PASS , stv
__device__ __forceinline__ u64 load_tag(const char* ws) {
    const u64 e = *(const __attribute__((address_space(4))) u64*)(ws + WS_EPOCH);
    return e * 0x9E3779B97F4A7C15ull + 0x632BE59BD9B4E019ull;
}
template <int E>
__device__ __forceinline__ u32x4 pack_kv(int i, const f32x16& a) {
    const int s = i & 1;
    if (E == 0) return PK4(a, 8 * s);
    auto x = __builtin_amdgcn_permlane32_swap(cvtpk(a[8 * s], a[8 * s + 1]), cvtpk(a[8 * s + 4], a[8 * s + 5]), false, false);
    auto y = __builtin_amdgcn_permlane32_swap(cvtpk(a[8 * s + 2], a[8 * s + 3]), cvtpk(a[8 * s + 6], a[8 * s + 7]), false, false);
    return (u32x4){x[0], y[0], x[1], y[1]};
}
#define PUT_KV(i, v, vo) BSTORE(v, rkv, vo, E ? ((i) & 1) * 512 + ((i) >> 1) * (128 * TS) : (i) * (64 * TS))
template <int E>
__device__ __forceinline__ void project(lds_ptr lds, char* ws, __amdgpu_buffer_rsrc_t rkv, __amdgpu_buffer_rsrc_t ro, int lane, int t, int p, int j, u64 tag PROF_ARG) {
    const int r32 = lane & 31, hh = lane >> 5;
    const unsigned q4 = (lane & 15) >> 2, wcol = ((lane >> 4) & 1) * 32 + (lane & 3) * 8;
    lds_cptr xa[4];
#pragma unroll
    for (int ks = 0; ks < 4; ++ks) xa[ks] = lds + (32 * t + r32) * 128 + (((hh ^ ((r32 >> 1) & 7)) * 16) ^ (ks * 32));
    unsigned w0o = L_W + (8 * hh + q4) * 128 + (q4 >> 1) * 64 + wcol, w1o = w0o ^ 64;
    PIN(w0o); PIN(w1o);
    const lds_cptr w0 = lds + w0o, w1 = lds + w1o, wq = lds + (8 * hh + q4) * 64 + wcol;
#define XRD(n) lds_frag(xa[(n) & 3] + (((n) >> 2) & 3) * 16384)
#define WRD(b, n) tr_frag(b + ((n) & 15) * 2048, b + ((n) & 15) * 2048 + 512)
#define YOF(n) ((n) >= 28 ? 32768 + ((n) & 3) * 2048 : ((n) & 15) * 2048)
#define YRD(b, n) tr_frag(b + YOF(n), b + YOF(n) + 512)
#define QOF(br, n) (((n) >= 28 ? L_Q15 - L_Q14 : ((n) >= 24 ? 0 : (((n) >> 2) & 3) * 16384)) + (br) * 4096 + ((n) & 3) * 1024)
#define QRD(br, n) tr_frag(((n) >= 24 ? wqh : wq) + QOF(br, n), ((n) >= 24 ? wqh : wq) + QOF(br, n) + 256)
#define MM(W, X, A) (E ? MFMA(X, W, A) : MFMA(W, X, A))
    bf16x8 xf[32], f0[4], f1[4];
    f32x16 a0 = f32x16{}, a1 = f32x16{};
    unsigned gwo = (lane & 15) == ((lane >> 4) & 1) ? L_GW + t * 1024 + hh * 16 : L_Z;
    PIN(gwo);
    f32x4 zg = f32x4{};
    bf16x8 gf[4];
#define RD1(n) do { xf[n] = XRD(n); f0[(n) & 3] = WRD(w0, n); f1[(n) & 3] = WRD(w1, n); SBAR(); } while (0)
#define GRD(n) gf[(n) & 3] = lds_frag(lds + gwo + (n) * 32)
#define GMM(n) zg = __builtin_amdgcn_mfma_f32_16x16x32_bf16(gf[(n) & 3], xf[n], zg, 0, 0, 0)
#define MM1(n) do { a0 = MM(f0[(n) & 3], xf[n], a0); a1 = MM(f1[(n) & 3], xf[n], a1); SBAR(); } while (0)
#pragma unroll
    for (int s = 0; s < 8; ++s) {
        if (s == 0) CBAR(); else BAR();
        if (s == 3) { if (E) BSTORE(((u32x4){0x3f803f80u, 0x3f803f80u, 0x3f803f80u, 0x3f803f80u}), rkv,
                                  (unsigned)((hh * 32 + 4 * (p >> 1) + (r32 >> 3)) * 64 + 4 * j + t) * TS + 2048 + (lane & 7) * 16, 0); }
        if (s == 5) BSTORE(((u32x4){~0u, ~0u, ~0u, ~0u}), ro, lane * 16, (unsigned)((4 * j + t) * 32 + 2 * p) * 1024);
        SBAR();
        RD1(4 * s); RD1(4 * s + 1);
        if (s) { GMM(4 * s - 4); GMM(4 * s - 3); GMM(4 * s - 2); GMM(4 * s - 1); SBAR(); }
        MM1(4 * s); RD1(4 * s + 2); MM1(4 * s + 1); RD1(4 * s + 3); MM1(4 * s + 2); MM1(4 * s + 3);
        if (s == 0) DRAIN();
        GRD(4 * s); GRD(4 * s + 1); GRD(4 * s + 2); GRD(4 * s + 3); SBAR();
    }
    GMM(28); GMM(29); GMM(30); GMM(31); SBAR();
    const unsigned tile = 4 * j + t;
    const unsigned vo = (unsigned)(4 * (p >> 1) * 64 + tile) * TS + (E ? (r32 >> 4) * (64 * TS) + 1024 + hh * 256 + (r32 & 15) * 16 : lane * 16);
    u32x4 pk[4] = {pack_kv<E>(0, a0), pack_kv<E>(1, a0), pack_kv<E>(2, a1), pack_kv<E>(3, a1)};
    PIN(pk[0]); PIN(pk[1]); PIN(pk[2]); PIN(pk[3]);
#define PRE8 2
#define XBAR
#define STPB 1
#define PUBN 15
#pragma unroll
    for (int i = 0; i < PRE8; ++i) PUT_KV(i, pk[i], vo);
    SBAR();
    BAR();
    if (lane < 16) {
        LAS float* gp = (LAS float*)(lds + L_G + t * 128) + lane;
        gp[0] = __builtin_amdgcn_rcpf(1.f + __expf(-zg[0])); gp[16] = __builtin_amdgcn_rcpf(1.f + __expf(-zg[1]));
    }
    SBAR();
    BAR(); SBAR();
    unsigned wqo = L_Q14 + (8 * hh + q4) * 64 + wcol;
    PIN(wqo);
    const lds_cptr wqh = lds + wqo;
    bf16x8 u0 = QRD(0, 0), u1 = QRD(1, 0), y0 = WRD(w0, 0), y1 = WRD(w1, 0);
    f32x16 b0 = f32x16{}, b1 = f32x16{}, aQ0 = f32x16{}, aQ1 = f32x16{};
    SBAR();
#pragma unroll
    for (int n = 0; n < 24; ++n) {
        aQ0 = MFMA(u0, xf[n], aQ0); SBAR();
        if (n == PUBN) { DRAIN(); if (lane == 0) __hip_atomic_store((GAS u64*)(ws + kvflag(0, p >> 1, tile, E)), tag, RLX_AGENT); SBAR(); }
        if ((n & 3) == 3) {
#pragma unroll
            for (int i = PRE8 + (n >> 2) * STPB; i < PRE8 + (n >> 2) * STPB + STPB; ++i) if (i < 4) PUT_KV(i, pk[i], vo);
            SBAR(); BAR(); SBAR();
        }
        u0 = QRD(0, n + 1); SBAR();
        aQ1 = MFMA(u1, xf[n], aQ1); SBAR();
        u1 = n < 23 ? QRD(1, n + 1) : QRD(0, 25); SBAR();
        b0 = MM(y0, xf[n], b0); SBAR(); y0 = n < 23 ? YRD(w0, n + 1) : QRD(0, 26); SBAR();
        b1 = MM(y1, xf[n], b1); SBAR(); y1 = n < 23 ? YRD(w1, n + 1) : QRD(0, 27); SBAR();
    }
    aQ0 = MFMA(u0, xf[24], aQ0); SBAR(); u0 = QRD(0, 28); SBAR(); aQ0 = MFMA(u1, xf[25], aQ0); SBAR(); u1 = QRD(0, 29); SBAR();
    aQ0 = MFMA(y0, xf[26], aQ0); SBAR(); y0 = QRD(0, 30); SBAR(); aQ0 = MFMA(y1, xf[27], aQ0); SBAR(); y1 = QRD(0, 31); SBAR();
    aQ0 = MFMA(u0, xf[28], aQ0); SBAR(); aQ0 = MFMA(u1, xf[29], aQ0); SBAR(); u1 = QRD(1, 24); SBAR();
    aQ0 = MFMA(y0, xf[30], aQ0); SBAR(); y0 = YRD(w0, 24); SBAR(); aQ0 = MFMA(y1, xf[31], aQ0); SBAR(); y1 = YRD(w1, 24); SBAR();
    const lds_ptr qd = lds + L_Q + t * 1024 + lane * 16;
#pragma unroll
    for (int n = 24; n < 32; ++n) {
        aQ1 = MFMA(u1, xf[n], aQ1); SBAR(); if (n < 31) u1 = QRD(1, n + 1); if (n > 24) PACE(); SBAR();
        b0 = MM(y0, xf[n], b0); SBAR(); if (n < 31) y0 = YRD(w0, n + 1); if (n > 24) PACE(); SBAR();
        if (n == 24) {
#pragma unroll
            for (int r = 8; r < 16; ++r) aQ0[r] *= QSCALE;
            *(LAS u32x4*)(qd + 4096) = PK4(aQ0, 8);
            SBAR();
        }
        b1 = MM(y1, xf[n], b1); SBAR();
        if (n == 24) { __builtin_amdgcn_s_setprio(2); cnt_arrive(lds + L_CNT + 12, lane); STAMP(4); SBAR(); }
        if (n < 31) y1 = YRD(w1, n + 1); if (n > 24) PACE(); SBAR();
    }
#pragma unroll
    for (int r = 0; r < 8; ++r) aQ0[r] *= QSCALE;
#pragma unroll
    for (int r = 0; r < 16; ++r) aQ1[r] *= QSCALE;
    *(LAS u32x4*)qd = PK4(aQ0, 0); *(LAS u32x4*)(qd + 8192) = PK4(aQ1, 0); *(LAS u32x4*)(qd + 12288) = PK4(aQ1, 8);
    cnt_arrive(lds + L_CNT + 8, lane);
    const unsigned vo2 = vo + 32 * 64 * TS;
#define YSLP 12
    PUT_KV(0, pack_kv<E>(0, b0), vo2); if (YSLP) __builtin_amdgcn_s_sleep(YSLP); PUT_KV(1, pack_kv<E>(1, b0), vo2); if (YSLP) __builtin_amdgcn_s_sleep(YSLP);
    PUT_KV(2, pack_kv<E>(2, b1), vo2); if (YSLP) __builtin_amdgcn_s_sleep(YSLP); PUT_KV(3, pack_kv<E>(3, b1), vo2);
    DRAIN();
    if (lane == 0) __hip_atomic_store((GAS u64*)(ws + kvflag(1, p >> 1, tile, E)), tag, RLX_AGENT);
    STAMP(19);
}

#define AB_WO (512 * 1024)
#define WOIN 2
#define CODE_LINES 256

struct Att {
    lds_ptr lds; char* ws; __amdgpu_buffer_rsrc_t rkv, ro;
    int lane, t, p, g, head; u64 tag; unsigned vk, vv0, vv1;
};
#define IDX 0xdddddddd
#define BLOADL(old, rs, vo, so) BLOAD(rs, vo, so)
#define LD3(K, V0, V1, rs, so) do { K = BLOAD(rs, a.vk, so); V0 = BLOAD(rs, a.vv0, so); V1 = BLOAD(rs, a.vv1, so); } while (0)
#define EX(X, r) X[r] = __builtin_amdgcn_exp2f(X[r])
#define STEP(X, Y, OP, VP, KN, QN, CN, O, V0, F1, F2) do { \
        EX(X, 0); EX(X, 1); SBAR(); OP = pv_hi(VP, Y, OP, IDX); SBAR(); F1; SBAR(); EX(X, 2); EX(X, 3); EX(X, 4); SBAR(); Y = MFMA(KN, QN, CN); SBAR(); \
        EX(X, 5); EX(X, 6); EX(X, 7); SBAR(); EX(X, 8); EX(X, 9); SBAR(); O = pv_lo(V0, X, O, IDX); SBAR(); F2; SBAR(); \
        EX(X, 10); EX(X, 11); EX(X, 12); EX(X, 13); EX(X, 14); EX(X, 15); SBAR(); } while (0)

__device__ __forceinline__ f32x16 lds_zero16(lds_cptr lds, int lane) {
    const volatile LAS f32x4* z = (const volatile LAS f32x4*)(lds + L_Z) + lane;
    const f32x4 a = *z, b = *z, c = *z, d = *z;
    f32x8 lo = __builtin_shufflevector(a, b, 0, 1, 2, 3, 4, 5, 6, 7), hi = __builtin_shufflevector(c, d, 0, 1, 2, 3, 4, 5, 6, 7);
    return __builtin_shufflevector(lo, hi, 0, 1, 2, 3, 4, 5, 6, 7, 8, 9, 10, 11, 12, 13, 14, 15);
}

template <int C, int BR>
__device__ __forceinline__ GAS u64* finish(const Att& a, f32x16 oo, float lrest, bf16x8 qa) {
    const float gt = *(const LAS float*)(a.lds + L_G + a.t * 128 + (a.lane & 31) * 4);
    auto lsw = __builtin_amdgcn_permlane32_swap(__float_as_uint(oo[8]), __float_as_uint(oo[8]), false, false);
    float l = __uint_as_float(lsw[0]) + lrest, z = 0.f;
#pragma unroll
    for (int r = 0; r < 8; ++r) z = __builtin_fmaf(oo[r], 0.f, z);
    if (__builtin_expect(__any(!(l + z < LMAX && l > LMIN)), 0)) {
        if (!BR) { wait_tags((const GAS u64*)(a.ws + kvflag(0, a.p >> 1, 0, 0) + a.lane * FSTR), a.tag); wait_tags((const GAS u64*)(a.ws + kvflag(0, a.p >> 1, 2, 0) + a.lane * FSTR), a.tag); }
        slow_rows(a.rkv, (unsigned)((BR * 32 + a.head) * 64) * TS, BR ? (a.g >= 8 ? a.g - 8 : 0) : 0, BR ? a.g : 63, a.g, BR, qa, a.vk, a.vv0, a.vv1, a.lds, a.lane, IDX, oo);
        auto l2 = __builtin_amdgcn_permlane32_swap(__float_as_uint(oo[8]), __float_as_uint(oo[8]), false, false);
        l = __uint_as_float(l2[0]);
    }
    const float il = __builtin_amdgcn_rcpf(l) * (BR ? 1.f - gt : gt);
    u32x4 v = (u32x4){cvtpk(oo[0] * il, oo[1] * il), cvtpk(oo[2] * il, oo[3] * il), cvtpk(oo[4] * il, oo[5] * il), cvtpk(oo[6] * il, oo[7] * il)};
    if (!C && !BR) v = __builtin_elementwise_min(v, (u32x4){~1u, ~1u, ~1u, ~1u});
    BSTORE(v, a.ro, a.vk, (unsigned)((BR * 64 + a.g) * 32 + a.head) * 1024);
    return (GAS u64*)(a.ws + WS_OFLAG) + ((BR * 64 + a.g) * 2 + C) * 16 + a.p;
}

template <int C>
__device__ __forceinline__ GAS u64* attn_full(const Att& a, f32x16 oo, f32x16 o1, f32x16 o2, f32x16 o3, f32x16 S1, __amdgpu_buffer_rsrc_t rwo, unsigned vwo, unsigned wi PROF_ARG) {
    const lds_ptr lds = a.lds; const int t = a.t, lane = a.lane, w = 4 * C + t;
    const f32x16 zc = f32x16{};
    bf16x8 ka, va0, va1, kb, vb0, vb1, kc, vc0, vc1, kd, vd0, vd1;
    const unsigned sBeg = (unsigned)(a.head * 64 + t) * TS, sEnd = sBeg + 64 * TS;
    unsigned so = (unsigned)(a.head * 64 + a.g) * TS;
#define NEXT_A() asm volatile("s_addk_i32 %0, %1" : "+s"(so) : "i"(4 * TS) : "scc")
#define WRAP_A() asm volatile("s_cmp_eq_u32 %0, %1\n\ts_cselect_b32 %0, %2, %0" : "+s"(so) : "s"(sEnd), "s"(sBeg) : "scc")
    LD3(ka, va0, va1, a.rkv, so); NEXT_A(); WRAP_A(); SBAR(); LD3(kb, vb0, vb1, a.rkv, so); NEXT_A(); WRAP_A(); SBAR();
    LD3(kc, vc0, vc1, a.rkv, so); NEXT_A(); WRAP_A(); SBAR();
    if (C) { cnt_wait(lds + L_CNT + 12); STAMP(4); }
    const lds_cptr qp = lds + L_Q + C * 4096 + lane * 16;
    bf16x8 qa = lds_frag(qp + t * 1024), q1 = lds_frag(qp + ((t + 1) & 3) * 1024), q2 = lds_frag(qp + ((t + 2) & 3) * 1024), q3 = lds_frag(qp + ((t + 3) & 3) * 1024);
    vd1 = lds_frag(lds + L_Z + lane * 16);
    PIN(q1); PIN(q2); PIN(q3); PIN(vd1); PIN(oo); PIN(o1); PIN(o2); PIN(o3); PIN(S1);
    __builtin_amdgcn_s_waitcnt(0x0F76);
    f32x16 S0 = MFMA(ka, qa, zc);
    PIN(S0);
#pragma unroll
    for (int i = 0; i < 12; ++i) __builtin_amdgcn_s_setprio(C);
    STAMP(3);
    SBAR();
    unsigned wso = 0;
#define NEXT_AW() do { if (C) asm volatile("s_addk_i32 %0, %3\n\ts_lshl_b32 %1, %2, 4" : "+s"(so), "=s"(wso) : "s"(wi), "i"(4 * TS) : "scc"); else NEXT_A(); } while (0)
#define WO_DMA() do { if (C) __builtin_amdgcn_raw_ptr_buffer_load_lds(rwo, (LAS void*)(lds + L_WO + t * 16384 + wi), 16, vwo, wso, 0, 0); } while (0)
#define WO_NEXT() do { if (C) asm volatile("s_addk_i32 %0, 0x400\n\ts_and_b32 %0, %0, 0x3fff" : "+s"(wi) :: "scc"); } while (0)
#define TILE(K, V0, V1, KN, LK, LV0, LV1, NW) do { \
        STEP(S0, S1, o3, LV1, K, q1, zc, oo, V0, LK = BLOADL(K, a.rkv, a.vk, so), LV0 = BLOADL(V0, a.rkv, a.vv0, so)); \
        STEP(S1, S0, oo, V1, K, q2, zc, o1, V0, LV1 = BLOADL(V1, a.rkv, a.vv1, so), NEXT_AW()); \
        STEP(S0, S1, o1, V1, K, q3, zc, o2, V0, WRAP_A(), WO_DMA()); \
        STEP(S1, S0, o2, V1, KN, qa, zc, o3, V0, __builtin_amdgcn_s_waitcnt(0x0F70 | (C ? NW : 6)), WO_NEXT()); } while (0)
#pragma nounroll
    for (int it = 0; it < 4; ++it) {
        TILE(ka, va0, va1, kb, kd, vd0, vd1, 7);
        TILE(kb, vb0, vb1, kc, ka, va0, va1, 8);
        TILE(kc, vc0, vc1, kd, kb, vb0, vb1, 9);
        TILE(kd, vd0, vd1, ka, kc, vc0, vc1, 9);
    }
    o3 = pv_hi(vd1, S1, o3, IDX);
    STAMP(6);
    if (C) cnt_wait(lds + L_CNT + 8); else __builtin_amdgcn_s_setprio(2);
#define PUT(O, sl) do { lds_ptr d_ = lds + L_PART + (w * 3 + sl) * 2048 + lane * 16; \
        *(LAS f32x4*)d_ = __builtin_shufflevector(O, O, 0, 1, 2, 3); *(LAS f32x4*)(d_ + 1024) = __builtin_shufflevector(O, O, 4, 5, 6, 7); } while (0)
    PUT(o1, 0); PUT(o2, 1); PUT(o3, 2);
    if (lane < 32) { LAS float* lp = (LAS float*)(lds + L_PL + w * 384) + lane; lp[0] = o1[8]; lp[32] = o2[8]; lp[64] = o3[8]; }
    if (C) { DRAIN(); cnt_arrive(lds + L_CNT, lane); cnt_wait(lds + L_CNT); } else CBAR();
    float lrest = 0.f;
#pragma unroll
    for (int s = 0; s < 3; ++s) {
        const int src = (C * 4 + ((t - 1 - s) & 3)) * 3 + s;
        const lds_cptr d = lds + L_PART + src * 2048 + lane * 16;
        const f32x4 x = *(const LAS f32x4*)d, y = *(const LAS f32x4*)(d + 1024);
#pragma unroll
        for (int r = 0; r < 4; ++r) { oo[r] += x[r]; oo[4 + r] += y[r]; }
        const float lp = *(const LAS float*)(lds + L_PL + src * 128 + (lane & 31) * 4);
        lrest = s ? lrest + lp : lp;
    }
    return finish<C, 0>(a, oo, lrest, qa);
}

template <int C>
__device__ __forceinline__ GAS u64* attn_window(const Att& a, GAS u64* pub PROF_ARG) {
    const lds_ptr lds = a.lds; const int t = a.t, lane = a.lane, g = a.g;
    const f32x16 zc = f32x16{};
    { const int i = lane & 15, kt = g - (i < 9 ? i : 0);
      wait_tags((const GAS u64*)(a.ws + kvflag(1, a.p >> 1, kt >= 0 ? kt : 0, (lane >> 4) & 1)), a.tag, pub, lane); }
    STAMP(25);
    bf16x8 k[9], v0[9], v1[9];
    const unsigned sB = (unsigned)((32 + a.head) * 64) * TS;
#define LDB(i) do { const int kt_ = g - (i); \
        const __amdgpu_buffer_rsrc_t rb_ = __builtin_amdgcn_make_buffer_rsrc(a.ws + WS_KV, 0, kt_ >= 0 ? KV_BYTES : 0u, 0x00020000); \
        LD3(k[i], v0[i], v1[i], rb_, sB + (unsigned)(kt_ >= 0 ? kt_ : 0) * TS); } while (0)
    LDB(0); SBAR(); LDB(1); SBAR(); LDB(2); SBAR();
    const bf16x8 qa = lds_frag(lds + L_Q + 8192 + C * 4096 + t * 1024 + lane * 16);
    const f32x16 cd = lds_f32x16(lds + L_MDIAG + lane * 16), cf = lds_f32x16(lds + L_MFAR + lane * 16);
    f32x16 oo = lds_zero16(lds, lane);
    f32x16 S0 = MFMA(k[0], qa, cd), S1 = MFMA(k[1], qa, zc);
    SBAR(); LDB(3); SBAR();
#pragma unroll
    for (int r = 0; r < 10; ++r) { EX(S0, r); SBAR(); }
    oo = pv_lo(v0[0], S0, oo, IDX); SBAR();
#pragma unroll
    for (int r = 10; r < 16; ++r) EX(S0, r);
    SBAR();
    STEP(S1, S0, oo, v1[0], k[2], qa, zc, oo, v0[1], LDB(4), );
    STEP(S0, S1, oo, v1[1], k[3], qa, zc, oo, v0[2], LDB(5), );
    STEP(S1, S0, oo, v1[2], k[4], qa, zc, oo, v0[3], LDB(6), );
    STEP(S0, S1, oo, v1[3], k[5], qa, zc, oo, v0[4], LDB(7), );
    STEP(S1, S0, oo, v1[4], k[6], qa, zc, oo, v0[5], LDB(8), );
    STEP(S0, S1, oo, v1[5], k[7], qa, zc, oo, v0[6], , );
    STEP(S1, S0, oo, v1[6], k[8], qa, cf, oo, v0[7], , );
    STEP(S0, S1, oo, v1[7], k[8], qa, cf, oo, v0[8], , );
    oo = pv_hi(v1[8], S0, oo, IDX);
    STAMP(7);
    return finish<C, 1>(a, oo, 0.f, qa);
}

__global__ void __launch_bounds__(512, 2)
gmix_kernel(const char* x, const char* wA, const char* woA, const char* wB, const char* woB, const char* gw, char* out, char* ws) {
    extern __shared__ __attribute__((aligned(16))) char smem[];
    const lds_ptr lds = (lds_ptr)smem;
    const unsigned lds0 = (unsigned)(uintptr_t)lds;
    const int tid = threadIdx.x, lane = tid & 63, r32 = lane & 31, hh = lane >> 5;
    const int w = __builtin_amdgcn_readfirstlane(tid >> 6);
    const int bid = blockIdx.x, xcd = bid & 7, rr = bid >> 3;
    const int p = 4 * (xcd & 3) + (rr & 3), j = 8 * (xcd >> 2) + (rr >> 2);
    const int jo = 2 * xcd + (rr >> 4), po = rr & 15;
    const int t = w & 3, c = w >> 2;
    const int e = p & 1;
    Rev stv;
    STAMP(0);
    u64 pc; asm volatile("s_getpc_b64 %0" : "=s"(pc));
    unsigned rot = (j + 2 * (p & 3)) & 7;
    unsigned vx0 = (128 * j + 32 * t + (lane >> 3)) * 1024 + (((lane & 7) ^ (lane >> 4)) * 16), vx1 = (vx0 ^ 64) + 7168, vx2 = vx0 + 14336, vx3 = vx1 + 14336;
    unsigned vw0 = (16 * t + (lane >> 3)) * 3072 + 1024 + 1024 * e + 128 * (p >> 1) + (((lane & 7) ^ (((lane >> 4) & 1) << 2)) * 16), vw1 = vw0 + 8 * 3072 - 1024;
    unsigned mx = lds0 + t * 4096, mw = lds0 + L_W + t * 2048, ow = rot * (64 * 3072);
    asm volatile("" : "+v"(vx0), "+v"(vx1), "+v"(vx2), "+v"(vx3), "+v"(vw0), "+v"(vw1));
    asm volatile("" : "+s"(mx), "+s"(mw), "+s"(rot), "+s"(ow));
    SBAR();
    asm volatile("" : "+s"(x), "+s"(wA), "+s"(woA), "+s"(wB), "+s"(woB), "+s"(gw), "+s"(out), "+s"(ws));
#define MK_ATT(C) Att a; a.lds = lds; a.ws = ws; a.lane = lane; a.t = t; a.p = p; a.g = 4 * j + t; a.head = 2 * p + C; \
    a.rkv = __builtin_amdgcn_make_buffer_rsrc(ws + WS_KV, 0, KV_BYTES, 0x00020000); a.ro = __builtin_amdgcn_make_buffer_rsrc(ws + WS_O, 0, O_BYTES, 0x00020000); \
    a.vk = lane * 16; a.vv0 = r32 < 16 ? 1024 + hh * 256 + r32 * 16 : (r32 == 16 ? 2048 : OOB); a.vv1 = r32 < 16 ? a.vv0 + 512 : a.vv0
    if (c) {
#define DMA1(s) do { const unsigned ks_ = ((s) + rot) & 7; \
        glds16x4(vx0, vx1, vx2, vx3, x + ks_ * 128, mx + ((s) & 3) * 16384); glds16x2(vw0, vw1, wA + ks_ * (64 * 3072), mw + ((s) & 3) * 8192); } while (0)
#define DMA2(s) do { const unsigned ks_ = (((s) + rot) & 7) * (64 * 3072); \
        glds16x2(vq0, vq1, wq + ks_, mq + ((s) == 14 ? L_Q14 : ((s) & 3) * 16384)); glds16x2(vw0, vw1, wB + ks_, mw + ((s) & 3) * 8192); SBAR(); } while (0)
#define LBAR(N) asm volatile("s_waitcnt vmcnt(" #N ")\n\ts_barrier" ::: "memory")
        STAMP(20);
        glds16x4(vx0, vx1, vx2, vx3, x + rot * 128, mx); glds16x2(vw0, vw1, wA + ow, mw);
        SBAR();
        DMA1(1);
        STAMP(22);
        SBAR();
#pragma nounroll
        for (int s = 2; s < 8; ++s) { LBAR(6); DMA1(s);
            if (s == 2) STAMP(1); if (s == 3) STAMP(2);
        }
        const unsigned vq0 = (32 * (t & 1) + (lane >> 2)) * 3072 + 64 * p + (lane & 3) * 16, vq1 = vq0 + 16 * 3072 - 1024, mq = lds0 + t * 2048;
        const char* wq = (t >> 1) ? wB : wA;
        LBAR(6); STAMP(14); DMA2(8);
        LBAR(4); STAMP(15);
        BAR();
        DMA2(9);
        MK_ATT(1);
        a.tag = load_tag(ws);
        asm volatile("" : "+s"(a.tag)); PIN(a.vk); PIN(a.vv0); PIN(a.vv1);
        const GAS u64* fp = (const GAS u64*)(ws + kvflag(0, p >> 1, t, 0) + r32 * FSTR);
        PIN(fp);
        LBAR(4); STAMP(16); DMA2(10);
        const unsigned k7 = ((7 + rot) & 7) * (64 * 3072), sx = mq + L_Q15, sw = mw + 32768;
        LBAR(4); STAMP(17); glds16(vq0, wq + k7, sx); DMA2(11);
        f32x16 oo = f32x16{}, o1 = f32x16{}; PIN(oo); PIN(o1);
        LBAR(5); STAMP(18); glds16(vw0, wB + k7, sw); DMA2(12);
        f32x16 o2 = f32x16{}, o3 = f32x16{}; PIN(o2); PIN(o3);
        LBAR(5); STAMP(19); glds16(vq1, wq + k7 + 1024, sx + 1024); DMA2(13);
        f32x16 S1 = f32x16{}; PIN(S1);
        const __amdgpu_buffer_rsrc_t rwo = __builtin_amdgcn_make_buffer_rsrc((void*)((t >> 1) ? woB : woA), 0, AB_WO, 0x00020000);
        unsigned vwo = ((t & 1) * 256 + (lane >> 2)) * 1024 + 64 * po + (lane & 3) * 16; PIN(vwo);
        LBAR(5); STAMP(21); glds16(vw1, wB + k7 + 1024, sw + 1024); DMA2(14);
        __builtin_amdgcn_s_setprio(1);
        LBAR(5); STAMP(24);
        LBAR(0); STAMP(29);
        wait_tags(fp, a.tag);
        STAMP(5);
        GAS u64* pub = attn_full<1>(a, oo, o1, o2, o3, S1, rwo, vwo, 4096 * (((rr >> 4) << 1) | (po & 1)) PROF_PASS);
        STAMP(23);
        pub = attn_window<1>(a, pub PROF_PASS);
        STAMP(8);
        const int go = 4 * jo + t;
        const lds_cptr wo = lds + L_WO + (4 * hh + ((lane & 15) >> 2)) * 64 + ((lane >> 4) & 1) * 32 + (lane & 3) * 8;
        const GAS u64* of_ = (const GAS u64*)(ws + WS_OFLAG);
        wait_tags(of_ + ((lane & 16) ? go * 32 + 16 : (64 + go) * 32) + (lane & 15), a.tag, pub, lane);
        STAMP(9);
#define OBR(n) ((n) >> 4 == 1 ? 0 : 1)
#define OHD(n) (2 * ((n) & 15) + ((n) >= 16))
#define OLD(n) BLOAD(a.ro, a.vk, (unsigned)((OBR(n) * 64 + go) * 32 + OHD(n)) * 1024)
#define OWF(n) tr_frag(wo + OBR(n) * 32768 + OHD(n) * 1024, wo + OBR(n) * 32768 + OHD(n) * 1024 + 512)
#define OIN 7
        bf16x8 oe[48];
#pragma unroll
        for (int n = 0; n < 48; ++n) {
            if (n == 32) wait_tags(of_ + (64 + go) * 32 + 16 + (lane & 15), a.tag);
            oe[n] = OLD(n); SBAR();
            if (n >= OIN - 1) __builtin_amdgcn_s_waitcnt(0x0F70 | (OIN - 1));
            SBAR();
        }
        bf16x8 wn[4];
#pragma unroll
        for (int n = 0; n < 4; ++n) { wn[n] = OWF(n); PIN(wn[n]); }
        __builtin_amdgcn_s_waitcnt(0x0F70);
#define NPRE 0
#define NMID 24
        f32x16 ya = f32x16{};
        bf16x8 wf[16], of[16];
#define OMM(n) do { ya = MFMA(wn[(n) & 3], oe[n], ya); SBAR(); if ((n) + 4 < 48) wn[(n) & 3] = OWF((n) + 4); \
            if ((n) >= 32) wf[(n) - 32] = tr_frag(wo + 2 * ((n) - 32) * 1024, wo + 2 * ((n) - 32) * 1024 + 512); SBAR(); } while (0)
#pragma unroll
        for (int n = 0; n < NPRE; ++n) { OMM(n); PACE(); }
        STAMP(10);
        BAR();
        BAR();
#pragma unroll
        for (int n = NPRE; n < NMID; ++n) OMM(n);
        STAMP(30);
        BAR();
        STAMP(11);
#define GRP_BAD(q) ({ unsigned mx_ = 0; _Pragma("unroll") for (int i = 4 * (q); i < 4 * (q) + 4; ++i) { const u32x4 u_ = __builtin_bit_cast(u32x4, of[i]); \
            mx_ = __builtin_elementwise_max(__builtin_elementwise_max(mx_, u_[0]), u_[1]); mx_ = __builtin_elementwise_max(__builtin_elementwise_max(mx_, u_[2]), u_[3]); } \
            __any(mx_ == ~0u); })
#define GRP_MM(q) do { _Pragma("unroll") for (int i = 4 * (q); i < 4 * (q) + 4; ++i) ya = MFMA(wf[i], of[i], ya); } while (0)
#define GRP_LD(q) do { _Pragma("unroll") for (int i = 4 * (q); i < 4 * (q) + 4; ++i) of[i] = BLOAD(a.ro, a.vk, (unsigned)(go * 32 + 2 * i) * 1024); } while (0)
#pragma unroll
        for (int n = NMID; n < 48; ++n) { if (n - NMID < 16) { of[n - NMID] = BLOAD(a.ro, a.vk, (unsigned)(go * 32 + 2 * (n - NMID)) * 1024); SBAR(); } OMM(n); }
        STAMP(31);
        int done = 0;
        if (!GRP_BAD(0)) { GRP_MM(0); done = 1; if (!GRP_BAD(1)) { GRP_MM(1); done = 2; if (!GRP_BAD(2)) { GRP_MM(2); done = 3; if (!GRP_BAD(3)) { GRP_MM(3); done = 4; } } } }
        if (done < 4) {
#pragma nounroll
            for (unsigned spins = 0; spins < SPIN_CAP; ++spins) {
                const bool b0 = GRP_BAD(0), b1 = GRP_BAD(1), b2 = GRP_BAD(2), b3 = GRP_BAD(3);
                if (!(b0 || b1 || b2 || b3)) break;
                asm volatile("" ::: "memory");
                if (b0) GRP_LD(0); if (b1) GRP_LD(1); if (b2) GRP_LD(2); if (b3) GRP_LD(3);
            }
            if (done < 1) GRP_MM(0); if (done < 2) GRP_MM(1); if (done < 3) GRP_MM(2); GRP_MM(3);
        }
#pragma unroll
        for (int s = 0; s < 2; ++s) {
            auto u = __builtin_amdgcn_permlane32_swap(cvtpk(ya[8 * s], ya[8 * s + 1]), cvtpk(ya[8 * s + 4], ya[8 * s + 5]), false, false);
            auto v = __builtin_amdgcn_permlane32_swap(cvtpk(ya[8 * s + 2], ya[8 * s + 3]), cvtpk(ya[8 * s + 6], ya[8 * s + 7]), false, false);
            *(GAS u32x4*)(out + (size_t)(32 * go + r32) * 1024 + 64 * po + 32 * s + 16 * hh) = (u32x4){u[0], v[0], u[1], v[1]};
        }
        if (bid == 0 && tid == 256) __hip_atomic_store((GAS u64*)(ws + WS_EPOCH), a.tag, RLX_AGENT);
        STAMP(12);
    } else {
        unsigned pf0 = 0;
        {
            const unsigned ln = rr * 4 + t;
            asm volatile("s_load_dword %0, %1, %2\n\ts_load_dword %0, %1, %3" : "+s"(pf0) : "s"(pc & ~127ull), "s"(ln * 128), "s"((ln + CODE_LINES - 128) * 128) : "memory");
        }
        glds16((((lane >> 3) + rot) & 7) * 128 + (lane & 7) * 16, gw, lds0 + L_GW + t * 1024);
#pragma unroll
        for (int m = 0; m < 2; ++m) {
            f32x4 mk;
#pragma unroll
            for (int i = 0; i < 4; ++i) { const bool le = (i + 8 * t + 4 * hh) <= r32; mk[i] = (le != (m != 0)) ? 0.f : -__builtin_inff(); }
            *(LAS f32x4*)(lds + L_MDIAG + m * 4096 + t * 1024 + lane * 16) = mk;
        }
        if (tid < 4) *(LAS unsigned*)(lds + L_CNT + tid * 4) = 0;
        *(LAS unsigned*)(lds + L_Z + tid * 4) = 0;
        u64 tag = load_tag(ws);
        asm volatile("s_waitcnt lgkmcnt(0)" : "+s"(pf0), "+s"(tag)::"memory");
        STAMP(21);
        {
            const __amdgpu_buffer_rsrc_t rkv = __builtin_amdgcn_make_buffer_rsrc(ws + WS_KV, 0, KV_BYTES, 0x00020000);
            const __amdgpu_buffer_rsrc_t ro = __builtin_amdgcn_make_buffer_rsrc(ws + WS_O, 0, O_BYTES, 0x00020000);
            if (e) project<1>(lds, ws, rkv, ro, lane, t, p, j, tag PROF_PASS); else project<0>(lds, ws, rkv, ro, lane, t, p, j, tag PROF_PASS);
        }
        cnt_wait(lds + L_CNT + 8);
        __builtin_amdgcn_s_sleep(BSLP);
        STAMP(24);
        MK_ATT(0);
        a.tag = tag;
        GAS u64* pub = attn_window<0>(a, nullptr PROF_PASS);
        STAMP(23);
        wait_tags((const GAS u64*)(ws + kvflag(0, p >> 1, t, 0) + r32 * FSTR), tag, pub, lane);
        STAMP(5);
        attn_full<0>(a, lds_zero16(lds, lane), lds_zero16(lds, lane), lds_zero16(lds, lane), lds_zero16(lds, lane), lds_zero16(lds, lane), a.rkv, 0, 0 PROF_PASS);
        BAR();
        STAMP(8);
        DRAIN(); BAR();
    }
    DRAIN();
    STAMP(13);
    if (c == RROLE) while (__builtin_amdgcn_s_memrealtime() < stv.r0 + 6000 + stv.d) __builtin_amdgcn_s_sleep(1);
}

extern "C" void kernel_launch(void* const* d_in, const int* in_sizes, int n_in, void* d_out, int out_size, void* d_ws, size_t ws_size,
                              hipStream_t stream) {
    (void)in_sizes; (void)n_in; (void)out_size; (void)ws_size;
    (void)hipFuncSetAttribute((const void*)gmix_kernel, hipFuncAttributeMaxDynamicSharedMemorySize, L_END);
    gmix_kernel<<<256, 512, L_END, stream>>>((const char*)d_in[0], (const char*)d_in[1], (const char*)d_in[2], (const char*)d_in[4],
                                             (const char*)d_in[5], (const char*)d_in[6], (char*)d_out, (char*)d_ws);
}
```

```cpp
#define REVEAL 99
#define RROLE 1
#define RCYC 0
#include <hip/hip_runtime.h>
#include <hip/hip_bf16.h>

typedef __attribute__((ext_vector_type(8))) __bf16 bf16x8;
typedef __attribute__((ext_vector_type(16))) __bf16 bf16x16;
typedef __attribute__((ext_vector_type(16))) float f32x16;
typedef __attribute__((ext_vector_type(8))) float f32x8;
typedef __attribute__((ext_vector_type(4))) float f32x4;
typedef __attribute__((ext_vector_type(4))) unsigned u32x4;
typedef __attribute__((ext_vector_type(2))) unsigned u32x2;
typedef __attribute__((ext_vector_type(2))) __bf16 bf16x2;
typedef short s16x4 __attribute__((ext_vector_type(4)));
typedef unsigned long long u64;
#define GAS __attribute__((address_space(1)))
#define LAS __attribute__((address_space(3)))
typedef LAS const char* lds_cptr;
typedef LAS char* lds_ptr;

constexpr unsigned WS_EPOCH = 0;
#define FSTR 8
constexpr unsigned WS_OFLAG = 65536;
constexpr unsigned WS_KVFLAG = 131072;
constexpr unsigned WS_KV = 1u << 20;
constexpr unsigned TS = 2176;
constexpr unsigned KV_BYTES = 2u * 32u * 64u * TS;
constexpr unsigned WS_O = 10u << 20;
constexpr unsigned O_BYTES = 4u << 20;
constexpr unsigned WS_STAMP = 16u << 20;
constexpr unsigned OOB = 0x7ff00000u;
constexpr unsigned L_W = 65536;
constexpr unsigned L_WO = 0, L_PART = 65536, L_PL = 114688, L_CNT = 117760, L_MDIAG = 118784, L_MFAR = 122880, L_Q = 126976, L_GW = 143360, L_Z = 147456, L_G = 148480, L_Q14 = 106496, L_Q15 = 148992, L_END = 157184;
constexpr float QSCALE = 0.25f * 1.4426950408889634f;
constexpr float LMAX = 1.2676506e30f, LMIN = 9.5367432e-7f;
constexpr unsigned SPIN_CAP = 1u << 17;

#define SBAR() __builtin_amdgcn_sched_barrier(0)
#define PIN(x) asm volatile("" : "+v"(x))
#define MFMA(a, b, c) __builtin_amdgcn_mfma_f32_32x32x16_bf16(a, b, c, 0, 0, 0)
#define BAR() asm volatile("s_barrier" ::: "memory")
#define CBAR() asm volatile("s_waitcnt lgkmcnt(0)\n\ts_barrier" ::: "memory")
#define DRAIN() asm volatile("s_waitcnt vmcnt(0)" ::: "memory")
#define PACEN 3
#define PACE() asm volatile("s_nop %0" :: "i"(PACEN) : "memory")
#define RLX_AGENT __ATOMIC_RELAXED, __HIP_MEMORY_SCOPE_AGENT
#define STAMP(i) do { if ((i) == 0 || (i) == REVEAL) { u64 a_, b_; SBAR(); asm volatile("s_memrealtime %0\n\ts_memtime %1\n\ts_waitcnt lgkmcnt(0)" : "=s"(a_), "=s"(b_) :: "memory"); \
    if ((i) == 0) { stv.r0 = a_; stv.c0 = b_; stv.d = 0; } else stv.d = RCYC ? (unsigned)(b_ - stv.c0) / 20u : (unsigned)(a_ - stv.r0); SBAR(); } } while (0)
struct Rev { u64 r0, c0; unsigned d; };

__device__ __forceinline__ void glds16(unsigned voff, const void* sbase, unsigned ldsaddr) {
    asm volatile("s_mov_b32 m0, %2\n\ts_nop 0\n\tglobal_load_lds_dwordx4 %0, %1" ::"v"(voff), "s"(sbase), "s"(ldsaddr) : "memory");
}
__device__ __forceinline__ void glds16x2(unsigned v0, unsigned v1, const void* sbase, unsigned ldsaddr) {
    asm volatile("s_mov_b32 m0, %3\n\ts_nop 0\n\tglobal_load_lds_dwordx4 %0, %2\n\tglobal_load_lds_dwordx4 %1, %2 offset:1024"
                 ::"v"(v0), "v"(v1), "s"(sbase), "s"(ldsaddr) : "memory");
}
__device__ __forceinline__ void glds16x4(unsigned v0, unsigned v1, unsigned v2, unsigned v3, const void* sbase, unsigned ldsaddr) {
    asm volatile("s_mov_b32 m0, %5\n\ts_nop 0\n\tglobal_load_lds_dwordx4 %0, %4\n\tglobal_load_lds_dwordx4 %1, %4 offset:1024\n\t"
                 "global_load_lds_dwordx4 %2, %4 offset:2048\n\tglobal_load_lds_dwordx4 %3, %4 offset:3072"
                 ::"v"(v0), "v"(v1), "v"(v2), "v"(v3), "s"(sbase), "s"(ldsaddr) : "memory");
}
__device__ __forceinline__ unsigned cvtpk(float lo, float hi) {
    typedef __attribute__((ext_vector_type(2))) float f32x2;
    return __builtin_bit_cast(unsigned, __builtin_convertvector((f32x2){lo, hi}, bf16x2));
}
__device__ __forceinline__ bf16x8 as_frag(u32x4 v) { return __builtin_bit_cast(bf16x8, v); }
__device__ __forceinline__ bf16x8 lds_frag(lds_cptr p) { return *(const LAS bf16x8*)p; }
__device__ __forceinline__ bf16x8 tr_frag(lds_cptr p0, lds_cptr p1) {
    s16x4 a = __builtin_amdgcn_ds_read_tr16_b64_v4i16((LAS s16x4*)p0);
    s16x4 b = __builtin_amdgcn_ds_read_tr16_b64_v4i16((LAS s16x4*)p1);
    return __builtin_bit_cast(bf16x8, __builtin_shufflevector(a, b, 0, 1, 2, 3, 4, 5, 6, 7));
}
__device__ __forceinline__ f32x16 pv_lo(bf16x8 v, const f32x16& p, f32x16 acc, int idx) {
    f32x8 h = __builtin_shufflevector(p, p, 0, 1, 2, 3, 4, 5, 6, 7);
    return __builtin_amdgcn_smfmac_f32_32x32x32_bf16(v, __builtin_bit_cast(bf16x16, h), acc, idx, 0, 0);
}
__device__ __forceinline__ f32x16 pv_hi(bf16x8 v, const f32x16& p, f32x16 acc, int idx) {
    f32x8 h = __builtin_shufflevector(p, p, 8, 9, 10, 11, 12, 13, 14, 15);
    return __builtin_amdgcn_smfmac_f32_32x32x32_bf16(v, __builtin_bit_cast(bf16x16, h), acc, idx, 0, 0);
}
#define MX3(a, b, c) __builtin_fmaxf(__builtin_fmaxf((a), (b)), (c))
__device__ __forceinline__ float max16(const f32x16& s) {
    float a = MX3(s[0], s[1], s[2]), b = MX3(s[3], s[4], s[5]);
    a = MX3(a, s[6], s[7]); b = MX3(b, s[8], s[9]); a = MX3(a, s[10], s[11]); b = MX3(b, s[12], s[13]);
    return MX3(a, b, __builtin_fmaxf(s[14], s[15]));
}
__device__ __forceinline__ float halfmax(float m) {
    auto r = __builtin_amdgcn_permlane32_swap(__float_as_uint(m), __float_as_uint(m), false, false);
    return __builtin_fmaxf(__uint_as_float(r[0]), __uint_as_float(r[1]));
}
__device__ __forceinline__ f32x16 lds_f32x16(lds_cptr p) {
    f32x4 a = *(const LAS f32x4*)p, b = *(const LAS f32x4*)(p + 1024), c = *(const LAS f32x4*)(p + 2048), d = *(const LAS f32x4*)(p + 3072);
    f32x8 lo = __builtin_shufflevector(a, b, 0, 1, 2, 3, 4, 5, 6, 7), hi = __builtin_shufflevector(c, d, 0, 1, 2, 3, 4, 5, 6, 7);
    return __builtin_shufflevector(lo, hi, 0, 1, 2, 3, 4, 5, 6, 7, 8, 9, 10, 11, 12, 13, 14, 15);
}
#define BLOAD(rs, vo, so) as_frag(__builtin_amdgcn_raw_buffer_load_b128(rs, vo, so, 16))
#define BSTORE(v, rs, vo, so) __builtin_amdgcn_raw_buffer_store_b128(v, rs, (vo) + (so), 0, 16)

#define POLLSLP 8
#define BSLP 44
__device__ __forceinline__ void wait_tags_end(u64 v, const GAS u64* p, u64 tag, GAS u64* pub, int lane) {
    if (pub) { DRAIN(); if (lane == 0) __hip_atomic_store(pub, tag, RLX_AGENT); }
#pragma nounroll
    for (unsigned spins = 0; spins < SPIN_CAP && !__all(v == tag); ++spins) {
        __builtin_amdgcn_s_sleep(POLLSLP);
        v = __hip_atomic_load(p, RLX_AGENT);
    }
    asm volatile("" ::: "memory");
}

__device__ __forceinline__ void wait_tags(const GAS u64* p, u64 tag, GAS u64* pub = nullptr, int lane = 0) {
    wait_tags_end(__hip_atomic_load(p, RLX_AGENT), p, tag, pub, lane);
}

__device__ __forceinline__ void cnt_arrive(lds_ptr cnt, int lane) {
    asm volatile("s_waitcnt lgkmcnt(0)" ::: "memory");
    if (lane == 0) __hip_atomic_fetch_add((LAS unsigned*)cnt, 1u, __ATOMIC_RELAXED, __HIP_MEMORY_SCOPE_WORKGROUP);
}
__device__ __forceinline__ void cnt_wait(lds_ptr cnt) {
#pragma nounroll
    for (unsigned spins = 0; spins < (1u << 22); ++spins) {
        if (__builtin_amdgcn_readfirstlane(__hip_atomic_load((LAS unsigned*)cnt, __ATOMIC_RELAXED, __HIP_MEMORY_SCOPE_WORKGROUP)) == 4) break;
        __builtin_amdgcn_s_sleep(1);
    }
    asm volatile("" ::: "memory");
}
__device__ __forceinline__ unsigned kvflag(int br, int quad, int tile, int type) {
    return WS_KVFLAG + (unsigned)((((br * 8 + quad) * 4 + (tile & 3)) * 2 + type) * 16 + (tile >> 2)) * FSTR;
}

__device__ __forceinline__ void slow_rows(__amdgpu_buffer_rsrc_t rs, unsigned sbase, int lo, int hi, int g, bool band, bf16x8 q,
                                          unsigned vk, unsigned vv0, unsigned vv1, lds_cptr lds, int lane, int idx, f32x16& acc) {
    float m = -__builtin_inff();
    for (int pass = 0; pass < 2; ++pass) {
        if (pass) { m = halfmax(m); acc = f32x16{}; }
        for (int kt = lo; kt <= hi; ++kt) {
            const unsigned so = sbase + (unsigned)kt * TS;
            const bf16x8 k = BLOAD(rs, vk, so), v0 = BLOAD(rs, vv0, so), v1 = BLOAD(rs, vv1, so);
            f32x16 c = f32x16{};
            if (band && kt == g) c = lds_f32x16(lds + L_MDIAG + lane * 16);
            if (band && kt == g - 8) c = lds_f32x16(lds + L_MFAR + lane * 16);
            f32x16 s = MFMA(k, q, c);
            if (!pass) m = __builtin_fmaxf(m, max16(s));
            else {
#pragma unroll
                for (int r = 0; r < 16; ++r) s[r] = __builtin_amdgcn_exp2f(s[r] - m);
                acc = pv_lo(v0, s, acc, idx); acc = pv_hi(v1, s, acc, idx);
            }
        }
    }
}

#define PK4(A, o) ((u32x4){cvtpk(A[o], A[o + 1]), cvtpk(A[o + 2], A[o + 3]), cvtpk(A[o + 4], A[o + 5]), cvtpk(A[o + 6], A[o + 7])})
#define PROF_ARG , Rev& stv
#define PROF_PASS , stv
__device__ __forceinline__ u64 load_tag(const char* ws) {
    const u64 e = *(const __attribute__((address_space(4))) u64*)(ws + WS_EPOCH);
    return e * 0x9E3779B97F4A7C15ull + 0x632BE59BD9B4E019ull;
}
template <int E>
__device__ __forceinline__ u32x4 pack_kv(int i, const f32x16& a) {
    const int s = i & 1;
    if (E == 0) return PK4(a, 8 * s);
    auto x = __builtin_amdgcn_permlane32_swap(cvtpk(a[8 * s], a[8 * s + 1]), cvtpk(a[8 * s + 4], a[8 * s + 5]), false, false);
    auto y = __builtin_amdgcn_permlane32_swap(cvtpk(a[8 * s + 2], a[8 * s + 3]), cvtpk(a[8 * s + 6], a[8 * s + 7]), false, false);
    return (u32x4){x[0], y[0], x[1], y[1]};
}
#define PUT_KV(i, v, vo) BSTORE(v, rkv, vo, E ? ((i) & 1) * 512 + ((i) >> 1) * (128 * TS) : (i) * (64 * TS))
template <int E>
__device__ __forceinline__ void project(lds_ptr lds, char* ws, __amdgpu_buffer_rsrc_t rkv, __amdgpu_buffer_rsrc_t ro, int lane, int t, int p, int j, u64 tag PROF_ARG) {
    const int r32 = lane & 31, hh = lane >> 5;
    const unsigned q4 = (lane & 15) >> 2, wcol = ((lane >> 4) & 1) * 32 + (lane & 3) * 8;
    lds_cptr xa[4];
#pragma unroll
    for (int ks = 0; ks < 4; ++ks) xa[ks] = lds + (32 * t + r32) * 128 + (((hh ^ ((r32 >> 1) & 7)) * 16) ^ (ks * 32));
    unsigned w0o = L_W + (8 * hh + q4) * 128 + (q4 >> 1) * 64 + wcol, w1o = w0o ^ 64;
    PIN(w0o); PIN(w1o);
    const lds_cptr w0 = lds + w0o, w1 = lds + w1o, wq = lds + (8 * hh + q4) * 64 + wcol;
#define XRD(n) lds_frag(xa[(n) & 3] + (((n) >> 2) & 3) * 16384)
#define WRD(b, n) tr_frag(b + ((n) & 15) * 2048, b + ((n) & 15) * 2048 + 512)
#define YOF(n) ((n) >= 28 ? 32768 + ((n) & 3) * 2048 : ((n) & 15) * 2048)
#define YRD(b, n) tr_frag(b + YOF(n), b + YOF(n) + 512)
#define QOF(br, n) (((n) >= 28 ? L_Q15 - L_Q14 : ((n) >= 24 ? 0 : (((n) >> 2) & 3) * 16384)) + (br) * 4096 + ((n) & 3) * 1024)
#define QRD(br, n) tr_frag(((n) >= 24 ? wqh : wq) + QOF(br, n), ((n) >= 24 ? wqh : wq) + QOF(br, n) + 256)
#define MM(W, X, A) (E ? MFMA(X, W, A) : MFMA(W, X, A))
    bf16x8 xf[32], f0[4], f1[4];
    f32x16 a0 = f32x16{}, a1 = f32x16{};
    unsigned gwo = (lane & 15) == ((lane >> 4) & 1) ? L_GW + t * 1024 + hh * 16 : L_Z;
    PIN(gwo);
    f32x4 zg = f32x4{};
    bf16x8 gf[4];
#define RD1(n) do { xf[n] = XRD(n); f0[(n) & 3] = WRD(w0, n); f1[(n) & 3] = WRD(w1, n); SBAR(); } while (0)
#define GRD(n) gf[(n) & 3] = lds_frag(lds + gwo + (n) * 32)
#define GMM(n) zg = __builtin_amdgcn_mfma_f32_16x16x32_bf16(gf[(n) & 3], xf[n], zg, 0, 0, 0)
#define MM1(n) do { a0 = MM(f0[(n) & 3], xf[n], a0); a1 = MM(f1[(n) & 3], xf[n], a1); SBAR(); } while (0)
#pragma unroll
    for (int s = 0; s < 8; ++s) {
        if (s == 0) CBAR(); else BAR();
        if (s == 3) { if (E) BSTORE(((u32x4){0x3f803f80u, 0x3f803f80u, 0x3f803f80u, 0x3f803f80u}), rkv,
                                  (unsigned)((hh * 32 + 4 * (p >> 1) + (r32 >> 3)) * 64 + 4 * j + t) * TS + 2048 + (lane & 7) * 16, 0); }
        if (s == 5) BSTORE(((u32x4){~0u, ~0u, ~0u, ~0u}), ro, lane * 16, (unsigned)((4 * j + t) * 32 + 2 * p) * 1024);
        SBAR();
        RD1(4 * s); RD1(4 * s + 1);
        if (s) { GMM(4 * s - 4); GMM(4 * s - 3); GMM(4 * s - 2); GMM(4 * s - 1); SBAR(); }
        MM1(4 * s); RD1(4 * s + 2); MM1(4 * s + 1); RD1(4 * s + 3); MM1(4 * s + 2); MM1(4 * s + 3);
        if (s == 0) DRAIN();
        GRD(4 * s); GRD(4 * s + 1); GRD(4 * s + 2); GRD(4 * s + 3); SBAR();
    }
    GMM(28); GMM(29); GMM(30); GMM(31); SBAR();
    const unsigned tile = 4 * j + t;
    const unsigned vo = (unsigned)(4 * (p >> 1) * 64 + tile) * TS + (E ? (r32 >> 4) * (64 * TS) + 1024 + hh * 256 + (r32 & 15) * 16 : lane * 16);
    u32x4 pk[4] = {pack_kv<E>(0, a0), pack_kv<E>(1, a0), pack_kv<E>(2, a1), pack_kv<E>(3, a1)};
    PIN(pk[0]); PIN(pk[1]); PIN(pk[2]); PIN(pk[3]);
#define PRE8 2
#define XBAR
#define STPB 1
#define PUBN 15
#pragma unroll
    for (int i = 0; i < PRE8; ++i) PUT_KV(i, pk[i], vo);
    SBAR();
    BAR();
    if (lane < 16) {
        LAS float* gp = (LAS float*)(lds + L_G + t * 128) + lane;
        gp[0] = __builtin_amdgcn_rcpf(1.f + __expf(-zg[0])); gp[16] = __builtin_amdgcn_rcpf(1.f + __expf(-zg[1]));
    }
    SBAR();
    BAR(); SBAR();
    unsigned wqo = L_Q14 + (8 * hh + q4) * 64 + wcol;
    PIN(wqo);
    const lds_cptr wqh = lds + wqo;
    bf16x8 u0 = QRD(0, 0), u1 = QRD(1, 0), y0 = WRD(w0, 0), y1 = WRD(w1, 0);
    f32x16 b0 = f32x16{}, b1 = f32x16{}, aQ0 = f32x16{}, aQ1 = f32x16{};
    SBAR();
#pragma unroll
    for (int n = 0; n < 24; ++n) {
        aQ0 = MFMA(u0, xf[n], aQ0); SBAR();
        if (n == PUBN) { DRAIN(); if (lane == 0) __hip_atomic_store((GAS u64*)(ws + kvflag(0, p >> 1, tile, E)), tag, RLX_AGENT); SBAR(); }
        if ((n & 3) == 3) {
#pragma unroll
            for (int i = PRE8 + (n >> 2) * STPB; i < PRE8 + (n >> 2) * STPB + STPB; ++i) if (i < 4) PUT_KV(i, pk[i], vo);
            SBAR(); BAR(); SBAR();
        }
        u0 = QRD(0, n + 1); SBAR();
        aQ1 = MFMA(u1, xf[n], aQ1); SBAR();
        u1 = n < 23 ? QRD(1, n + 1) : QRD(0, 25); SBAR();
        b0 = MM(y0, xf[n], b0); SBAR(); y0 = n < 23 ? YRD(w0, n + 1) : QRD(0, 26); SBAR();
        b1 = MM(y1, xf[n], b1); SBAR(); y1 = n < 23 ? YRD(w1, n + 1) : QRD(0, 27); SBAR();
    }
    aQ0 = MFMA(u0, xf[24], aQ0); SBAR(); u0 = QRD(0, 28); SBAR(); aQ0 = MFMA(u1, xf[25], aQ0); SBAR(); u1 = QRD(0, 29); SBAR();
    aQ0 = MFMA(y0, xf[26], aQ0); SBAR(); y0 = QRD(0, 30); SBAR(); aQ0 = MFMA(y1, xf[27], aQ0); SBAR(); y1 = QRD(0, 31); SBAR();
    aQ0 = MFMA(u0, xf[28], aQ0); SBAR(); aQ0 = MFMA(u1, xf[29], aQ0); SBAR(); u1 = QRD(1, 24); SBAR();
    aQ0 = MFMA(y0, xf[30], aQ0); SBAR(); y0 = YRD(w0, 24); SBAR(); aQ0 = MFMA(y1, xf[31], aQ0); SBAR(); y1 = YRD(w1, 24); SBAR();
    const lds_ptr qd = lds + L_Q + t * 1024 + lane * 16;
#pragma unroll
    for (int n = 24; n < 32; ++n) {
        aQ1 = MFMA(u1, xf[n], aQ1); SBAR(); if (n < 31) u1 = QRD(1, n + 1); if (n > 24) PACE(); SBAR();
        b0 = MM(y0, xf[n], b0); SBAR(); if (n < 31) y0 = YRD(w0, n + 1); if (n > 24) PACE(); SBAR();
        if (n == 24) {
#pragma unroll
            for (int r = 8; r < 16; ++r) aQ0[r] *= QSCALE;
            *(LAS u32x4*)(qd + 4096) = PK4(aQ0, 8);
            SBAR();
        }
        b1 = MM(y1, xf[n], b1); SBAR();
        if (n == 24) { __builtin_amdgcn_s_setprio(2); cnt_arrive(lds + L_CNT + 12, lane); STAMP(4); SBAR(); }
        if (n < 31) y1 = YRD(w1, n + 1); if (n > 24) PACE(); SBAR();
    }
#pragma unroll
    for (int r = 0; r < 8; ++r) aQ0[r] *= QSCALE;
#pragma unroll
    for (int r = 0; r < 16; ++r) aQ1[r] *= QSCALE;
    *(LAS u32x4*)qd = PK4(aQ0, 0); *(LAS u32x4*)(qd + 8192) = PK4(aQ1, 0); *(LAS u32x4*)(qd + 12288) = PK4(aQ1, 8);
    cnt_arrive(lds + L_CNT + 8, lane);
    const unsigned vo2 = vo + 32 * 64 * TS;
#define YSLP 12
    PUT_KV(0, pack_kv<E>(0, b0), vo2); if (YSLP) __builtin_amdgcn_s_sleep(YSLP); PUT_KV(1, pack_kv<E>(1, b0), vo2); if (YSLP) __builtin_amdgcn_s_sleep(YSLP);
    PUT_KV(2, pack_kv<E>(2, b1), vo2); if (YSLP) __builtin_amdgcn_s_sleep(YSLP); PUT_KV(3, pack_kv<E>(3, b1), vo2);
    DRAIN();
    if (lane == 0) __hip_atomic_store((GAS u64*)(ws + kvflag(1, p >> 1, tile, E)), tag, RLX_AGENT);
    STAMP(19);
}

#define AB_WO (512 * 1024)
#define WOIN 2
#define CODE_LINES 256

struct Att {
    lds_ptr lds; char* ws; __amdgpu_buffer_rsrc_t rkv, ro;
    int lane, t, p, g, head; u64 tag; unsigned vk, vv0, vv1;
};
#define IDX 0xdddddddd
#define BLOADL(old, rs, vo, so) BLOAD(rs, vo, so)
#define LD3(K, V0, V1, rs, so) do { K = BLOAD(rs, a.vk, so); V0 = BLOAD(rs, a.vv0, so); V1 = BLOAD(rs, a.vv1, so); } while (0)
#define EX(X, r) X[r] = __builtin_amdgcn_exp2f(X[r])
#define STEP(X, Y, OP, VP, KN, QN, CN, O, V0, F1, F2) do { \
        EX(X, 0); EX(X, 1); SBAR(); OP = pv_hi(VP, Y, OP, IDX); SBAR(); F1; SBAR(); EX(X, 2); EX(X, 3); EX(X, 4); SBAR(); Y = MFMA(KN, QN, CN); SBAR(); \
        EX(X, 5); EX(X, 6); EX(X, 7); SBAR(); EX(X, 8); EX(X, 9); SBAR(); O = pv_lo(V0, X, O, IDX); SBAR(); F2; SBAR(); \
        EX(X, 10); EX(X, 11); EX(X, 12); EX(X, 13); EX(X, 14); EX(X, 15); SBAR(); } while (0)

__device__ __forceinline__ f32x16 lds_zero16(lds_cptr lds, int lane) {
    const volatile LAS f32x4* z = (const volatile LAS f32x4*)(lds + L_Z) + lane;
    const f32x4 a = *z, b = *z, c = *z, d = *z;
    f32x8 lo = __builtin_shufflevector(a, b, 0, 1, 2, 3, 4, 5, 6, 7), hi = __builtin_shufflevector(c, d, 0, 1, 2, 3, 4, 5, 6, 7);
    return __builtin_shufflevector(lo, hi, 0, 1, 2, 3, 4, 5, 6, 7, 8, 9, 10, 11, 12, 13, 14, 15);
}

template <int C, int BR>
__device__ __forceinline__ GAS u64* finish(const Att& a, f32x16 oo, float lrest, bf16x8 qa) {
    const float gt = *(const LAS float*)(a.lds + L_G + a.t * 128 + (a.lane & 31) * 4);
    auto lsw = __builtin_amdgcn_permlane32_swap(__float_as_uint(oo[8]), __float_as_uint(oo[8]), false, false);
    float l = __uint_as_float(lsw[0]) + lrest, z = 0.f;
#pragma unroll
    for (int r = 0; r < 8; ++r) z = __builtin_fmaf(oo[r], 0.f, z);
    if (__builtin_expect(__any(!(l + z < LMAX && l > LMIN)), 0)) {
        if (!BR) { wait_tags((const GAS u64*)(a.ws + kvflag(0, a.p >> 1, 0, 0) + a.lane * FSTR), a.tag); wait_tags((const GAS u64*)(a.ws + kvflag(0, a.p >> 1, 2, 0) + a.lane * FSTR), a.tag); }
        slow_rows(a.rkv, (unsigned)((BR * 32 + a.head) * 64) * TS, BR ? (a.g >= 8 ? a.g - 8 : 0) : 0, BR ? a.g : 63, a.g, BR, qa, a.vk, a.vv0, a.vv1, a.lds, a.lane, IDX, oo);
        auto l2 = __builtin_amdgcn_permlane32_swap(__float_as_uint(oo[8]), __float_as_uint(oo[8]), false, false);
        l = __uint_as_float(l2[0]);
    }
    const float il = __builtin_amdgcn_rcpf(l) * (BR ? 1.f - gt : gt);
    u32x4 v = (u32x4){cvtpk(oo[0] * il, oo[1] * il), cvtpk(oo[2] * il, oo[3] * il), cvtpk(oo[4] * il, oo[5] * il), cvtpk(oo[6] * il, oo[7] * il)};
    if (!C && !BR) v = __builtin_elementwise_min(v, (u32x4){~1u, ~1u, ~1u, ~1u});
    BSTORE(v, a.ro, a.vk, (unsigned)((BR * 64 + a.g) * 32 + a.head) * 1024);
    return (GAS u64*)(a.ws + WS_OFLAG) + ((BR * 64 + a.g) * 2 + C) * 16 + a.p;
}

template <int C>
__device__ __forceinline__ GAS u64* attn_full(const Att& a, f32x16 oo, f32x16 o1, f32x16 o2, f32x16 o3, f32x16 S1, __amdgpu_buffer_rsrc_t rwo, unsigned vwo, unsigned wi PROF_ARG) {
    const lds_ptr lds = a.lds; const int t = a.t, lane = a.lane, w = 4 * C + t;
    const f32x16 zc = f32x16{};
    bf16x8 ka, va0, va1, kb, vb0, vb1, kc, vc0, vc1, kd, vd0, vd1;
    const unsigned sBeg = (unsigned)(a.head * 64 + t) * TS, sEnd = sBeg + 64 * TS;
    unsigned so = (unsigned)(a.head * 64 + a.g) * TS;
#define NEXT_A() asm volatile("s_addk_i32 %0, %1" : "+s"(so) : "i"(4 * TS) : "scc")
#define WRAP_A() asm volatile("s_cmp_eq_u32 %0, %1\n\ts_cselect_b32 %0, %2, %0" : "+s"(so) : "s"(sEnd), "s"(sBeg) : "scc")
    LD3(ka, va0, va1, a.rkv, so); NEXT_A(); WRAP_A(); SBAR(); LD3(kb, vb0, vb1, a.rkv, so); NEXT_A(); WRAP_A(); SBAR();
    LD3(kc, vc0, vc1, a.rkv, so); NEXT_A(); WRAP_A(); SBAR();
    if (C) { cnt_wait(lds + L_CNT + 12); STAMP(4); }
    const lds_cptr qp = lds + L_Q + C * 4096 + lane * 16;
    bf16x8 qa = lds_frag(qp + t * 1024), q1 = lds_frag(qp + ((t + 1) & 3) * 1024), q2 = lds_frag(qp + ((t + 2) & 3) * 1024), q3 = lds_frag(qp + ((t + 3) & 3) * 1024);
    vd1 = lds_frag(lds + L_Z + lane * 16);
    PIN(q1); PIN(q2); PIN(q3); PIN(vd1); PIN(oo); PIN(o1); PIN(o2); PIN(o3); PIN(S1);
    __builtin_amdgcn_s_waitcnt(0x0F76);
    f32x16 S0 = MFMA(ka, qa, zc);
    PIN(S0);
#pragma unroll
    for (int i = 0; i < 12; ++i) __builtin_amdgcn_s_setprio(C);
    STAMP(3);
    SBAR();
    unsigned wso = 0;
#define NEXT_AW() do { if (C) asm volatile("s_addk_i32 %0, %3\n\ts_lshl_b32 %1, %2, 4" : "+s"(so), "=s"(wso) : "s"(wi), "i"(4 * TS) : "scc"); else NEXT_A(); } while (0)
#define WO_DMA() do { if (C) __builtin_amdgcn_raw_ptr_buffer_load_lds(rwo, (LAS void*)(lds + L_WO + t * 16384 + wi), 16, vwo, wso, 0, 0); } while (0)
#define WO_NEXT() do { if (C) asm volatile("s_addk_i32 %0, 0x400\n\ts_and_b32 %0, %0, 0x3fff" : "+s"(wi) :: "scc"); } while (0)
#define TILE(K, V0, V1, KN, LK, LV0, LV1, NW) do { \
        STEP(S0, S1, o3, LV1, K, q1, zc, oo, V0, LK = BLOADL(K, a.rkv, a.vk, so), LV0 = BLOADL(V0, a.rkv, a.vv0, so)); \
        STEP(S1, S0, oo, V1, K, q2, zc, o1, V0, LV1 = BLOADL(V1, a.rkv, a.vv1, so), NEXT_AW()); \
        STEP(S0, S1, o1, V1, K, q3, zc, o2, V0, WRAP_A(), WO_DMA()); \
        STEP(S1, S0, o2, V1, KN, qa, zc, o3, V0, __builtin_amdgcn_s_waitcnt(0x0F70 | (C ? NW : 6)), WO_NEXT()); } while (0)
#pragma nounroll
    for (int it = 0; it < 4; ++it) {
        TILE(ka, va0, va1, kb, kd, vd0, vd1, 7);
        TILE(kb, vb0, vb1, kc, ka, va0, va1, 8);
        TILE(kc, vc0, vc1, kd, kb, vb0, vb1, 9);
        TILE(kd, vd0, vd1, ka, kc, vc0, vc1, 9);
    }
    o3 = pv_hi(vd1, S1, o3, IDX);
    STAMP(6);
    if (C) cnt_wait(lds + L_CNT + 8); else __builtin_amdgcn_s_setprio(2);
#define PUT(O, sl) do { lds_ptr d_ = lds + L_PART + (w * 3 + sl) * 2048 + lane * 16; \
        *(LAS f32x4*)d_ = __builtin_shufflevector(O, O, 0, 1, 2, 3); *(LAS f32x4*)(d_ + 1024) = __builtin_shufflevector(O, O, 4, 5, 6, 7); } while (0)
    PUT(o1, 0); PUT(o2, 1); PUT(o3, 2);
    if (lane < 32) { LAS float* lp = (LAS float*)(lds + L_PL + w * 384) + lane; lp[0] = o1[8]; lp[32] = o2[8]; lp[64] = o3[8]; }
    if (C) { DRAIN(); cnt_arrive(lds + L_CNT, lane); cnt_wait(lds + L_CNT); } else CBAR();
    float lrest = 0.f;
#pragma unroll
    for (int s = 0; s < 3; ++s) {
        const int src = (C * 4 + ((t - 1 - s) & 3)) * 3 + s;
        const lds_cptr d = lds + L_PART + src * 2048 + lane * 16;
        const f32x4 x = *(const LAS f32x4*)d, y = *(const LAS f32x4*)(d + 1024);
#pragma unroll
        for (int r = 0; r < 4; ++r) { oo[r] += x[r]; oo[4 + r] += y[r]; }
        const float lp = *(const LAS float*)(lds + L_PL + src * 128 + (lane & 31) * 4);
        lrest = s ? lrest + lp : lp;
    }
    return finish<C, 0>(a, oo, lrest, qa);
}

template <int C>
__device__ __forceinline__ GAS u64* attn_window(const Att& a, GAS u64* pub PROF_ARG) {
    const lds_ptr lds = a.lds; const int t = a.t, lane = a.lane, g = a.g;
    const f32x16 zc = f32x16{};
    { const int i = lane & 15, kt = g - (i < 9 ? i : 0);
      wait_tags((const GAS u64*)(a.ws + kvflag(1, a.p >> 1, kt >= 0 ? kt : 0, (lane >> 4) & 1)), a.tag, pub, lane); }
    STAMP(25);
    bf16x8 k[9], v0[9], v1[9];
    const unsigned sB = (unsigned)((32 + a.head) * 64) * TS;
#define LDB(i) do { const int kt_ = g - (i); \
        const __amdgpu_buffer_rsrc_t rb_ = __builtin_amdgcn_make_buffer_rsrc(a.ws + WS_KV, 0, kt_ >= 0 ? KV_BYTES : 0u, 0x00020000); \
        LD3(k[i], v0[i], v1[i], rb_, sB + (unsigned)(kt_ >= 0 ? kt_ : 0) * TS); } while (0)
    LDB(0); SBAR(); LDB(1); SBAR(); LDB(2); SBAR();
    const bf16x8 qa = lds_frag(lds + L_Q + 8192 + C * 4096 + t * 1024 + lane * 16);
    const f32x16 cd = lds_f32x16(lds + L_MDIAG + lane * 16), cf = lds_f32x16(lds + L_MFAR + lane * 16);
    f32x16 oo = lds_zero16(lds, lane);
    f32x16 S0 = MFMA(k[0], qa, cd), S1 = MFMA(k[1], qa, zc);
    SBAR(); LDB(3); SBAR();
#pragma unroll
    for (int r = 0; r < 10; ++r) { EX(S0, r); SBAR(); }
    oo = pv_lo(v0[0], S0, oo, IDX); SBAR();
#pragma unroll
    for (int r = 10; r < 16; ++r) EX(S0, r);
    SBAR();
    STEP(S1, S0, oo, v1[0], k[2], qa, zc, oo, v0[1], LDB(4), );
    STEP(S0, S1, oo, v1[1], k[3], qa, zc, oo, v0[2], LDB(5), );
    STEP(S1, S0, oo, v1[2], k[4], qa, zc, oo, v0[3], LDB(6), );
    STEP(S0, S1, oo, v1[3], k[5], qa, zc, oo, v0[4], LDB(7), );
    STEP(S1, S0, oo, v1[4], k[6], qa, zc, oo, v0[5], LDB(8), );
    STEP(S0, S1, oo, v1[5], k[7], qa, zc, oo, v0[6], , );
    STEP(S1, S0, oo, v1[6], k[8], qa, cf, oo, v0[7], , );
    STEP(S0, S1, oo, v1[7], k[8], qa, cf, oo, v0[8], , );
    oo = pv_hi(v1[8], S0, oo, IDX);
    STAMP(7);
    return finish<C, 1>(a, oo, 0.f, qa);
}

__global__ void __launch_bounds__(512, 2)
gmix_kernel(const char* x, const char* wA, const char* woA, const char* wB, const char* woB, const char* gw, char* out, char* ws) {
    extern __shared__ __attribute__((aligned(16))) char smem[];
    const lds_ptr lds = (lds_ptr)smem;
    const unsigned lds0 = (unsigned)(uintptr_t)lds;
    const int tid = threadIdx.x, lane = tid & 63, r32 = lane & 31, hh = lane >> 5;
    const int w = __builtin_amdgcn_readfirstlane(tid >> 6);
    const int bid = blockIdx.x, xcd = bid & 7, rr = bid >> 3;
    const int p = 4 * (xcd & 3) + (rr & 3), j = 8 * (xcd >> 2) + (rr >> 2);
    const int jo = 2 * xcd + (rr >> 4), po = rr & 15;
    const int t = w & 3, c = w >> 2;
    const int e = p & 1;
    Rev stv;
    STAMP(0);
    u64 pc; asm volatile("s_getpc_b64 %0" : "=s"(pc));
    unsigned rot = (j + 2 * (p & 3)) & 7;
    unsigned vx0 = (128 * j + 32 * t + (lane >> 3)) * 1024 + (((lane & 7) ^ (lane >> 4)) * 16), vx1 = (vx0 ^ 64) + 7168, vx2 = vx0 + 14336, vx3 = vx1 + 14336;
    unsigned vw0 = (16 * t + (lane >> 3)) * 3072 + 1024 + 1024 * e + 128 * (p >> 1) + (((lane & 7) ^ (((lane >> 4) & 1) << 2)) * 16), vw1 = vw0 + 8 * 3072 - 1024;
    unsigned mx = lds0 + t * 4096, mw = lds0 + L_W + t * 2048, ow = rot * (64 * 3072);
    asm volatile("" : "+v"(vx0), "+v"(vx1), "+v"(vx2), "+v"(vx3), "+v"(vw0), "+v"(vw1));
    asm volatile("" : "+s"(mx), "+s"(mw), "+s"(rot), "+s"(ow));
    SBAR();
    asm volatile("" : "+s"(x), "+s"(wA), "+s"(woA), "+s"(wB), "+s"(woB), "+s"(gw), "+s"(out), "+s"(ws));
#define MK_ATT(C) Att a; a.lds = lds; a.ws = ws; a.lane = lane; a.t = t; a.p = p; a.g = 4 * j + t; a.head = 2 * p + C; \
    a.rkv = __builtin_amdgcn_make_buffer_rsrc(ws + WS_KV, 0, KV_BYTES, 0x00020000); a.ro = __builtin_amdgcn_make_buffer_rsrc(ws + WS_O, 0, O_BYTES, 0x00020000); \
    a.vk = lane * 16; a.vv0 = r32 < 16 ? 1024 + hh * 256 + r32 * 16 : (r32 == 16 ? 2048 : OOB); a.vv1 = r32 < 16 ? a.vv0 + 512 : a.vv0
    if (c) {
#define DMA1(s) do { const unsigned ks_ = ((s) + rot) & 7; \
        glds16x4(vx0, vx1, vx2, vx3, x + ks_ * 128, mx + ((s) & 3) * 16384); glds16x2(vw0, vw1, wA + ks_ * (64 * 3072), mw + ((s) & 3) * 8192); } while (0)
#define DMA2(s) do { const unsigned ks_ = (((s) + rot) & 7) * (64 * 3072); \
        glds16x2(vq0, vq1, wq + ks_, mq + ((s) == 14 ? L_Q14 : ((s) & 3) * 16384)); glds16x2(vw0, vw1, wB + ks_, mw + ((s) & 3) * 8192); SBAR(); } while (0)
#define LBAR(N) asm volatile("s_waitcnt vmcnt(" #N ")\n\ts_barrier" ::: "memory")
        STAMP(20);
        glds16x4(vx0, vx1, vx2, vx3, x + rot * 128, mx); glds16x2(vw0, vw1, wA + ow, mw);
        SBAR();
        DMA1(1);
        STAMP(22);
        SBAR();
#pragma nounroll
        for (int s = 2; s < 8; ++s) { LBAR(6); DMA1(s);
            if (s == 2) STAMP(1); if (s == 3) STAMP(2);
        }
        const unsigned vq0 = (32 * (t & 1) + (lane >> 2)) * 3072 + 64 * p + (lane & 3) * 16, vq1 = vq0 + 16 * 3072 - 1024, mq = lds0 + t * 2048;
        const char* wq = (t >> 1) ? wB : wA;
        LBAR(6); STAMP(14); DMA2(8);
        LBAR(4); STAMP(15);
        BAR();
        DMA2(9);
        MK_ATT(1);
        a.tag = load_tag(ws);
        asm volatile("" : "+s"(a.tag)); PIN(a.vk); PIN(a.vv0); PIN(a.vv1);
        const GAS u64* fp = (const GAS u64*)(ws + kvflag(0, p >> 1, t, 0) + r32 * FSTR);
        PIN(fp);
        LBAR(4); STAMP(16); DMA2(10);
        const unsigned k7 = ((7 + rot) & 7) * (64 * 3072), sx = mq + L_Q15, sw = mw + 32768;
        LBAR(4); STAMP(17); glds16(vq0, wq + k7, sx); DMA2(11);
        f32x16 oo = f32x16{}, o1 = f32x16{}; PIN(oo); PIN(o1);
        LBAR(5); STAMP(18); glds16(vw0, wB + k7, sw); DMA2(12);
        f32x16 o2 = f32x16{}, o3 = f32x16{}; PIN(o2); PIN(o3);
        LBAR(5); STAMP(19); glds16(vq1, wq + k7 + 1024, sx + 1024); DMA2(13);
        f32x16 S1 = f32x16{}; PIN(S1);
        const __amdgpu_buffer_rsrc_t rwo = __builtin_amdgcn_make_buffer_rsrc((void*)((t >> 1) ? woB : woA), 0, AB_WO, 0x00020000);
        unsigned vwo = ((t & 1) * 256 + (lane >> 2)) * 1024 + 64 * po + (lane & 3) * 16; PIN(vwo);
        LBAR(5); STAMP(21); glds16(vw1, wB + k7 + 1024, sw + 1024); DMA2(14);
        __builtin_amdgcn_s_setprio(1);
        LBAR(5); STAMP(24);
        LBAR(0); STAMP(29);
        wait_tags(fp, a.tag);
        STAMP(5);
        GAS u64* pub = attn_full<1>(a, oo, o1, o2, o3, S1, rwo, vwo, 4096 * (((rr >> 4) << 1) | (po & 1)) PROF_PASS);
        STAMP(23);
        pub = attn_window<1>(a, pub PROF_PASS);
        STAMP(8);
        const int go = 4 * jo + t;
        const lds_cptr wo = lds + L_WO + (4 * hh + ((lane & 15) >> 2)) * 64 + ((lane >> 4) & 1) * 32 + (lane & 3) * 8;
        const GAS u64* of_ = (const GAS u64*)(ws + WS_OFLAG);
        wait_tags(of_ + ((lane & 16) ? go * 32 + 16 : (64 + go) * 32) + (lane & 15), a.tag, pub, lane);
        STAMP(9);
#define OBR(n) ((n) >> 4 == 1 ? 0 : 1)
#define OHD(n) (2 * ((n) & 15) + ((n) >= 16))
#define OLD(n) BLOAD(a.ro, a.vk, (unsigned)((OBR(n) * 64 + go) * 32 + OHD(n)) * 1024)
#define OWF(n) tr_frag(wo + OBR(n) * 32768 + OHD(n) * 1024, wo + OBR(n) * 32768 + OHD(n) * 1024 + 512)
#define OIN 7
        bf16x8 oe[48];
#pragma unroll
        for (int n = 0; n < 48; ++n) {
            if (n == 32) wait_tags(of_ + (64 + go) * 32 + 16 + (lane & 15), a.tag);
            oe[n] = OLD(n); SBAR();
            if (n >= OIN - 1) __builtin_amdgcn_s_waitcnt(0x0F70 | (OIN - 1));
            SBAR();
        }
        bf16x8 wn[4];
#pragma unroll
        for (int n = 0; n < 4; ++n) { wn[n] = OWF(n); PIN(wn[n]); }
        __builtin_amdgcn_s_waitcnt(0x0F70);
#define NPRE 0
#define NMID 24
        f32x16 ya = f32x16{};
        bf16x8 wf[16], of[16];
#define OMM(n) do { ya = MFMA(wn[(n) & 3], oe[n], ya); SBAR(); if ((n) + 4 < 48) wn[(n) & 3] = OWF((n) + 4); \
            if ((n) >= 32) wf[(n) - 32] = tr_frag(wo + 2 * ((n) - 32) * 1024, wo + 2 * ((n) - 32) * 1024 + 512); SBAR(); } while (0)
#pragma unroll
        for (int n = 0; n < NPRE; ++n) { OMM(n); PACE(); }
        STAMP(10);
        BAR();
        BAR();
#pragma unroll
        for (int n = NPRE; n < NMID; ++n) OMM(n);
        STAMP(30);
        BAR();
        STAMP(11);
#define GRP_BAD(q) ({ unsigned mx_ = 0; _Pragma("unroll") for (int i = 4 * (q); i < 4 * (q) + 4; ++i) { const u32x4 u_ = __builtin_bit_cast(u32x4, of[i]); \
            mx_ = __builtin_elementwise_max(__builtin_elementwise_max(mx_, u_[0]), u_[1]); mx_ = __builtin_elementwise_max(__builtin_elementwise_max(mx_, u_[2]), u_[3]); } \
            __any(mx_ == ~0u); })
#define GRP_MM(q) do { _Pragma("unroll") for (int i = 4 * (q); i < 4 * (q) + 4; ++i) ya = MFMA(wf[i], of[i], ya); } while (0)
#define GRP_LD(q) do { _Pragma("unroll") for (int i = 4 * (q); i < 4 * (q) + 4; ++i) of[i] = BLOAD(a.ro, a.vk, (unsigned)(go * 32 + 2 * i) * 1024); } while (0)
#pragma unroll
        for (int n = NMID; n < 48; ++n) { if (n - NMID < 16) { of[n - NMID] = BLOAD(a.ro, a.vk, (unsigned)(go * 32 + 2 * (n - NMID)) * 1024); SBAR(); } OMM(n); }
        STAMP(31);
        int done = 0;
        if (!GRP_BAD(0)) { GRP_MM(0); done = 1; if (!GRP_BAD(1)) { GRP_MM(1); done = 2; if (!GRP_BAD(2)) { GRP_MM(2); done = 3; if (!GRP_BAD(3)) { GRP_MM(3); done = 4; } } } }
        if (done < 4) {
#pragma nounroll
            for (unsigned spins = 0; spins < SPIN_CAP; ++spins) {
                const bool b0 = GRP_BAD(0), b1 = GRP_BAD(1), b2 = GRP_BAD(2), b3 = GRP_BAD(3);
                if (!(b0 || b1 || b2 || b3)) break;
                asm volatile("" ::: "memory");
                if (b0) GRP_LD(0); if (b1) GRP_LD(1); if (b2) GRP_LD(2); if (b3) GRP_LD(3);
            }
            if (done < 1) GRP_MM(0); if (done < 2) GRP_MM(1); if (done < 3) GRP_MM(2); GRP_MM(3);
        }
#pragma unroll
        for (int s = 0; s < 2; ++s) {
            auto u = __builtin_amdgcn_permlane32_swap(cvtpk(ya[8 * s], ya[8 * s + 1]), cvtpk(ya[8 * s + 4], ya[8 * s + 5]), false, false);
            auto v = __builtin_amdgcn_permlane32_swap(cvtpk(ya[8 * s + 2], ya[8 * s + 3]), cvtpk(ya[8 * s + 6], ya[8 * s + 7]), false, false);
#define OUT_AUX 0
            __builtin_amdgcn_raw_buffer_store_b128(((u32x4){u[0], v[0], u[1], v[1]}), __builtin_amdgcn_make_buffer_rsrc(out, 0, 2048 * 1024, 0x00020000),
                                                   (32 * go + r32) * 1024 + 64 * po + 32 * s + 16 * hh, 0, OUT_AUX);
        }
        if (bid == 0 && tid == 256) __hip_atomic_store((GAS u64*)(ws + WS_EPOCH), a.tag, RLX_AGENT);
        STAMP(12);
    } else {
        unsigned pf0 = 0;
        {
            const unsigned ln = rr * 4 + t;
            asm volatile("s_load_dword %0, %1, %2\n\ts_load_dword %0, %1, %3" : "+s"(pf0) : "s"(pc & ~127ull), "s"(ln * 128), "s"((ln + CODE_LINES - 128) * 128) : "memory");
        }
        glds16((((lane >> 3) + rot) & 7) * 128 + (lane & 7) * 16, gw, lds0 + L_GW + t * 1024);
#pragma unroll
        for (int m = 0; m < 2; ++m) {
            f32x4 mk;
#pragma unroll
            for (int i = 0; i < 4; ++i) { const bool le = (i + 8 * t + 4 * hh) <= r32; mk[i] = (le != (m != 0)) ? 0.f : -__builtin_inff(); }
            *(LAS f32x4*)(lds + L_MDIAG + m * 4096 + t * 1024 + lane * 16) = mk;
        }
        if (tid < 4) *(LAS unsigned*)(lds + L_CNT + tid * 4) = 0;
        *(LAS unsigned*)(lds + L_Z + tid * 4) = 0;
        u64 tag = load_tag(ws);
        asm volatile("s_waitcnt lgkmcnt(0)" : "+s"(pf0), "+s"(tag)::"memory");
        STAMP(21);
        {
            const __amdgpu_buffer_rsrc_t rkv = __builtin_amdgcn_make_buffer_rsrc(ws + WS_KV, 0, KV_BYTES, 0x00020000);
            const __amdgpu_buffer_rsrc_t ro = __builtin_amdgcn_make_buffer_rsrc(ws + WS_O, 0, O_BYTES, 0x00020000);
            if (e) project<1>(lds, ws, rkv, ro, lane, t, p, j, tag PROF_PASS); else project<0>(lds, ws, rkv, ro, lane, t, p, j, tag PROF_PASS);
        }
        cnt_wait(lds + L_CNT + 8);
        __builtin_amdgcn_s_sleep(BSLP);
        STAMP(24);
        MK_ATT(0);
        a.tag = tag;
        GAS u64* pub = attn_window<0>(a, nullptr PROF_PASS);
        STAMP(23);
        wait_tags((const GAS u64*)(ws + kvflag(0, p >> 1, t, 0) + r32 * FSTR), tag, pub, lane);
        STAMP(5);
        attn_full<0>(a, lds_zero16(lds, lane), lds_zero16(lds, lane), lds_zero16(lds, lane), lds_zero16(lds, lane), lds_zero16(lds, lane), a.rkv, 0, 0 PROF_PASS);
        BAR();
        STAMP(8);
        DRAIN(); BAR();
    }
    DRAIN();
    STAMP(13);
    if (c == RROLE && bid == 0) while (__builtin_amdgcn_s_memrealtime() < stv.r0 + 6000 + stv.d) __builtin_amdgcn_s_sleep(1);
}

extern "C" void kernel_launch(void* const* d_in, const int* in_sizes, int n_in, void* d_out, int out_size, void* d_ws, size_t ws_size,
                              hipStream_t stream) {
    (void)in_sizes; (void)n_in; (void)out_size; (void)ws_size;
    (void)hipFuncSetAttribute((const void*)gmix_kernel, hipFuncAttributeMaxDynamicSharedMemorySize, L_END);
    gmix_kernel<<<256, 512, L_END, stream>>>((const char*)d_in[0], (const char*)d_in[1], (const char*)d_in[2], (const char*)d_in[4],
                                             (const char*)d_in[5], (const char*)d_in[6], (char*)d_out, (char*)d_ws);
}
```

```cpp
#include <hip/hip_runtime.h>
#include <hip/hip_bf16.h>

typedef __attribute__((ext_vector_type(8))) __bf16 bf16x8;
typedef __attribute__((ext_vector_type(16))) __bf16 bf16x16;
typedef __attribute__((ext_vector_type(16))) float f32x16;
typedef __attribute__((ext_vector_type(8))) float f32x8;
typedef __attribute__((ext_vector_type(4))) float f32x4;
typedef __attribute__((ext_vector_type(4))) unsigned u32x4;
typedef __attribute__((ext_vector_type(2))) unsigned u32x2;
typedef __attribute__((ext_vector_type(2))) __bf16 bf16x2;
typedef short s16x4 __attribute__((ext_vector_type(4)));
typedef unsigned long long u64;
#define GAS __attribute__((address_space(1)))
#define LAS __attribute__((address_space(3)))
typedef LAS const char* lds_cptr;
typedef LAS char* lds_ptr;

constexpr unsigned WS_EPOCH = 0;
#define FSTR 8
constexpr unsigned WS_OFLAG = 65536;
constexpr unsigned WS_KVFLAG = 131072;
constexpr unsigned WS_KV = 1u << 20;
constexpr unsigned TS = 2176;
constexpr unsigned KV_BYTES = 2u * 32u * 64u * TS;
constexpr unsigned WS_O = 10u << 20;
constexpr unsigned O_BYTES = 4u << 20;
constexpr unsigned WS_STAMP = 16u << 20;
constexpr unsigned OOB = 0x7ff00000u;
constexpr unsigned L_W = 65536;
constexpr unsigned L_WO = 0, L_PART = 65536, L_PL = 114688, L_CNT = 117760, L_MDIAG = 118784, L_MFAR = 122880, L_Q = 126976, L_GW = 143360, L_Z = 147456, L_G = 148480, L_Q14 = 106496, L_Q15 = 148992, L_END = 157184;
constexpr float QSCALE = 0.25f * 1.4426950408889634f;
constexpr float LMAX = 1.2676506e30f, LMIN = 9.5367432e-7f;
constexpr unsigned SPIN_CAP = 1u << 17;

#define SBAR() __builtin_amdgcn_sched_barrier(0)
#define PIN(x) asm volatile("" : "+v"(x))
#define MFMA(a, b, c) __builtin_amdgcn_mfma_f32_32x32x16_bf16(a, b, c, 0, 0, 0)
#define BAR() asm volatile("s_barrier" ::: "memory")
#define CBAR() asm volatile("s_waitcnt lgkmcnt(0)\n\ts_barrier" ::: "memory")
#define DRAIN() asm volatile("s_waitcnt vmcnt(0)" ::: "memory")
#define PACEN 3
#define PACE() asm volatile("s_nop %0" :: "i"(PACEN) : "memory")
#define RLX_AGENT __ATOMIC_RELAXED, __HIP_MEMORY_SCOPE_AGENT
#define STAMP(i)

__device__ __forceinline__ void glds16(unsigned voff, const void* sbase, unsigned ldsaddr) {
    asm volatile("s_mov_b32 m0, %2\n\ts_nop 0\n\tglobal_load_lds_dwordx4 %0, %1" ::"v"(voff), "s"(sbase), "s"(ldsaddr) : "memory");
}
__device__ __forceinline__ void glds16x2(unsigned v0, unsigned v1, const void* sbase, unsigned ldsaddr) {
    asm volatile("s_mov_b32 m0, %3\n\ts_nop 0\n\tglobal_load_lds_dwordx4 %0, %2\n\tglobal_load_lds_dwordx4 %1, %2 offset:1024"
                 ::"v"(v0), "v"(v1), "s"(sbase), "s"(ldsaddr) : "memory");
}
__device__ __forceinline__ void glds16x4(unsigned v0, unsigned v1, unsigned v2, unsigned v3, const void* sbase, unsigned ldsaddr) {
    asm volatile("s_mov_b32 m0, %5\n\ts_nop 0\n\tglobal_load_lds_dwordx4 %0, %4\n\tglobal_load_lds_dwordx4 %1, %4 offset:1024\n\t"
                 "global_load_lds_dwordx4 %2, %4 offset:2048\n\tglobal_load_lds_dwordx4 %3, %4 offset:3072"
                 ::"v"(v0), "v"(v1), "v"(v2), "v"(v3), "s"(sbase), "s"(ldsaddr) : "memory");
}
__device__ __forceinline__ unsigned cvtpk(float lo, float hi) {
    typedef __attribute__((ext_vector_type(2))) float f32x2;
    return __builtin_bit_cast(unsigned, __builtin_convertvector((f32x2){lo, hi}, bf16x2));
}
__device__ __forceinline__ bf16x8 as_frag(u32x4 v) { return __builtin_bit_cast(bf16x8, v); }
__device__ __forceinline__ bf16x8 lds_frag(lds_cptr p) { return *(const LAS bf16x8*)p; }
__device__ __forceinline__ bf16x8 tr_frag(lds_cptr p0, lds_cptr p1) {
    s16x4 a = __builtin_amdgcn_ds_read_tr16_b64_v4i16((LAS s16x4*)p0);
    s16x4 b = __builtin_amdgcn_ds_read_tr16_b64_v4i16((LAS s16x4*)p1);
    return __builtin_bit_cast(bf16x8, __builtin_shufflevector(a, b, 0, 1, 2, 3, 4, 5, 6, 7));
}
__device__ __forceinline__ f32x16 pv_lo(bf16x8 v, const f32x16& p, f32x16 acc, int idx) {
    f32x8 h = __builtin_shufflevector(p, p, 0, 1, 2, 3, 4, 5, 6, 7);
    return __builtin_amdgcn_smfmac_f32_32x32x32_bf16(v, __builtin_bit_cast(bf16x16, h), acc, idx, 0, 0);
}
__device__ __forceinline__ f32x16 pv_hi(bf16x8 v, const f32x16& p, f32x16 acc, int idx) {
    f32x8 h = __builtin_shufflevector(p, p, 8, 9, 10, 11, 12, 13, 14, 15);
    return __builtin_amdgcn_smfmac_f32_32x32x32_bf16(v, __builtin_bit_cast(bf16x16, h), acc, idx, 0, 0);
}
#define MX3(a, b, c) __builtin_fmaxf(__builtin_fmaxf((a), (b)), (c))
__device__ __forceinline__ float max16(const f32x16& s) {
    float a = MX3(s[0], s[1], s[2]), b = MX3(s[3], s[4], s[5]);
    a = MX3(a, s[6], s[7]); b = MX3(b, s[8], s[9]); a = MX3(a, s[10], s[11]); b = MX3(b, s[12], s[13]);
    return MX3(a, b, __builtin_fmaxf(s[14], s[15]));
}
__device__ __forceinline__ float halfmax(float m) {
    auto r = __builtin_amdgcn_permlane32_swap(__float_as_uint(m), __float_as_uint(m), false, false);
    return __builtin_fmaxf(__uint_as_float(r[0]), __uint_as_float(r[1]));
}
__device__ __forceinline__ f32x16 lds_f32x16(lds_cptr p) {
    f32x4 a = *(const LAS f32x4*)p, b = *(const LAS f32x4*)(p + 1024), c = *(const LAS f32x4*)(p + 2048), d = *(const LAS f32x4*)(p + 3072);
    f32x8 lo = __builtin_shufflevector(a, b, 0, 1, 2, 3, 4, 5, 6, 7), hi = __builtin_shufflevector(c, d, 0, 1, 2, 3, 4, 5, 6, 7);
    return __builtin_shufflevector(lo, hi, 0, 1, 2, 3, 4, 5, 6, 7, 8, 9, 10, 11, 12, 13, 14, 15);
}
#define BLOAD(rs, vo, so) as_frag(__builtin_amdgcn_raw_buffer_load_b128(rs, vo, so, 16))
#define BSTORE(v, rs, vo, so) __builtin_amdgcn_raw_buffer_store_b128(v, rs, (vo) + (so), 0, 16)

#define POLLSLP 8
#define BSLP 36
__device__ __forceinline__ void wait_tags_end(u64 v, const GAS u64* p, u64 tag, GAS u64* pub, int lane) {
    if (pub) { DRAIN(); if (lane == 0) __hip_atomic_store(pub, tag, RLX_AGENT); }
    if (__builtin_expect(!__all(v == tag), 0)) {
#pragma nounroll
        for (unsigned spins = 0; spins < SPIN_CAP; ++spins) {
            __builtin_amdgcn_s_sleep(POLLSLP);
            if (__all(__hip_atomic_load(p, RLX_AGENT) == tag)) break;
        }
    }
    asm volatile("" ::: "memory");
}

__device__ __forceinline__ void wait_tags(const GAS u64* p, u64 tag, GAS u64* pub = nullptr, int lane = 0) {
    wait_tags_end(__hip_atomic_load(p, RLX_AGENT), p, tag, pub, lane);
}

__device__ __forceinline__ void cnt_arrive(lds_ptr cnt, int lane) {
    asm volatile("s_waitcnt lgkmcnt(0)" ::: "memory");
    if (lane == 0) __hip_atomic_fetch_add((LAS unsigned*)cnt, 1u, __ATOMIC_RELAXED, __HIP_MEMORY_SCOPE_WORKGROUP);
}
__device__ __forceinline__ void cnt_wait(lds_ptr cnt) {
#pragma nounroll
    for (unsigned spins = 0; spins < (1u << 22); ++spins) {
        if (__builtin_amdgcn_readfirstlane(__hip_atomic_load((LAS unsigned*)cnt, __ATOMIC_RELAXED, __HIP_MEMORY_SCOPE_WORKGROUP)) == 4) break;
        __builtin_amdgcn_s_sleep(1);
    }
    asm volatile("" ::: "memory");
}
__device__ __forceinline__ unsigned kvflag(int br, int quad, int tile, int type) {
    return WS_KVFLAG + (unsigned)((((br * 8 + quad) * 4 + (tile & 3)) * 2 + type) * 16 + (tile >> 2)) * FSTR;
}

__device__ __forceinline__ void slow_rows(__amdgpu_buffer_rsrc_t rs, unsigned sbase, int lo, int hi, int g, bool band, bf16x8 q,
                                          unsigned vk, unsigned vv0, unsigned vv1, lds_cptr lds, int lane, int idx, f32x16& acc) {
    float m = -__builtin_inff();
    for (int pass = 0; pass < 2; ++pass) {
        if (pass) { m = halfmax(m); acc = f32x16{}; }
        for (int kt = lo; kt <= hi; ++kt) {
            const unsigned so = sbase + (unsigned)kt * TS;
            const bf16x8 k = BLOAD(rs, vk, so), v0 = BLOAD(rs, vv0, so), v1 = BLOAD(rs, vv1, so);
            f32x16 c = f32x16{};
            if (band && kt == g) c = lds_f32x16(lds + L_MDIAG + lane * 16);
            if (band && kt == g - 8) c = lds_f32x16(lds + L_MFAR + lane * 16);
            f32x16 s = MFMA(k, q, c);
            if (!pass) m = __builtin_fmaxf(m, max16(s));
            else {
#pragma unroll
                for (int r = 0; r < 16; ++r) s[r] = __builtin_amdgcn_exp2f(s[r] - m);
                acc = pv_lo(v0, s, acc, idx); acc = pv_hi(v1, s, acc, idx);
            }
        }
    }
}

#define PK4(A, o) ((u32x4){cvtpk(A[o], A[o + 1]), cvtpk(A[o + 2], A[o + 3]), cvtpk(A[o + 4], A[o + 5]), cvtpk(A[o + 6], A[o + 7])})
#define PROF_ARG
#define PROF_PASS
template <int E>
__device__ __forceinline__ u32x4 pack_kv(int i, const f32x16& a) {
    const int s = i & 1;
    if (E == 0) return PK4(a, 8 * s);
    auto x = __builtin_amdgcn_permlane32_swap(cvtpk(a[8 * s], a[8 * s + 1]), cvtpk(a[8 * s + 4], a[8 * s + 5]), false, false);
    auto y = __builtin_amdgcn_permlane32_swap(cvtpk(a[8 * s + 2], a[8 * s + 3]), cvtpk(a[8 * s + 6], a[8 * s + 7]), false, false);
    return (u32x4){x[0], y[0], x[1], y[1]};
}
#define PUT_KV(i, v, vo) BSTORE(v, rkv, vo, E ? ((i) & 1) * 512 + ((i) >> 1) * (128 * TS) : (i) * (64 * TS))
template <int E>
__device__ __forceinline__ void project(lds_ptr lds, char* ws, __amdgpu_buffer_rsrc_t rkv, __amdgpu_buffer_rsrc_t ro, int lane, int t, int p, int j, u64& tag PROF_ARG) {
    const int r32 = lane & 31, hh = lane >> 5;
    const unsigned q4 = (lane & 15) >> 2, wcol = ((lane >> 4) & 1) * 32 + (lane & 3) * 8;
    lds_cptr xa[4];
#pragma unroll
    for (int ks = 0; ks < 4; ++ks) xa[ks] = lds + (32 * t + r32) * 128 + (((hh ^ ((r32 >> 1) & 7)) * 16) ^ (ks * 32));
    unsigned w0o = L_W + (8 * hh + q4) * 128 + (q4 >> 1) * 64 + wcol, w1o = w0o ^ 64;
    PIN(w0o); PIN(w1o);
    const lds_cptr w0 = lds + w0o, w1 = lds + w1o, wq = lds + (8 * hh + q4) * 64 + wcol;
#define XRD(n) lds_frag(xa[(n) & 3] + (((n) >> 2) & 3) * 16384)
#define WRD(b, n) tr_frag(b + ((n) & 15) * 2048, b + ((n) & 15) * 2048 + 512)
#define YOF(n) ((n) >= 28 ? 32768 + ((n) & 3) * 2048 : ((n) & 15) * 2048)
#define YRD(b, n) tr_frag(b + YOF(n), b + YOF(n) + 512)
#define QOF(br, n) (((n) >= 28 ? L_Q15 - L_Q14 : ((n) >= 24 ? 0 : (((n) >> 2) & 3) * 16384)) + (br) * 4096 + ((n) & 3) * 1024)
#define QRD(br, n) tr_frag(((n) >= 24 ? wqh : wq) + QOF(br, n), ((n) >= 24 ? wqh : wq) + QOF(br, n) + 256)
#define MM(W, X, A) (E ? MFMA(X, W, A) : MFMA(W, X, A))
    bf16x8 xf[32], f0[4], f1[4];
    f32x16 a0 = f32x16{}, a1 = f32x16{};
    unsigned gwo = (lane & 15) == ((lane >> 4) & 1) ? L_GW + t * 1024 + hh * 16 : L_Z;
    PIN(gwo);
    f32x4 zg = f32x4{};
    bf16x8 gf[4];
#define RD1(n) do { xf[n] = XRD(n); f0[(n) & 3] = WRD(w0, n); f1[(n) & 3] = WRD(w1, n); SBAR(); } while (0)
#define GRD(n) gf[(n) & 3] = lds_frag(lds + gwo + (n) * 32)
#define GMM(n) zg = __builtin_amdgcn_mfma_f32_16x16x32_bf16(gf[(n) & 3], xf[n], zg, 0, 0, 0)
#define MM1(n) do { a0 = MM(f0[(n) & 3], xf[n], a0); a1 = MM(f1[(n) & 3], xf[n], a1); SBAR(); } while (0)
#pragma unroll
    for (int s = 0; s < 8; ++s) {
        if (s == 0) CBAR(); else BAR();
        SBAR();
        RD1(4 * s); RD1(4 * s + 1);
        if (s) { MM1(4 * s - 1); GMM(4 * s - 4); GMM(4 * s - 3); GMM(4 * s - 2); GMM(4 * s - 1); SBAR(); }
        RD1(4 * s + 2); MM1(4 * s); RD1(4 * s + 3); MM1(4 * s + 1);
        if (s == 0) DRAIN();
        GRD(4 * s); GRD(4 * s + 1); GRD(4 * s + 2); GRD(4 * s + 3); SBAR();
        MM1(4 * s + 2);
        if (s == 2) { if (E) BSTORE(((u32x4){0x3f803f80u, 0x3f803f80u, 0x3f803f80u, 0x3f803f80u}), rkv,
                                  (unsigned)((hh * 32 + 4 * (p >> 1) + (r32 >> 3)) * 64 + 4 * j + t) * TS + 2048 + (lane & 7) * 16, 0); }
        if (s == 4) BSTORE(((u32x4){~0u, ~0u, ~0u, ~0u}), ro, lane * 16, (unsigned)((4 * j + t) * 32 + 2 * p) * 1024);
        SBAR();
    }
    const unsigned tile = 4 * j + t;
    unsigned vo = (unsigned)(4 * (p >> 1) * 64 + tile) * TS + (E ? (r32 >> 4) * (64 * TS) + 1024 + hh * 256 + (r32 & 15) * 16 : lane * 16);
    unsigned wqo = L_Q14 + (8 * hh + q4) * 64 + wcol;
    PIN(vo); PIN(wqo);
    const lds_cptr wqh = lds + wqo;
    MM1(31); GMM(28); GMM(29); GMM(30); GMM(31); SBAR();
#define PRE8 2
#define ST8 4
#define PUBN 9
    u32x4 pk[4];
#pragma unroll
    for (int i = 0; i < PRE8; ++i) { pk[i] = pack_kv<E>(i, i < 2 ? a0 : a1); PUT_KV(i, pk[i], vo); }
    SBAR();
    BAR(); SBAR();
    bf16x8 u0 = QRD(0, 0), u1 = QRD(1, 0), y0 = QRD(0, 1), y1 = QRD(1, 1);
    const u32x2 tg = *(const LAS u32x2*)(lds + L_CNT + 16);
    f32x16 b0 = f32x16{}, b1 = f32x16{}, aQ0 = f32x16{}, aQ1 = f32x16{};
    SBAR();
#define XPK(i) do { if ((i) >= PRE8) { pk[i] = pack_kv<E>(i, (i) < 2 ? a0 : a1); PIN(pk[i]); if ((i) < ST8) PUT_KV(i, pk[i], vo); SBAR(); } } while (0)
    XPK(0);
    aQ0 = MFMA(u0, xf[0], aQ0); SBAR(); u0 = QRD(0, 2); SBAR(); XPK(1);
    aQ1 = MFMA(u1, xf[0], aQ1); SBAR(); u1 = QRD(1, 2); SBAR();
    aQ0 = MFMA(y0, xf[1], aQ0); SBAR(); y0 = QRD(0, 3); SBAR(); XPK(2);
    aQ1 = MFMA(y1, xf[1], aQ1); SBAR(); y1 = QRD(1, 3); SBAR();
    aQ0 = MFMA(u0, xf[2], aQ0); SBAR(); u0 = YRD(w0, 0); SBAR(); XPK(3);
    aQ1 = MFMA(u1, xf[2], aQ1); SBAR(); u1 = YRD(w1, 0); SBAR();
    aQ0 = MFMA(y0, xf[3], aQ0); SBAR(); y0 = YRD(w0, 1); SBAR();
    float ge0 = __expf(-zg[0]), ge1 = __expf(-zg[1]); PIN(ge0); PIN(ge1); SBAR();
    aQ1 = MFMA(y1, xf[3], aQ1); SBAR(); y1 = YRD(w1, 1); SBAR();
    ge0 = __builtin_amdgcn_rcpf(1.f + ge0); ge1 = __builtin_amdgcn_rcpf(1.f + ge1); PIN(ge0); PIN(ge1); SBAR();
    b0 = MM(u0, xf[0], b0); SBAR(); u0 = YRD(w0, 2); SBAR();
    { LAS float* gp = (LAS float*)(lds + (lane < 16 ? L_G + t * 128 : L_PL)) + lane; gp[0] = ge0; gp[16] = ge1; } SBAR();
    b1 = MM(u1, xf[0], b1); SBAR(); u1 = YRD(w1, 2); SBAR();
    tag = ((u64)(unsigned)__builtin_amdgcn_readfirstlane(tg[1]) << 32) | (unsigned)__builtin_amdgcn_readfirstlane(tg[0]);
    asm volatile("" : "+s"(tag)); SBAR();
    b0 = MM(y0, xf[1], b0); SBAR(); y0 = YRD(w0, 3); SBAR();
    b1 = MM(y1, xf[1], b1); SBAR(); y1 = YRD(w1, 3); SBAR();
    b0 = MM(u0, xf[2], b0); SBAR();
    if (ST8 < 4) { PUT_KV(ST8 & 3, pk[ST8 & 3], vo); SBAR(); }
    BAR(); SBAR();
    u0 = QRD(0, 4); SBAR();
    b1 = MM(u1, xf[2], b1); SBAR(); u1 = QRD(1, 4); SBAR();
    b0 = MM(y0, xf[3], b0); SBAR(); y0 = YRD(w0, 4); SBAR();
    b1 = MM(y1, xf[3], b1); SBAR(); y1 = YRD(w1, 4); SBAR();
#pragma unroll
    for (int n = 4; n < 24; ++n) {
        aQ0 = MFMA(u0, xf[n], aQ0); SBAR();
        if (n == PUBN) { DRAIN(); if (lane == 0) __hip_atomic_store((GAS u64*)(ws + kvflag(0, p >> 1, tile, E)), tag, RLX_AGENT); SBAR(); }
        if ((n & 3) == 3) {
            if (ST8 + (n >> 2) < 4) { PUT_KV((ST8 + (n >> 2)) & 3, pk[(ST8 + (n >> 2)) & 3], vo); SBAR(); }
            BAR(); SBAR();
        }
        u0 = QRD(0, n + 1); SBAR();
        aQ1 = MFMA(u1, xf[n], aQ1); SBAR();
        u1 = n < 23 ? QRD(1, n + 1) : QRD(0, 25); SBAR();
        b0 = MM(y0, xf[n], b0); SBAR(); y0 = n < 23 ? YRD(w0, n + 1) : QRD(0, 26); SBAR();
        b1 = MM(y1, xf[n], b1); SBAR(); y1 = n < 23 ? YRD(w1, n + 1) : QRD(0, 27); SBAR();
    }
    aQ0 = MFMA(u0, xf[24], aQ0); SBAR(); u0 = QRD(0, 28); SBAR(); aQ0 = MFMA(u1, xf[25], aQ0); SBAR(); u1 = QRD(0, 29); SBAR();
    aQ0 = MFMA(y0, xf[26], aQ0); SBAR(); y0 = QRD(0, 30); SBAR(); aQ0 = MFMA(y1, xf[27], aQ0); SBAR(); y1 = QRD(0, 31); SBAR();
    aQ0 = MFMA(u0, xf[28], aQ0); SBAR(); aQ0 = MFMA(u1, xf[29], aQ0); SBAR(); u1 = QRD(1, 24); SBAR();
    aQ0 = MFMA(y0, xf[30], aQ0); SBAR(); y0 = YRD(w0, 24); SBAR(); aQ0 = MFMA(y1, xf[31], aQ0); SBAR(); y1 = YRD(w1, 24); SBAR();
    const lds_ptr qd = lds + L_Q + t * 1024 + lane * 16;
#pragma unroll
    for (int n = 24; n < 32; ++n) {
        aQ1 = MFMA(u1, xf[n], aQ1); SBAR();
        if (n == 24) {
#pragma unroll
            for (int r = 8; r < 16; ++r) aQ0[r] *= QSCALE;
            *(LAS u32x4*)(qd + 4096) = PK4(aQ0, 8);
            __builtin_amdgcn_s_setprio(2);
            CBAR(); STAMP(4); SBAR();
        }
        if (n < 31) u1 = QRD(1, n + 1); PACE(); SBAR();
        b0 = MM(y0, xf[n], b0); SBAR(); if (n < 31) y0 = YRD(w0, n + 1); PACE(); SBAR();
        b1 = MM(y1, xf[n], b1); SBAR(); if (n < 31) y1 = YRD(w1, n + 1); PACE(); SBAR();
    }
#pragma unroll
    for (int r = 0; r < 8; ++r) aQ0[r] *= QSCALE;
#pragma unroll
    for (int r = 0; r < 16; ++r) aQ1[r] *= QSCALE;
    *(LAS u32x4*)qd = PK4(aQ0, 0); *(LAS u32x4*)(qd + 8192) = PK4(aQ1, 0); *(LAS u32x4*)(qd + 12288) = PK4(aQ1, 8);
    cnt_arrive(lds + L_CNT + 8, lane);
    const unsigned vo2 = vo + 32 * 64 * TS;
#define YSLP 8
    PUT_KV(0, pack_kv<E>(0, b0), vo2); if (YSLP) __builtin_amdgcn_s_sleep(YSLP); PUT_KV(1, pack_kv<E>(1, b0), vo2); if (YSLP) __builtin_amdgcn_s_sleep(YSLP);
    PUT_KV(2, pack_kv<E>(2, b1), vo2); if (YSLP) __builtin_amdgcn_s_sleep(YSLP); PUT_KV(3, pack_kv<E>(3, b1), vo2);
    DRAIN();
    if (lane == 0) __hip_atomic_store((GAS u64*)(ws + kvflag(1, p >> 1, tile, E)), tag, RLX_AGENT);
    STAMP(19);
}

#define AB_WO (512 * 1024)
#define WOIN 2
#define CODE_LINES 256

struct Att {
    lds_ptr lds; char* ws; __amdgpu_buffer_rsrc_t rkv, ro;
    int lane, t, p, g, head, idx; u64 tag; unsigned vk, vv0, vv1, so, sBeg, sEnd; lds_cptr zp, qp[4];
};
#define IDX a.idx
#define BLOADL(old, rs, vo, so) BLOAD(rs, vo, so)
#define LD3(K, V0, V1, rs, so) do { K = BLOAD(rs, a.vk, so); V0 = BLOAD(rs, a.vv0, so); V1 = BLOAD(rs, a.vv1, so); } while (0)
#define EX(X, r) X[r] = __builtin_amdgcn_exp2f(X[r])
#define STEP(X, Y, OP, VP, KN, QN, CN, O, V0, F1, F2) do { \
        EX(X, 0); EX(X, 1); SBAR(); OP = pv_hi(VP, Y, OP, IDX); SBAR(); F1; SBAR(); EX(X, 2); EX(X, 3); EX(X, 4); SBAR(); Y = MFMA(KN, QN, CN); SBAR(); \
        EX(X, 5); EX(X, 6); EX(X, 7); SBAR(); EX(X, 8); EX(X, 9); SBAR(); O = pv_lo(V0, X, O, IDX); SBAR(); F2; SBAR(); \
        EX(X, 10); EX(X, 11); EX(X, 12); EX(X, 13); EX(X, 14); EX(X, 15); SBAR(); } while (0)

__device__ __forceinline__ f32x16 lds_zero16(lds_cptr zp) {
    const volatile LAS f32x4* z = (const volatile LAS f32x4*)zp;
    const f32x4 a = *z, b = *z, c = *z, d = *z;
    f32x8 lo = __builtin_shufflevector(a, b, 0, 1, 2, 3, 4, 5, 6, 7), hi = __builtin_shufflevector(c, d, 0, 1, 2, 3, 4, 5, 6, 7);
    return __builtin_shufflevector(lo, hi, 0, 1, 2, 3, 4, 5, 6, 7, 8, 9, 10, 11, 12, 13, 14, 15);
}

template <int C, int BR>
__device__ __forceinline__ GAS u64* finish(const Att& a, f32x16 oo, float lrest, bf16x8 qa) {
    const float gt = *(const LAS float*)(a.lds + L_G + a.t * 128 + (a.lane & 31) * 4);
    auto lsw = __builtin_amdgcn_permlane32_swap(__float_as_uint(oo[8]), __float_as_uint(oo[8]), false, false);
    float l = __uint_as_float(lsw[0]) + lrest, z = 0.f;
#pragma unroll
    for (int r = 0; r < 8; ++r) z = __builtin_fmaf(oo[r], 0.f, z);
    if (__builtin_expect(__any(!(l + z < LMAX && l > LMIN)), 0)) {
        if (!BR) { wait_tags((const GAS u64*)(a.ws + kvflag(0, a.p >> 1, 0, 0) + a.lane * FSTR), a.tag); wait_tags((const GAS u64*)(a.ws + kvflag(0, a.p >> 1, 2, 0) + a.lane * FSTR), a.tag); }
        slow_rows(a.rkv, (unsigned)((BR * 32 + a.head) * 64) * TS, BR ? (a.g >= 8 ? a.g - 8 : 0) : 0, BR ? a.g : 63, a.g, BR, qa, a.vk, a.vv0, a.vv1, a.lds, a.lane, IDX, oo);
        auto l2 = __builtin_amdgcn_permlane32_swap(__float_as_uint(oo[8]), __float_as_uint(oo[8]), false, false);
        l = __uint_as_float(l2[0]);
    }
    const float il = __builtin_amdgcn_rcpf(l) * (BR ? 1.f - gt : gt);
    u32x4 v = (u32x4){cvtpk(oo[0] * il, oo[1] * il), cvtpk(oo[2] * il, oo[3] * il), cvtpk(oo[4] * il, oo[5] * il), cvtpk(oo[6] * il, oo[7] * il)};
    if (!C && !BR) v = __builtin_elementwise_min(v, (u32x4){~1u, ~1u, ~1u, ~1u});
    BSTORE(v, a.ro, a.vk, (unsigned)((BR * 64 + a.g) * 32 + a.head) * 1024);
    return (GAS u64*)(a.ws + WS_OFLAG) + ((BR * 64 + a.g) * 2 + C) * 16 + a.p;
}

template <int C>
__device__ __forceinline__ GAS u64* attn_full(const Att& a, f32x16 oo, f32x16 o1, f32x16 o2, f32x16 o3, f32x16 S1, __amdgpu_buffer_rsrc_t rwo, unsigned vwo, unsigned wi PROF_ARG) {
    const lds_ptr lds = a.lds; const int t = a.t, lane = a.lane, w = 4 * C + t;
    const f32x16 zc = f32x16{};
    bf16x8 ka, va0, va1, kb, vb0, vb1, kc, vc0, vc1, kd, vd0, vd1;
    const unsigned sBeg = a.sBeg, sEnd = a.sEnd;
    unsigned so = a.so;
#define NEXT_A() asm volatile("s_addk_i32 %0, %1" : "+s"(so) : "i"(4 * TS) : "scc")
#define WRAP_A() asm volatile("s_cmp_eq_u32 %0, %1\n\ts_cselect_b32 %0, %2, %0" : "+s"(so) : "s"(sEnd), "s"(sBeg) : "scc")
#define STEP_A() asm volatile("s_addk_i32 %0, %3\n\ts_cmp_eq_u32 %0, %1\n\ts_cselect_b32 %0, %2, %0" : "+s"(so) : "s"(sEnd), "s"(sBeg), "i"(4 * TS) : "scc")
    LD3(ka, va0, va1, a.rkv, so); STEP_A(); SBAR(); LD3(kb, vb0, vb1, a.rkv, so); STEP_A(); SBAR();
    LD3(kc, vc0, vc1, a.rkv, so); STEP_A(); SBAR();
    vd1 = lds_frag(a.zp);
    if (C) { BAR(); STAMP(4); }
    bf16x8 qa = lds_frag(a.qp[0]); SBAR();
    bf16x8 q1 = lds_frag(a.qp[1]), q2 = lds_frag(a.qp[2]), q3 = lds_frag(a.qp[3]);
    SBAR();
    f32x16 S0 = MFMA(ka, qa, zc);
    PIN(S0); SBAR();
    PIN(q1); PIN(q2); PIN(q3); PIN(vd1); PIN(oo); PIN(o1); PIN(o2); PIN(o3); PIN(S1);
    __builtin_amdgcn_s_waitcnt(0x0F76);
#pragma unroll
    for (int i = 0; i < 8; ++i) __builtin_amdgcn_s_setprio(C);
    STAMP(3);
    SBAR();
    unsigned wso = 0;
#define NEXT_AW() do { if (C) asm volatile("s_addk_i32 %0, %3\n\ts_lshl_b32 %1, %2, 4" : "+s"(so), "=s"(wso) : "s"(wi), "i"(4 * TS) : "scc"); else NEXT_A(); } while (0)
#define WO_DMA() do { if (C) __builtin_amdgcn_raw_ptr_buffer_load_lds(rwo, (LAS void*)(lds + L_WO + t * 16384 + wi), 16, vwo, wso, 0, 0); } while (0)
#define WO_NEXT() do { if (C) asm volatile("s_addk_i32 %0, 0x400\n\ts_and_b32 %0, %0, 0x3fff" : "+s"(wi) :: "scc"); } while (0)
#define TILE(K, V0, V1, KN, LK, LV0, LV1, NW) do { \
        STEP(S0, S1, o3, LV1, K, q1, zc, oo, V0, LK = BLOADL(K, a.rkv, a.vk, so), LV0 = BLOADL(V0, a.rkv, a.vv0, so)); \
        STEP(S1, S0, oo, V1, K, q2, zc, o1, V0, LV1 = BLOADL(V1, a.rkv, a.vv1, so), NEXT_AW()); \
        STEP(S0, S1, o1, V1, K, q3, zc, o2, V0, WRAP_A(), WO_DMA()); \
        STEP(S1, S0, o2, V1, KN, qa, zc, o3, V0, __builtin_amdgcn_s_waitcnt(0x0F70 | (C ? NW : 6)), WO_NEXT()); } while (0)
#pragma nounroll
    for (int it = 0; it < 4; ++it) {
        TILE(ka, va0, va1, kb, kd, vd0, vd1, 7);
        TILE(kb, vb0, vb1, kc, ka, va0, va1, 8);
        TILE(kc, vc0, vc1, kd, kb, vb0, vb1, 9);
        TILE(kd, vd0, vd1, ka, kc, vc0, vc1, 9);
    }
    o3 = pv_hi(vd1, S1, o3, IDX);
    STAMP(6);
    if (C) cnt_wait(lds + L_CNT + 8); else __builtin_amdgcn_s_setprio(2);
#define PUT(O, sl) do { lds_ptr d_ = lds + L_PART + (w * 3 + sl) * 2048 + lane * 16; \
        *(LAS f32x4*)d_ = __builtin_shufflevector(O, O, 0, 1, 2, 3); *(LAS f32x4*)(d_ + 1024) = __builtin_shufflevector(O, O, 4, 5, 6, 7); } while (0)
    PUT(o1, 0); PUT(o2, 1); PUT(o3, 2);
    if (lane < 32) { LAS float* lp = (LAS float*)(lds + L_PL + w * 384) + lane; lp[0] = o1[8]; lp[32] = o2[8]; lp[64] = o3[8]; }
    if (C) { DRAIN(); cnt_arrive(lds + L_CNT, lane); cnt_wait(lds + L_CNT); } else CBAR();
    float lrest = 0.f;
#pragma unroll
    for (int s = 0; s < 3; ++s) {
        const int src = (C * 4 + ((t - 1 - s) & 3)) * 3 + s;
        const lds_cptr d = lds + L_PART + src * 2048 + lane * 16;
        const f32x4 x = *(const LAS f32x4*)d, y = *(const LAS f32x4*)(d + 1024);
#pragma unroll
        for (int r = 0; r < 4; ++r) { oo[r] += x[r]; oo[4 + r] += y[r]; }
        const float lp = *(const LAS float*)(lds + L_PL + src * 128 + (lane & 31) * 4);
        lrest = s ? lrest + lp : lp;
    }
    return finish<C, 0>(a, oo, lrest, qa);
}

template <int C>
__device__ __forceinline__ GAS u64* attn_window(const Att& a, GAS u64* pub PROF_ARG) {
    const lds_ptr lds = a.lds; const int t = a.t, lane = a.lane, g = a.g;
    const f32x16 zc = f32x16{};
    { const int i = lane & 15, kt = g - (i < 9 ? i : 0);
      wait_tags((const GAS u64*)(a.ws + kvflag(1, a.p >> 1, kt >= 0 ? kt : 0, (lane >> 4) & 1)), a.tag, pub, lane); }
    STAMP(25);
    bf16x8 k[9], v0[9], v1[9];
    const unsigned sB = (unsigned)((32 + a.head) * 64) * TS;
#define LDB(i) do { const int kt_ = g - (i); \
        const __amdgpu_buffer_rsrc_t rb_ = __builtin_amdgcn_make_buffer_rsrc(a.ws + WS_KV, 0, kt_ >= 0 ? KV_BYTES : 0u, 0x00020000); \
        LD3(k[i], v0[i], v1[i], rb_, sB + (unsigned)(kt_ >= 0 ? kt_ : 0) * TS); } while (0)
    LDB(0); SBAR(); LDB(1); SBAR(); LDB(2); SBAR();
    const bf16x8 qa = lds_frag(lds + L_Q + 8192 + C * 4096 + t * 1024 + lane * 16);
    const f32x16 cd = lds_f32x16(lds + L_MDIAG + lane * 16), cf = lds_f32x16(lds + L_MFAR + lane * 16);
    f32x16 oo = lds_zero16(a.zp);
    f32x16 S0 = MFMA(k[0], qa, cd), S1 = MFMA(k[1], qa, zc);
    SBAR(); LDB(3); SBAR();
#pragma unroll
    for (int r = 0; r < 10; ++r) { EX(S0, r); SBAR(); }
    oo = pv_lo(v0[0], S0, oo, IDX); SBAR();
#pragma unroll
    for (int r = 10; r < 16; ++r) EX(S0, r);
    SBAR();
    STEP(S1, S0, oo, v1[0], k[2], qa, zc, oo, v0[1], LDB(4), );
    STEP(S0, S1, oo, v1[1], k[3], qa, zc, oo, v0[2], LDB(5), );
    STEP(S1, S0, oo, v1[2], k[4], qa, zc, oo, v0[3], LDB(6), );
    STEP(S0, S1, oo, v1[3], k[5], qa, zc, oo, v0[4], LDB(7), );
    STEP(S1, S0, oo, v1[4], k[6], qa, zc, oo, v0[5], LDB(8), );
    STEP(S0, S1, oo, v1[5], k[7], qa, zc, oo, v0[6], , );
    STEP(S1, S0, oo, v1[6], k[8], qa, cf, oo, v0[7], , );
    STEP(S0, S1, oo, v1[7], k[8], qa, cf, oo, v0[8], , );
    oo = pv_hi(v1[8], S0, oo, IDX);
    STAMP(7);
    return finish<C, 1>(a, oo, 0.f, qa);
}

__global__ void __launch_bounds__(512, 2)
gmix_kernel(const char* x, const char* wA, const char* woA, const char* wB, const char* woB, const char* gw, char* out, char* ws) {
    extern __shared__ __attribute__((aligned(16))) char smem[];
    const lds_ptr lds = (lds_ptr)smem;
    const unsigned lds0 = (unsigned)(uintptr_t)lds;
    const int tid = threadIdx.x, lane = tid & 63, r32 = lane & 31, hh = lane >> 5;
    const int w = __builtin_amdgcn_readfirstlane(tid >> 6);
    const int bid = blockIdx.x, xcd = bid & 7, rr = bid >> 3;
    const int p = 4 * (xcd & 3) + (rr & 3), j = 8 * (xcd >> 2) + (rr >> 2);
    const int jo = 2 * xcd + (rr >> 4), po = rr & 15;
    const int t = w & 3, c = w >> 2;
    const int e = p & 1;
    STAMP(0);
    {
        u64 pc; const unsigned ln = ((bid & ~7) | (tid >> 6)) * 128;
        asm volatile("s_getpc_b64 %0\n\ts_mov_b32 m0, %2\n\ts_nop 0\n\tglobal_load_lds_dword %1, %0" : "=&s"(pc) : "v"(ln), "s"(lds0 + L_PL) : "memory");
        SBAR();
    }
    unsigned rot = (j + 2 * (p & 3)) & 7;
    unsigned vx0 = (128 * j + 32 * t + (lane >> 3)) * 1024 + (((lane & 7) ^ (lane >> 4)) * 16), vx1 = (vx0 ^ 64) + 7168, vx2 = vx0 + 14336, vx3 = vx1 + 14336;
    unsigned vw0 = (16 * t + (lane >> 3)) * 3072 + 1024 + 1024 * e + 128 * (p >> 1) + (((lane & 7) ^ (((lane >> 4) & 1) << 2)) * 16), vw1 = vw0 + 8 * 3072 - 1024;
    unsigned mx = lds0 + t * 4096, mw = lds0 + L_W + t * 2048, ow = rot * (64 * 3072);
    asm volatile("" : "+v"(vx0), "+v"(vx1), "+v"(vx2), "+v"(vx3), "+v"(vw0), "+v"(vw1));
    asm volatile("" : "+s"(mx), "+s"(mw), "+s"(rot), "+s"(ow));
    SBAR();
    asm volatile("" : "+s"(x), "+s"(wA), "+s"(woA), "+s"(wB), "+s"(woB), "+s"(gw), "+s"(out), "+s"(ws));
#define MK_ATT(C) Att a; a.lds = lds; a.ws = ws; a.lane = lane; a.t = t; a.p = p; a.g = 4 * j + t; a.head = 2 * p + C; a.idx = 0xdddddddd; \
    a.rkv = __builtin_amdgcn_make_buffer_rsrc(ws + WS_KV, 0, KV_BYTES, 0x00020000); a.ro = __builtin_amdgcn_make_buffer_rsrc(ws + WS_O, 0, O_BYTES, 0x00020000); \
    a.vk = lane * 16; a.vv0 = r32 < 16 ? 1024 + hh * 256 + r32 * 16 : (r32 == 16 ? 2048 : OOB); a.vv1 = r32 < 16 ? a.vv0 + 512 : a.vv0; a.zp = lds + L_Z + lane * 16; \
    a.sBeg = (unsigned)(a.head * 64 + t) * TS; a.sEnd = a.sBeg + 64 * TS; a.so = (unsigned)(a.head * 64 + a.g) * TS; \
    _Pragma("unroll") for (int i = 0; i < 4; ++i) a.qp[i] = lds + L_Q + C * 4096 + ((t + i) & 3) * 1024 + lane * 16
    if (c) {
#define DMA1(s) do { const unsigned ks_ = ((s) + rot) & 7; \
        glds16x4(vx0, vx1, vx2, vx3, x + ks_ * 128, mx + ((s) & 3) * 16384); glds16x2(vw0, vw1, wA + ks_ * (64 * 3072), mw + ((s) & 3) * 8192); } while (0)
#define DMA2(s) do { const unsigned ks_ = (((s) + rot) & 7) * (64 * 3072); \
        glds16x2(vq0, vq1, wq + ks_, mq + ((s) == 14 ? L_Q14 : ((s) & 3) * 16384)); glds16x2(vw0, vw1, wB + ks_, mw + ((s) & 3) * 8192); SBAR(); } while (0)
#define LBAR(N) asm volatile("s_waitcnt vmcnt(" #N ")\n\ts_barrier" ::: "memory")
        STAMP(20);
        glds16x4(vx0, vx1, vx2, vx3, x + rot * 128, mx); glds16x2(vw0, vw1, wA + ow, mw);
        SBAR();
        const unsigned k1 = (1 + rot) & 7;
        glds16x2(vx0, vx1, x + k1 * 128, mx + 16384);
        SBAR();
        const u64 ep = *(const __attribute__((address_space(4))) u64*)(ws + WS_EPOCH);
        unsigned vq0 = (32 * (t & 1) + (lane >> 2)) * 3072 + 64 * p + (lane & 3) * 16, vq1 = vq0 + 16 * 3072 - 1024;
        unsigned vwo = ((t & 1) * 256 + (lane >> 2)) * 1024 + 64 * po + (lane & 3) * 16;
        MK_ATT(1);
        const GAS u64* fp = (const GAS u64*)(ws + kvflag(0, p >> 1, t, 0) + r32 * FSTR);
        PIN(vq0); PIN(vq1); PIN(vwo); PIN(a.vk); PIN(a.vv0); PIN(a.vv1); PIN(a.qp[0]); PIN(a.qp[1]); PIN(a.qp[2]); PIN(a.qp[3]); PIN(a.zp); PIN(fp); PIN(a.idx);
        SBAR();
        STAMP(30);
        glds16x2(vx0, vx1, x + k1 * 128 + 16384, mx + 16384 + 2048); glds16x2(vw0, vw1, wA + k1 * (64 * 3072), mw + 8192);
        STAMP(22);
        SBAR();
#pragma nounroll
        for (int s = 2; s < 8; ++s) { LBAR(6); DMA1(s);
        }
        const unsigned mq = lds0 + t * 2048;
        const char* wq = (t >> 1) ? wB : wA;
        LBAR(6); STAMP(14); DMA2(8);
        LBAR(4); STAMP(15); DMA2(9);
        a.tag = ep * 0x9E3779B97F4A7C15ull + 0x632BE59BD9B4E019ull;
        *(LAS u64*)(lds + L_CNT + 16) = a.tag;
        asm volatile("s_waitcnt lgkmcnt(0)" ::: "memory");
        LBAR(4); STAMP(16); DMA2(10);
        const unsigned k7 = ((7 + rot) & 7) * (64 * 3072), sx = mq + L_Q15, sw = mw + 32768;
        LBAR(4); STAMP(17); glds16(vq0, wq + k7, sx); DMA2(11);
        f32x16 oo = lds_zero16(a.zp), o1 = lds_zero16(a.zp); PIN(oo); PIN(o1);
        LBAR(5); STAMP(18); glds16(vw0, wB + k7, sw); DMA2(12);
        f32x16 o2 = lds_zero16(a.zp), o3 = lds_zero16(a.zp); PIN(o2); PIN(o3);
        LBAR(5); STAMP(19); glds16(vq1, wq + k7 + 1024, sx + 1024); DMA2(13);
        f32x16 S1 = lds_zero16(a.zp); PIN(S1);
        LBAR(5); STAMP(21); glds16(vw1, wB + k7 + 1024, sw + 1024); DMA2(14);
        __amdgpu_buffer_rsrc_t rwo = __builtin_amdgcn_make_buffer_rsrc((void*)((t >> 1) ? woB : woA), 0, AB_WO, 0x00020000);
        unsigned wi0 = 4096 * (((rr >> 4) << 1) | (po & 1));
        asm volatile("" : "+s"(a.rkv), "+s"(rwo), "+s"(a.so), "+s"(a.sBeg), "+s"(a.sEnd), "+s"(wi0));
        __builtin_amdgcn_s_setprio(1);
        LBAR(5); STAMP(24);
        const u64 fv = __hip_atomic_load(fp, RLX_AGENT);
        LBAR(1); STAMP(29);
        wait_tags_end(fv, fp, a.tag, nullptr, lane);
        STAMP(5);
        GAS u64* pub = attn_full<1>(a, oo, o1, o2, o3, S1, rwo, vwo, wi0 PROF_PASS);
        STAMP(23);
        pub = attn_window<1>(a, pub PROF_PASS);
        STAMP(8);
        const int go = 4 * jo + t;
        const lds_cptr wo = lds + L_WO + (4 * hh + ((lane & 15) >> 2)) * 64 + ((lane >> 4) & 1) * 32 + (lane & 3) * 8;
        const GAS u64* of_ = (const GAS u64*)(ws + WS_OFLAG);
        wait_tags(of_ + ((lane & 16) ? go * 32 + 16 : (64 + go) * 32) + (lane & 15), a.tag, pub, lane);
        STAMP(9);
#define OBR(n) ((n) >> 4 == 1 ? 0 : 1)
#define OHD(n) (2 * ((n) & 15) + ((n) >= 16))
#define OLD(n) BLOAD(a.ro, a.vk, (unsigned)((OBR(n) * 64 + go) * 32 + OHD(n)) * 1024)
#define OWF(n) tr_frag(wo + OBR(n) * 32768 + OHD(n) * 1024, wo + OBR(n) * 32768 + OHD(n) * 1024 + 512)
#define OIN 7
        bf16x8 oe[48];
#pragma unroll
        for (int n = 0; n < 48; ++n) {
            if (n == 32) wait_tags(of_ + (64 + go) * 32 + 16 + (lane & 15), a.tag);
            oe[n] = OLD(n); SBAR();
            if (n >= OIN - 1) __builtin_amdgcn_s_waitcnt(0x0F70 | (OIN - 1));
            SBAR();
        }
        bf16x8 wn[4];
#pragma unroll
        for (int n = 0; n < 4; ++n) { wn[n] = OWF(n); PIN(wn[n]); }
        __builtin_amdgcn_s_waitcnt(0x0F70);
#define NPRE 0
#define NMID 24
        f32x16 ya = f32x16{};
        bf16x8 wf[16], of[16];
#define OMM(n) do { ya = MFMA(wn[(n) & 3], oe[n], ya); SBAR(); if ((n) + 4 < 48) wn[(n) & 3] = OWF((n) + 4); \
            if ((n) >= 32) wf[(n) - 32] = tr_frag(wo + 2 * ((n) - 32) * 1024, wo + 2 * ((n) - 32) * 1024 + 512); SBAR(); } while (0)
#pragma unroll
        for (int n = 0; n < NPRE; ++n) { OMM(n); PACE(); }
        STAMP(10);
        BAR();
        BAR();
#pragma unroll
        for (int n = NPRE; n < NMID; ++n) OMM(n);
        BAR();
        STAMP(11);
#define GRP_BAD(q) ({ unsigned mx_ = 0; _Pragma("unroll") for (int i = 4 * (q); i < 4 * (q) + 4; ++i) { const u32x4 u_ = __builtin_bit_cast(u32x4, of[i]); \
            mx_ = __builtin_elementwise_max(__builtin_elementwise_max(mx_, u_[0]), u_[1]); mx_ = __builtin_elementwise_max(__builtin_elementwise_max(mx_, u_[2]), u_[3]); } \
            __any(mx_ == ~0u); })
#define GRP_MM(q) do { _Pragma("unroll") for (int i = 4 * (q); i < 4 * (q) + 4; ++i) ya = MFMA(wf[i], of[i], ya); } while (0)
#define GRP_LD(q) do { _Pragma("unroll") for (int i = 4 * (q); i < 4 * (q) + 4; ++i) of[i] = BLOAD(a.ro, a.vk, (unsigned)(go * 32 + 2 * i) * 1024); } while (0)
#pragma unroll
        for (int n = NMID; n < 48; ++n) { if (n - NMID < 16) { of[n - NMID] = BLOAD(a.ro, a.vk, (unsigned)(go * 32 + 2 * (n - NMID)) * 1024); SBAR(); } OMM(n); }
        STAMP(31);
        int done = 0;
        if (!GRP_BAD(0)) { GRP_MM(0); done = 1; if (!GRP_BAD(1)) { GRP_MM(1); done = 2; if (!GRP_BAD(2)) { GRP_MM(2); done = 3; if (!GRP_BAD(3)) { GRP_MM(3); done = 4; } } } }
        if (done < 4) {
#pragma nounroll
            for (unsigned spins = 0; spins < SPIN_CAP; ++spins) {
                const bool b0 = GRP_BAD(0), b1 = GRP_BAD(1), b2 = GRP_BAD(2), b3 = GRP_BAD(3);
                if (!(b0 || b1 || b2 || b3)) break;
                asm volatile("" ::: "memory");
                if (b0) GRP_LD(0); if (b1) GRP_LD(1); if (b2) GRP_LD(2); if (b3) GRP_LD(3);
            }
            if (done < 1) GRP_MM(0); if (done < 2) GRP_MM(1); if (done < 3) GRP_MM(2); GRP_MM(3);
        }
#pragma unroll
        for (int s = 0; s < 2; ++s) {
            auto u = __builtin_amdgcn_permlane32_swap(cvtpk(ya[8 * s], ya[8 * s + 1]), cvtpk(ya[8 * s + 4], ya[8 * s + 5]), false, false);
            auto v = __builtin_amdgcn_permlane32_swap(cvtpk(ya[8 * s + 2], ya[8 * s + 3]), cvtpk(ya[8 * s + 6], ya[8 * s + 7]), false, false);
#define OUT_AUX 0
            __builtin_amdgcn_raw_buffer_store_b128(((u32x4){u[0], v[0], u[1], v[1]}), __builtin_amdgcn_make_buffer_rsrc(out, 0, 2048 * 1024, 0x00020000),
                                                   (32 * go + r32) * 1024 + 64 * po + 32 * s + 16 * hh, 0, OUT_AUX);
        }
        if (bid == 0 && tid == 256) __hip_atomic_store((GAS u64*)(ws + WS_EPOCH), a.tag, RLX_AGENT);
        STAMP(12);
    } else {
        glds16((((lane >> 3) + rot) & 7) * 128 + (lane & 7) * 16, gw, lds0 + L_GW + t * 1024);
#pragma unroll
        for (int m = 0; m < 2; ++m) {
            f32x4 mk;
#pragma unroll
            for (int i = 0; i < 4; ++i) { const bool le = (i + 8 * t + 4 * hh) <= r32; mk[i] = (le != (m != 0)) ? 0.f : -__builtin_inff(); }
            *(LAS f32x4*)(lds + L_MDIAG + m * 4096 + t * 1024 + lane * 16) = mk;
        }
        if (tid < 4) *(LAS unsigned*)(lds + L_CNT + tid * 4) = 0;
        *(LAS unsigned*)(lds + L_Z + tid * 4) = 0;
        u64 tag;
        STAMP(21);
        {
            const __amdgpu_buffer_rsrc_t rkv = __builtin_amdgcn_make_buffer_rsrc(ws + WS_KV, 0, KV_BYTES, 0x00020000);
            const __amdgpu_buffer_rsrc_t ro = __builtin_amdgcn_make_buffer_rsrc(ws + WS_O, 0, O_BYTES, 0x00020000);
            if (e) project<1>(lds, ws, rkv, ro, lane, t, p, j, tag PROF_PASS); else project<0>(lds, ws, rkv, ro, lane, t, p, j, tag PROF_PASS);
        }
        cnt_wait(lds + L_CNT + 8);
        __builtin_amdgcn_s_sleep(BSLP);
        STAMP(24);
        MK_ATT(0);
        a.tag = tag;
        GAS u64* pub = attn_window<0>(a, nullptr PROF_PASS);
        STAMP(23);
        wait_tags((const GAS u64*)(ws + kvflag(0, p >> 1, t, 0) + r32 * FSTR), tag, pub, lane);
        STAMP(5);
        attn_full<0>(a, lds_zero16(a.zp), lds_zero16(a.zp), lds_zero16(a.zp), lds_zero16(a.zp), lds_zero16(a.zp), a.rkv, 0, 0 PROF_PASS);
        BAR();
        STAMP(8);
        DRAIN(); BAR();
    }
}

extern "C" void kernel_launch(void* const* d_in, const int* in_sizes, int n_in, void* d_out, int out_size, void* d_ws, size_t ws_size,
                              hipStream_t stream) {
    (void)in_sizes; (void)n_in; (void)out_size; (void)ws_size;
    (void)hipFuncSetAttribute((const void*)gmix_kernel, hipFuncAttributeMaxDynamicSharedMemorySize, L_END);
    gmix_kernel<<<256, 512, L_END, stream>>>((const char*)d_in[0], (const char*)d_in[1], (const char*)d_in[2], (const char*)d_in[4],
                                             (const char*)d_in[5], (const char*)d_in[6], (char*)d_out, (char*)d_ws);
}
```

```cpp
#define REVEAL 99
#define RROLE 1
#define RCYC 0
#include <hip/hip_runtime.h>
#include <hip/hip_bf16.h>

typedef __attribute__((ext_vector_type(8))) __bf16 bf16x8;
typedef __attribute__((ext_vector_type(16))) __bf16 bf16x16;
typedef __attribute__((ext_vector_type(16))) float f32x16;
typedef __attribute__((ext_vector_type(8))) float f32x8;
typedef __attribute__((ext_vector_type(4))) float f32x4;
typedef __attribute__((ext_vector_type(4))) unsigned u32x4;
typedef __attribute__((ext_vector_type(2))) unsigned u32x2;
typedef __attribute__((ext_vector_type(2))) __bf16 bf16x2;
typedef short s16x4 __attribute__((ext_vector_type(4)));
typedef unsigned long long u64;
#define GAS __attribute__((address_space(1)))
#define LAS __attribute__((address_space(3)))
typedef LAS const char* lds_cptr;
typedef LAS char* lds_ptr;

constexpr unsigned WS_EPOCH = 0;
#define FSTR 8
constexpr unsigned WS_OFLAG = 65536;
constexpr unsigned WS_KVFLAG = 131072;
constexpr unsigned WS_KV = 1u << 20;
constexpr unsigned TS = 2176;
constexpr unsigned KV_BYTES = 2u * 32u * 64u * TS;
constexpr unsigned WS_O = 10u << 20;
constexpr unsigned O_BYTES = 4u << 20;
constexpr unsigned WS_STAMP = 16u << 20;
constexpr unsigned OOB = 0x7ff00000u;
constexpr unsigned L_W = 65536;
constexpr unsigned L_WO = 0, L_PART = 65536, L_PL = 114688, L_CNT = 117760, L_MDIAG = 118784, L_MFAR = 122880, L_Q = 126976, L_GW = 143360, L_Z = 147456, L_G = 148480, L_Q14 = 106496, L_Q15 = 148992, L_END = 157184;
constexpr float QSCALE = 0.25f * 1.4426950408889634f;
constexpr float LMAX = 1.2676506e30f, LMIN = 9.5367432e-7f;
constexpr unsigned SPIN_CAP = 1u << 17;

#define SBAR() __builtin_amdgcn_sched_barrier(0)
#define PIN(x) asm volatile("" : "+v"(x))
#define MFMA(a, b, c) __builtin_amdgcn_mfma_f32_32x32x16_bf16(a, b, c, 0, 0, 0)
#define BAR() asm volatile("s_barrier" ::: "memory")
#define CBAR() asm volatile("s_waitcnt lgkmcnt(0)\n\ts_barrier" ::: "memory")
#define DRAIN() asm volatile("s_waitcnt vmcnt(0)" ::: "memory")
#define PACEN 3
#define PACE() asm volatile("s_nop %0" :: "i"(PACEN) : "memory")
#define RLX_AGENT __ATOMIC_RELAXED, __HIP_MEMORY_SCOPE_AGENT
#define STAMP(i) do { if ((i) == 0 || (i) == REVEAL) { u64 a_, b_; SBAR(); asm volatile("s_memrealtime %0\n\ts_memtime %1\n\ts_waitcnt lgkmcnt(0)" : "=s"(a_), "=s"(b_) :: "memory"); \
    if ((i) == 0) { stv.r0 = a_; stv.c0 = b_; stv.d = 0; } else stv.d = RCYC ? (unsigned)(b_ - stv.c0) / 20u : (unsigned)(a_ - stv.r0); SBAR(); } } while (0)
struct Rev { u64 r0, c0; unsigned d; };

__device__ __forceinline__ void glds16(unsigned voff, const void* sbase, unsigned ldsaddr) {
    asm volatile("s_mov_b32 m0, %2\n\ts_nop 0\n\tglobal_load_lds_dwordx4 %0, %1" ::"v"(voff), "s"(sbase), "s"(ldsaddr) : "memory");
}
__device__ __forceinline__ void glds16x2(unsigned v0, unsigned v1, const void* sbase, unsigned ldsaddr) {
    asm volatile("s_mov_b32 m0, %3\n\ts_nop 0\n\tglobal_load_lds_dwordx4 %0, %2\n\tglobal_load_lds_dwordx4 %1, %2 offset:1024"
                 ::"v"(v0), "v"(v1), "s"(sbase), "s"(ldsaddr) : "memory");
}
__device__ __forceinline__ void glds16x4(unsigned v0, unsigned v1, unsigned v2, unsigned v3, const void* sbase, unsigned ldsaddr) {
    asm volatile("s_mov_b32 m0, %5\n\ts_nop 0\n\tglobal_load_lds_dwordx4 %0, %4\n\tglobal_load_lds_dwordx4 %1, %4 offset:1024\n\t"
                 "global_load_lds_dwordx4 %2, %4 offset:2048\n\tglobal_load_lds_dwordx4 %3, %4 offset:3072"
                 ::"v"(v0), "v"(v1), "v"(v2), "v"(v3), "s"(sbase), "s"(ldsaddr) : "memory");
}
__device__ __forceinline__ unsigned cvtpk(float lo, float hi) {
    typedef __attribute__((ext_vector_type(2))) float f32x2;
    return __builtin_bit_cast(unsigned, __builtin_convertvector((f32x2){lo, hi}, bf16x2));
}
__device__ __forceinline__ bf16x8 as_frag(u32x4 v) { return __builtin_bit_cast(bf16x8, v); }
__device__ __forceinline__ bf16x8 lds_frag(lds_cptr p) { return *(const LAS bf16x8*)p; }
__device__ __forceinline__ bf16x8 tr_frag(lds_cptr p0, lds_cptr p1) {
    s16x4 a = __builtin_amdgcn_ds_read_tr16_b64_v4i16((LAS s16x4*)p0);
    s16x4 b = __builtin_amdgcn_ds_read_tr16_b64_v4i16((LAS s16x4*)p1);
    return __builtin_bit_cast(bf16x8, __builtin_shufflevector(a, b, 0, 1, 2, 3, 4, 5, 6, 7));
}
__device__ __forceinline__ f32x16 pv_lo(bf16x8 v, const f32x16& p, f32x16 acc, int idx) {
    f32x8 h = __builtin_shufflevector(p, p, 0, 1, 2, 3, 4, 5, 6, 7);
    return __builtin_amdgcn_smfmac_f32_32x32x32_bf16(v, __builtin_bit_cast(bf16x16, h), acc, idx, 0, 0);
}
__device__ __forceinline__ f32x16 pv_hi(bf16x8 v, const f32x16& p, f32x16 acc, int idx) {
    f32x8 h = __builtin_shufflevector(p, p, 8, 9, 10, 11, 12, 13, 14, 15);
    return __builtin_amdgcn_smfmac_f32_32x32x32_bf16(v, __builtin_bit_cast(bf16x16, h), acc, idx, 0, 0);
}
#define MX3(a, b, c) __builtin_fmaxf(__builtin_fmaxf((a), (b)), (c))
__device__ __forceinline__ float max16(const f32x16& s) {
    float a = MX3(s[0], s[1], s[2]), b = MX3(s[3], s[4], s[5]);
    a = MX3(a, s[6], s[7]); b = MX3(b, s[8], s[9]); a = MX3(a, s[10], s[11]); b = MX3(b, s[12], s[13]);
    return MX3(a, b, __builtin_fmaxf(s[14], s[15]));
}
__device__ __forceinline__ float halfmax(float m) {
    auto r = __builtin_amdgcn_permlane32_swap(__float_as_uint(m), __float_as_uint(m), false, false);
    return __builtin_fmaxf(__uint_as_float(r[0]), __uint_as_float(r[1]));
}
__device__ __forceinline__ f32x16 lds_f32x16(lds_cptr p) {
    f32x4 a = *(const LAS f32x4*)p, b = *(const LAS f32x4*)(p + 1024), c = *(const LAS f32x4*)(p + 2048), d = *(const LAS f32x4*)(p + 3072);
    f32x8 lo = __builtin_shufflevector(a, b, 0, 1, 2, 3, 4, 5, 6, 7), hi = __builtin_shufflevector(c, d, 0, 1, 2, 3, 4, 5, 6, 7);
    return __builtin_shufflevector(lo, hi, 0, 1, 2, 3, 4, 5, 6, 7, 8, 9, 10, 11, 12, 13, 14, 15);
}
#define BLOAD(rs, vo, so) as_frag(__builtin_amdgcn_raw_buffer_load_b128(rs, vo, so, 16))
#define BSTORE(v, rs, vo, so) __builtin_amdgcn_raw_buffer_store_b128(v, rs, (vo) + (so), 0, 16)

#define POLLSLP 8
#define BSLP 36
__device__ __forceinline__ void wait_tags_end(u64 v, const GAS u64* p, u64 tag, GAS u64* pub, int lane) {
    if (pub) { DRAIN(); if (lane == 0) __hip_atomic_store(pub, tag, RLX_AGENT); }
    if (__builtin_expect(!__all(v == tag), 0)) {
#pragma nounroll
        for (unsigned spins = 0; spins < SPIN_CAP; ++spins) {
            __builtin_amdgcn_s_sleep(POLLSLP);
            if (__all(__hip_atomic_load(p, RLX_AGENT) == tag)) break;
        }
    }
    asm volatile("" ::: "memory");
}

__device__ __forceinline__ void wait_tags(const GAS u64* p, u64 tag, GAS u64* pub = nullptr, int lane = 0) {
    wait_tags_end(__hip_atomic_load(p, RLX_AGENT), p, tag, pub, lane);
}

__device__ __forceinline__ void cnt_arrive(lds_ptr cnt, int lane) {
    asm volatile("s_waitcnt lgkmcnt(0)" ::: "memory");
    if (lane == 0) __hip_atomic_fetch_add((LAS unsigned*)cnt, 1u, __ATOMIC_RELAXED, __HIP_MEMORY_SCOPE_WORKGROUP);
}
__device__ __forceinline__ void cnt_wait(lds_ptr cnt) {
#pragma nounroll
    for (unsigned spins = 0; spins < (1u << 22); ++spins) {
        if (__builtin_amdgcn_readfirstlane(__hip_atomic_load((LAS unsigned*)cnt, __ATOMIC_RELAXED, __HIP_MEMORY_SCOPE_WORKGROUP)) == 4) break;
        __builtin_amdgcn_s_sleep(1);
    }
    asm volatile("" ::: "memory");
}
__device__ __forceinline__ unsigned kvflag(int br, int quad, int tile, int type) {
    return WS_KVFLAG + (unsigned)((((br * 8 + quad) * 4 + (tile & 3)) * 2 + type) * 16 + (tile >> 2)) * FSTR;
}

__device__ __forceinline__ void slow_rows(__amdgpu_buffer_rsrc_t rs, unsigned sbase, int lo, int hi, int g, bool band, bf16x8 q,
                                          unsigned vk, unsigned vv0, unsigned vv1, lds_cptr lds, int lane, int idx, f32x16& acc) {
    float m = -__builtin_inff();
    for (int pass = 0; pass < 2; ++pass) {
        if (pass) { m = halfmax(m); acc = f32x16{}; }
        for (int kt = lo; kt <= hi; ++kt) {
            const unsigned so = sbase + (unsigned)kt * TS;
            const bf16x8 k = BLOAD(rs, vk, so), v0 = BLOAD(rs, vv0, so), v1 = BLOAD(rs, vv1, so);
            f32x16 c = f32x16{};
            if (band && kt == g) c = lds_f32x16(lds + L_MDIAG + lane * 16);
            if (band && kt == g - 8) c = lds_f32x16(lds + L_MFAR + lane * 16);
            f32x16 s = MFMA(k, q, c);
            if (!pass) m = __builtin_fmaxf(m, max16(s));
            else {
#pragma unroll
                for (int r = 0; r < 16; ++r) s[r] = __builtin_amdgcn_exp2f(s[r] - m);
                acc = pv_lo(v0, s, acc, idx); acc = pv_hi(v1, s, acc, idx);
            }
        }
    }
}

#define PK4(A, o) ((u32x4){cvtpk(A[o], A[o + 1]), cvtpk(A[o + 2], A[o + 3]), cvtpk(A[o + 4], A[o + 5]), cvtpk(A[o + 6], A[o + 7])})
#define PROF_ARG , Rev& stv
#define PROF_PASS , stv
template <int E>
__device__ __forceinline__ u32x4 pack_kv(int i, const f32x16& a) {
    const int s = i & 1;
    if (E == 0) return PK4(a, 8 * s);
    auto x = __builtin_amdgcn_permlane32_swap(cvtpk(a[8 * s], a[8 * s + 1]), cvtpk(a[8 * s + 4], a[8 * s + 5]), false, false);
    auto y = __builtin_amdgcn_permlane32_swap(cvtpk(a[8 * s + 2], a[8 * s + 3]), cvtpk(a[8 * s + 6], a[8 * s + 7]), false, false);
    return (u32x4){x[0], y[0], x[1], y[1]};
}
#define PUT_KV(i, v, vo) BSTORE(v, rkv, vo, E ? ((i) & 1) * 512 + ((i) >> 1) * (128 * TS) : (i) * (64 * TS))
template <int E>
__device__ __forceinline__ void project(lds_ptr lds, char* ws, __amdgpu_buffer_rsrc_t rkv, __amdgpu_buffer_rsrc_t ro, int lane, int t, int p, int j, u64& tag PROF_ARG) {
    const int r32 = lane & 31, hh = lane >> 5;
    const unsigned q4 = (lane & 15) >> 2, wcol = ((lane >> 4) & 1) * 32 + (lane & 3) * 8;
    lds_cptr xa[4];
#pragma unroll
    for (int ks = 0; ks < 4; ++ks) xa[ks] = lds + (32 * t + r32) * 128 + (((hh ^ ((r32 >> 1) & 7)) * 16) ^ (ks * 32));
    unsigned w0o = L_W + (8 * hh + q4) * 128 + (q4 >> 1) * 64 + wcol, w1o = w0o ^ 64;
    PIN(w0o); PIN(w1o);
    const lds_cptr w0 = lds + w0o, w1 = lds + w1o, wq = lds + (8 * hh + q4) * 64 + wcol;
#define XRD(n) lds_frag(xa[(n) & 3] + (((n) >> 2) & 3) * 16384)
#define WRD(b, n) tr_frag(b + ((n) & 15) * 2048, b + ((n) & 15) * 2048 + 512)
#define YOF(n) ((n) >= 28 ? 32768 + ((n) & 3) * 2048 : ((n) & 15) * 2048)
#define YRD(b, n) tr_frag(b + YOF(n), b + YOF(n) + 512)
#define QOF(br, n) (((n) >= 28 ? L_Q15 - L_Q14 : ((n) >= 24 ? 0 : (((n) >> 2) & 3) * 16384)) + (br) * 4096 + ((n) & 3) * 1024)
#define QRD(br, n) tr_frag(((n) >= 24 ? wqh : wq) + QOF(br, n), ((n) >= 24 ? wqh : wq) + QOF(br, n) + 256)
#define MM(W, X, A) (E ? MFMA(X, W, A) : MFMA(W, X, A))
    bf16x8 xf[32], f0[4], f1[4];
    f32x16 a0 = f32x16{}, a1 = f32x16{};
    unsigned gwo = (lane & 15) == ((lane >> 4) & 1) ? L_GW + t * 1024 + hh * 16 : L_Z;
    PIN(gwo);
    f32x4 zg = f32x4{};
    bf16x8 gf[4];
#define RD1(n) do { xf[n] = XRD(n); f0[(n) & 3] = WRD(w0, n); f1[(n) & 3] = WRD(w1, n); SBAR(); } while (0)
#define GRD(n) gf[(n) & 3] = lds_frag(lds + gwo + (n) * 32)
#define GMM(n) zg = __builtin_amdgcn_mfma_f32_16x16x32_bf16(gf[(n) & 3], xf[n], zg, 0, 0, 0)
#define MM1(n) do { a0 = MM(f0[(n) & 3], xf[n], a0); a1 = MM(f1[(n) & 3], xf[n], a1); SBAR(); } while (0)
#pragma unroll
    for (int s = 0; s < 8; ++s) {
        if (s == 0) CBAR(); else BAR();
        SBAR();
        RD1(4 * s); RD1(4 * s + 1);
        if (s) { MM1(4 * s - 1); GMM(4 * s - 4); GMM(4 * s - 3); GMM(4 * s - 2); GMM(4 * s - 1); SBAR(); }
        RD1(4 * s + 2); MM1(4 * s); RD1(4 * s + 3); MM1(4 * s + 1);
        if (s == 0) DRAIN();
        GRD(4 * s); GRD(4 * s + 1); GRD(4 * s + 2); GRD(4 * s + 3); SBAR();
        MM1(4 * s + 2);
        if (s == 2) { if (E) BSTORE(((u32x4){0x3f803f80u, 0x3f803f80u, 0x3f803f80u, 0x3f803f80u}), rkv,
                                  (unsigned)((hh * 32 + 4 * (p >> 1) + (r32 >> 3)) * 64 + 4 * j + t) * TS + 2048 + (lane & 7) * 16, 0); }
        if (s == 4) BSTORE(((u32x4){~0u, ~0u, ~0u, ~0u}), ro, lane * 16, (unsigned)((4 * j + t) * 32 + 2 * p) * 1024);
        SBAR();
    }
    const unsigned tile = 4 * j + t;
    unsigned vo = (unsigned)(4 * (p >> 1) * 64 + tile) * TS + (E ? (r32 >> 4) * (64 * TS) + 1024 + hh * 256 + (r32 & 15) * 16 : lane * 16);
    unsigned wqo = L_Q14 + (8 * hh + q4) * 64 + wcol;
    PIN(vo); PIN(wqo);
    const lds_cptr wqh = lds + wqo;
    MM1(31); GMM(28); GMM(29); GMM(30); GMM(31); SBAR();
#define PRE8 2
#define ST8 4
#define PUBN 9
    u32x4 pk[4];
#pragma unroll
    for (int i = 0; i < PRE8; ++i) { pk[i] = pack_kv<E>(i, i < 2 ? a0 : a1); PUT_KV(i, pk[i], vo); }
    SBAR();
    BAR(); SBAR();
    bf16x8 u0 = QRD(0, 0), u1 = QRD(1, 0), y0 = QRD(0, 1), y1 = QRD(1, 1);
    const u32x2 tg = *(const LAS u32x2*)(lds + L_CNT + 16);
    f32x16 b0 = f32x16{}, b1 = f32x16{}, aQ0 = f32x16{}, aQ1 = f32x16{};
    SBAR();
#define XPK(i) do { if ((i) >= PRE8) { pk[i] = pack_kv<E>(i, (i) < 2 ? a0 : a1); PIN(pk[i]); if ((i) < ST8) PUT_KV(i, pk[i], vo); SBAR(); } } while (0)
    XPK(0);
    aQ0 = MFMA(u0, xf[0], aQ0); SBAR(); u0 = QRD(0, 2); SBAR(); XPK(1);
    aQ1 = MFMA(u1, xf[0], aQ1); SBAR(); u1 = QRD(1, 2); SBAR();
    aQ0 = MFMA(y0, xf[1], aQ0); SBAR(); y0 = QRD(0, 3); SBAR(); XPK(2);
    aQ1 = MFMA(y1, xf[1], aQ1); SBAR(); y1 = QRD(1, 3); SBAR();
    aQ0 = MFMA(u0, xf[2], aQ0); SBAR(); u0 = YRD(w0, 0); SBAR(); XPK(3);
    aQ1 = MFMA(u1, xf[2], aQ1); SBAR(); u1 = YRD(w1, 0); SBAR();
    aQ0 = MFMA(y0, xf[3], aQ0); SBAR(); y0 = YRD(w0, 1); SBAR();
    float ge0 = __expf(-zg[0]), ge1 = __expf(-zg[1]); PIN(ge0); PIN(ge1); SBAR();
    aQ1 = MFMA(y1, xf[3], aQ1); SBAR(); y1 = YRD(w1, 1); SBAR();
    ge0 = __builtin_amdgcn_rcpf(1.f + ge0); ge1 = __builtin_amdgcn_rcpf(1.f + ge1); PIN(ge0); PIN(ge1); SBAR();
    b0 = MM(u0, xf[0], b0); SBAR(); u0 = YRD(w0, 2); SBAR();
    { LAS float* gp = (LAS float*)(lds + (lane < 16 ? L_G + t * 128 : L_PL)) + lane; gp[0] = ge0; gp[16] = ge1; } SBAR();
    b1 = MM(u1, xf[0], b1); SBAR(); u1 = YRD(w1, 2); SBAR();
    tag = ((u64)(unsigned)__builtin_amdgcn_readfirstlane(tg[1]) << 32) | (unsigned)__builtin_amdgcn_readfirstlane(tg[0]);
    asm volatile("" : "+s"(tag)); SBAR();
    b0 = MM(y0, xf[1], b0); SBAR(); y0 = YRD(w0, 3); SBAR();
    b1 = MM(y1, xf[1], b1); SBAR(); y1 = YRD(w1, 3); SBAR();
    b0 = MM(u0, xf[2], b0); SBAR();
    if (ST8 < 4) { PUT_KV(ST8 & 3, pk[ST8 & 3], vo); SBAR(); }
    BAR(); SBAR();
    u0 = QRD(0, 4); SBAR();
    b1 = MM(u1, xf[2], b1); SBAR(); u1 = QRD(1, 4); SBAR();
    b0 = MM(y0, xf[3], b0); SBAR(); y0 = YRD(w0, 4); SBAR();
    b1 = MM(y1, xf[3], b1); SBAR(); y1 = YRD(w1, 4); SBAR();
#pragma unroll
    for (int n = 4; n < 24; ++n) {
        aQ0 = MFMA(u0, xf[n], aQ0); SBAR();
        if (n == PUBN) { DRAIN(); if (lane == 0) __hip_atomic_store((GAS u64*)(ws + kvflag(0, p >> 1, tile, E)), tag, RLX_AGENT); SBAR(); }
        if ((n & 3) == 3) {
            if (ST8 + (n >> 2) < 4) { PUT_KV((ST8 + (n >> 2)) & 3, pk[(ST8 + (n >> 2)) & 3], vo); SBAR(); }
            BAR(); SBAR();
        }
        u0 = QRD(0, n + 1); SBAR();
        aQ1 = MFMA(u1, xf[n], aQ1); SBAR();
        u1 = n < 23 ? QRD(1, n + 1) : QRD(0, 25); SBAR();
        b0 = MM(y0, xf[n], b0); SBAR(); y0 = n < 23 ? YRD(w0, n + 1) : QRD(0, 26); SBAR();
        b1 = MM(y1, xf[n], b1); SBAR(); y1 = n < 23 ? YRD(w1, n + 1) : QRD(0, 27); SBAR();
    }
    aQ0 = MFMA(u0, xf[24], aQ0); SBAR(); u0 = QRD(0, 28); SBAR(); aQ0 = MFMA(u1, xf[25], aQ0); SBAR(); u1 = QRD(0, 29); SBAR();
    aQ0 = MFMA(y0, xf[26], aQ0); SBAR(); y0 = QRD(0, 30); SBAR(); aQ0 = MFMA(y1, xf[27], aQ0); SBAR(); y1 = QRD(0, 31); SBAR();
    aQ0 = MFMA(u0, xf[28], aQ0); SBAR(); aQ0 = MFMA(u1, xf[29], aQ0); SBAR(); u1 = QRD(1, 24); SBAR();
    aQ0 = MFMA(y0, xf[30], aQ0); SBAR(); y0 = YRD(w0, 24); SBAR(); aQ0 = MFMA(y1, xf[31], aQ0); SBAR(); y1 = YRD(w1, 24); SBAR();
    const lds_ptr qd = lds + L_Q + t * 1024 + lane * 16;
#pragma unroll
    for (int n = 24; n < 32; ++n) {
        aQ1 = MFMA(u1, xf[n], aQ1); SBAR();
        if (n == 24) {
#pragma unroll
            for (int r = 8; r < 16; ++r) aQ0[r] *= QSCALE;
            *(LAS u32x4*)(qd + 4096) = PK4(aQ0, 8);
            __builtin_amdgcn_s_setprio(2);
            CBAR(); STAMP(4); SBAR();
        }
        if (n < 31) u1 = QRD(1, n + 1); PACE(); SBAR();
        b0 = MM(y0, xf[n], b0); SBAR(); if (n < 31) y0 = YRD(w0, n + 1); PACE(); SBAR();
        b1 = MM(y1, xf[n], b1); SBAR(); if (n < 31) y1 = YRD(w1, n + 1); PACE(); SBAR();
    }
#pragma unroll
    for (int r = 0; r < 8; ++r) aQ0[r] *= QSCALE;
#pragma unroll
    for (int r = 0; r < 16; ++r) aQ1[r] *= QSCALE;
    *(LAS u32x4*)qd = PK4(aQ0, 0); *(LAS u32x4*)(qd + 8192) = PK4(aQ1, 0); *(LAS u32x4*)(qd + 12288) = PK4(aQ1, 8);
    cnt_arrive(lds + L_CNT + 8, lane);
    const unsigned vo2 = vo + 32 * 64 * TS;
#define YSLP 8
    PUT_KV(0, pack_kv<E>(0, b0), vo2); if (YSLP) __builtin_amdgcn_s_sleep(YSLP); PUT_KV(1, pack_kv<E>(1, b0), vo2); if (YSLP) __builtin_amdgcn_s_sleep(YSLP);
    PUT_KV(2, pack_kv<E>(2, b1), vo2); if (YSLP) __builtin_amdgcn_s_sleep(YSLP); PUT_KV(3, pack_kv<E>(3, b1), vo2);
    DRAIN();
    if (lane == 0) __hip_atomic_store((GAS u64*)(ws + kvflag(1, p >> 1, tile, E)), tag, RLX_AGENT);
    STAMP(19);
}

#define AB_WO (512 * 1024)
#define WOIN 2
#define CODE_LINES 256

struct Att {
    lds_ptr lds; char* ws; __amdgpu_buffer_rsrc_t rkv, ro;
    int lane, t, p, g, head, idx; u64 tag; unsigned vk, vv0, vv1, so, sBeg, sEnd; lds_cptr zp, qp[4];
};
#define IDX a.idx
#define BLOADL(old, rs, vo, so) BLOAD(rs, vo, so)
#define LD3(K, V0, V1, rs, so) do { K = BLOAD(rs, a.vk, so); V0 = BLOAD(rs, a.vv0, so); V1 = BLOAD(rs, a.vv1, so); } while (0)
#define EX(X, r) X[r] = __builtin_amdgcn_exp2f(X[r])
#define STEP(X, Y, OP, VP, KN, QN, CN, O, V0, F1, F2) do { \
        EX(X, 0); EX(X, 1); SBAR(); OP = pv_hi(VP, Y, OP, IDX); SBAR(); F1; SBAR(); EX(X, 2); EX(X, 3); EX(X, 4); SBAR(); Y = MFMA(KN, QN, CN); SBAR(); \
        EX(X, 5); EX(X, 6); EX(X, 7); SBAR(); EX(X, 8); EX(X, 9); SBAR(); O = pv_lo(V0, X, O, IDX); SBAR(); F2; SBAR(); \
        EX(X, 10); EX(X, 11); EX(X, 12); EX(X, 13); EX(X, 14); EX(X, 15); SBAR(); } while (0)

__device__ __forceinline__ f32x16 lds_zero16(lds_cptr zp) {
    const volatile LAS f32x4* z = (const volatile LAS f32x4*)zp;
    const f32x4 a = *z, b = *z, c = *z, d = *z;
    f32x8 lo = __builtin_shufflevector(a, b, 0, 1, 2, 3, 4, 5, 6, 7), hi = __builtin_shufflevector(c, d, 0, 1, 2, 3, 4, 5, 6, 7);
    return __builtin_shufflevector(lo, hi, 0, 1, 2, 3, 4, 5, 6, 7, 8, 9, 10, 11, 12, 13, 14, 15);
}

template <int C, int BR>
__device__ __forceinline__ GAS u64* finish(const Att& a, f32x16 oo, float lrest, bf16x8 qa) {
    const float gt = *(const LAS float*)(a.lds + L_G + a.t * 128 + (a.lane & 31) * 4);
    auto lsw = __builtin_amdgcn_permlane32_swap(__float_as_uint(oo[8]), __float_as_uint(oo[8]), false, false);
    float l = __uint_as_float(lsw[0]) + lrest, z = 0.f;
#pragma unroll
    for (int r = 0; r < 8; ++r) z = __builtin_fmaf(oo[r], 0.f, z);
    if (__builtin_expect(__any(!(l + z < LMAX && l > LMIN)), 0)) {
        if (!BR) { wait_tags((const GAS u64*)(a.ws + kvflag(0, a.p >> 1, 0, 0) + a.lane * FSTR), a.tag); wait_tags((const GAS u64*)(a.ws + kvflag(0, a.p >> 1, 2, 0) + a.lane * FSTR), a.tag); }
        slow_rows(a.rkv, (unsigned)((BR * 32 + a.head) * 64) * TS, BR ? (a.g >= 8 ? a.g - 8 : 0) : 0, BR ? a.g : 63, a.g, BR, qa, a.vk, a.vv0, a.vv1, a.lds, a.lane, IDX, oo);
        auto l2 = __builtin_amdgcn_permlane32_swap(__float_as_uint(oo[8]), __float_as_uint(oo[8]), false, false);
        l = __uint_as_float(l2[0]);
    }
    const float il = __builtin_amdgcn_rcpf(l) * (BR ? 1.f - gt : gt);
    u32x4 v = (u32x4){cvtpk(oo[0] * il, oo[1] * il), cvtpk(oo[2] * il, oo[3] * il), cvtpk(oo[4] * il, oo[5] * il), cvtpk(oo[6] * il, oo[7] * il)};
    if (!C && !BR) v = __builtin_elementwise_min(v, (u32x4){~1u, ~1u, ~1u, ~1u});
    BSTORE(v, a.ro, a.vk, (unsigned)((BR * 64 + a.g) * 32 + a.head) * 1024);
    return (GAS u64*)(a.ws + WS_OFLAG) + ((BR * 64 + a.g) * 2 + C) * 16 + a.p;
}

template <int C>
__device__ __forceinline__ GAS u64* attn_full(const Att& a, f32x16 oo, f32x16 o1, f32x16 o2, f32x16 o3, f32x16 S1, __amdgpu_buffer_rsrc_t rwo, unsigned vwo, unsigned wi PROF_ARG) {
    const lds_ptr lds = a.lds; const int t = a.t, lane = a.lane, w = 4 * C + t;
    const f32x16 zc = f32x16{};
    bf16x8 ka, va0, va1, kb, vb0, vb1, kc, vc0, vc1, kd, vd0, vd1;
    const unsigned sBeg = a.sBeg, sEnd = a.sEnd;
    unsigned so = a.so;
#define NEXT_A() asm volatile("s_addk_i32 %0, %1" : "+s"(so) : "i"(4 * TS) : "scc")
#define WRAP_A() asm volatile("s_cmp_eq_u32 %0, %1\n\ts_cselect_b32 %0, %2, %0" : "+s"(so) : "s"(sEnd), "s"(sBeg) : "scc")
#define STEP_A() asm volatile("s_addk_i32 %0, %3\n\ts_cmp_eq_u32 %0, %1\n\ts_cselect_b32 %0, %2, %0" : "+s"(so) : "s"(sEnd), "s"(sBeg), "i"(4 * TS) : "scc")
    LD3(ka, va0, va1, a.rkv, so); STEP_A(); SBAR(); LD3(kb, vb0, vb1, a.rkv, so); STEP_A(); SBAR();
    LD3(kc, vc0, vc1, a.rkv, so); STEP_A(); SBAR();
    vd1 = lds_frag(a.zp);
    if (C) { BAR(); STAMP(4); }
    bf16x8 qa = lds_frag(a.qp[0]); SBAR();
    bf16x8 q1 = lds_frag(a.qp[1]), q2 = lds_frag(a.qp[2]), q3 = lds_frag(a.qp[3]);
    SBAR();
    f32x16 S0 = MFMA(ka, qa, zc);
    PIN(S0); SBAR();
    PIN(q1); PIN(q2); PIN(q3); PIN(vd1); PIN(oo); PIN(o1); PIN(o2); PIN(o3); PIN(S1);
    __builtin_amdgcn_s_waitcnt(0x0F76);
#pragma unroll
    for (int i = 0; i < 8; ++i) __builtin_amdgcn_s_setprio(C);
    STAMP(3);
    SBAR();
    unsigned wso = 0;
#define NEXT_AW() do { if (C) asm volatile("s_addk_i32 %0, %3\n\ts_lshl_b32 %1, %2, 4" : "+s"(so), "=s"(wso) : "s"(wi), "i"(4 * TS) : "scc"); else NEXT_A(); } while (0)
#define WO_DMA() do { if (C) __builtin_amdgcn_raw_ptr_buffer_load_lds(rwo, (LAS void*)(lds + L_WO + t * 16384 + wi), 16, vwo, wso, 0, 0); } while (0)
#define WO_NEXT() do { if (C) asm volatile("s_addk_i32 %0, 0x400\n\ts_and_b32 %0, %0, 0x3fff" : "+s"(wi) :: "scc"); } while (0)
#define TILE(K, V0, V1, KN, LK, LV0, LV1, NW) do { \
        STEP(S0, S1, o3, LV1, K, q1, zc, oo, V0, LK = BLOADL(K, a.rkv, a.vk, so), LV0 = BLOADL(V0, a.rkv, a.vv0, so)); \
        STEP(S1, S0, oo, V1, K, q2, zc, o1, V0, LV1 = BLOADL(V1, a.rkv, a.vv1, so), NEXT_AW()); \
        STEP(S0, S1, o1, V1, K, q3, zc, o2, V0, WRAP_A(), WO_DMA()); \
        STEP(S1, S0, o2, V1, KN, qa, zc, o3, V0, __builtin_amdgcn_s_waitcnt(0x0F70 | (C ? NW : 6)), WO_NEXT()); } while (0)
#pragma nounroll
    for (int it = 0; it < 4; ++it) {
        TILE(ka, va0, va1, kb, kd, vd0, vd1, 7);
        TILE(kb, vb0, vb1, kc, ka, va0, va1, 8);
        TILE(kc, vc0, vc1, kd, kb, vb0, vb1, 9);
        TILE(kd, vd0, vd1, ka, kc, vc0, vc1, 9);
    }
    o3 = pv_hi(vd1, S1, o3, IDX);
    STAMP(6);
    if (C) cnt_wait(lds + L_CNT + 8); else __builtin_amdgcn_s_setprio(2);
#define PUT(O, sl) do { lds_ptr d_ = lds + L_PART + (w * 3 + sl) * 2048 + lane * 16; \
        *(LAS f32x4*)d_ = __builtin_shufflevector(O, O, 0, 1, 2, 3); *(LAS f32x4*)(d_ + 1024) = __builtin_shufflevector(O, O, 4, 5, 6, 7); } while (0)
    PUT(o1, 0); PUT(o2, 1); PUT(o3, 2);
    if (lane < 32) { LAS float* lp = (LAS float*)(lds + L_PL + w * 384) + lane; lp[0] = o1[8]; lp[32] = o2[8]; lp[64] = o3[8]; }
    if (C) { DRAIN(); cnt_arrive(lds + L_CNT, lane); cnt_wait(lds + L_CNT); } else CBAR();
    float lrest = 0.f;
#pragma unroll
    for (int s = 0; s < 3; ++s) {
        const int src = (C * 4 + ((t - 1 - s) & 3)) * 3 + s;
        const lds_cptr d = lds + L_PART + src * 2048 + lane * 16;
        const f32x4 x = *(const LAS f32x4*)d, y = *(const LAS f32x4*)(d + 1024);
#pragma unroll
        for (int r = 0; r < 4; ++r) { oo[r] += x[r]; oo[4 + r] += y[r]; }
        const float lp = *(const LAS float*)(lds + L_PL + src * 128 + (lane & 31) * 4);
        lrest = s ? lrest + lp : lp;
    }
    return finish<C, 0>(a, oo, lrest, qa);
}

template <int C>
__device__ __forceinline__ GAS u64* attn_window(const Att& a, GAS u64* pub PROF_ARG) {
    const lds_ptr lds = a.lds; const int t = a.t, lane = a.lane, g = a.g;
    const f32x16 zc = f32x16{};
    { const int i = lane & 15, kt = g - (i < 9 ? i : 0);
      wait_tags((const GAS u64*)(a.ws + kvflag(1, a.p >> 1, kt >= 0 ? kt : 0, (lane >> 4) & 1)), a.tag, pub, lane); }
    STAMP(25);
    bf16x8 k[9], v0[9], v1[9];
    const unsigned sB = (unsigned)((32 + a.head) * 64) * TS;
#define LDB(i) do { const int kt_ = g - (i); \
        const __amdgpu_buffer_rsrc_t rb_ = __builtin_amdgcn_make_buffer_rsrc(a.ws + WS_KV, 0, kt_ >= 0 ? KV_BYTES : 0u, 0x00020000); \
        LD3(k[i], v0[i], v1[i], rb_, sB + (unsigned)(kt_ >= 0 ? kt_ : 0) * TS); } while (0)
    LDB(0); SBAR(); LDB(1); SBAR(); LDB(2); SBAR();
    const bf16x8 qa = lds_frag(lds + L_Q + 8192 + C * 4096 + t * 1024 + lane * 16);
    const f32x16 cd = lds_f32x16(lds + L_MDIAG + lane * 16), cf = lds_f32x16(lds + L_MFAR + lane * 16);
    f32x16 oo = lds_zero16(a.zp);
    f32x16 S0 = MFMA(k[0], qa, cd), S1 = MFMA(k[1], qa, zc);
    SBAR(); LDB(3); SBAR();
#pragma unroll
    for (int r = 0; r < 10; ++r) { EX(S0, r); SBAR(); }
    oo = pv_lo(v0[0], S0, oo, IDX); SBAR();
#pragma unroll
    for (int r = 10; r < 16; ++r) EX(S0, r);
    SBAR();
    STEP(S1, S0, oo, v1[0], k[2], qa, zc, oo, v0[1], LDB(4), );
    STEP(S0, S1, oo, v1[1], k[3], qa, zc, oo, v0[2], LDB(5), );
    STEP(S1, S0, oo, v1[2], k[4], qa, zc, oo, v0[3], LDB(6), );
    STEP(S0, S1, oo, v1[3], k[5], qa, zc, oo, v0[4], LDB(7), );
    STEP(S1, S0, oo, v1[4], k[6], qa, zc, oo, v0[5], LDB(8), );
    STEP(S0, S1, oo, v1[5], k[7], qa, zc, oo, v0[6], , );
    STEP(S1, S0, oo, v1[6], k[8], qa, cf, oo, v0[7], , );
    STEP(S0, S1, oo, v1[7], k[8], qa, cf, oo, v0[8], , );
    oo = pv_hi(v1[8], S0, oo, IDX);
    STAMP(7);
    return finish<C, 1>(a, oo, 0.f, qa);
}

__global__ void __launch_bounds__(512, 2)
gmix_kernel(const char* x, const char* wA, const char* woA, const char* wB, const char* woB, const char* gw, char* out, char* ws) {
    extern __shared__ __attribute__((aligned(16))) char smem[];
    const lds_ptr lds = (lds_ptr)smem;
    const unsigned lds0 = (unsigned)(uintptr_t)lds;
    const int tid = threadIdx.x, lane = tid & 63, r32 = lane & 31, hh = lane >> 5;
    const int w = __builtin_amdgcn_readfirstlane(tid >> 6);
    const int bid = blockIdx.x, xcd = bid & 7, rr = bid >> 3;
    const int p = 4 * (xcd & 3) + (rr & 3), j = 8 * (xcd >> 2) + (rr >> 2);
    const int jo = 2 * xcd + (rr >> 4), po = rr & 15;
    const int t = w & 3, c = w >> 2;
    const int e = p & 1;
    Rev stv;
    STAMP(0);
    {
        u64 pc; const unsigned ln = ((bid & ~7) | (tid >> 6)) * 128;
        asm volatile("s_getpc_b64 %0\n\ts_mov_b32 m0, %2\n\ts_nop 0\n\tglobal_load_lds_dword %1, %0" : "=&s"(pc) : "v"(ln), "s"(lds0 + L_PL) : "memory");
        SBAR();
    }
    unsigned rot = (j + 2 * (p & 3)) & 7;
    unsigned vx0 = (128 * j + 32 * t + (lane >> 3)) * 1024 + (((lane & 7) ^ (lane >> 4)) * 16), vx1 = (vx0 ^ 64) + 7168, vx2 = vx0 + 14336, vx3 = vx1 + 14336;
    unsigned vw0 = (16 * t + (lane >> 3)) * 3072 + 1024 + 1024 * e + 128 * (p >> 1) + (((lane & 7) ^ (((lane >> 4) & 1) << 2)) * 16), vw1 = vw0 + 8 * 3072 - 1024;
    unsigned mx = lds0 + t * 4096, mw = lds0 + L_W + t * 2048, ow = rot * (64 * 3072);
    asm volatile("" : "+v"(vx0), "+v"(vx1), "+v"(vx2), "+v"(vx3), "+v"(vw0), "+v"(vw1));
    asm volatile("" : "+s"(mx), "+s"(mw), "+s"(rot), "+s"(ow));
    SBAR();
    asm volatile("" : "+s"(x), "+s"(wA), "+s"(woA), "+s"(wB), "+s"(woB), "+s"(gw), "+s"(out), "+s"(ws));
#define MK_ATT(C) Att a; a.lds = lds; a.ws = ws; a.lane = lane; a.t = t; a.p = p; a.g = 4 * j + t; a.head = 2 * p + C; a.idx = 0xdddddddd; \
    a.rkv = __builtin_amdgcn_make_buffer_rsrc(ws + WS_KV, 0, KV_BYTES, 0x00020000); a.ro = __builtin_amdgcn_make_buffer_rsrc(ws + WS_O, 0, O_BYTES, 0x00020000); \
    a.vk = lane * 16; a.vv0 = r32 < 16 ? 1024 + hh * 256 + r32 * 16 : (r32 == 16 ? 2048 : OOB); a.vv1 = r32 < 16 ? a.vv0 + 512 : a.vv0; a.zp = lds + L_Z + lane * 16; \
    a.sBeg = (unsigned)(a.head * 64 + t) * TS; a.sEnd = a.sBeg + 64 * TS; a.so = (unsigned)(a.head * 64 + a.g) * TS; \
    _Pragma("unroll") for (int i = 0; i < 4; ++i) a.qp[i] = lds + L_Q + C * 4096 + ((t + i) & 3) * 1024 + lane * 16
    if (c) {
#define DMA1(s) do { const unsigned ks_ = ((s) + rot) & 7; \
        glds16x4(vx0, vx1, vx2, vx3, x + ks_ * 128, mx + ((s) & 3) * 16384); glds16x2(vw0, vw1, wA + ks_ * (64 * 3072), mw + ((s) & 3) * 8192); } while (0)
#define DMA2(s) do { const unsigned ks_ = (((s) + rot) & 7) * (64 * 3072); \
        glds16x2(vq0, vq1, wq + ks_, mq + ((s) == 14 ? L_Q14 : ((s) & 3) * 16384)); glds16x2(vw0, vw1, wB + ks_, mw + ((s) & 3) * 8192); SBAR(); } while (0)
#define LBAR(N) asm volatile("s_waitcnt vmcnt(" #N ")\n\ts_barrier" ::: "memory")
        STAMP(20);
        glds16x4(vx0, vx1, vx2, vx3, x + rot * 128, mx); glds16x2(vw0, vw1, wA + ow, mw);
        SBAR();
        const unsigned k1 = (1 + rot) & 7;
        glds16x2(vx0, vx1, x + k1 * 128, mx + 16384);
        SBAR();
        const u64 ep = *(const __attribute__((address_space(4))) u64*)(ws + WS_EPOCH);
        unsigned vq0 = (32 * (t & 1) + (lane >> 2)) * 3072 + 64 * p + (lane & 3) * 16, vq1 = vq0 + 16 * 3072 - 1024;
        unsigned vwo = ((t & 1) * 256 + (lane >> 2)) * 1024 + 64 * po + (lane & 3) * 16;
        MK_ATT(1);
        const GAS u64* fp = (const GAS u64*)(ws + kvflag(0, p >> 1, t, 0) + r32 * FSTR);
        PIN(vq0); PIN(vq1); PIN(vwo); PIN(a.vk); PIN(a.vv0); PIN(a.vv1); PIN(a.qp[0]); PIN(a.qp[1]); PIN(a.qp[2]); PIN(a.qp[3]); PIN(a.zp); PIN(fp); PIN(a.idx);
        SBAR();
        STAMP(30);
        glds16x2(vx0, vx1, x + k1 * 128 + 16384, mx + 16384 + 2048); glds16x2(vw0, vw1, wA + k1 * (64 * 3072), mw + 8192);
        STAMP(22);
        SBAR();
#pragma nounroll
        for (int s = 2; s < 8; ++s) { LBAR(6); DMA1(s);
            if (s == 2) STAMP(1); if (s == 3) STAMP(2);
        }
        const unsigned mq = lds0 + t * 2048;
        const char* wq = (t >> 1) ? wB : wA;
        LBAR(6); STAMP(14); DMA2(8);
        LBAR(4); STAMP(15); DMA2(9);
        a.tag = ep * 0x9E3779B97F4A7C15ull + 0x632BE59BD9B4E019ull;
        *(LAS u64*)(lds + L_CNT + 16) = a.tag;
        asm volatile("s_waitcnt lgkmcnt(0)" ::: "memory");
        LBAR(4); STAMP(16); DMA2(10);
        const unsigned k7 = ((7 + rot) & 7) * (64 * 3072), sx = mq + L_Q15, sw = mw + 32768;
        LBAR(4); STAMP(17); glds16(vq0, wq + k7, sx); DMA2(11);
        f32x16 oo = lds_zero16(a.zp), o1 = lds_zero16(a.zp); PIN(oo); PIN(o1);
        LBAR(5); STAMP(18); glds16(vw0, wB + k7, sw); DMA2(12);
        f32x16 o2 = lds_zero16(a.zp), o3 = lds_zero16(a.zp); PIN(o2); PIN(o3);
        LBAR(5); STAMP(19); glds16(vq1, wq + k7 + 1024, sx + 1024); DMA2(13);
        f32x16 S1 = lds_zero16(a.zp); PIN(S1);
        LBAR(5); STAMP(21); glds16(vw1, wB + k7 + 1024, sw + 1024); DMA2(14);
        __amdgpu_buffer_rsrc_t rwo = __builtin_amdgcn_make_buffer_rsrc((void*)((t >> 1) ? woB : woA), 0, AB_WO, 0x00020000);
        unsigned wi0 = 4096 * (((rr >> 4) << 1) | (po & 1));
        asm volatile("" : "+s"(a.rkv), "+s"(rwo), "+s"(a.so), "+s"(a.sBeg), "+s"(a.sEnd), "+s"(wi0));
        __builtin_amdgcn_s_setprio(1);
        LBAR(5); STAMP(24);
        const u64 fv = __hip_atomic_load(fp, RLX_AGENT);
        LBAR(1); STAMP(29);
        wait_tags_end(fv, fp, a.tag, nullptr, lane);
        STAMP(5);
        GAS u64* pub = attn_full<1>(a, oo, o1, o2, o3, S1, rwo, vwo, wi0 PROF_PASS);
        STAMP(23);
        pub = attn_window<1>(a, pub PROF_PASS);
        STAMP(8);
        const int go = 4 * jo + t;
        const lds_cptr wo = lds + L_WO + (4 * hh + ((lane & 15) >> 2)) * 64 + ((lane >> 4) & 1) * 32 + (lane & 3) * 8;
        const GAS u64* of_ = (const GAS u64*)(ws + WS_OFLAG);
        wait_tags(of_ + ((lane & 16) ? go * 32 + 16 : (64 + go) * 32) + (lane & 15), a.tag, pub, lane);
        STAMP(9);
#define OBR(n) ((n) >> 4 == 1 ? 0 : 1)
#define OHD(n) (2 * ((n) & 15) + ((n) >= 16))
#define OLD(n) BLOAD(a.ro, a.vk, (unsigned)((OBR(n) * 64 + go) * 32 + OHD(n)) * 1024)
#define OWF(n) tr_frag(wo + OBR(n) * 32768 + OHD(n) * 1024, wo + OBR(n) * 32768 + OHD(n) * 1024 + 512)
#define OIN 7
        bf16x8 oe[48];
#pragma unroll
        for (int n = 0; n < 48; ++n) {
            if (n == 32) wait_tags(of_ + (64 + go) * 32 + 16 + (lane & 15), a.tag);
            oe[n] = OLD(n); SBAR();
            if (n >= OIN - 1) __builtin_amdgcn_s_waitcnt(0x0F70 | (OIN - 1));
            SBAR();
        }
        bf16x8 wn[4];
#pragma unroll
        for (int n = 0; n < 4; ++n) { wn[n] = OWF(n); PIN(wn[n]); }
        __builtin_amdgcn_s_waitcnt(0x0F70);
#define NPRE 0
#define NMID 24
        f32x16 ya = f32x16{};
        bf16x8 wf[16], of[16];
#define OMM(n) do { ya = MFMA(wn[(n) & 3], oe[n], ya); SBAR(); if ((n) + 4 < 48) wn[(n) & 3] = OWF((n) + 4); \
            if ((n) >= 32) wf[(n) - 32] = tr_frag(wo + 2 * ((n) - 32) * 1024, wo + 2 * ((n) - 32) * 1024 + 512); SBAR(); } while (0)
#pragma unroll
        for (int n = 0; n < NPRE; ++n) { OMM(n); PACE(); }
        STAMP(10);
        BAR();
        BAR();
#pragma unroll
        for (int n = NPRE; n < NMID; ++n) OMM(n);
        BAR();
        STAMP(11);
#define GRP_BAD(q) ({ unsigned mx_ = 0; _Pragma("unroll") for (int i = 4 * (q); i < 4 * (q) + 4; ++i) { const u32x4 u_ = __builtin_bit_cast(u32x4, of[i]); \
            mx_ = __builtin_elementwise_max(__builtin_elementwise_max(mx_, u_[0]), u_[1]); mx_ = __builtin_elementwise_max(__builtin_elementwise_max(mx_, u_[2]), u_[3]); } \
            __any(mx_ == ~0u); })
#define GRP_MM(q) do { _Pragma("unroll") for (int i = 4 * (q); i < 4 * (q) + 4; ++i) ya = MFMA(wf[i], of[i], ya); } while (0)
#define GRP_LD(q) do { _Pragma("unroll") for (int i = 4 * (q); i < 4 * (q) + 4; ++i) of[i] = BLOAD(a.ro, a.vk, (unsigned)(go * 32 + 2 * i) * 1024); } while (0)
#pragma unroll
        for (int n = NMID; n < 48; ++n) { if (n - NMID < 16) { of[n - NMID] = BLOAD(a.ro, a.vk, (unsigned)(go * 32 + 2 * (n - NMID)) * 1024); SBAR(); } OMM(n); }
        STAMP(31);
        int done = 0;
        if (!GRP_BAD(0)) { GRP_MM(0); done = 1; if (!GRP_BAD(1)) { GRP_MM(1); done = 2; if (!GRP_BAD(2)) { GRP_MM(2); done = 3; if (!GRP_BAD(3)) { GRP_MM(3); done = 4; } } } }
        if (done < 4) {
#pragma nounroll
            for (unsigned spins = 0; spins < SPIN_CAP; ++spins) {
                const bool b0 = GRP_BAD(0), b1 = GRP_BAD(1), b2 = GRP_BAD(2), b3 = GRP_BAD(3);
                if (!(b0 || b1 || b2 || b3)) break;
                asm volatile("" ::: "memory");
                if (b0) GRP_LD(0); if (b1) GRP_LD(1); if (b2) GRP_LD(2); if (b3) GRP_LD(3);
            }
            if (done < 1) GRP_MM(0); if (done < 2) GRP_MM(1); if (done < 3) GRP_MM(2); GRP_MM(3);
        }
#pragma unroll
        for (int s = 0; s < 2; ++s) {
            auto u = __builtin_amdgcn_permlane32_swap(cvtpk(ya[8 * s], ya[8 * s + 1]), cvtpk(ya[8 * s + 4], ya[8 * s + 5]), false, false);
            auto v = __builtin_amdgcn_permlane32_swap(cvtpk(ya[8 * s + 2], ya[8 * s + 3]), cvtpk(ya[8 * s + 6], ya[8 * s + 7]), false, false);
#define OUT_AUX 0
            __builtin_amdgcn_raw_buffer_store_b128(((u32x4){u[0], v[0], u[1], v[1]}), __builtin_amdgcn_make_buffer_rsrc(out, 0, 2048 * 1024, 0x00020000),
                                                   (32 * go + r32) * 1024 + 64 * po + 32 * s + 16 * hh, 0, OUT_AUX);
        }
        if (bid == 0 && tid == 256) __hip_atomic_store((GAS u64*)(ws + WS_EPOCH), a.tag, RLX_AGENT);
        STAMP(12);
    } else {
        glds16((((lane >> 3) + rot) & 7) * 128 + (lane & 7) * 16, gw, lds0 + L_GW + t * 1024);
#pragma unroll
        for (int m = 0; m < 2; ++m) {
            f32x4 mk;
#pragma unroll
            for (int i = 0; i < 4; ++i) { const bool le = (i + 8 * t + 4 * hh) <= r32; mk[i] = (le != (m != 0)) ? 0.f : -__builtin_inff(); }
            *(LAS f32x4*)(lds + L_MDIAG + m * 4096 + t * 1024 + lane * 16) = mk;
        }
        if (tid < 4) *(LAS unsigned*)(lds + L_CNT + tid * 4) = 0;
        *(LAS unsigned*)(lds + L_Z + tid * 4) = 0;
        u64 tag;
        STAMP(21);
        {
            const __amdgpu_buffer_rsrc_t rkv = __builtin_amdgcn_make_buffer_rsrc(ws + WS_KV, 0, KV_BYTES, 0x00020000);
            const __amdgpu_buffer_rsrc_t ro = __builtin_amdgcn_make_buffer_rsrc(ws + WS_O, 0, O_BYTES, 0x00020000);
            if (e) project<1>(lds, ws, rkv, ro, lane, t, p, j, tag PROF_PASS); else project<0>(lds, ws, rkv, ro, lane, t, p, j, tag PROF_PASS);
        }
        cnt_wait(lds + L_CNT + 8);
        __builtin_amdgcn_s_sleep(BSLP);
        STAMP(24);
        MK_ATT(0);
        a.tag = tag;
        GAS u64* pub = attn_window<0>(a, nullptr PROF_PASS);
        STAMP(23);
        wait_tags((const GAS u64*)(ws + kvflag(0, p >> 1, t, 0) + r32 * FSTR), tag, pub, lane);
        STAMP(5);
        attn_full<0>(a, lds_zero16(a.zp), lds_zero16(a.zp), lds_zero16(a.zp), lds_zero16(a.zp), lds_zero16(a.zp), a.rkv, 0, 0 PROF_PASS);
        BAR();
        STAMP(8);
        DRAIN(); BAR();
    }
    DRAIN();
    STAMP(13);
    if (c == RROLE && bid == 0) while (__builtin_amdgcn_s_memrealtime() < stv.r0 + 6000 + stv.d) __builtin_amdgcn_s_sleep(1);
}

extern "C" void kernel_launch(void* const* d_in, const int* in_sizes, int n_in, void* d_out, int out_size, void* d_ws, size_t ws_size,
                              hipStream_t stream) {
    (void)in_sizes; (void)n_in; (void)out_size; (void)ws_size;
    (void)hipFuncSetAttribute((const void*)gmix_kernel, hipFuncAttributeMaxDynamicSharedMemorySize, L_END);
    gmix_kernel<<<256, 512, L_END, stream>>>((const char*)d_in[0], (const char*)d_in[1], (const char*)d_in[2], (const char*)d_in[4],
                                             (const char*)d_in[5], (const char*)d_in[6], (char*)d_out, (char*)d_ws);
}
```

```cpp
#define ROTEXPR j+2*p
#include <hip/hip_runtime.h>
#include <hip/hip_bf16.h>

typedef __attribute__((ext_vector_type(8))) __bf16 bf16x8;
typedef __attribute__((ext_vector_type(16))) __bf16 bf16x16;
typedef __attribute__((ext_vector_type(16))) float f32x16;
typedef __attribute__((ext_vector_type(8))) float f32x8;
typedef __attribute__((ext_vector_type(4))) float f32x4;
typedef __attribute__((ext_vector_type(4))) unsigned u32x4;
typedef __attribute__((ext_vector_type(2))) unsigned u32x2;
typedef __attribute__((ext_vector_type(2))) __bf16 bf16x2;
typedef short s16x4 __attribute__((ext_vector_type(4)));
typedef unsigned long long u64;
#define GAS __attribute__((address_space(1)))
#define LAS __attribute__((address_space(3)))
typedef LAS const char* lds_cptr;
typedef LAS char* lds_ptr;

constexpr unsigned WS_EPOCH = 0;
#define FSTR 8
constexpr unsigned WS_OFLAG = 65536;
constexpr unsigned WS_KVFLAG = 131072;
constexpr unsigned WS_KV = 1u << 20;
constexpr unsigned TS = 2176;
constexpr unsigned KV_BYTES = 2u * 32u * 64u * TS;
constexpr unsigned WS_O = 10u << 20;
constexpr unsigned O_BYTES = 4u << 20;
constexpr unsigned WS_STAMP = 16u << 20;
constexpr unsigned OOB = 0x7ff00000u;
constexpr unsigned L_W = 65536;
constexpr unsigned L_WO = 0, L_PART = 65536, L_PL = 114688, L_CNT = 117760, L_MDIAG = 118784, L_MFAR = 122880, L_Q = 126976, L_GW = 143360, L_Z = 147456, L_G = 148480, L_Q14 = 106496, L_Q15 = 148992, L_END = 157184;
constexpr float QSCALE = 0.25f * 1.4426950408889634f;
constexpr float LMAX = 1.2676506e30f, LMIN = 9.5367432e-7f;
constexpr unsigned SPIN_CAP = 1u << 17;

#define SBAR() __builtin_amdgcn_sched_barrier(0)
#define PIN(x) asm volatile("" : "+v"(x))
#define MFMA(a, b, c) __builtin_amdgcn_mfma_f32_32x32x16_bf16(a, b, c, 0, 0, 0)
#define BAR() asm volatile("s_barrier" ::: "memory")
#define CBAR() asm volatile("s_waitcnt lgkmcnt(0)\n\ts_barrier" ::: "memory")
#define DRAIN() asm volatile("s_waitcnt vmcnt(0)" ::: "memory")
#define PACEN 3
#define PACE() asm volatile("s_nop %0" :: "i"(PACEN) : "memory")
#define RLX_AGENT __ATOMIC_RELAXED, __HIP_MEMORY_SCOPE_AGENT
#define STAMP(i)

__device__ __forceinline__ void glds16(unsigned voff, const void* sbase, unsigned ldsaddr) {
    asm volatile("s_mov_b32 m0, %2\n\ts_nop 0\n\tglobal_load_lds_dwordx4 %0, %1" ::"v"(voff), "s"(sbase), "s"(ldsaddr) : "memory");
}
__device__ __forceinline__ void glds16x2(unsigned v0, unsigned v1, const void* sbase, unsigned ldsaddr) {
    asm volatile("s_mov_b32 m0, %3\n\ts_nop 0\n\tglobal_load_lds_dwordx4 %0, %2\n\tglobal_load_lds_dwordx4 %1, %2 offset:1024"
                 ::"v"(v0), "v"(v1), "s"(sbase), "s"(ldsaddr) : "memory");
}
__device__ __forceinline__ void glds16x4(unsigned v0, unsigned v1, unsigned v2, unsigned v3, const void* sbase, unsigned ldsaddr) {
    asm volatile("s_mov_b32 m0, %5\n\ts_nop 0\n\tglobal_load_lds_dwordx4 %0, %4\n\tglobal_load_lds_dwordx4 %1, %4 offset:1024\n\t"
                 "global_load_lds_dwordx4 %2, %4 offset:2048\n\tglobal_load_lds_dwordx4 %3, %4 offset:3072"
                 ::"v"(v0), "v"(v1), "v"(v2), "v"(v3), "s"(sbase), "s"(ldsaddr) : "memory");
}
__device__ __forceinline__ unsigned cvtpk(float lo, float hi) {
    typedef __attribute__((ext_vector_type(2))) float f32x2;
    return __builtin_bit_cast(unsigned, __builtin_convertvector((f32x2){lo, hi}, bf16x2));
}
__device__ __forceinline__ bf16x8 as_frag(u32x4 v) { return __builtin_bit_cast(bf16x8, v); }
__device__ __forceinline__ bf16x8 lds_frag(lds_cptr p) { return *(const LAS bf16x8*)p; }
__device__ __forceinline__ bf16x8 tr_frag(lds_cptr p0, lds_cptr p1) {
    s16x4 a = __builtin_amdgcn_ds_read_tr16_b64_v4i16((LAS s16x4*)p0);
    s16x4 b = __builtin_amdgcn_ds_read_tr16_b64_v4i16((LAS s16x4*)p1);
    return __builtin_bit_cast(bf16x8, __builtin_shufflevector(a, b, 0, 1, 2, 3, 4, 5, 6, 7));
}
__device__ __forceinline__ f32x16 pv_lo(bf16x8 v, const f32x16& p, f32x16 acc, int idx) {
    f32x8 h = __builtin_shufflevector(p, p, 0, 1, 2, 3, 4, 5, 6, 7);
    return __builtin_amdgcn_smfmac_f32_32x32x32_bf16(v, __builtin_bit_cast(bf16x16, h), acc, idx, 0, 0);
}
__device__ __forceinline__ f32x16 pv_hi(bf16x8 v, const f32x16& p, f32x16 acc, int idx) {
    f32x8 h = __builtin_shufflevector(p, p, 8, 9, 10, 11, 12, 13, 14, 15);
    return __builtin_amdgcn_smfmac_f32_32x32x32_bf16(v, __builtin_bit_cast(bf16x16, h), acc, idx, 0, 0);
}
#define MX3(a, b, c) __builtin_fmaxf(__builtin_fmaxf((a), (b)), (c))
__device__ __forceinline__ float max16(const f32x16& s) {
    float a = MX3(s[0], s[1], s[2]), b = MX3(s[3], s[4], s[5]);
    a = MX3(a, s[6], s[7]); b = MX3(b, s[8], s[9]); a = MX3(a, s[10], s[11]); b = MX3(b, s[12], s[13]);
    return MX3(a, b, __builtin_fmaxf(s[14], s[15]));
}
__device__ __forceinline__ float halfmax(float m) {
    auto r = __builtin_amdgcn_permlane32_swap(__float_as_uint(m), __float_as_uint(m), false, false);
    return __builtin_fmaxf(__uint_as_float(r[0]), __uint_as_float(r[1]));
}
__device__ __forceinline__ f32x16 lds_f32x16(lds_cptr p) {
    f32x4 a = *(const LAS f32x4*)p, b = *(const LAS f32x4*)(p + 1024), c = *(const LAS f32x4*)(p + 2048), d = *(const LAS f32x4*)(p + 3072);
    f32x8 lo = __builtin_shufflevector(a, b, 0, 1, 2, 3, 4, 5, 6, 7), hi = __builtin_shufflevector(c, d, 0, 1, 2, 3, 4, 5, 6, 7);
    return __builtin_shufflevector(lo, hi, 0, 1, 2, 3, 4, 5, 6, 7, 8, 9, 10, 11, 12, 13, 14, 15);
}
#define BLOAD(rs, vo, so) as_frag(__builtin_amdgcn_raw_buffer_load_b128(rs, vo, so, 16))
#define BSTORE(v, rs, vo, so) __builtin_amdgcn_raw_buffer_store_b128(v, rs, (vo) + (so), 0, 16)

#define POLLSLP 8
#define BSLP 36
__device__ __forceinline__ void wait_tags_end(u64 v, const GAS u64* p, u64 tag, GAS u64* pub, int lane) {
    if (pub) { DRAIN(); if (lane == 0) __hip_atomic_store(pub, tag, RLX_AGENT); }
    if (__builtin_expect(!__all(v == tag), 0)) {
#pragma nounroll
        for (unsigned spins = 0; spins < SPIN_CAP; ++spins) {
            __builtin_amdgcn_s_sleep(POLLSLP);
            if (__all(__hip_atomic_load(p, RLX_AGENT) == tag)) break;
        }
    }
    asm volatile("" ::: "memory");
}

__device__ __forceinline__ void wait_tags(const GAS u64* p, u64 tag, GAS u64* pub = nullptr, int lane = 0) {
    wait_tags_end(__hip_atomic_load(p, RLX_AGENT), p, tag, pub, lane);
}

__device__ __forceinline__ void cnt_arrive(lds_ptr cnt, int lane) {
    asm volatile("s_waitcnt lgkmcnt(0)" ::: "memory");
    if (lane == 0) __hip_atomic_fetch_add((LAS unsigned*)cnt, 1u, __ATOMIC_RELAXED, __HIP_MEMORY_SCOPE_WORKGROUP);
}
__device__ __forceinline__ void cnt_wait(lds_ptr cnt) {
#pragma nounroll
    for (unsigned spins = 0; spins < (1u << 22); ++spins) {
        if (__builtin_amdgcn_readfirstlane(__hip_atomic_load((LAS unsigned*)cnt, __ATOMIC_RELAXED, __HIP_MEMORY_SCOPE_WORKGROUP)) == 4) break;
        __builtin_amdgcn_s_sleep(1);
    }
    asm volatile("" ::: "memory");
}
__device__ __forceinline__ unsigned kvflag(int br, int quad, int tile, int type) {
    return WS_KVFLAG + (unsigned)((((br * 8 + quad) * 4 + (tile & 3)) * 2 + type) * 16 + (tile >> 2)) * FSTR;
}

__device__ __forceinline__ void slow_rows(__amdgpu_buffer_rsrc_t rs, unsigned sbase, int lo, int hi, int g, bool band, bf16x8 q,
                                          unsigned vk, unsigned vv0, unsigned vv1, lds_cptr lds, int lane, int idx, f32x16& acc) {
    float m = -__builtin_inff();
    for (int pass = 0; pass < 2; ++pass) {
        if (pass) { m = halfmax(m); acc = f32x16{}; }
        for (int kt = lo; kt <= hi; ++kt) {
            const unsigned so = sbase + (unsigned)kt * TS;
            const bf16x8 k = BLOAD(rs, vk, so), v0 = BLOAD(rs, vv0, so), v1 = BLOAD(rs, vv1, so);
            f32x16 c = f32x16{};
            if (band && kt == g) c = lds_f32x16(lds + L_MDIAG + lane * 16);
            if (band && kt == g - 8) c = lds_f32x16(lds + L_MFAR + lane * 16);
            f32x16 s = MFMA(k, q, c);
            if (!pass) m = __builtin_fmaxf(m, max16(s));
            else {
#pragma unroll
                for (int r = 0; r < 16; ++r) s[r] = __builtin_amdgcn_exp2f(s[r] - m);
                acc = pv_lo(v0, s, acc, idx); acc = pv_hi(v1, s, acc, idx);
            }
        }
    }
}

#define PK4(A, o) ((u32x4){cvtpk(A[o], A[o + 1]), cvtpk(A[o + 2], A[o + 3]), cvtpk(A[o + 4], A[o + 5]), cvtpk(A[o + 6], A[o + 7])})
#define PROF_ARG
#define PROF_PASS
template <int E>
__device__ __forceinline__ u32x4 pack_kv(int i, const f32x16& a) {
    const int s = i & 1;
    if (E == 0) return PK4(a, 8 * s);
    auto x = __builtin_amdgcn_permlane32_swap(cvtpk(a[8 * s], a[8 * s + 1]), cvtpk(a[8 * s + 4], a[8 * s + 5]), false, false);
    auto y = __builtin_amdgcn_permlane32_swap(cvtpk(a[8 * s + 2], a[8 * s + 3]), cvtpk(a[8 * s + 6], a[8 * s + 7]), false, false);
    return (u32x4){x[0], y[0], x[1], y[1]};
}
#define PUT_KV(i, v, vo) BSTORE(v, rkv, vo, E ? ((i) & 1) * 512 + ((i) >> 1) * (128 * TS) : (i) * (64 * TS))
template <int E>
__device__ __forceinline__ void project(lds_ptr lds, char* ws, __amdgpu_buffer_rsrc_t rkv, __amdgpu_buffer_rsrc_t ro, int lane, int t, int p, int j, u64& tag PROF_ARG) {
    const int r32 = lane & 31, hh = lane >> 5;
    const unsigned q4 = (lane & 15) >> 2, wcol = ((lane >> 4) & 1) * 32 + (lane & 3) * 8;
    lds_cptr xa[4];
#pragma unroll
    for (int ks = 0; ks < 4; ++ks) xa[ks] = lds + (32 * t + r32) * 128 + (((hh ^ ((r32 >> 1) & 7)) * 16) ^ (ks * 32));
    unsigned w0o = L_W + (8 * hh + q4) * 128 + (q4 >> 1) * 64 + wcol, w1o = w0o ^ 64;
    PIN(w0o); PIN(w1o);
    const lds_cptr w0 = lds + w0o, w1 = lds + w1o, wq = lds + (8 * hh + q4) * 64 + wcol;
#define XRD(n) lds_frag(xa[(n) & 3] + (((n) >> 2) & 3) * 16384)
#define WRD(b, n) tr_frag(b + ((n) & 15) * 2048, b + ((n) & 15) * 2048 + 512)
#define YOF(n) ((n) >= 28 ? 32768 + ((n) & 3) * 2048 : ((n) & 15) * 2048)
#define YRD(b, n) tr_frag(b + YOF(n), b + YOF(n) + 512)
#define QOF(br, n) (((n) >= 28 ? L_Q15 - L_Q14 : ((n) >= 24 ? 0 : (((n) >> 2) & 3) * 16384)) + (br) * 4096 + ((n) & 3) * 1024)
#define QRD(br, n) tr_frag(((n) >= 24 ? wqh : wq) + QOF(br, n), ((n) >= 24 ? wqh : wq) + QOF(br, n) + 256)
#define MM(W, X, A) (E ? MFMA(X, W, A) : MFMA(W, X, A))
    bf16x8 xf[32], f0[4], f1[4];
    f32x16 a0 = f32x16{}, a1 = f32x16{};
    unsigned gwo = (lane & 15) == ((lane >> 4) & 1) ? L_GW + t * 1024 + hh * 16 : L_Z;
    PIN(gwo);
    f32x4 zg = f32x4{};
    bf16x8 gf[4];
#define RD1(n) do { xf[n] = XRD(n); f0[(n) & 3] = WRD(w0, n); f1[(n) & 3] = WRD(w1, n); SBAR(); } while (0)
#define GRD(n) gf[(n) & 3] = lds_frag(lds + gwo + (n) * 32)
#define GMM(n) zg = __builtin_amdgcn_mfma_f32_16x16x32_bf16(gf[(n) & 3], xf[n], zg, 0, 0, 0)
#define MM1(n) do { a0 = MM(f0[(n) & 3], xf[n], a0); a1 = MM(f1[(n) & 3], xf[n], a1); SBAR(); } while (0)
#pragma unroll
    for (int s = 0; s < 8; ++s) {
        if (s == 0) CBAR(); else BAR();
        SBAR();
        RD1(4 * s); RD1(4 * s + 1);
        if (s) { MM1(4 * s - 1); GMM(4 * s - 4); GMM(4 * s - 3); GMM(4 * s - 2); GMM(4 * s - 1); SBAR(); }
        RD1(4 * s + 2); MM1(4 * s); RD1(4 * s + 3); MM1(4 * s + 1);
        if (s == 0) DRAIN();
        GRD(4 * s); GRD(4 * s + 1); GRD(4 * s + 2); GRD(4 * s + 3); SBAR();
        MM1(4 * s + 2);
        if (s == 2) { if (E) BSTORE(((u32x4){0x3f803f80u, 0x3f803f80u, 0x3f803f80u, 0x3f803f80u}), rkv,
                                  (unsigned)((hh * 32 + 4 * (p >> 1) + (r32 >> 3)) * 64 + 4 * j + t) * TS + 2048 + (lane & 7) * 16, 0); }
        if (s == 4) BSTORE(((u32x4){~0u, ~0u, ~0u, ~0u}), ro, lane * 16, (unsigned)((4 * j + t) * 32 + 2 * p) * 1024);
        SBAR();
    }
    const unsigned tile = 4 * j + t;
    unsigned vo = (unsigned)(4 * (p >> 1) * 64 + tile) * TS + (E ? (r32 >> 4) * (64 * TS) + 1024 + hh * 256 + (r32 & 15) * 16 : lane * 16);
    unsigned wqo = L_Q14 + (8 * hh + q4) * 64 + wcol;
    PIN(vo); PIN(wqo);
    const lds_cptr wqh = lds + wqo;
    MM1(31); GMM(28); GMM(29); GMM(30); GMM(31); SBAR();
#define PRE8 2
#define ST8 4
#define PUBN 9
    u32x4 pk[4];
#pragma unroll
    for (int i = 0; i < PRE8; ++i) { pk[i] = pack_kv<E>(i, i < 2 ? a0 : a1); PUT_KV(i, pk[i], vo); }
    SBAR();
    BAR(); SBAR();
    bf16x8 u0 = QRD(0, 0), u1 = QRD(1, 0), y0 = QRD(0, 1), y1 = QRD(1, 1);
    const u32x2 tg = *(const LAS u32x2*)(lds + L_CNT + 16);
    f32x16 b0 = f32x16{}, b1 = f32x16{}, aQ0 = f32x16{}, aQ1 = f32x16{};
    SBAR();
#define XPK(i) do { if ((i) >= PRE8) { pk[i] = pack_kv<E>(i, (i) < 2 ? a0 : a1); PIN(pk[i]); if ((i) < ST8) PUT_KV(i, pk[i], vo); SBAR(); } } while (0)
    XPK(0);
    aQ0 = MFMA(u0, xf[0], aQ0); SBAR(); u0 = QRD(0, 2); SBAR(); XPK(1);
    aQ1 = MFMA(u1, xf[0], aQ1); SBAR(); u1 = QRD(1, 2); SBAR();
    aQ0 = MFMA(y0, xf[1], aQ0); SBAR(); y0 = QRD(0, 3); SBAR(); XPK(2);
    aQ1 = MFMA(y1, xf[1], aQ1); SBAR(); y1 = QRD(1, 3); SBAR();
    aQ0 = MFMA(u0, xf[2], aQ0); SBAR(); u0 = YRD(w0, 0); SBAR(); XPK(3);
    aQ1 = MFMA(u1, xf[2], aQ1); SBAR(); u1 = YRD(w1, 0); SBAR();
    aQ0 = MFMA(y0, xf[3], aQ0); SBAR(); y0 = YRD(w0, 1); SBAR();
    float ge0 = __expf(-zg[0]), ge1 = __expf(-zg[1]); PIN(ge0); PIN(ge1); SBAR();
    aQ1 = MFMA(y1, xf[3], aQ1); SBAR(); y1 = YRD(w1, 1); SBAR();
    ge0 = __builtin_amdgcn_rcpf(1.f + ge0); ge1 = __builtin_amdgcn_rcpf(1.f + ge1); PIN(ge0); PIN(ge1); SBAR();
    b0 = MM(u0, xf[0], b0); SBAR(); u0 = YRD(w0, 2); SBAR();
    { LAS float* gp = (LAS float*)(lds + (lane < 16 ? L_G + t * 128 : L_PL)) + lane; gp[0] = ge0; gp[16] = ge1; } SBAR();
    b1 = MM(u1, xf[0], b1); SBAR(); u1 = YRD(w1, 2); SBAR();
    tag = ((u64)(unsigned)__builtin_amdgcn_readfirstlane(tg[1]) << 32) | (unsigned)__builtin_amdgcn_readfirstlane(tg[0]);
    asm volatile("" : "+s"(tag)); SBAR();
    b0 = MM(y0, xf[1], b0); SBAR(); y0 = YRD(w0, 3); SBAR();
    b1 = MM(y1, xf[1], b1); SBAR(); y1 = YRD(w1, 3); SBAR();
    b0 = MM(u0, xf[2], b0); SBAR();
    if (ST8 < 4) { PUT_KV(ST8 & 3, pk[ST8 & 3], vo); SBAR(); }
    BAR(); SBAR();
    u0 = QRD(0, 4); SBAR();
    b1 = MM(u1, xf[2], b1); SBAR(); u1 = QRD(1, 4); SBAR();
    b0 = MM(y0, xf[3], b0); SBAR(); y0 = YRD(w0, 4); SBAR();
    b1 = MM(y1, xf[3], b1); SBAR(); y1 = YRD(w1, 4); SBAR();
#pragma unroll
    for (int n = 4; n < 24; ++n) {
        aQ0 = MFMA(u0, xf[n], aQ0); SBAR();
        if (n == PUBN) { DRAIN(); if (lane == 0) __hip_atomic_store((GAS u64*)(ws + kvflag(0, p >> 1, tile, E)), tag, RLX_AGENT); SBAR(); }
        if ((n & 3) == 3) {
            if (ST8 + (n >> 2) < 4) { PUT_KV((ST8 + (n >> 2)) & 3, pk[(ST8 + (n >> 2)) & 3], vo); SBAR(); }
            BAR(); SBAR();
        }
        u0 = QRD(0, n + 1); SBAR();
        aQ1 = MFMA(u1, xf[n], aQ1); SBAR();
        u1 = n < 23 ? QRD(1, n + 1) : QRD(0, 25); SBAR();
        b0 = MM(y0, xf[n], b0); SBAR(); y0 = n < 23 ? YRD(w0, n + 1) : QRD(0, 26); SBAR();
        b1 = MM(y1, xf[n], b1); SBAR(); y1 = n < 23 ? YRD(w1, n + 1) : QRD(0, 27); SBAR();
    }
    aQ0 = MFMA(u0, xf[24], aQ0); SBAR(); u0 = QRD(0, 28); SBAR(); aQ0 = MFMA(u1, xf[25], aQ0); SBAR(); u1 = QRD(0, 29); SBAR();
    aQ0 = MFMA(y0, xf[26], aQ0); SBAR(); y0 = QRD(0, 30); SBAR(); aQ0 = MFMA(y1, xf[27], aQ0); SBAR(); y1 = QRD(0, 31); SBAR();
    aQ0 = MFMA(u0, xf[28], aQ0); SBAR(); aQ0 = MFMA(u1, xf[29], aQ0); SBAR(); u1 = QRD(1, 24); SBAR();
    aQ0 = MFMA(y0, xf[30], aQ0); SBAR(); y0 = YRD(w0, 24); SBAR(); aQ0 = MFMA(y1, xf[31], aQ0); SBAR(); y1 = YRD(w1, 24); SBAR();
    const lds_ptr qd = lds + L_Q + t * 1024 + lane * 16;
#pragma unroll
    for (int n = 24; n < 32; ++n) {
        aQ1 = MFMA(u1, xf[n], aQ1); SBAR();
        if (n == 24) {
#pragma unroll
            for (int r = 8; r < 16; ++r) aQ0[r] *= QSCALE;
            *(LAS u32x4*)(qd + 4096) = PK4(aQ0, 8);
            __builtin_amdgcn_s_setprio(2);
            CBAR(); STAMP(4); SBAR();
        }
        if (n < 31) u1 = QRD(1, n + 1); PACE(); SBAR();
        b0 = MM(y0, xf[n], b0); SBAR(); if (n < 31) y0 = YRD(w0, n + 1); PACE(); SBAR();
        b1 = MM(y1, xf[n], b1); SBAR(); if (n < 31) y1 = YRD(w1, n + 1); PACE(); SBAR();
    }
#pragma unroll
    for (int r = 0; r < 8; ++r) aQ0[r] *= QSCALE;
#pragma unroll
    for (int r = 0; r < 16; ++r) aQ1[r] *= QSCALE;
    *(LAS u32x4*)qd = PK4(aQ0, 0); *(LAS u32x4*)(qd + 8192) = PK4(aQ1, 0); *(LAS u32x4*)(qd + 12288) = PK4(aQ1, 8);
    cnt_arrive(lds + L_CNT + 8, lane);
    const unsigned vo2 = vo + 32 * 64 * TS;
#define YSLP 8
    PUT_KV(0, pack_kv<E>(0, b0), vo2); if (YSLP) __builtin_amdgcn_s_sleep(YSLP); PUT_KV(1, pack_kv<E>(1, b0), vo2); if (YSLP) __builtin_amdgcn_s_sleep(YSLP);
    PUT_KV(2, pack_kv<E>(2, b1), vo2); if (YSLP) __builtin_amdgcn_s_sleep(YSLP); PUT_KV(3, pack_kv<E>(3, b1), vo2);
    DRAIN();
    if (lane == 0) __hip_atomic_store((GAS u64*)(ws + kvflag(1, p >> 1, tile, E)), tag, RLX_AGENT);
    STAMP(19);
}

#define AB_WO (512 * 1024)
#define WOIN 2
#define CODE_LINES 256

struct Att {
    lds_ptr lds; char* ws; __amdgpu_buffer_rsrc_t rkv, ro;
    int lane, t, p, g, head, idx; u64 tag; unsigned vk, vv0, vv1, so, sBeg, sEnd; lds_cptr zp, qp[4];
};
#define IDX a.idx
#define BLOADL(old, rs, vo, so) BLOAD(rs, vo, so)
#define LD3(K, V0, V1, rs, so) do { K = BLOAD(rs, a.vk, so); V0 = BLOAD(rs, a.vv0, so); V1 = BLOAD(rs, a.vv1, so); } while (0)
#define EX(X, r) X[r] = __builtin_amdgcn_exp2f(X[r])
#define STEP(X, Y, OP, VP, KN, QN, CN, O, V0, F1, F2) do { \
        EX(X, 0); EX(X, 1); SBAR(); OP = pv_hi(VP, Y, OP, IDX); SBAR(); F1; SBAR(); EX(X, 2); EX(X, 3); EX(X, 4); SBAR(); Y = MFMA(KN, QN, CN); SBAR(); \
        EX(X, 5); EX(X, 6); EX(X, 7); SBAR(); EX(X, 8); EX(X, 9); SBAR(); O = pv_lo(V0, X, O, IDX); SBAR(); F2; SBAR(); \
        EX(X, 10); EX(X, 11); EX(X, 12); EX(X, 13); EX(X, 14); EX(X, 15); SBAR(); } while (0)

__device__ __forceinline__ f32x16 lds_zero16(lds_cptr zp) {
    const volatile LAS f32x4* z = (const volatile LAS f32x4*)zp;
    const f32x4 a = *z, b = *z, c = *z, d = *z;
    f32x8 lo = __builtin_shufflevector(a, b, 0, 1, 2, 3, 4, 5, 6, 7), hi = __builtin_shufflevector(c, d, 0, 1, 2, 3, 4, 5, 6, 7);
    return __builtin_shufflevector(lo, hi, 0, 1, 2, 3, 4, 5, 6, 7, 8, 9, 10, 11, 12, 13, 14, 15);
}

template <int C, int BR>
__device__ __forceinline__ GAS u64* finish(const Att& a, f32x16 oo, float lrest, bf16x8 qa) {
    const float gt = *(const LAS float*)(a.lds + L_G + a.t * 128 + (a.lane & 31) * 4);
    auto lsw = __builtin_amdgcn_permlane32_swap(__float_as_uint(oo[8]), __float_as_uint(oo[8]), false, false);
    float l = __uint_as_float(lsw[0]) + lrest, z = 0.f;
#pragma unroll
    for (int r = 0; r < 8; ++r) z = __builtin_fmaf(oo[r], 0.f, z);
    if (__builtin_expect(__any(!(l + z < LMAX && l > LMIN)), 0)) {
        if (!BR) { wait_tags((const GAS u64*)(a.ws + kvflag(0, a.p >> 1, 0, 0) + a.lane * FSTR), a.tag); wait_tags((const GAS u64*)(a.ws + kvflag(0, a.p >> 1, 2, 0) + a.lane * FSTR), a.tag); }
        slow_rows(a.rkv, (unsigned)((BR * 32 + a.head) * 64) * TS, BR ? (a.g >= 8 ? a.g - 8 : 0) : 0, BR ? a.g : 63, a.g, BR, qa, a.vk, a.vv0, a.vv1, a.lds, a.lane, IDX, oo);
        auto l2 = __builtin_amdgcn_permlane32_swap(__float_as_uint(oo[8]), __float_as_uint(oo[8]), false, false);
        l = __uint_as_float(l2[0]);
    }
    const float il = __builtin_amdgcn_rcpf(l) * (BR ? 1.f - gt : gt);
    u32x4 v = (u32x4){cvtpk(oo[0] * il, oo[1] * il), cvtpk(oo[2] * il, oo[3] * il), cvtpk(oo[4] * il, oo[5] * il), cvtpk(oo[6] * il, oo[7] * il)};
    if (!C && !BR) v = __builtin_elementwise_min(v, (u32x4){~1u, ~1u, ~1u, ~1u});
    BSTORE(v, a.ro, a.vk, (unsigned)((BR * 64 + a.g) * 32 + a.head) * 1024);
    return (GAS u64*)(a.ws + WS_OFLAG) + ((BR * 64 + a.g) * 2 + C) * 16 + a.p;
}

template <int C>
__device__ __forceinline__ GAS u64* attn_full(const Att& a, f32x16 oo, f32x16 o1, f32x16 o2, f32x16 o3, f32x16 S1, __amdgpu_buffer_rsrc_t rwo, unsigned vwo, unsigned wi PROF_ARG) {
    const lds_ptr lds = a.lds; const int t = a.t, lane = a.lane, w = 4 * C + t;
    const f32x16 zc = f32x16{};
    bf16x8 ka, va0, va1, kb, vb0, vb1, kc, vc0, vc1, kd, vd0, vd1;
    const unsigned sBeg = a.sBeg, sEnd = a.sEnd;
    unsigned so = a.so;
#define NEXT_A() asm volatile("s_addk_i32 %0, %1" : "+s"(so) : "i"(4 * TS) : "scc")
#define WRAP_A() asm volatile("s_cmp_eq_u32 %0, %1\n\ts_cselect_b32 %0, %2, %0" : "+s"(so) : "s"(sEnd), "s"(sBeg) : "scc")
#define STEP_A() asm volatile("s_addk_i32 %0, %3\n\ts_cmp_eq_u32 %0, %1\n\ts_cselect_b32 %0, %2, %0" : "+s"(so) : "s"(sEnd), "s"(sBeg), "i"(4 * TS) : "scc")
    LD3(ka, va0, va1, a.rkv, so); STEP_A(); SBAR(); LD3(kb, vb0, vb1, a.rkv, so); STEP_A(); SBAR();
    LD3(kc, vc0, vc1, a.rkv, so); STEP_A(); SBAR();
    vd1 = lds_frag(a.zp);
    if (C) { BAR(); STAMP(4); }
    bf16x8 qa = lds_frag(a.qp[0]); SBAR();
    bf16x8 q1 = lds_frag(a.qp[1]), q2 = lds_frag(a.qp[2]), q3 = lds_frag(a.qp[3]);
    SBAR();
    f32x16 S0 = MFMA(ka, qa, zc);
    PIN(S0); SBAR();
    PIN(q1); PIN(q2); PIN(q3); PIN(vd1); PIN(oo); PIN(o1); PIN(o2); PIN(o3); PIN(S1);
    __builtin_amdgcn_s_waitcnt(0x0F76);
#pragma unroll
    for (int i = 0; i < 8; ++i) __builtin_amdgcn_s_setprio(C);
    STAMP(3);
    SBAR();
    unsigned wso = 0;
#define NEXT_AW() do { if (C) asm volatile("s_addk_i32 %0, %3\n\ts_lshl_b32 %1, %2, 4" : "+s"(so), "=s"(wso) : "s"(wi), "i"(4 * TS) : "scc"); else NEXT_A(); } while (0)
#define WO_DMA() do { if (C) __builtin_amdgcn_raw_ptr_buffer_load_lds(rwo, (LAS void*)(lds + L_WO + t * 16384 + wi), 16, vwo, wso, 0, 0); } while (0)
#define WO_NEXT() do { if (C) asm volatile("s_addk_i32 %0, 0x400\n\ts_and_b32 %0, %0, 0x3fff" : "+s"(wi) :: "scc"); } while (0)
#define TILE(K, V0, V1, KN, LK, LV0, LV1, NW) do { \
        STEP(S0, S1, o3, LV1, K, q1, zc, oo, V0, LK = BLOADL(K, a.rkv, a.vk, so), LV0 = BLOADL(V0, a.rkv, a.vv0, so)); \
        STEP(S1, S0, oo, V1, K, q2, zc, o1, V0, LV1 = BLOADL(V1, a.rkv, a.vv1, so), NEXT_AW()); \
        STEP(S0, S1, o1, V1, K, q3, zc, o2, V0, WRAP_A(), WO_DMA()); \
        STEP(S1, S0, o2, V1, KN, qa, zc, o3, V0, __builtin_amdgcn_s_waitcnt(0x0F70 | (C ? NW : 6)), WO_NEXT()); } while (0)
#pragma nounroll
    for (int it = 0; it < 4; ++it) {
        TILE(ka, va0, va1, kb, kd, vd0, vd1, 7);
        TILE(kb, vb0, vb1, kc, ka, va0, va1, 8);
        TILE(kc, vc0, vc1, kd, kb, vb0, vb1, 9);
        TILE(kd, vd0, vd1, ka, kc, vc0, vc1, 9);
    }
    o3 = pv_hi(vd1, S1, o3, IDX);
    STAMP(6);
    if (C) cnt_wait(lds + L_CNT + 8); else __builtin_amdgcn_s_setprio(2);
#define PUT(O, sl) do { lds_ptr d_ = lds + L_PART + (w * 3 + sl) * 2048 + lane * 16; \
        *(LAS f32x4*)d_ = __builtin_shufflevector(O, O, 0, 1, 2, 3); *(LAS f32x4*)(d_ + 1024) = __builtin_shufflevector(O, O, 4, 5, 6, 7); } while (0)
    PUT(o1, 0); PUT(o2, 1); PUT(o3, 2);
    if (lane < 32) { LAS float* lp = (LAS float*)(lds + L_PL + w * 384) + lane; lp[0] = o1[8]; lp[32] = o2[8]; lp[64] = o3[8]; }
    if (C) { DRAIN(); cnt_arrive(lds + L_CNT, lane); cnt_wait(lds + L_CNT); } else CBAR();
    float lrest = 0.f;
#pragma unroll
    for (int s = 0; s < 3; ++s) {
        const int src = (C * 4 + ((t - 1 - s) & 3)) * 3 + s;
        const lds_cptr d = lds + L_PART + src * 2048 + lane * 16;
        const f32x4 x = *(const LAS f32x4*)d, y = *(const LAS f32x4*)(d + 1024);
#pragma unroll
        for (int r = 0; r < 4; ++r) { oo[r] += x[r]; oo[4 + r] += y[r]; }
        const float lp = *(const LAS float*)(lds + L_PL + src * 128 + (lane & 31) * 4);
        lrest = s ? lrest + lp : lp;
    }
    return finish<C, 0>(a, oo, lrest, qa);
}

template <int C>
__device__ __forceinline__ GAS u64* attn_window(const Att& a, GAS u64* pub PROF_ARG) {
    const lds_ptr lds = a.lds; const int t = a.t, lane = a.lane, g = a.g;
    const f32x16 zc = f32x16{};
    { const int i = lane & 15, kt = g - (i < 9 ? i : 0);
      wait_tags((const GAS u64*)(a.ws + kvflag(1, a.p >> 1, kt >= 0 ? kt : 0, (lane >> 4) & 1)), a.tag, pub, lane); }
    STAMP(25);
    bf16x8 k[9], v0[9], v1[9];
    const unsigned sB = (unsigned)((32 + a.head) * 64) * TS;
#define LDB(i) do { const int kt_ = g - (i); \
        const __amdgpu_buffer_rsrc_t rb_ = __builtin_amdgcn_make_buffer_rsrc(a.ws + WS_KV, 0, kt_ >= 0 ? KV_BYTES : 0u, 0x00020000); \
        LD3(k[i], v0[i], v1[i], rb_, sB + (unsigned)(kt_ >= 0 ? kt_ : 0) * TS); } while (0)
    LDB(0); SBAR(); LDB(1); SBAR(); LDB(2); SBAR();
    const bf16x8 qa = lds_frag(lds + L_Q + 8192 + C * 4096 + t * 1024 + lane * 16);
    const f32x16 cd = lds_f32x16(lds + L_MDIAG + lane * 16), cf = lds_f32x16(lds + L_MFAR + lane * 16);
    f32x16 oo = lds_zero16(a.zp);
    f32x16 S0 = MFMA(k[0], qa, cd), S1 = MFMA(k[1], qa, zc);
    SBAR(); LDB(3); SBAR();
#pragma unroll
    for (int r = 0; r < 10; ++r) { EX(S0, r); SBAR(); }
    oo = pv_lo(v0[0], S0, oo, IDX); SBAR();
#pragma unroll
    for (int r = 10; r < 16; ++r) EX(S0, r);
    SBAR();
    STEP(S1, S0, oo, v1[0], k[2], qa, zc, oo, v0[1], LDB(4), );
    STEP(S0, S1, oo, v1[1], k[3], qa, zc, oo, v0[2], LDB(5), );
    STEP(S1, S0, oo, v1[2], k[4], qa, zc, oo, v0[3], LDB(6), );
    STEP(S0, S1, oo, v1[3], k[5], qa, zc, oo, v0[4], LDB(7), );
    STEP(S1, S0, oo, v1[4], k[6], qa, zc, oo, v0[5], LDB(8), );
    STEP(S0, S1, oo, v1[5], k[7], qa, zc, oo, v0[6], , );
    STEP(S1, S0, oo, v1[6], k[8], qa, cf, oo, v0[7], , );
    STEP(S0, S1, oo, v1[7], k[8], qa, cf, oo, v0[8], , );
    oo = pv_hi(v1[8], S0, oo, IDX);
    STAMP(7);
    return finish<C, 1>(a, oo, 0.f, qa);
}

__global__ void __launch_bounds__(512, 2)
gmix_kernel(const char* x, const char* wA, const char* woA, const char* wB, const char* woB, const char* gw, char* out, char* ws) {
    extern __shared__ __attribute__((aligned(16))) char smem[];
    const lds_ptr lds = (lds_ptr)smem;
    const unsigned lds0 = (unsigned)(uintptr_t)lds;
    const int tid = threadIdx.x, lane = tid & 63, r32 = lane & 31, hh = lane >> 5;
    const int w = __builtin_amdgcn_readfirstlane(tid >> 6);
    const int bid = blockIdx.x, xcd = bid & 7, rr = bid >> 3;
    const int p = 4 * (xcd & 3) + (rr & 3), j = 8 * (xcd >> 2) + (rr >> 2);
    const int jo = 2 * xcd + (rr >> 4), po = rr & 15;
    const int t = w & 3, c = w >> 2;
    const int e = p & 1;
    STAMP(0);
    {
        u64 pc; const unsigned ln = ((bid & ~7) | (tid >> 6)) * 128;
        asm volatile("s_getpc_b64 %0\n\ts_mov_b32 m0, %2\n\ts_nop 0\n\tglobal_load_lds_dword %1, %0" : "=&s"(pc) : "v"(ln), "s"(lds0 + L_PL) : "memory");
        SBAR();
    }
    unsigned rot = (ROTEXPR) & 7;
    unsigned vx0 = (128 * j + 32 * t + (lane >> 3)) * 1024 + (((lane & 7) ^ (lane >> 4)) * 16), vx1 = (vx0 ^ 64) + 7168, vx2 = vx0 + 14336, vx3 = vx1 + 14336;
    unsigned vw0 = (16 * t + (lane >> 3)) * 3072 + 1024 + 1024 * e + 128 * (p >> 1) + (((lane & 7) ^ (((lane >> 4) & 1) << 2)) * 16), vw1 = vw0 + 8 * 3072 - 1024;
    unsigned mx = lds0 + t * 4096, mw = lds0 + L_W + t * 2048, ow = rot * (64 * 3072);
    asm volatile("" : "+v"(vx0), "+v"(vx1), "+v"(vx2), "+v"(vx3), "+v"(vw0), "+v"(vw1));
    asm volatile("" : "+s"(mx), "+s"(mw), "+s"(rot), "+s"(ow));
    SBAR();
    asm volatile("" : "+s"(x), "+s"(wA), "+s"(woA), "+s"(wB), "+s"(woB), "+s"(gw), "+s"(out), "+s"(ws));
#define MK_ATT(C) Att a; a.lds = lds; a.ws = ws; a.lane = lane; a.t = t; a.p = p; a.g = 4 * j + t; a.head = 2 * p + C; a.idx = 0xdddddddd; \
    a.rkv = __builtin_amdgcn_make_buffer_rsrc(ws + WS_KV, 0, KV_BYTES, 0x00020000); a.ro = __builtin_amdgcn_make_buffer_rsrc(ws + WS_O, 0, O_BYTES, 0x00020000); \
    a.vk = lane * 16; a.vv0 = r32 < 16 ? 1024 + hh * 256 + r32 * 16 : (r32 == 16 ? 2048 : OOB); a.vv1 = r32 < 16 ? a.vv0 + 512 : a.vv0; a.zp = lds + L_Z + lane * 16; \
    a.sBeg = (unsigned)(a.head * 64 + t) * TS; a.sEnd = a.sBeg + 64 * TS; a.so = (unsigned)(a.head * 64 + a.g) * TS; \
    _Pragma("unroll") for (int i = 0; i < 4; ++i) a.qp[i] = lds + L_Q + C * 4096 + ((t + i) & 3) * 1024 + lane * 16
    if (c) {
#define DMA1(s) do { const unsigned ks_ = ((s) + rot) & 7; \
        glds16x4(vx0, vx1, vx2, vx3, x + ks_ * 128, mx + ((s) & 3) * 16384); glds16x2(vw0, vw1, wA + ks_ * (64 * 3072), mw + ((s) & 3) * 8192); } while (0)
#define DMA2(s) do { const unsigned ks_ = (((s) + rot) & 7) * (64 * 3072); \
        glds16x2(vq0, vq1, wq + ks_, mq + ((s) == 14 ? L_Q14 : ((s) & 3) * 16384)); glds16x2(vw0, vw1, wB + ks_, mw + ((s) & 3) * 8192); SBAR(); } while (0)
#define LBAR(N) asm volatile("s_waitcnt vmcnt(" #N ")\n\ts_barrier" ::: "memory")
        STAMP(20);
        glds16x4(vx0, vx1, vx2, vx3, x + rot * 128, mx); glds16x2(vw0, vw1, wA + ow, mw);
        SBAR();
        const unsigned k1 = (1 + rot) & 7;
        glds16x2(vx0, vx1, x + k1 * 128, mx + 16384);
        SBAR();
        const u64 ep = *(const __attribute__((address_space(4))) u64*)(ws + WS_EPOCH);
        unsigned vq0 = (32 * (t & 1) + (lane >> 2)) * 3072 + 64 * p + (lane & 3) * 16, vq1 = vq0 + 16 * 3072 - 1024;
        unsigned vwo = ((t & 1) * 256 + (lane >> 2)) * 1024 + 64 * po + (lane & 3) * 16;
        MK_ATT(1);
        const GAS u64* fp = (const GAS u64*)(ws + kvflag(0, p >> 1, t, 0) + r32 * FSTR);
        PIN(vq0); PIN(vq1); PIN(vwo); PIN(a.vk); PIN(a.vv0); PIN(a.vv1); PIN(a.qp[0]); PIN(a.qp[1]); PIN(a.qp[2]); PIN(a.qp[3]); PIN(a.zp); PIN(fp); PIN(a.idx);
        SBAR();
        STAMP(30);
        glds16x2(vx0, vx1, x + k1 * 128 + 16384, mx + 16384 + 2048); glds16x2(vw0, vw1, wA + k1 * (64 * 3072), mw + 8192);
        STAMP(22);
        SBAR();
#pragma nounroll
        for (int s = 2; s < 8; ++s) { LBAR(6); DMA1(s);
        }
        const unsigned mq = lds0 + t * 2048;
        const char* wq = (t >> 1) ? wB : wA;
        LBAR(6); STAMP(14); DMA2(8);
        LBAR(4); STAMP(15); DMA2(9);
        a.tag = ep * 0x9E3779B97F4A7C15ull + 0x632BE59BD9B4E019ull;
        *(LAS u64*)(lds + L_CNT + 16) = a.tag;
        asm volatile("s_waitcnt lgkmcnt(0)" ::: "memory");
        LBAR(4); STAMP(16); DMA2(10);
        const unsigned k7 = ((7 + rot) & 7) * (64 * 3072), sx = mq + L_Q15, sw = mw + 32768;
        LBAR(4); STAMP(17); glds16(vq0, wq + k7, sx); DMA2(11);
        f32x16 oo = lds_zero16(a.zp), o1 = lds_zero16(a.zp); PIN(oo); PIN(o1);
        LBAR(5); STAMP(18); glds16(vw0, wB + k7, sw); DMA2(12);
        f32x16 o2 = lds_zero16(a.zp), o3 = lds_zero16(a.zp); PIN(o2); PIN(o3);
        LBAR(5); STAMP(19); glds16(vq1, wq + k7 + 1024, sx + 1024); DMA2(13);
        f32x16 S1 = lds_zero16(a.zp); PIN(S1);
        LBAR(5); STAMP(21); glds16(vw1, wB + k7 + 1024, sw + 1024); DMA2(14);
        __amdgpu_buffer_rsrc_t rwo = __builtin_amdgcn_make_buffer_rsrc((void*)((t >> 1) ? woB : woA), 0, AB_WO, 0x00020000);
        unsigned wi0 = 4096 * (((rr >> 4) << 1) | (po & 1));
        asm volatile("" : "+s"(a.rkv), "+s"(rwo), "+s"(a.so), "+s"(a.sBeg), "+s"(a.sEnd), "+s"(wi0));
        __builtin_amdgcn_s_setprio(1);
        LBAR(5); STAMP(24);
        const u64 fv = __hip_atomic_load(fp, RLX_AGENT);
        LBAR(1); STAMP(29);
        wait_tags_end(fv, fp, a.tag, nullptr, lane);
        STAMP(5);
        GAS u64* pub = attn_full<1>(a, oo, o1, o2, o3, S1, rwo, vwo, wi0 PROF_PASS);
        STAMP(23);
        pub = attn_window<1>(a, pub PROF_PASS);
        STAMP(8);
        const int go = 4 * jo + t;
        const lds_cptr wo = lds + L_WO + (4 * hh + ((lane & 15) >> 2)) * 64 + ((lane >> 4) & 1) * 32 + (lane & 3) * 8;
        const GAS u64* of_ = (const GAS u64*)(ws + WS_OFLAG);
        wait_tags(of_ + ((lane & 16) ? go * 32 + 16 : (64 + go) * 32) + (lane & 15), a.tag, pub, lane);
        STAMP(9);
#define OBR(n) ((n) >> 4 == 1 ? 0 : 1)
#define OHD(n) (2 * ((n) & 15) + ((n) >= 16))
#define OLD(n) BLOAD(a.ro, a.vk, (unsigned)((OBR(n) * 64 + go) * 32 + OHD(n)) * 1024)
#define OWF(n) tr_frag(wo + OBR(n) * 32768 + OHD(n) * 1024, wo + OBR(n) * 32768 + OHD(n) * 1024 + 512)
#define OIN 7
        bf16x8 oe[48];
#pragma unroll
        for (int n = 0; n < 48; ++n) {
            if (n == 32) wait_tags(of_ + (64 + go) * 32 + 16 + (lane & 15), a.tag);
            oe[n] = OLD(n); SBAR();
            if (n >= OIN - 1) __builtin_amdgcn_s_waitcnt(0x0F70 | (OIN - 1));
            SBAR();
        }
        bf16x8 wn[4];
#pragma unroll
        for (int n = 0; n < 4; ++n) { wn[n] = OWF(n); PIN(wn[n]); }
        __builtin_amdgcn_s_waitcnt(0x0F70);
#define NPRE 0
#define NMID 24
        f32x16 ya = f32x16{};
        bf16x8 wf[16], of[16];
#define OMM(n) do { ya = MFMA(wn[(n) & 3], oe[n], ya); SBAR(); if ((n) + 4 < 48) wn[(n) & 3] = OWF((n) + 4); \
            if ((n) >= 32) wf[(n) - 32] = tr_frag(wo + 2 * ((n) - 32) * 1024, wo + 2 * ((n) - 32) * 1024 + 512); SBAR(); } while (0)
#pragma unroll
        for (int n = 0; n < NPRE; ++n) { OMM(n); PACE(); }
        STAMP(10);
        BAR();
        BAR();
#pragma unroll
        for (int n = NPRE; n < NMID; ++n) OMM(n);
        BAR();
        STAMP(11);
#define GRP_BAD(q) ({ unsigned mx_ = 0; _Pragma("unroll") for (int i = 4 * (q); i < 4 * (q) + 4; ++i) { const u32x4 u_ = __builtin_bit_cast(u32x4, of[i]); \
            mx_ = __builtin_elementwise_max(__builtin_elementwise_max(mx_, u_[0]), u_[1]); mx_ = __builtin_elementwise_max(__builtin_elementwise_max(mx_, u_[2]), u_[3]); } \
            __any(mx_ == ~0u); })
#define GRP_MM(q) do { _Pragma("unroll") for (int i = 4 * (q); i < 4 * (q) + 4; ++i) ya = MFMA(wf[i], of[i], ya); } while (0)
#define GRP_LD(q) do { _Pragma("unroll") for (int i = 4 * (q); i < 4 * (q) + 4; ++i) of[i] = BLOAD(a.ro, a.vk, (unsigned)(go * 32 + 2 * i) * 1024); } while (0)
#pragma unroll
        for (int n = NMID; n < 48; ++n) { if (n - NMID < 16) { of[n - NMID] = BLOAD(a.ro, a.vk, (unsigned)(go * 32 + 2 * (n - NMID)) * 1024); SBAR(); } OMM(n); }
        STAMP(31);
        int done = 0;
        if (!GRP_BAD(0)) { GRP_MM(0); done = 1; if (!GRP_BAD(1)) { GRP_MM(1); done = 2; if (!GRP_BAD(2)) { GRP_MM(2); done = 3; if (!GRP_BAD(3)) { GRP_MM(3); done = 4; } } } }
        if (done < 4) {
#pragma nounroll
            for (unsigned spins = 0; spins < SPIN_CAP; ++spins) {
                const bool b0 = GRP_BAD(0), b1 = GRP_BAD(1), b2 = GRP_BAD(2), b3 = GRP_BAD(3);
                if (!(b0 || b1 || b2 || b3)) break;
                asm volatile("" ::: "memory");
                if (b0) GRP_LD(0); if (b1) GRP_LD(1); if (b2) GRP_LD(2); if (b3) GRP_LD(3);
            }
            if (done < 1) GRP_MM(0); if (done < 2) GRP_MM(1); if (done < 3) GRP_MM(2); GRP_MM(3);
        }
#pragma unroll
        for (int s = 0; s < 2; ++s) {
            auto u = __builtin_amdgcn_permlane32_swap(cvtpk(ya[8 * s], ya[8 * s + 1]), cvtpk(ya[8 * s + 4], ya[8 * s + 5]), false, false);
            auto v = __builtin_amdgcn_permlane32_swap(cvtpk(ya[8 * s + 2], ya[8 * s + 3]), cvtpk(ya[8 * s + 6], ya[8 * s + 7]), false, false);
#define OUT_AUX 0
            __builtin_amdgcn_raw_buffer_store_b128(((u32x4){u[0], v[0], u[1], v[1]}), __builtin_amdgcn_make_buffer_rsrc(out, 0, 2048 * 1024, 0x00020000),
                                                   (32 * go + r32) * 1024 + 64 * po + 32 * s + 16 * hh, 0, OUT_AUX);
        }
        if (bid == 0 && tid == 256) __hip_atomic_store((GAS u64*)(ws + WS_EPOCH), a.tag, RLX_AGENT);
        STAMP(12);
    } else {
        glds16((((lane >> 3) + rot) & 7) * 128 + (lane & 7) * 16, gw, lds0 + L_GW + t * 1024);
#pragma unroll
        for (int m = 0; m < 2; ++m) {
            f32x4 mk;
#pragma unroll
            for (int i = 0; i < 4; ++i) { const bool le = (i + 8 * t + 4 * hh) <= r32; mk[i] = (le != (m != 0)) ? 0.f : -__builtin_inff(); }
            *(LAS f32x4*)(lds + L_MDIAG + m * 4096 + t * 1024 + lane * 16) = mk;
        }
        if (tid < 4) *(LAS unsigned*)(lds + L_CNT + tid * 4) = 0;
        *(LAS unsigned*)(lds + L_Z + tid * 4) = 0;
        u64 tag;
        STAMP(21);
        {
            const __amdgpu_buffer_rsrc_t rkv = __builtin_amdgcn_make_buffer_rsrc(ws + WS_KV, 0, KV_BYTES, 0x00020000);
            const __amdgpu_buffer_rsrc_t ro = __builtin_amdgcn_make_buffer_rsrc(ws + WS_O, 0, O_BYTES, 0x00020000);
            if (e) project<1>(lds, ws, rkv, ro, lane, t, p, j, tag PROF_PASS); else project<0>(lds, ws, rkv, ro, lane, t, p, j, tag PROF_PASS);
        }
        cnt_wait(lds + L_CNT + 8);
        __builtin_amdgcn_s_sleep(BSLP);
        STAMP(24);
        MK_ATT(0);
        a.tag = tag;
        GAS u64* pub = attn_window<0>(a, nullptr PROF_PASS);
        STAMP(23);
        wait_tags((const GAS u64*)(ws + kvflag(0, p >> 1, t, 0) + r32 * FSTR), tag, pub, lane);
        STAMP(5);
        attn_full<0>(a, lds_zero16(a.zp), lds_zero16(a.zp), lds_zero16(a.zp), lds_zero16(a.zp), lds_zero16(a.zp), a.rkv, 0, 0 PROF_PASS);
        BAR();
        STAMP(8);
        DRAIN(); BAR();
    }
}

extern "C" void kernel_launch(void* const* d_in, const int* in_sizes, int n_in, void* d_out, int out_size, void* d_ws, size_t ws_size,
                              hipStream_t stream) {
    (void)in_sizes; (void)n_in; (void)out_size; (void)ws_size;
    (void)hipFuncSetAttribute((const void*)gmix_kernel, hipFuncAttributeMaxDynamicSharedMemorySize, L_END);
    gmix_kernel<<<256, 512, L_END, stream>>>((const char*)d_in[0], (const char*)d_in[1], (const char*)d_in[2], (const char*)d_in[4],
                                             (const char*)d_in[5], (const char*)d_in[6], (char*)d_out, (char*)d_ws);
}
```

```cpp
#define PACEN 6
#include <hip/hip_runtime.h>
#include <hip/hip_bf16.h>

typedef __attribute__((ext_vector_type(8))) __bf16 bf16x8;
typedef __attribute__((ext_vector_type(16))) __bf16 bf16x16;
typedef __attribute__((ext_vector_type(16))) float f32x16;
typedef __attribute__((ext_vector_type(8))) float f32x8;
typedef __attribute__((ext_vector_type(4))) float f32x4;
typedef __attribute__((ext_vector_type(4))) unsigned u32x4;
typedef __attribute__((ext_vector_type(2))) unsigned u32x2;
typedef __attribute__((ext_vector_type(2))) __bf16 bf16x2;
typedef short s16x4 __attribute__((ext_vector_type(4)));
typedef unsigned long long u64;
#define GAS __attribute__((address_space(1)))
#define LAS __attribute__((address_space(3)))
typedef LAS const char* lds_cptr;
typedef LAS char* lds_ptr;

constexpr unsigned WS_EPOCH = 0;
#define FSTR 8
constexpr unsigned WS_OFLAG = 65536;
constexpr unsigned WS_KVFLAG = 131072;
constexpr unsigned WS_KV = 1u << 20;
constexpr unsigned TS = 2176;
constexpr unsigned KV_BYTES = 2u * 32u * 64u * TS;
#define OSTR 32896
constexpr unsigned WS_O = 10u << 20;
constexpr unsigned OS = OSTR;
constexpr unsigned O_BYTES = 128u * OS;
constexpr unsigned WS_STAMP = 16u << 20;
constexpr unsigned OOB = 0x7ff00000u;
constexpr unsigned L_W = 65536;
constexpr unsigned L_WO = 0, L_PART = 65536, PREC = 7168, L_Q14 = 106496, L_Q15 = 114688, L_MDIAG = 122880, L_MFAR = 126976, L_Q = 131072,
                   L_GW = 147456, L_Z = 151552, L_G = 152576, L_CNT = 153088, L_SCR = 153600, L_END = 154112;
constexpr float QSCALE = 0.25f * 1.4426950408889634f;
constexpr float LMAX = 1.2676506e30f, LMIN = 9.5367432e-7f;
constexpr unsigned SPIN_CAP = 1u << 17;

#define SBAR() __builtin_amdgcn_sched_barrier(0)
#define PIN(x) asm volatile("" : "+v"(x))
#define MFMA(a, b, c) __builtin_amdgcn_mfma_f32_32x32x16_bf16(a, b, c, 0, 0, 0)
#define BAR() asm volatile("s_barrier" ::: "memory")
#define CBAR() asm volatile("s_waitcnt lgkmcnt(0)\n\ts_barrier" ::: "memory")
#define DRAIN() asm volatile("s_waitcnt vmcnt(0)" ::: "memory")
#define PACE() asm volatile("s_nop %0" :: "i"(PACEN) : "memory")
#define RLX_AGENT __ATOMIC_RELAXED, __HIP_MEMORY_SCOPE_AGENT
#define STAMP(i)

__device__ __forceinline__ void glds16(unsigned voff, const void* sbase, unsigned ldsaddr) {
    asm volatile("s_mov_b32 m0, %2\n\ts_nop 0\n\tglobal_load_lds_dwordx4 %0, %1" ::"v"(voff), "s"(sbase), "s"(ldsaddr) : "memory");
}
__device__ __forceinline__ void glds16x2(unsigned v0, unsigned v1, const void* sbase, unsigned ldsaddr) {
    asm volatile("s_mov_b32 m0, %3\n\ts_nop 0\n\tglobal_load_lds_dwordx4 %0, %2\n\tglobal_load_lds_dwordx4 %1, %2 offset:1024"
                 ::"v"(v0), "v"(v1), "s"(sbase), "s"(ldsaddr) : "memory");
}
__device__ __forceinline__ void glds16x4(unsigned v0, unsigned v1, unsigned v2, unsigned v3, const void* sbase, unsigned ldsaddr) {
    asm volatile("s_mov_b32 m0, %5\n\ts_nop 0\n\tglobal_load_lds_dwordx4 %0, %4\n\tglobal_load_lds_dwordx4 %1, %4 offset:1024\n\t"
                 "global_load_lds_dwordx4 %2, %4 offset:2048\n\tglobal_load_lds_dwordx4 %3, %4 offset:3072"
                 ::"v"(v0), "v"(v1), "v"(v2), "v"(v3), "s"(sbase), "s"(ldsaddr) : "memory");
}
__device__ __forceinline__ unsigned cvtpk(float lo, float hi) {
    typedef __attribute__((ext_vector_type(2))) float f32x2;
    return __builtin_bit_cast(unsigned, __builtin_convertvector((f32x2){lo, hi}, bf16x2));
}
__device__ __forceinline__ bf16x8 as_frag(u32x4 v) { return __builtin_bit_cast(bf16x8, v); }
__device__ __forceinline__ bf16x8 lds_frag(lds_cptr p) { return *(const LAS bf16x8*)p; }
__device__ __forceinline__ bf16x8 tr_frag(lds_cptr p0, lds_cptr p1) {
    s16x4 a = __builtin_amdgcn_ds_read_tr16_b64_v4i16((LAS s16x4*)p0);
    s16x4 b = __builtin_amdgcn_ds_read_tr16_b64_v4i16((LAS s16x4*)p1);
    return __builtin_bit_cast(bf16x8, __builtin_shufflevector(a, b, 0, 1, 2, 3, 4, 5, 6, 7));
}
__device__ __forceinline__ f32x16 pv_lo(bf16x8 v, const f32x16& p, f32x16 acc, int idx) {
    f32x8 h = __builtin_shufflevector(p, p, 0, 1, 2, 3, 4, 5, 6, 7);
    return __builtin_amdgcn_smfmac_f32_32x32x32_bf16(v, __builtin_bit_cast(bf16x16, h), acc, idx, 0, 0);
}
__device__ __forceinline__ f32x16 pv_hi(bf16x8 v, const f32x16& p, f32x16 acc, int idx) {
    f32x8 h = __builtin_shufflevector(p, p, 8, 9, 10, 11, 12, 13, 14, 15);
    return __builtin_amdgcn_smfmac_f32_32x32x32_bf16(v, __builtin_bit_cast(bf16x16, h), acc, idx, 0, 0);
}
#define MX3(a, b, c) __builtin_fmaxf(__builtin_fmaxf((a), (b)), (c))
__device__ __forceinline__ float max16(const f32x16& s) {
    float a = MX3(s[0], s[1], s[2]), b = MX3(s[3], s[4], s[5]);
    a = MX3(a, s[6], s[7]); b = MX3(b, s[8], s[9]); a = MX3(a, s[10], s[11]); b = MX3(b, s[12], s[13]);
    return MX3(a, b, __builtin_fmaxf(s[14], s[15]));
}
__device__ __forceinline__ float halfmax(float m) {
    auto r = __builtin_amdgcn_permlane32_swap(__float_as_uint(m), __float_as_uint(m), false, false);
    return __builtin_fmaxf(__uint_as_float(r[0]), __uint_as_float(r[1]));
}
__device__ __forceinline__ f32x16 lds_f32x16(lds_cptr p) {
    f32x4 a = *(const LAS f32x4*)p, b = *(const LAS f32x4*)(p + 1024), c = *(const LAS f32x4*)(p + 2048), d = *(const LAS f32x4*)(p + 3072);
    f32x8 lo = __builtin_shufflevector(a, b, 0, 1, 2, 3, 4, 5, 6, 7), hi = __builtin_shufflevector(c, d, 0, 1, 2, 3, 4, 5, 6, 7);
    return __builtin_shufflevector(lo, hi, 0, 1, 2, 3, 4, 5, 6, 7, 8, 9, 10, 11, 12, 13, 14, 15);
}
#define BLOAD(rs, vo, so) as_frag(__builtin_amdgcn_raw_buffer_load_b128(rs, vo, so, 16))
#define BSTORE(v, rs, vo, so) __builtin_amdgcn_raw_buffer_store_b128(v, rs, (vo) + (so), 0, 16)

#define POLLSLP 8
#define BSLP 36
__device__ __forceinline__ void wait_tags_end(u64 v, const GAS u64* p, u64 tag, GAS u64* pub, int lane) {
    if (pub) { DRAIN(); if (lane == 0) __hip_atomic_store(pub, tag, RLX_AGENT); }
    if (__builtin_expect(!__all(v == tag), 0)) {
#pragma nounroll
        for (unsigned spins = 0; spins < SPIN_CAP; ++spins) {
            __builtin_amdgcn_s_sleep(POLLSLP);
            if (__all(__hip_atomic_load(p, RLX_AGENT) == tag)) break;
        }
    }
    asm volatile("" ::: "memory");
}

__device__ __forceinline__ void wait_tags(const GAS u64* p, u64 tag, GAS u64* pub = nullptr, int lane = 0) {
    wait_tags_end(__hip_atomic_load(p, RLX_AGENT), p, tag, pub, lane);
}

__device__ __forceinline__ void cnt_arrive(lds_ptr cnt, int lane) {
    asm volatile("s_waitcnt lgkmcnt(0)" ::: "memory");
    if (lane == 0) __hip_atomic_fetch_add((LAS unsigned*)cnt, 1u, __ATOMIC_RELAXED, __HIP_MEMORY_SCOPE_WORKGROUP);
}
__device__ __forceinline__ void cnt_wait(lds_ptr cnt) {
#pragma nounroll
    for (unsigned spins = 0; spins < (1u << 22); ++spins) {
        if (__builtin_amdgcn_readfirstlane(__hip_atomic_load((LAS unsigned*)cnt, __ATOMIC_RELAXED, __HIP_MEMORY_SCOPE_WORKGROUP)) == 4) break;
        __builtin_amdgcn_s_sleep(1);
    }
    asm volatile("" ::: "memory");
}
__device__ __forceinline__ unsigned kvflag(int br, int quad, int tile, int type) {
    return WS_KVFLAG + (unsigned)(((((br * 8 + quad) * 4 + (tile & 3)) * 2 + (tile >> 5)) * 2 + type) * 8 + ((tile >> 2) & 7)) * FSTR;
}

__device__ __forceinline__ void slow_rows(__amdgpu_buffer_rsrc_t rs, unsigned sbase, int lo, int hi, int g, bool band, bf16x8 q,
                                          unsigned vk, unsigned vv0, unsigned vv1, lds_cptr lds, int lane, int idx, f32x16& acc) {
    float m = -__builtin_inff();
    for (int pass = 0; pass < 2; ++pass) {
        if (pass) { m = halfmax(m); acc = f32x16{}; }
        for (int kt = lo; kt <= hi; ++kt) {
            const unsigned so = sbase + (unsigned)kt * TS;
            const bf16x8 k = BLOAD(rs, vk, so), v0 = BLOAD(rs, vv0, so), v1 = BLOAD(rs, vv1, so);
            f32x16 c = f32x16{};
            if (band && kt == g) c = lds_f32x16(lds + L_MDIAG + lane * 16);
            if (band && kt == g - 8) c = lds_f32x16(lds + L_MFAR + lane * 16);
            f32x16 s = MFMA(k, q, c);
            if (!pass) m = __builtin_fmaxf(m, max16(s));
            else {
#pragma unroll
                for (int r = 0; r < 16; ++r) s[r] = __builtin_amdgcn_exp2f(s[r] - m);
                acc = pv_lo(v0, s, acc, idx); acc = pv_hi(v1, s, acc, idx);
            }
        }
    }
}

#define PK4(A, o) ((u32x4){cvtpk(A[o], A[o + 1]), cvtpk(A[o + 2], A[o + 3]), cvtpk(A[o + 4], A[o + 5]), cvtpk(A[o + 6], A[o + 7])})
#define PROF_ARG
#define PROF_PASS
template <int E>
__device__ __forceinline__ u32x4 pack_kv(int i, const f32x16& a) {
    const int s = i & 1;
    if (E == 0) return PK4(a, 8 * s);
    auto x = __builtin_amdgcn_permlane32_swap(cvtpk(a[8 * s], a[8 * s + 1]), cvtpk(a[8 * s + 4], a[8 * s + 5]), false, false);
    auto y = __builtin_amdgcn_permlane32_swap(cvtpk(a[8 * s + 2], a[8 * s + 3]), cvtpk(a[8 * s + 6], a[8 * s + 7]), false, false);
    return (u32x4){x[0], y[0], x[1], y[1]};
}
#define PUT_KV(i, v, vo) BSTORE(v, rkv, vo, ((i) < 2 ? 64 * TS : 0) + ((i) & 1) * (E ? 512 : 128 * TS))
#define QSWAP 1
#define PBX 4
template <int E>
__device__ __forceinline__ void project(lds_ptr lds, char* ws, __amdgpu_buffer_rsrc_t rkv, __amdgpu_buffer_rsrc_t ro, int lane, int t, int p, int j, u64& tag PROF_ARG) {
    const int r32 = lane & 31, hh = lane >> 5, qd4 = (p >> 1) ^ QSWAP;
    const unsigned q4 = (lane & 15) >> 2, wcol = ((lane >> 4) & 1) * 32 + (lane & 3) * 8;
    lds_cptr xa[4];
#pragma unroll
    for (int ks = 0; ks < 4; ++ks) xa[ks] = lds + (32 * t + r32) * 128 + (((hh ^ ((r32 >> 1) & 7)) * 16) ^ (ks * 32));
    unsigned w1o = L_W + (8 * hh + q4) * 128 + ((lane >> 4) & 1) * 64 + (q4 >> 1) * 32 + (lane & 3) * 8, w0o = w1o ^ 32;
    PIN(w0o); PIN(w1o);
    const lds_cptr w0 = lds + w0o, w1 = lds + w1o, wq = lds + (8 * hh + q4) * 64 + wcol;
#define XRD(n) lds_frag(xa[(n) & 3] + (((n) >> 2) & 3) * 16384)
#define WRD(b, n) tr_frag(b + ((n) & 15) * 2048, b + ((n) & 15) * 2048 + 512)
#define YOF(n) ((n) >= 28 ? 32768 + ((n) & 3) * 2048 : ((n) & 15) * 2048)
#define YRD(b, n) tr_frag(b + YOF(n), b + YOF(n) + 512)
#define QOF(br, n) (((n) >= 28 ? L_Q15 - L_Q14 : ((n) >= 24 ? 0 : (((n) >> 2) & 3) * 16384)) + (br) * 4096 + (((n) & 3) ^ E) * 1024)
#define QRD(br, n) tr_frag(((n) >= 24 ? wqh : wq) + QOF(br, n), ((n) >= 24 ? wqh : wq) + QOF(br, n) + 256)
#define MM(W, X, A) (E ? MFMA(X, W, A) : MFMA(W, X, A))
    bf16x8 xf[32], f0[4], f1[4];
    f32x16 a0 = f32x16{}, a1 = f32x16{};
    unsigned gwo = (lane & 15) == ((lane >> 4) & 1) ? L_GW + t * 1024 + hh * 16 : L_Z;
    PIN(gwo);
    f32x4 zg = f32x4{};
    bf16x8 gf[4];
#define RD1(n) do { xf[n] = XRD(n); f0[(n) & 3] = WRD(w0, n); f1[(n) & 3] = WRD(w1, n); SBAR(); } while (0)
#define GRD(n) gf[(n) & 3] = lds_frag(lds + gwo + (n) * 32)
#define GMM(n) zg = __builtin_amdgcn_mfma_f32_16x16x32_bf16(gf[(n) & 3], xf[n], zg, 0, 0, 0)
#define MM1(n) do { a0 = MM(f0[(n) & 3], xf[n], a0); a1 = MM(f1[(n) & 3], xf[n], a1); SBAR(); } while (0)
#pragma unroll
    for (int s = 0; s < 8; ++s) {
        if (s == 0) CBAR(); else BAR();
        SBAR();
        RD1(4 * s); RD1(4 * s + 1);
        if (s) { MM1(4 * s - 1); GMM(4 * s - 4); GMM(4 * s - 3); GMM(4 * s - 2); GMM(4 * s - 1); SBAR(); }
        RD1(4 * s + 2); MM1(4 * s); RD1(4 * s + 3); MM1(4 * s + 1);
        if (s == 0) DRAIN();
        GRD(4 * s); GRD(4 * s + 1); GRD(4 * s + 2); GRD(4 * s + 3); SBAR();
        MM1(4 * s + 2);
        SBAR();
    }
    const unsigned tile = 4 * j + t;
    unsigned vo = (unsigned)(4 * qd4 * 64 + tile) * TS + (E ? (r32 >> 4) * (128 * TS) + 1024 + hh * 256 + (r32 & 15) * 16 : lane * 16);
    unsigned wqo = L_Q14 + (8 * hh + q4) * 64 + wcol;
    PIN(vo); PIN(wqo);
    const lds_cptr wqh = lds + wqo;
    MM1(31); GMM(28); GMM(29); GMM(30); GMM(31); SBAR();
#define PRE8 2
#define PUBN 7
#define LATE0 99
    u32x4 pk[4];
#pragma unroll
    for (int i = 0; i < 4; ++i) { pk[i] = pack_kv<E>(i, i < 2 ? a0 : a1); PIN(pk[i]); if (i < PRE8) PUT_KV(i, pk[i], vo); SBAR(); }
    if (!E) BSTORE(((u32x4){0x3f803f80u, 0x3f803f80u, 0x3f803f80u, 0x3f803f80u}), rkv,
                   (unsigned)((hh * 32 + 4 * (qd4 ^ (hh * (PBX >> 1))) + (r32 >> 3)) * 64 + tile) * TS + 2048 + (lane & 7) * 16, 0);
    { LAS float* gp = (LAS float*)(lds + (lane < 16 ? L_G + t * 128 : L_SCR)) + lane;
      gp[0] = __builtin_amdgcn_rcpf(1.f + __expf(-zg[0])); gp[16] = __builtin_amdgcn_rcpf(1.f + __expf(-zg[1])); }
    SBAR();
    BAR(); SBAR();
    bf16x8 u0 = QRD(0, 0), u1 = QRD(1, 0), y0 = YRD(w0, 0), y1 = YRD(w1, 0);
    const u32x2 tg = *(const LAS u32x2*)(lds + L_CNT + 16);
    f32x16 b0 = f32x16{}, b1 = f32x16{}, aQ0 = f32x16{}, aQ1 = f32x16{};
    SBAR();
#pragma unroll
    for (int n = 0; n < 24; ++n) {
        aQ0 = MFMA(u0, xf[n], aQ0); SBAR();
        if (n == 1) { tag = ((u64)(unsigned)__builtin_amdgcn_readfirstlane(tg[1]) << 32) | (unsigned)__builtin_amdgcn_readfirstlane(tg[0]); asm volatile("" : "+s"(tag)); SBAR(); }
        if (n == PUBN) { DRAIN(); if (lane == 0) __hip_atomic_store((GAS u64*)(ws + kvflag(0, qd4, tile, E)), tag, RLX_AGENT); SBAR(); }
        if ((n & 3) == 3) {
            if (PRE8 + (n >> 2) < 2) { PUT_KV((PRE8 + (n >> 2)) & 3, pk[(PRE8 + (n >> 2)) & 3], vo); SBAR(); }
            if (n == LATE0) { PUT_KV(2, pk[2], vo); SBAR(); }
            if (n == LATE0 + 4) { PUT_KV(3, pk[3], vo); SBAR(); }
            BAR(); SBAR();
        }
        u0 = QRD(0, n + 1); SBAR();
        aQ1 = MFMA(u1, xf[n], aQ1); SBAR();
        u1 = n < 23 ? QRD(1, n + 1) : QRD(0, 25); SBAR();
        b0 = MM(y0, xf[n], b0); SBAR(); y0 = n < 23 ? YRD(w0, n + 1) : QRD(0, 26); SBAR();
        b1 = MM(y1, xf[n], b1); SBAR(); y1 = n < 23 ? YRD(w1, n + 1) : QRD(0, 27); SBAR();
    }
    aQ0 = MFMA(u0, xf[24], aQ0); SBAR(); u0 = QRD(0, 28); SBAR(); aQ0 = MFMA(u1, xf[25], aQ0); SBAR(); u1 = QRD(0, 29); SBAR();
    aQ0 = MFMA(y0, xf[26], aQ0); SBAR(); y0 = QRD(0, 30); SBAR(); aQ0 = MFMA(y1, xf[27], aQ0); SBAR(); y1 = QRD(0, 31); SBAR();
    aQ0 = MFMA(u0, xf[28], aQ0); SBAR(); aQ0 = MFMA(u1, xf[29], aQ0); SBAR(); u1 = QRD(1, 24); SBAR();
    aQ0 = MFMA(y0, xf[30], aQ0); SBAR(); y0 = YRD(w0, 24); SBAR(); aQ0 = MFMA(y1, xf[31], aQ0); SBAR(); y1 = YRD(w1, 24); SBAR();
    const lds_ptr qd = lds + L_Q + t * 1024 + lane * 16;
#pragma unroll
    for (int n = 24; n < 32; ++n) {
        aQ1 = MFMA(u1, xf[n], aQ1); SBAR();
        if (n == 24) {
#pragma unroll
            for (int r = 8; r < 16; ++r) aQ0[r] *= QSCALE;
            *(LAS u32x4*)(qd + 4096) = PK4(aQ0, 8);
            __builtin_amdgcn_s_setprio(2);
            CBAR(); STAMP(4); SBAR();
        }
        if (n < 31) u1 = QRD(1, n + 1); PACE(); SBAR();
        b0 = MM(y0, xf[n], b0); SBAR(); if (n < 31) y0 = YRD(w0, n + 1); PACE(); SBAR();
        b1 = MM(y1, xf[n], b1); SBAR(); if (n < 31) y1 = YRD(w1, n + 1); PACE(); SBAR();
    }
#pragma unroll
    for (int r = 0; r < 8; ++r) aQ0[r] *= QSCALE;
#pragma unroll
    for (int r = 0; r < 16; ++r) aQ1[r] *= QSCALE;
    *(LAS u32x4*)qd = PK4(aQ0, 0); *(LAS u32x4*)(qd + 8192) = PK4(aQ1, 0); *(LAS u32x4*)(qd + 12288) = PK4(aQ1, 8);
    cnt_arrive(lds + L_CNT + 8, lane);
    const unsigned vo2 = vo + (32 + 4 * ((qd4 ^ (PBX >> 1)) - qd4)) * 64 * TS;
#define YSLP 8
    u32x4 py[4] = {pack_kv<E>(0, b0), pack_kv<E>(1, b0), pack_kv<E>(2, b1), pack_kv<E>(3, b1)};
    PIN(py[0]); PIN(py[1]); PIN(py[2]); PIN(py[3]);
#define YSTAG 2
    if (t & 1) __builtin_amdgcn_s_sleep(YSTAG);
    if (t & 2) __builtin_amdgcn_s_sleep(2 * YSTAG);
    if (LATE0 > 23) { PUT_KV(2, pk[2], vo); __builtin_amdgcn_s_sleep(YSLP); }
    if (LATE0 > 19) { PUT_KV(3, pk[3], vo); __builtin_amdgcn_s_sleep(YSLP); }
    PUT_KV(0, py[0], vo2); __builtin_amdgcn_s_sleep(YSLP); PUT_KV(1, py[1], vo2); __builtin_amdgcn_s_sleep(YSLP);
    PUT_KV(2, py[2], vo2); __builtin_amdgcn_s_sleep(YSLP); PUT_KV(3, py[3], vo2);
    DRAIN();
    if (lane == 0) __hip_atomic_store((GAS u64*)(ws + kvflag(1, qd4, tile, E)), tag, RLX_AGENT);
    BSTORE(((u32x4){~0u, ~0u, ~0u, ~0u}), ro, lane * 16, tile * OS + 2 * p * 1024);
    STAMP(19);
}

#define AB_WO (512 * 1024)
#define ROTEXPR j + 2 * (p & 3)
#define WOX 1
#define WOIN 2
#define CODE_LINES 280

struct Att {
    lds_ptr lds; char* ws; __amdgpu_buffer_rsrc_t rkv, ro;
    int lane, t, p, g, head, idx; u64 tag; unsigned vk, vv0, vv1, sBeg; lds_cptr zp, qp[4];
};
#define IDX a.idx
#define LD3(K, V0, V1, rs, so) do { K = BLOAD(rs, a.vk, so); V0 = BLOAD(rs, a.vv0, so); V1 = BLOAD(rs, a.vv1, so); } while (0)
#define EX(X, r) X[r] = __builtin_amdgcn_exp2f(X[r])
#define STEP(X, Y, OP, VP, KN, QN, CN, O, V0, F1, F3, F2) do { \
        EX(X, 0); EX(X, 1); SBAR(); OP = pv_hi(VP, Y, OP, IDX); SBAR(); F1; SBAR(); EX(X, 2); EX(X, 3); EX(X, 4); SBAR(); Y = MFMA(KN, QN, CN); SBAR(); F3; SBAR(); \
        EX(X, 5); EX(X, 6); EX(X, 7); SBAR(); EX(X, 8); EX(X, 9); SBAR(); O = pv_lo(V0, X, O, IDX); SBAR(); F2; SBAR(); \
        EX(X, 10); EX(X, 11); EX(X, 12); EX(X, 13); EX(X, 14); EX(X, 15); SBAR(); } while (0)

__device__ __forceinline__ f32x16 lds_zero16(lds_cptr zp) {
    const volatile LAS f32x4* z = (const volatile LAS f32x4*)zp;
    const f32x4 a = *z, b = *z, c = *z, d = *z;
    f32x8 lo = __builtin_shufflevector(a, b, 0, 1, 2, 3, 4, 5, 6, 7), hi = __builtin_shufflevector(c, d, 0, 1, 2, 3, 4, 5, 6, 7);
    return __builtin_shufflevector(lo, hi, 0, 1, 2, 3, 4, 5, 6, 7, 8, 9, 10, 11, 12, 13, 14, 15);
}

template <int C, int BR>
__device__ __forceinline__ void finish(const Att& a, f32x16 o, float gf, bf16x8 q, unsigned vo) {
    f32x8 y = __builtin_shufflevector(o, o, 0, 1, 2, 3, 4, 5, 6, 7) * (__builtin_amdgcn_rcpf(o[8]) * gf);
    PIN(y);
    const float chk = __builtin_fmaf(((y[0] + y[2]) + (y[4] + y[6])) + ((y[1] + y[3]) + (y[5] + y[7])), 0.f, o[8]);
    if (__builtin_expect(__any(__builtin_amdgcn_fmed3f(chk, LMIN, LMAX) != chk), 0)) {
        if (!BR) { wait_tags((const GAS u64*)(a.ws + kvflag(!C, a.p >> 1, 0, 0) + a.lane * FSTR), a.tag); wait_tags((const GAS u64*)(a.ws + kvflag(!C, a.p >> 1, 2, 0) + a.lane * FSTR), a.tag); }
        slow_rows(a.rkv, (unsigned)((BR * 32 + (a.head ^ (BR * 2 * PBX))) * 64) * TS, BR ? (a.g >= 8 ? a.g - 8 : 0) : 0, BR ? a.g : 63, a.g, BR, q, a.vk, a.vv0, a.vv1, a.lds, a.lane, IDX, o);
        y = __builtin_shufflevector(o, o, 0, 1, 2, 3, 4, 5, 6, 7) * (__builtin_amdgcn_rcpf(o[8]) * gf);
        PIN(y);
    }
    u32x4 v = (u32x4){cvtpk(y[0], y[1]), cvtpk(y[2], y[3]), cvtpk(y[4], y[5]), cvtpk(y[6], y[7])};
    if (!C && !BR) v = __builtin_elementwise_min(v, (u32x4){~1u, ~1u, ~1u, ~1u});
    BSTORE(v, a.ro, vo, 0);
}
#define OFLAG(BR, C) ((GAS u64*)(a.ws + WS_OFLAG) + (((BR) * 64 + a.g) * 2 + (C)) * 16 + (a.p ^ ((BR) * PBX)))
#define OVOFF(BR) (a.vk + (unsigned)((BR) * 64 + a.g) * OS + (a.head ^ ((BR) * 2 * PBX)) * 1024)
#define GATE() (*(const LAS float*)(a.lds + L_G + a.t * 128 + (a.lane & 31) * 4))

struct KV3 { bf16x8 k[3], v0[3], v1[3]; };
#define LDB(K, V0, V1, i) do { const int kt_ = a.g - (i); \
        const __amdgpu_buffer_rsrc_t rb_ = __builtin_amdgcn_make_buffer_rsrc(a.ws + WS_KV, 0, kt_ >= 0 ? KV_BYTES : 0u, 0x00020000); \
        LD3(K, V0, V1, rb_, (unsigned)((32 + (a.head ^ (2 * PBX))) * 64 + (kt_ >= 0 ? kt_ : 0)) * TS); } while (0)
#define LDB_FIRST(f) do { LDB(f.k[0], f.v0[0], f.v1[0], 0); SBAR(); LDB(f.k[1], f.v0[1], f.v1[1], 1); SBAR(); LDB(f.k[2], f.v0[2], f.v1[2], 2); SBAR(); } while (0)
#define LDA_FIRST(f) do { LD3(f.k[0], f.v0[0], f.v1[0], a.rkv, so0[0]); SBAR(); LD3(f.k[1], f.v0[1], f.v1[1], a.rkv, so0[1]); SBAR(); \
        LD3(f.k[2], f.v0[2], f.v1[2], a.rkv, so0[2]); SBAR(); } while (0)
__device__ __forceinline__ unsigned window_flag(const Att& a) {
    const int i = a.lane & 15, kt = a.g - (i < 9 ? i : 0);
    return kvflag(1, a.p >> 1, kt >= 0 ? kt : 0, (a.lane >> 4) & 1);
}

template <int C>
__device__ __forceinline__ void attn_full(const Att& a, f32x16 o0, f32x16 o1, f32x16 o2, f32x16 o3, f32x16 S1, unsigned so,
                                          __amdgpu_buffer_rsrc_t rwo, unsigned vwo, unsigned wi, const GAS u64* fp2, const GAS u64* fp3, KV3& nxt PROF_ARG) {
    const lds_ptr lds = a.lds; const int t = a.t, lane = a.lane;
    const f32x16 zc = f32x16{};
    bf16x8 ka = nxt.k[0], va0 = nxt.v0[0], va1 = nxt.v1[0], kb = nxt.k[1], vb0 = nxt.v0[1], vb1 = nxt.v1[1], kc = nxt.k[2], vc0 = nxt.v0[2], vc1 = nxt.v1[2], kd, vd0, vd1;
    const unsigned sBeg = a.sBeg, jb = a.g >> 2;
    unsigned ki = 3;
    vd1 = lds_frag(a.zp);
    lds_ptr pw = lds + L_PART + (4 * C + t) * PREC + lane * 16;
    lds_cptr pr0 = lds + L_PART + (4 * C + ((t - 1) & 3)) * PREC + lane * 16, pr1 = lds + L_PART + (4 * C + ((t - 2) & 3)) * PREC + lane * 16,
             pr2 = lds + L_PART + (4 * C + ((t - 3) & 3)) * PREC + lane * 16;
    unsigned ovo = OVOFF(0); float gf = 0.f;
    if (C) { BAR(); STAMP(4); }
    else { gf = GATE(); PIN(pw); PIN(pr0); PIN(pr1); PIN(pr2); PIN(ovo); PIN(gf);
           DRAIN(); if (lane == 0) __hip_atomic_store(OFLAG(1, 0), a.tag, RLX_AGENT); }
    bf16x8 q0 = lds_frag(a.qp[0]); SBAR();
    bf16x8 q1 = lds_frag(a.qp[1]), q2 = lds_frag(a.qp[2]), q3 = lds_frag(a.qp[3]);
    SBAR();
    f32x16 S0 = MFMA(ka, q0, zc);
    PIN(S0); SBAR();
    PIN(q1); PIN(q2); PIN(q3); PIN(vd1); PIN(o0); PIN(o1); PIN(o2); PIN(o3); PIN(S1);
    __builtin_amdgcn_s_waitcnt(0x0F76);
#pragma unroll
    for (int i = 0; i < 8; ++i) __builtin_amdgcn_s_setprio(C);
    STAMP(3);
    SBAR();
    unsigned wso = 0;
    u64 fv = a.tag;
#define NEXT1() asm volatile("s_add_i32 %0, %0, 1\n\ts_xor_b32 %1, %0, %2" : "+s"(ki), "=s"(so) : "s"(jb) : "scc")
#define NEXT2() asm volatile("s_and_b32 %0, %0, 15" : "+s"(so) :: "scc")
#define NEXT3() asm volatile("s_mulk_i32 %0, %2\n\ts_add_i32 %0, %0, %1" : "+s"(so) : "s"(sBeg), "i"(4 * TS) : "scc")
#define WO_OFF() do { if (C) asm volatile("s_lshl_b32 %0, %1, 4" : "=s"(wso) : "s"(wi) : "scc"); } while (0)
#define WO_DMA() do { if (C) __builtin_amdgcn_raw_ptr_buffer_load_lds(rwo, (LAS void*)(lds + L_WO + t * 16384 + wi), 16, vwo, wso, 0, 0); } while (0)
#define WO_NEXT() do { if (C) asm volatile("s_addk_i32 %0, 0x400\n\ts_and_b32 %0, %0, 0x3fff" : "+s"(wi) :: "scc"); } while (0)
#define TILE(K, V0, V1, KN, LK, LV0, LV1, NW, G) do { \
        STEP(S0, S1, o3, LV1, K, q1, zc, o0, V0, LK = BLOAD(a.rkv, a.vk, so), WO_OFF(), LV0 = BLOAD(a.rkv, a.vv0, so)); \
        STEP(S1, S0, o0, V1, K, q2, zc, o1, V0, LV1 = BLOAD(a.rkv, a.vv1, so), , WO_DMA()); \
        STEP(S0, S1, o1, V1, K, q3, zc, o2, V0, NEXT1(), NEXT2(), NEXT3()); \
        STEP(S1, S0, o2, V1, KN, q0, zc, o3, V0, __builtin_amdgcn_s_waitcnt(0x0F70 | (C ? NW : 6)), G, WO_NEXT()); } while (0)
#define PUTA(O, sl) (*(LAS f32x4*)(pw + (sl) * 2048) = __builtin_shufflevector(O, O, 0, 1, 2, 3))
#define PUTB(O, sl) (*(LAS f32x4*)(pw + (sl) * 2048 + 1024) = __builtin_shufflevector(O, O, 4, 5, 6, 7))
#define PUTL(O, sl) (*(LAS float*)(pw + 6144 + (sl) * 4) = O[8])
    f32x4 x0, y0, x1, y1, x2, y2; float l0, l1, l2;
#define GETP() do { x0 = *(const LAS f32x4*)pr0; x1 = *(const LAS f32x4*)(pr1 + 2048); x2 = *(const LAS f32x4*)(pr2 + 4096); \
        y0 = *(const LAS f32x4*)(pr0 + 1024); y1 = *(const LAS f32x4*)(pr1 + 3072); y2 = *(const LAS f32x4*)(pr2 + 5120); \
        l0 = *(const LAS float*)(pr0 + 6144); l1 = *(const LAS float*)(pr1 + 6148); l2 = *(const LAS float*)(pr2 + 6152); } while (0)
    if (C) {
#pragma nounroll
        for (int it = 0; it < 4; ++it) {
            TILE(ka, va0, va1, kb, kd, vd0, vd1, 7, );
            PIN(S0); PIN(S1); PIN(o3); wait_tags_end(fv, fp2, a.tag, nullptr, 0);
            TILE(kb, vb0, vb1, kc, ka, va0, va1, 8, );
            TILE(kc, vc0, vc1, kd, kb, vb0, vb1, 9, fv = __hip_atomic_load(fp2, RLX_AGENT));
            TILE(kd, vd0, vd1, ka, kc, vc0, vc1, 10, );
        }
        o3 = pv_hi(vd1, S1, o3, IDX);
        STAMP(6);
        const u64 f3 = __hip_atomic_load(fp3, RLX_AGENT);
        cnt_wait(lds + L_CNT + 8);
        PUTA(o0, 0); PUTB(o0, 0); PUTL(o0, 0); PUTA(o1, 1); PUTB(o1, 1); PUTL(o1, 1); PUTA(o2, 2); PUTB(o2, 2); PUTL(o2, 2);
        DRAIN(); cnt_arrive(lds + L_CNT, lane);
        wait_tags_end(f3, fp3, a.tag, nullptr, 0);
        LDB_FIRST(nxt);
        cnt_wait(lds + L_CNT);
        GETP(); gf = GATE();
    } else {
        TILE(ka, va0, va1, kb, kd, vd0, vd1, 0, );
        TILE(kb, vb0, vb1, kc, ka, va0, va1, 0, );
#pragma nounroll
        for (int it = 0; it < 3; ++it) {
            TILE(kc, vc0, vc1, kd, kb, vb0, vb1, 0, );
            TILE(kd, vd0, vd1, ka, kc, vc0, vc1, 0, );
            TILE(ka, va0, va1, kb, kd, vd0, vd1, 0, );
            TILE(kb, vb0, vb1, kc, ka, va0, va1, 0, );
        }
        STEP(S0, S1, o3, vb1, kc, q1, zc, o0, vc0, , , );
        STEP(S1, S0, o0, vc1, kc, q2, zc, o1, vc0, , , );
        STEP(S0, S1, o1, vc1, kd, q0, zc, o2, vc0, __builtin_amdgcn_s_waitcnt(0x0F73), , );
        STEP(S1, S0, o2, vc1, kd, q1, zc, o0, vd0, , , );
        STEP(S0, S1, o0, vd1, kd, q2, zc, o1, vd0, , , );
        STEP(S1, S0, o1, vd1, kc, q3, zc, o2, vd0, PUTA(o0, 0), PUTL(o0, 0), PUTB(o0, 0));
        STEP(S0, S1, o2, vd1, kd, q3, zc, o3, vc0, PUTA(o1, 1), PUTL(o1, 1), PUTB(o1, 1));
        PUTA(o2, 2); PUTB(o2, 2); SBAR();
        EX(S1, 0); EX(S1, 1); SBAR(); o3 = pv_hi(vc1, S0, o3, IDX); SBAR(); PUTL(o2, 2); __builtin_amdgcn_s_setprio(2); SBAR();
        EX(S1, 2); EX(S1, 3); EX(S1, 4); EX(S1, 5); EX(S1, 6); EX(S1, 7); SBAR(); EX(S1, 8); EX(S1, 9); SBAR();
        o3 = pv_lo(vd0, S1, o3, IDX); PIN(S1); SBAR();
        CBAR(); GETP(); SBAR();
        EX(S1, 10); EX(S1, 11); EX(S1, 12); EX(S1, 13); EX(S1, 14); EX(S1, 15); SBAR();
        o3 = pv_hi(vd1, S1, o3, IDX); SBAR();
        STAMP(6);
    }
    x0 = (x0 + x1) + x2; y0 = (y0 + y1) + y2; l0 = (l0 + l1) + l2;
    PIN(x0); PIN(y0); PIN(l0); PIN(o3);
#pragma unroll
    for (int r = 0; r < 4; ++r) { o3[r] += x0[r]; o3[4 + r] += y0[r]; }
    o3[8] += l0;
    finish<C, 0>(a, o3, gf, q3, ovo);
}

template <int C>
__device__ __forceinline__ void attn_window(const Att& a, KV3& f, const unsigned (&so0)[4] PROF_ARG) {
    const lds_ptr lds = a.lds; const int t = a.t, lane = a.lane;
    const f32x16 zc = f32x16{};
    bf16x8 k[9], v0[9], v1[9];
#pragma unroll
    for (int i = 0; i < 3; ++i) { k[i] = f.k[i]; v0[i] = f.v0[i]; v1[i] = f.v1[i]; }
#define LDW(i) LDB(k[i], v0[i], v1[i], i)
    const bf16x8 qa = lds_frag(lds + L_Q + 8192 + C * 4096 + t * 1024 + lane * 16);
    const f32x16 cd = lds_f32x16(lds + L_MDIAG + lane * 16), cf = lds_f32x16(lds + L_MFAR + lane * 16);
    f32x16 oo = lds_zero16(a.zp);
    const float gt = GATE();
    f32x16 S0 = MFMA(k[0], qa, cd), S1 = MFMA(k[1], qa, zc);
    SBAR(); LDW(3); SBAR();
#pragma unroll
    for (int r = 0; r < 10; ++r) { EX(S0, r); SBAR(); }
    oo = pv_lo(v0[0], S0, oo, IDX); SBAR();
#pragma unroll
    for (int r = 10; r < 16; ++r) EX(S0, r);
    SBAR();
    STEP(S1, S0, oo, v1[0], k[2], qa, zc, oo, v0[1], LDW(4), , );
    STEP(S0, S1, oo, v1[1], k[3], qa, zc, oo, v0[2], LDW(5), , );
    STEP(S1, S0, oo, v1[2], k[4], qa, zc, oo, v0[3], LDW(6), , );
    STEP(S0, S1, oo, v1[3], k[5], qa, zc, oo, v0[4], LDW(7), , );
    STEP(S1, S0, oo, v1[4], k[6], qa, zc, oo, v0[5], LDW(8), , );
    const GAS u64* fa = (const GAS u64*)(a.ws + kvflag(1, a.p >> 1, t, 0) + (lane & 31) * FSTR);
    u64 fv = 0;
    STEP(S0, S1, oo, v1[5], k[7], qa, zc, oo, v0[6], if (!C) fv = __hip_atomic_load(fa, RLX_AGENT), , );
    STEP(S1, S0, oo, v1[6], k[8], qa, cf, oo, v0[7], , , );
    if (C) { PIN(S0); PIN(S1); PIN(oo); DRAIN(); if (lane == 0) __hip_atomic_store(OFLAG(0, C), a.tag, RLX_AGENT); }
    STEP(S0, S1, oo, v1[7], k[8], qa, cf, oo, v0[8], , , );
    oo = pv_hi(v1[8], S0, oo, IDX);
    STAMP(7);
    if (!C) { PIN(oo); wait_tags_end(fv, fa, a.tag, nullptr, 0); LDA_FIRST(f); }
    finish<C, 1>(a, oo, 1.f - gt, qa, OVOFF(1));
}

__global__ void __launch_bounds__(512, 2)
gmix_kernel(const char* x, const char* wA, const char* woA, const char* wB, const char* woB, const char* gw, char* out, char* ws) {
    extern __shared__ __attribute__((aligned(16))) char smem[];
    const lds_ptr lds = (lds_ptr)smem;
    const unsigned lds0 = (unsigned)(uintptr_t)lds;
    const int tid = threadIdx.x, lane = tid & 63, r32 = lane & 31, hh = lane >> 5;
    const int w = __builtin_amdgcn_readfirstlane(tid >> 6);
    const int bid = blockIdx.x, xcd = bid & 7, rr = bid >> 3;
    const int p = 4 * (xcd & 3) + (rr & 3), j = 8 * (xcd >> 2) + (rr >> 2);
    const int jo = 2 * xcd + (rr >> 4), po = rr & 15;
    const int t = w & 3, c = w >> 2;
    const int e = p & 1;
    STAMP(0);
    {
        u64 pc; const unsigned ln = (((bid & ~7) | (tid >> 6)) + (bid < CODE_LINES - 256 ? (tid & 32) * 8 : 0)) * 128;
        asm volatile("s_getpc_b64 %0\n\ts_mov_b32 m0, %2\n\ts_nop 0\n\tglobal_load_lds_dword %1, %0" : "=&s"(pc) : "v"(ln), "s"(lds0 + L_SCR) : "memory");
        SBAR();
    }
    unsigned rot = (ROTEXPR) & 7;
    unsigned vx0 = (128 * j + 32 * t + (lane >> 3)) * 1024 + (((lane & 7) ^ (lane >> 4)) * 16), vx1 = (vx0 ^ 64) + 7168, vx2 = vx0 + 14336, vx3 = vx1 + 14336;
    unsigned vw0 = (16 * t + (lane >> 3)) * 3072 + 1024 + 1024 * e + 128 * ((p >> 1) ^ QSWAP) + (((lane & 7) ^ (((lane >> 4) & 1) << 1)) * 16), vw1 = vw0 + 8 * 3072 - 1024;
    unsigned mx = lds0 + t * 4096, mw = lds0 + L_W + t * 2048, ow = rot * (64 * 3072);
    asm volatile("" : "+v"(vx0), "+v"(vx1), "+v"(vx2), "+v"(vx3), "+v"(vw0), "+v"(vw1));
    asm volatile("" : "+s"(mx), "+s"(mw), "+s"(rot), "+s"(ow));
    SBAR();
    asm volatile("" : "+s"(x), "+s"(wA), "+s"(woA), "+s"(wB), "+s"(woB), "+s"(gw), "+s"(out), "+s"(ws));
#define MK_ATT(C) Att a; a.lds = lds; a.ws = ws; a.lane = lane; a.t = t; a.p = p; a.g = 4 * j + t; a.head = 2 * p + C; a.idx = 0xdddddddd; \
    a.rkv = __builtin_amdgcn_make_buffer_rsrc(ws + WS_KV, 0, KV_BYTES, 0x00020000); a.ro = __builtin_amdgcn_make_buffer_rsrc(ws + WS_O, 0, O_BYTES, 0x00020000); \
    a.vk = lane * 16; a.vv0 = r32 < 16 ? 1024 + hh * 256 + r32 * 16 : ((r32 & ~4) == 16 ? 2048 : OOB); a.vv1 = r32 < 16 ? a.vv0 + 512 : a.vv0; a.zp = lds + L_Z + lane * 16; \
    a.sBeg = (unsigned)(a.head * 64 + t) * TS; \
    _Pragma("unroll") for (int i = 0; i < 4; ++i) a.qp[i] = lds + L_Q + C * 4096 + ((t + 1 + i) & 3) * 1024 + lane * 16
#define MK_SO() unsigned so0[4]; _Pragma("unroll") for (int i = 0; i < 4; ++i) so0[i] = a.sBeg + (j ^ i) * (4 * TS)
    if (c) {
#define DMA1(s) do { const unsigned ks_ = ((s) + rot) & 7; \
        glds16x4(vx0, vx1, vx2, vx3, x + ks_ * 128, mx + ((s) & 3) * 16384); glds16x2(vw0, vw1, wA + ks_ * (64 * 3072), mw + ((s) & 3) * 8192); } while (0)
#define DMA2(s) do { const unsigned ks_ = (((s) + rot) & 7) * (64 * 3072); \
        glds16x2(vq0, vq1, wq + ks_, mq + ((s) == 14 ? L_Q14 : ((s) & 3) * 16384)); glds16x2(vw0, vw1, wy + ks_, mw + ((s) & 3) * 8192); SBAR(); } while (0)
#define LBAR(N) asm volatile("s_waitcnt vmcnt(" #N ")\n\ts_barrier" ::: "memory")
        STAMP(20);
        glds16x4(vx0, vx1, vx2, vx3, x + rot * 128, mx); glds16x2(vw0, vw1, wA + ow, mw);
        SBAR();
        const unsigned k1 = (1 + rot) & 7;
        glds16x2(vx0, vx1, x + k1 * 128, mx + 16384);
        SBAR();
        const u64 ep = *(const __attribute__((address_space(4))) u64*)(ws + WS_EPOCH);
        unsigned vq0 = (32 * (t & 1) + 16 * e + (lane >> 2)) * 3072 + 64 * (p ^ ((t >> 1) * PBX)) + (lane & 3) * 16 + 1024, vq1 = vq0 + (e ? -16 * 3072 : 16 * 3072) - 1024;
        unsigned vwo = ((t & 1) * 256 + (lane >> 2)) * 1024 + 64 * po + (lane & 3) * 16;
        MK_ATT(1);
        const GAS u64* fp = (const GAS u64*)(ws + kvflag(0, p >> 1, (j >> 3) * 32 + t, 0) + (lane & 15) * FSTR);
        const GAS u64* fp2 = (const GAS u64*)(ws + kvflag(0, p >> 1, ((j >> 3) ^ 1) * 32 + t, 0) + (lane & 15) * FSTR);
        const GAS u64* fp3 = (const GAS u64*)(ws + (hh ? WS_OFLAG + ((64 + 4 * jo + t) * 32 + (lane & 15)) * 8 : window_flag(a)));
        PIN(vq0); PIN(vq1); PIN(vwo); PIN(a.vk); PIN(a.vv0); PIN(a.vv1); PIN(a.qp[0]); PIN(a.qp[1]); PIN(a.qp[2]); PIN(a.qp[3]); PIN(a.zp); PIN(fp); PIN(fp2); PIN(fp3); PIN(a.idx);
        SBAR();
        STAMP(30);
        glds16x2(vx0, vx1, x + k1 * 128 + 16384, mx + 16384 + 2048); glds16x2(vw0, vw1, wA + k1 * (64 * 3072), mw + 8192);
        STAMP(22);
        SBAR();
#pragma nounroll
        for (int s = 2; s < 8; ++s) { LBAR(6); DMA1(s);
        }
        const unsigned mq = lds0 + t * 2048;
        const char* wq = ((t >> 1) ? wB : wA) - 1024;
        const char* wy = wB + 128 * ((((p >> 1) ^ QSWAP) ^ (PBX >> 1)) - ((p >> 1) ^ QSWAP));
        LBAR(6); STAMP(14); DMA2(8);
        LBAR(4); STAMP(15); DMA2(9);
        a.tag = ep * 0x9E3779B97F4A7C15ull + 0x632BE59BD9B4E019ull;
        *(LAS u64*)(lds + L_CNT + 16) = a.tag;
        asm volatile("s_waitcnt lgkmcnt(0)" ::: "memory");
        LBAR(4); STAMP(16); DMA2(10);
        const unsigned k7 = ((7 + rot) & 7) * (64 * 3072), sx = mq + L_Q15, sw = mw + 32768;
        LBAR(4); STAMP(17); glds16(vq0, wq + k7, sx); DMA2(11);
        f32x16 oo = lds_zero16(a.zp), o1 = lds_zero16(a.zp); PIN(oo); PIN(o1);
        LBAR(5); STAMP(18); glds16(vw0, wy + k7, sw); DMA2(12);
        f32x16 o2 = lds_zero16(a.zp), o3 = lds_zero16(a.zp); PIN(o2); PIN(o3);
        LBAR(5); STAMP(19); glds16(vq1, wq + k7 + 1024, sx + 1024); DMA2(13);
        f32x16 S1 = lds_zero16(a.zp); PIN(S1);
        LBAR(5); STAMP(21); glds16(vw1, wy + k7 + 1024, sw + 1024); DMA2(14);
        __amdgpu_buffer_rsrc_t rwo = __builtin_amdgcn_make_buffer_rsrc((void*)((t >> 1) ? woB : woA), 0, AB_WO, 0x00020000);
        unsigned wi0 = 4096 * (((rr >> 4) << 1) | (po & 1)) + WOX * 1024 * (xcd & 3);
        MK_SO();
        asm volatile("" : "+s"(a.rkv), "+s"(rwo), "+s"(a.sBeg), "+s"(wi0), "+s"(so0[0]), "+s"(so0[1]), "+s"(so0[2]), "+s"(so0[3]));
        __builtin_amdgcn_s_setprio(1);
        LBAR(5); STAMP(24);
        const u64 fv = __hip_atomic_load(fp, RLX_AGENT);
        LBAR(1); STAMP(29);
        wait_tags_end(fv, fp, a.tag, nullptr, lane);
        STAMP(5);
        KV3 kw;
        LDA_FIRST(kw);
        attn_full<1>(a, oo, o1, o2, o3, S1, so0[3], rwo, vwo, wi0, fp2, fp3, kw PROF_PASS);
        STAMP(23);
        attn_window<1>(a, kw, so0 PROF_PASS);
        STAMP(8);
        const int go = 4 * jo + t;
        const lds_cptr wo = lds + L_WO + (4 * hh + ((lane & 15) >> 2)) * 64 + ((lane >> 4) & 1) * 32 + (lane & 3) * 8;
        const GAS u64* of_ = (const GAS u64*)(ws + WS_OFLAG);
#define OBR(n) ((n) >> 4 == 1 ? 0 : 1)
#define OHD(n) (2 * ((n) & 15) + ((n) >= 16))
#define OLD(n) BLOAD(a.ro, a.vk, (unsigned)(OBR(n) * 64 + go) * OS + OHD(n) * 1024)
#define OWF(n) tr_frag(wo + OBR(n) * 32768 + OHD(n) * 1024, wo + OBR(n) * 32768 + OHD(n) * 1024 + 512)
#define OIN 6
        bf16x8 oe[48];
#pragma unroll
        for (int n = 0; n < 48; ++n) {
            if (n == 16) { wait_tags(of_ + go * 32 + 16 + (lane & 15), a.tag, OFLAG(1, 1), lane); STAMP(9); }
            if (n == 32) wait_tags(of_ + (64 + go) * 32 + 16 + (lane & 15), a.tag);
            oe[n] = OLD(n); SBAR();
            if (n >= OIN - 1) __builtin_amdgcn_s_waitcnt(0x0F70 | (OIN - 1));
            SBAR();
        }
        bf16x8 wn[4];
#pragma unroll
        for (int n = 0; n < 4; ++n) { wn[n] = OWF(n); PIN(wn[n]); }
        __builtin_amdgcn_s_waitcnt(0x0F70);
#define NEARLY 16
#define NMID 32
        unsigned sg[4]; lds_cptr wg[4];
#pragma unroll
        for (int q = 0; q < 4; ++q) { const int gq = ((p >> 2) + 1 + q) & 3; sg[q] = (unsigned)go * OS + gq * 8192; wg[q] = wo + gq * 8192; PIN(wg[q]); }
        unsigned vout = (32 * go + r32) * 1024 + 64 * po + 16 * hh;
        __amdgpu_buffer_rsrc_t rout = __builtin_amdgcn_make_buffer_rsrc(out, 0, 2048 * 1024, 0x00020000);
        PIN(vout); asm volatile("" : "+s"(sg[0]), "+s"(sg[1]), "+s"(sg[2]), "+s"(sg[3]), "+s"(rout));
        f32x16 ya = f32x16{};
        bf16x8 wf[16], of[16];
#define FLD(i) of[i] = BLOAD(a.ro, a.vk, sg[(i) >> 2] + ((i) & 3) * 2048)
#define GRP_BAD(q) ({ unsigned mx_ = 0; _Pragma("unroll") for (int i = 4 * (q); i < 4 * (q) + 4; ++i) { const u32x4 u_ = __builtin_bit_cast(u32x4, of[i]); \
            mx_ = __builtin_elementwise_max(__builtin_elementwise_max(mx_, u_[0]), u_[1]); mx_ = __builtin_elementwise_max(__builtin_elementwise_max(mx_, u_[2]), u_[3]); } \
            __any(mx_ == ~0u); })
#define GRP_LD(q) do { _Pragma("unroll") for (int i = 4 * (q); i < 4 * (q) + 4; ++i) FLD(i); } while (0)
        STAMP(10);
        BAR();
        BAR();
#pragma unroll
        for (int n = 0; n < 48; ++n) {
            ya = MFMA(wn[n & 3], oe[n], ya); SBAR();
            if (n >= NEARLY && n < NEARLY + 12) FLD((n - NEARLY) & 15);
            if (n >= NMID && n < NMID + 4) FLD((n - NMID + 12) & 15);
            if (n + 4 < 48) wn[n & 3] = OWF(n + 4);
            if (n >= 32) wf[n - 32] = tr_frag(wg[(n - 32) >> 2] + (n & 3) * 2048, wg[(n - 32) >> 2] + (n & 3) * 2048 + 512);
            SBAR();
        }
        STAMP(31);
        f32x16 yb = lds_zero16(a.zp), yc = lds_zero16(a.zp), yd = lds_zero16(a.zp);
        unsigned pend = 15;
#define GRP_TRY(q, Y) do { if (pend & (1u << (q))) { if (!GRP_BAD(q)) { \
            _Pragma("unroll") for (int i = 4 * (q); i < 4 * (q) + 4; ++i) Y = MFMA(wf[i], of[i], Y); pend ^= 1u << (q); } } } while (0)
        unsigned spins = SPIN_CAP;
#pragma nounroll
        do {
            GRP_TRY(0, ya); GRP_TRY(1, yb); GRP_TRY(2, yc); GRP_TRY(3, yd);
            asm volatile("s_sub_u32 %1, %1, 1\n\ts_cselect_b32 %0, 0, %0" : "+s"(pend), "+s"(spins) :: "memory", "scc");
            if (pend & 1) GRP_LD(0); if (pend & 2) GRP_LD(1); if (pend & 4) GRP_LD(2); if (pend & 8) GRP_LD(3);
        } while (pend);
        ya = (ya + yb) + (yc + yd);
#pragma unroll
        for (int s = 0; s < 2; ++s) {
            auto u = __builtin_amdgcn_permlane32_swap(cvtpk(ya[8 * s], ya[8 * s + 1]), cvtpk(ya[8 * s + 4], ya[8 * s + 5]), false, false);
            auto v = __builtin_amdgcn_permlane32_swap(cvtpk(ya[8 * s + 2], ya[8 * s + 3]), cvtpk(ya[8 * s + 6], ya[8 * s + 7]), false, false);
#define OUT_AUX 0
            __builtin_amdgcn_raw_buffer_store_b128(((u32x4){u[0], v[0], u[1], v[1]}), rout, vout + 32 * s, 0, OUT_AUX);
        }
        if (bid == 0 && tid == 256) __hip_atomic_store((GAS u64*)(ws + WS_EPOCH), a.tag, RLX_AGENT);
        STAMP(12);
    } else {
        glds16((((lane >> 3) + rot) & 7) * 128 + (lane & 7) * 16, gw, lds0 + L_GW + t * 1024);
#pragma unroll
        for (int m = 0; m < 2; ++m) {
            f32x4 mk;
#pragma unroll
            for (int i = 0; i < 4; ++i) { const bool le = (i + 8 * t + 4 * hh) <= r32; mk[i] = (le != (m != 0)) ? 0.f : -__builtin_inff(); }
            *(LAS f32x4*)(lds + L_MDIAG + m * 4096 + t * 1024 + lane * 16) = mk;
        }
        if (tid < 4) *(LAS unsigned*)(lds + L_CNT + tid * 4) = 0;
        *(LAS unsigned*)(lds + L_Z + tid * 4) = 0;
        u64 tag;
        STAMP(21);
        {
            const __amdgpu_buffer_rsrc_t rkv = __builtin_amdgcn_make_buffer_rsrc(ws + WS_KV, 0, KV_BYTES, 0x00020000);
            const __amdgpu_buffer_rsrc_t ro = __builtin_amdgcn_make_buffer_rsrc(ws + WS_O, 0, O_BYTES, 0x00020000);
            if (e) project<1>(lds, ws, rkv, ro, lane, t, p, j, tag PROF_PASS); else project<0>(lds, ws, rkv, ro, lane, t, p, j, tag PROF_PASS);
        }
        cnt_wait(lds + L_CNT + 8);
        __builtin_amdgcn_s_sleep(BSLP);
        STAMP(24);
        MK_ATT(0);
        a.tag = tag;
        wait_tags((const GAS u64*)(ws + window_flag(a)), tag);
        STAMP(25);
        KV3 kw;
        LDB_FIRST(kw);
        MK_SO();
        attn_window<0>(a, kw, so0 PROF_PASS);
        STAMP(23);
        PIN(a.idx);
        attn_full<0>(a, lds_zero16(a.zp), lds_zero16(a.zp), lds_zero16(a.zp), lds_zero16(a.zp), lds_zero16(a.zp), so0[3], a.rkv, 0, 0, nullptr, nullptr, kw PROF_PASS);
        BAR();
        STAMP(8);
    }
}

extern "C" void kernel_launch(void* const* d_in, const int* in_sizes, int n_in, void* d_out, int out_size, void* d_ws, size_t ws_size,
                              hipStream_t stream) {
    (void)in_sizes; (void)n_in; (void)out_size; (void)ws_size;
    (void)hipFuncSetAttribute((const void*)gmix_kernel, hipFuncAttributeMaxDynamicSharedMemorySize, L_END);
    gmix_kernel<<<256, 512, L_END, stream>>>((const char*)d_in[0], (const char*)d_in[1], (const char*)d_in[2], (const char*)d_in[4],
                                             (const char*)d_in[5], (const char*)d_in[6], (char*)d_out, (char*)d_ws);
}
```

```cpp
#define REVEAL 99
#define RROLE 1
#define RCYC 0
#include <hip/hip_runtime.h>
#include <hip/hip_bf16.h>

typedef __attribute__((ext_vector_type(8))) __bf16 bf16x8;
typedef __attribute__((ext_vector_type(16))) __bf16 bf16x16;
typedef __attribute__((ext_vector_type(16))) float f32x16;
typedef __attribute__((ext_vector_type(8))) float f32x8;
typedef __attribute__((ext_vector_type(4))) float f32x4;
typedef __attribute__((ext_vector_type(4))) unsigned u32x4;
typedef __attribute__((ext_vector_type(2))) unsigned u32x2;
typedef __attribute__((ext_vector_type(2))) __bf16 bf16x2;
typedef short s16x4 __attribute__((ext_vector_type(4)));
typedef unsigned long long u64;
#define GAS __attribute__((address_space(1)))
#define LAS __attribute__((address_space(3)))
typedef LAS const char* lds_cptr;
typedef LAS char* lds_ptr;

constexpr unsigned WS_EPOCH = 0;
#define FSTR 8
constexpr unsigned WS_OFLAG = 65536;
constexpr unsigned WS_KVFLAG = 131072;
constexpr unsigned WS_KV = 1u << 20;
constexpr unsigned TS = 2176;
constexpr unsigned KV_BYTES = 2u * 32u * 64u * TS;
#define OSTR 32896
constexpr unsigned WS_O = 10u << 20;
constexpr unsigned OS = OSTR;
constexpr unsigned O_BYTES = 128u * OS;
constexpr unsigned WS_STAMP = 16u << 20;
constexpr unsigned OOB = 0x7ff00000u;
constexpr unsigned L_W = 65536;
constexpr unsigned L_WO = 0, L_PART = 65536, PREC = 7168, L_Q14 = 106496, L_Q15 = 114688, L_MDIAG = 122880, L_MFAR = 126976, L_Q = 131072,
                   L_GW = 147456, L_Z = 151552, L_G = 152576, L_CNT = 153088, L_SCR = 153600, L_END = 154112;
constexpr float QSCALE = 0.25f * 1.4426950408889634f;
constexpr float LMAX = 1.2676506e30f, LMIN = 9.5367432e-7f;
constexpr unsigned SPIN_CAP = 1u << 17;

#define SBAR() __builtin_amdgcn_sched_barrier(0)
#define PIN(x) asm volatile("" : "+v"(x))
#define MFMA(a, b, c) __builtin_amdgcn_mfma_f32_32x32x16_bf16(a, b, c, 0, 0, 0)
#define BAR() asm volatile("s_barrier" ::: "memory")
#define CBAR() asm volatile("s_waitcnt lgkmcnt(0)\n\ts_barrier" ::: "memory")
#define DRAIN() asm volatile("s_waitcnt vmcnt(0)" ::: "memory")
#define PACEN 3
#define PACE() asm volatile("s_nop %0" :: "i"(PACEN) : "memory")
#define RLX_AGENT __ATOMIC_RELAXED, __HIP_MEMORY_SCOPE_AGENT
#define STAMP(i) do { if ((i) == 0 || (i) == REVEAL) { u64 a_, b_; SBAR(); asm volatile("s_memrealtime %0\n\ts_memtime %1\n\ts_waitcnt lgkmcnt(0)" : "=s"(a_), "=s"(b_) :: "memory"); \
    if ((i) == 0) { stv.r0 = a_; stv.c0 = b_; stv.d = 0; } else stv.d = RCYC ? (unsigned)(b_ - stv.c0) / 20u : (unsigned)(a_ - stv.r0); SBAR(); } } while (0)
struct Rev { u64 r0, c0; unsigned d; };

__device__ __forceinline__ void glds16(unsigned voff, const void* sbase, unsigned ldsaddr) {
    asm volatile("s_mov_b32 m0, %2\n\ts_nop 0\n\tglobal_load_lds_dwordx4 %0, %1" ::"v"(voff), "s"(sbase), "s"(ldsaddr) : "memory");
}
__device__ __forceinline__ void glds16x2(unsigned v0, unsigned v1, const void* sbase, unsigned ldsaddr) {
    asm volatile("s_mov_b32 m0, %3\n\ts_nop 0\n\tglobal_load_lds_dwordx4 %0, %2\n\tglobal_load_lds_dwordx4 %1, %2 offset:1024"
                 ::"v"(v0), "v"(v1), "s"(sbase), "s"(ldsaddr) : "memory");
}
__device__ __forceinline__ void glds16x4(unsigned v0, unsigned v1, unsigned v2, unsigned v3, const void* sbase, unsigned ldsaddr) {
    asm volatile("s_mov_b32 m0, %5\n\ts_nop 0\n\tglobal_load_lds_dwordx4 %0, %4\n\tglobal_load_lds_dwordx4 %1, %4 offset:1024\n\t"
                 "global_load_lds_dwordx4 %2, %4 offset:2048\n\tglobal_load_lds_dwordx4 %3, %4 offset:3072"
                 ::"v"(v0), "v"(v1), "v"(v2), "v"(v3), "s"(sbase), "s"(ldsaddr) : "memory");
}
__device__ __forceinline__ unsigned cvtpk(float lo, float hi) {
    typedef __attribute__((ext_vector_type(2))) float f32x2;
    return __builtin_bit_cast(unsigned, __builtin_convertvector((f32x2){lo, hi}, bf16x2));
}
__device__ __forceinline__ bf16x8 as_frag(u32x4 v) { return __builtin_bit_cast(bf16x8, v); }
__device__ __forceinline__ bf16x8 lds_frag(lds_cptr p) { return *(const LAS bf16x8*)p; }
__device__ __forceinline__ bf16x8 tr_frag(lds_cptr p0, lds_cptr p1) {
    s16x4 a = __builtin_amdgcn_ds_read_tr16_b64_v4i16((LAS s16x4*)p0);
    s16x4 b = __builtin_amdgcn_ds_read_tr16_b64_v4i16((LAS s16x4*)p1);
    return __builtin_bit_cast(bf16x8, __builtin_shufflevector(a, b, 0, 1, 2, 3, 4, 5, 6, 7));
}
__device__ __forceinline__ f32x16 pv_lo(bf16x8 v, const f32x16& p, f32x16 acc, int idx) {
    f32x8 h = __builtin_shufflevector(p, p, 0, 1, 2, 3, 4, 5, 6, 7);
    return __builtin_amdgcn_smfmac_f32_32x32x32_bf16(v, __builtin_bit_cast(bf16x16, h), acc, idx, 0, 0);
}
__device__ __forceinline__ f32x16 pv_hi(bf16x8 v, const f32x16& p, f32x16 acc, int idx) {
    f32x8 h = __builtin_shufflevector(p, p, 8, 9, 10, 11, 12, 13, 14, 15);
    return __builtin_amdgcn_smfmac_f32_32x32x32_bf16(v, __builtin_bit_cast(bf16x16, h), acc, idx, 0, 0);
}
#define MX3(a, b, c) __builtin_fmaxf(__builtin_fmaxf((a), (b)), (c))
__device__ __forceinline__ float max16(const f32x16& s) {
    float a = MX3(s[0], s[1], s[2]), b = MX3(s[3], s[4], s[5]);
    a = MX3(a, s[6], s[7]); b = MX3(b, s[8], s[9]); a = MX3(a, s[10], s[11]); b = MX3(b, s[12], s[13]);
    return MX3(a, b, __builtin_fmaxf(s[14], s[15]));
}
__device__ __forceinline__ float halfmax(float m) {
    auto r = __builtin_amdgcn_permlane32_swap(__float_as_uint(m), __float_as_uint(m), false, false);
    return __builtin_fmaxf(__uint_as_float(r[0]), __uint_as_float(r[1]));
}
__device__ __forceinline__ f32x16 lds_f32x16(lds_cptr p) {
    f32x4 a = *(const LAS f32x4*)p, b = *(const LAS f32x4*)(p + 1024), c = *(const LAS f32x4*)(p + 2048), d = *(const LAS f32x4*)(p + 3072);
    f32x8 lo = __builtin_shufflevector(a, b, 0, 1, 2, 3, 4, 5, 6, 7), hi = __builtin_shufflevector(c, d, 0, 1, 2, 3, 4, 5, 6, 7);
    return __builtin_shufflevector(lo, hi, 0, 1, 2, 3, 4, 5, 6, 7, 8, 9, 10, 11, 12, 13, 14, 15);
}
#define BLOAD(rs, vo, so) as_frag(__builtin_amdgcn_raw_buffer_load_b128(rs, vo, so, 16))
#define BSTORE(v, rs, vo, so) __builtin_amdgcn_raw_buffer_store_b128(v, rs, (vo) + (so), 0, 16)

#define POLLSLP 8
#define BSLP 36
__device__ __forceinline__ void wait_tags_end(u64 v, const GAS u64* p, u64 tag, GAS u64* pub, int lane) {
    if (pub) { DRAIN(); if (lane == 0) __hip_atomic_store(pub, tag, RLX_AGENT); }
    if (__builtin_expect(!__all(v == tag), 0)) {
#pragma nounroll
        for (unsigned spins = 0; spins < SPIN_CAP; ++spins) {
            __builtin_amdgcn_s_sleep(POLLSLP);
            if (__all(__hip_atomic_load(p, RLX_AGENT) == tag)) break;
        }
    }
    asm volatile("" ::: "memory");
}

__device__ __forceinline__ void wait_tags(const GAS u64* p, u64 tag, GAS u64* pub = nullptr, int lane = 0) {
    wait_tags_end(__hip_atomic_load(p, RLX_AGENT), p, tag, pub, lane);
}

__device__ __forceinline__ void cnt_arrive(lds_ptr cnt, int lane) {
    asm volatile("s_waitcnt lgkmcnt(0)" ::: "memory");
    if (lane == 0) __hip_atomic_fetch_add((LAS unsigned*)cnt, 1u, __ATOMIC_RELAXED, __HIP_MEMORY_SCOPE_WORKGROUP);
}
__device__ __forceinline__ void cnt_wait(lds_ptr cnt) {
#pragma nounroll
    for (unsigned spins = 0; spins < (1u << 22); ++spins) {
        if (__builtin_amdgcn_readfirstlane(__hip_atomic_load((LAS unsigned*)cnt, __ATOMIC_RELAXED, __HIP_MEMORY_SCOPE_WORKGROUP)) == 4) break;
        __builtin_amdgcn_s_sleep(1);
    }
    asm volatile("" ::: "memory");
}
__device__ __forceinline__ unsigned kvflag(int br, int quad, int tile, int type) {
    return WS_KVFLAG + (unsigned)(((((br * 8 + quad) * 4 + (tile & 3)) * 2 + (tile >> 5)) * 2 + type) * 8 + ((tile >> 2) & 7)) * FSTR;
}

__device__ __forceinline__ void slow_rows(__amdgpu_buffer_rsrc_t rs, unsigned sbase, int lo, int hi, int g, bool band, bf16x8 q,
                                          unsigned vk, unsigned vv0, unsigned vv1, lds_cptr lds, int lane, int idx, f32x16& acc) {
    float m = -__builtin_inff();
    for (int pass = 0; pass < 2; ++pass) {
        if (pass) { m = halfmax(m); acc = f32x16{}; }
        for (int kt = lo; kt <= hi; ++kt) {
            const unsigned so = sbase + (unsigned)kt * TS;
            const bf16x8 k = BLOAD(rs, vk, so), v0 = BLOAD(rs, vv0, so), v1 = BLOAD(rs, vv1, so);
            f32x16 c = f32x16{};
            if (band && kt == g) c = lds_f32x16(lds + L_MDIAG + lane * 16);
            if (band && kt == g - 8) c = lds_f32x16(lds + L_MFAR + lane * 16);
            f32x16 s = MFMA(k, q, c);
            if (!pass) m = __builtin_fmaxf(m, max16(s));
            else {
#pragma unroll
                for (int r = 0; r < 16; ++r) s[r] = __builtin_amdgcn_exp2f(s[r] - m);
                acc = pv_lo(v0, s, acc, idx); acc = pv_hi(v1, s, acc, idx);
            }
        }
    }
}

#define PK4(A, o) ((u32x4){cvtpk(A[o], A[o + 1]), cvtpk(A[o + 2], A[o + 3]), cvtpk(A[o + 4], A[o + 5]), cvtpk(A[o + 6], A[o + 7])})
#define PROF_ARG , Rev& stv
#define PROF_PASS , stv
template <int E>
__device__ __forceinline__ u32x4 pack_kv(int i, const f32x16& a) {
    const int s = i & 1;
    if (E == 0) return PK4(a, 8 * s);
    auto x = __builtin_amdgcn_permlane32_swap(cvtpk(a[8 * s], a[8 * s + 1]), cvtpk(a[8 * s + 4], a[8 * s + 5]), false, false);
    auto y = __builtin_amdgcn_permlane32_swap(cvtpk(a[8 * s + 2], a[8 * s + 3]), cvtpk(a[8 * s + 6], a[8 * s + 7]), false, false);
    return (u32x4){x[0], y[0], x[1], y[1]};
}
#define PUT_KV(i, v, vo) BSTORE(v, rkv, vo, ((i) < 2 ? 64 * TS : 0) + ((i) & 1) * (E ? 512 : 128 * TS))
#define QSWAP 1
#define PBX 4
template <int E>
__device__ __forceinline__ void project(lds_ptr lds, char* ws, __amdgpu_buffer_rsrc_t rkv, __amdgpu_buffer_rsrc_t ro, int lane, int t, int p, int j, u64& tag PROF_ARG) {
    const int r32 = lane & 31, hh = lane >> 5, qd4 = (p >> 1) ^ QSWAP;
    const unsigned q4 = (lane & 15) >> 2, wcol = ((lane >> 4) & 1) * 32 + (lane & 3) * 8;
    lds_cptr xa[4];
#pragma unroll
    for (int ks = 0; ks < 4; ++ks) xa[ks] = lds + (32 * t + r32) * 128 + (((hh ^ ((r32 >> 1) & 7)) * 16) ^ (ks * 32));
    unsigned w1o = L_W + (8 * hh + q4) * 128 + ((lane >> 4) & 1) * 64 + (q4 >> 1) * 32 + (lane & 3) * 8, w0o = w1o ^ 32;
    PIN(w0o); PIN(w1o);
    const lds_cptr w0 = lds + w0o, w1 = lds + w1o, wq = lds + (8 * hh + q4) * 64 + wcol;
#define XRD(n) lds_frag(xa[(n) & 3] + (((n) >> 2) & 3) * 16384)
#define WRD(b, n) tr_frag(b + ((n) & 15) * 2048, b + ((n) & 15) * 2048 + 512)
#define YOF(n) ((n) >= 28 ? 32768 + ((n) & 3) * 2048 : ((n) & 15) * 2048)
#define YRD(b, n) tr_frag(b + YOF(n), b + YOF(n) + 512)
#define QOF(br, n) (((n) >= 28 ? L_Q15 - L_Q14 : ((n) >= 24 ? 0 : (((n) >> 2) & 3) * 16384)) + (br) * 4096 + (((n) & 3) ^ E) * 1024)
#define QRD(br, n) tr_frag(((n) >= 24 ? wqh : wq) + QOF(br, n), ((n) >= 24 ? wqh : wq) + QOF(br, n) + 256)
#define MM(W, X, A) (E ? MFMA(X, W, A) : MFMA(W, X, A))
    bf16x8 xf[32], f0[4], f1[4];
    f32x16 a0 = f32x16{}, a1 = f32x16{};
    unsigned gwo = (lane & 15) == ((lane >> 4) & 1) ? L_GW + t * 1024 + hh * 16 : L_Z;
    PIN(gwo);
    f32x4 zg = f32x4{};
    bf16x8 gf[4];
#define RD1(n) do { xf[n] = XRD(n); f0[(n) & 3] = WRD(w0, n); f1[(n) & 3] = WRD(w1, n); SBAR(); } while (0)
#define GRD(n) gf[(n) & 3] = lds_frag(lds + gwo + (n) * 32)
#define GMM(n) zg = __builtin_amdgcn_mfma_f32_16x16x32_bf16(gf[(n) & 3], xf[n], zg, 0, 0, 0)
#define MM1(n) do { a0 = MM(f0[(n) & 3], xf[n], a0); a1 = MM(f1[(n) & 3], xf[n], a1); SBAR(); } while (0)
#pragma unroll
    for (int s = 0; s < 8; ++s) {
        if (s == 0) CBAR(); else BAR();
        SBAR();
        RD1(4 * s); RD1(4 * s + 1);
        if (s) { MM1(4 * s - 1); GMM(4 * s - 4); GMM(4 * s - 3); GMM(4 * s - 2); GMM(4 * s - 1); SBAR(); }
        RD1(4 * s + 2); MM1(4 * s); RD1(4 * s + 3); MM1(4 * s + 1);
        if (s == 0) DRAIN();
        GRD(4 * s); GRD(4 * s + 1); GRD(4 * s + 2); GRD(4 * s + 3); SBAR();
        MM1(4 * s + 2);
        SBAR();
    }
    const unsigned tile = 4 * j + t;
    unsigned vo = (unsigned)(4 * qd4 * 64 + tile) * TS + (E ? (r32 >> 4) * (128 * TS) + 1024 + hh * 256 + (r32 & 15) * 16 : lane * 16);
    unsigned wqo = L_Q14 + (8 * hh + q4) * 64 + wcol;
    PIN(vo); PIN(wqo);
    const lds_cptr wqh = lds + wqo;
    MM1(31); GMM(28); GMM(29); GMM(30); GMM(31); SBAR();
#define PRE8 2
#define PUBN 7
#define LATE0 99
    u32x4 pk[4];
#pragma unroll
    for (int i = 0; i < 4; ++i) { pk[i] = pack_kv<E>(i, i < 2 ? a0 : a1); PIN(pk[i]); if (i < PRE8) PUT_KV(i, pk[i], vo); SBAR(); }
    if (!E) BSTORE(((u32x4){0x3f803f80u, 0x3f803f80u, 0x3f803f80u, 0x3f803f80u}), rkv,
                   (unsigned)((hh * 32 + 4 * (qd4 ^ (hh * (PBX >> 1))) + (r32 >> 3)) * 64 + tile) * TS + 2048 + (lane & 7) * 16, 0);
    { LAS float* gp = (LAS float*)(lds + (lane < 16 ? L_G + t * 128 : L_SCR)) + lane;
      gp[0] = __builtin_amdgcn_rcpf(1.f + __expf(-zg[0])); gp[16] = __builtin_amdgcn_rcpf(1.f + __expf(-zg[1])); }
    SBAR();
    BAR(); SBAR();
    bf16x8 u0 = QRD(0, 0), u1 = QRD(1, 0), y0 = YRD(w0, 0), y1 = YRD(w1, 0);
    const u32x2 tg = *(const LAS u32x2*)(lds + L_CNT + 16);
    f32x16 b0 = f32x16{}, b1 = f32x16{}, aQ0 = f32x16{}, aQ1 = f32x16{};
    SBAR();
#pragma unroll
    for (int n = 0; n < 24; ++n) {
        aQ0 = MFMA(u0, xf[n], aQ0); SBAR();
        if (n == 1) { tag = ((u64)(unsigned)__builtin_amdgcn_readfirstlane(tg[1]) << 32) | (unsigned)__builtin_amdgcn_readfirstlane(tg[0]); asm volatile("" : "+s"(tag)); SBAR(); }
        if (n == PUBN) { DRAIN(); if (lane == 0) __hip_atomic_store((GAS u64*)(ws + kvflag(0, qd4, tile, E)), tag, RLX_AGENT); SBAR(); }
        if ((n & 3) == 3) {
            if (PRE8 + (n >> 2) < 2) { PUT_KV((PRE8 + (n >> 2)) & 3, pk[(PRE8 + (n >> 2)) & 3], vo); SBAR(); }
            if (n == LATE0) { PUT_KV(2, pk[2], vo); SBAR(); }
            if (n == LATE0 + 4) { PUT_KV(3, pk[3], vo); SBAR(); }
            BAR(); SBAR();
        }
        u0 = QRD(0, n + 1); SBAR();
        aQ1 = MFMA(u1, xf[n], aQ1); SBAR();
        u1 = n < 23 ? QRD(1, n + 1) : QRD(0, 25); SBAR();
        b0 = MM(y0, xf[n], b0); SBAR(); y0 = n < 23 ? YRD(w0, n + 1) : QRD(0, 26); SBAR();
        b1 = MM(y1, xf[n], b1); SBAR(); y1 = n < 23 ? YRD(w1, n + 1) : QRD(0, 27); SBAR();
    }
    aQ0 = MFMA(u0, xf[24], aQ0); SBAR(); u0 = QRD(0, 28); SBAR(); aQ0 = MFMA(u1, xf[25], aQ0); SBAR(); u1 = QRD(0, 29); SBAR();
    aQ0 = MFMA(y0, xf[26], aQ0); SBAR(); y0 = QRD(0, 30); SBAR(); aQ0 = MFMA(y1, xf[27], aQ0); SBAR(); y1 = QRD(0, 31); SBAR();
    aQ0 = MFMA(u0, xf[28], aQ0); SBAR(); aQ0 = MFMA(u1, xf[29], aQ0); SBAR(); u1 = QRD(1, 24); SBAR();
    aQ0 = MFMA(y0, xf[30], aQ0); SBAR(); y0 = YRD(w0, 24); SBAR(); aQ0 = MFMA(y1, xf[31], aQ0); SBAR(); y1 = YRD(w1, 24); SBAR();
    const lds_ptr qd = lds + L_Q + t * 1024 + lane * 16;
#pragma unroll
    for (int n = 24; n < 32; ++n) {
        aQ1 = MFMA(u1, xf[n], aQ1); SBAR();
        if (n == 24) {
#pragma unroll
            for (int r = 8; r < 16; ++r) aQ0[r] *= QSCALE;
            *(LAS u32x4*)(qd + 4096) = PK4(aQ0, 8);
            __builtin_amdgcn_s_setprio(2);
            CBAR(); STAMP(4); SBAR();
        }
        if (n < 31) u1 = QRD(1, n + 1); PACE(); SBAR();
        b0 = MM(y0, xf[n], b0); SBAR(); if (n < 31) y0 = YRD(w0, n + 1); PACE(); SBAR();
        b1 = MM(y1, xf[n], b1); SBAR(); if (n < 31) y1 = YRD(w1, n + 1); PACE(); SBAR();
    }
#pragma unroll
    for (int r = 0; r < 8; ++r) aQ0[r] *= QSCALE;
#pragma unroll
    for (int r = 0; r < 16; ++r) aQ1[r] *= QSCALE;
    *(LAS u32x4*)qd = PK4(aQ0, 0); *(LAS u32x4*)(qd + 8192) = PK4(aQ1, 0); *(LAS u32x4*)(qd + 12288) = PK4(aQ1, 8);
    cnt_arrive(lds + L_CNT + 8, lane);
    const unsigned vo2 = vo + (32 + 4 * ((qd4 ^ (PBX >> 1)) - qd4)) * 64 * TS;
#define YSLP 8
    u32x4 py[4] = {pack_kv<E>(0, b0), pack_kv<E>(1, b0), pack_kv<E>(2, b1), pack_kv<E>(3, b1)};
    PIN(py[0]); PIN(py[1]); PIN(py[2]); PIN(py[3]);
#define YSTAG 2
    if (t & 1) __builtin_amdgcn_s_sleep(YSTAG);
    if (t & 2) __builtin_amdgcn_s_sleep(2 * YSTAG);
    if (LATE0 > 23) { PUT_KV(2, pk[2], vo); __builtin_amdgcn_s_sleep(YSLP); }
    if (LATE0 > 19) { PUT_KV(3, pk[3], vo); __builtin_amdgcn_s_sleep(YSLP); }
    PUT_KV(0, py[0], vo2); __builtin_amdgcn_s_sleep(YSLP); PUT_KV(1, py[1], vo2); __builtin_amdgcn_s_sleep(YSLP);
    PUT_KV(2, py[2], vo2); __builtin_amdgcn_s_sleep(YSLP); PUT_KV(3, py[3], vo2);
    DRAIN();
    if (lane == 0) __hip_atomic_store((GAS u64*)(ws + kvflag(1, qd4, tile, E)), tag, RLX_AGENT);
    BSTORE(((u32x4){~0u, ~0u, ~0u, ~0u}), ro, lane * 16, tile * OS + 2 * p * 1024);
    STAMP(19);
}

#define AB_WO (512 * 1024)
#define ROTEXPR j + 2 * (p & 3)
#define WOX 1
#define WOIN 2
#define CODE_LINES 280

struct Att {
    lds_ptr lds; char* ws; __amdgpu_buffer_rsrc_t rkv, ro;
    int lane, t, p, g, head, idx; u64 tag; unsigned vk, vv0, vv1, sBeg; lds_cptr zp, qp[4];
};
#define IDX a.idx
#define LD3(K, V0, V1, rs, so) do { K = BLOAD(rs, a.vk, so); V0 = BLOAD(rs, a.vv0, so); V1 = BLOAD(rs, a.vv1, so); } while (0)
#define EX(X, r) X[r] = __builtin_amdgcn_exp2f(X[r])
#define STEP(X, Y, OP, VP, KN, QN, CN, O, V0, F1, F3, F2) do { \
        EX(X, 0); EX(X, 1); SBAR(); OP = pv_hi(VP, Y, OP, IDX); SBAR(); F1; SBAR(); EX(X, 2); EX(X, 3); EX(X, 4); SBAR(); Y = MFMA(KN, QN, CN); SBAR(); F3; SBAR(); \
        EX(X, 5); EX(X, 6); EX(X, 7); SBAR(); EX(X, 8); EX(X, 9); SBAR(); O = pv_lo(V0, X, O, IDX); SBAR(); F2; SBAR(); \
        EX(X, 10); EX(X, 11); EX(X, 12); EX(X, 13); EX(X, 14); EX(X, 15); SBAR(); } while (0)

__device__ __forceinline__ f32x16 lds_zero16(lds_cptr zp) {
    const volatile LAS f32x4* z = (const volatile LAS f32x4*)zp;
    const f32x4 a = *z, b = *z, c = *z, d = *z;
    f32x8 lo = __builtin_shufflevector(a, b, 0, 1, 2, 3, 4, 5, 6, 7), hi = __builtin_shufflevector(c, d, 0, 1, 2, 3, 4, 5, 6, 7);
    return __builtin_shufflevector(lo, hi, 0, 1, 2, 3, 4, 5, 6, 7, 8, 9, 10, 11, 12, 13, 14, 15);
}

template <int C, int BR>
__device__ __forceinline__ void finish(const Att& a, f32x16 o, float gf, bf16x8 q, unsigned vo) {
    f32x8 y = __builtin_shufflevector(o, o, 0, 1, 2, 3, 4, 5, 6, 7) * (__builtin_amdgcn_rcpf(o[8]) * gf);
    PIN(y);
    const float chk = __builtin_fmaf(((y[0] + y[2]) + (y[4] + y[6])) + ((y[1] + y[3]) + (y[5] + y[7])), 0.f, o[8]);
    if (__builtin_expect(__any(__builtin_amdgcn_fmed3f(chk, LMIN, LMAX) != chk), 0)) {
        if (!BR) { wait_tags((const GAS u64*)(a.ws + kvflag(!C, a.p >> 1, 0, 0) + a.lane * FSTR), a.tag); wait_tags((const GAS u64*)(a.ws + kvflag(!C, a.p >> 1, 2, 0) + a.lane * FSTR), a.tag); }
        slow_rows(a.rkv, (unsigned)((BR * 32 + (a.head ^ (BR * 2 * PBX))) * 64) * TS, BR ? (a.g >= 8 ? a.g - 8 : 0) : 0, BR ? a.g : 63, a.g, BR, q, a.vk, a.vv0, a.vv1, a.lds, a.lane, IDX, o);
        y = __builtin_shufflevector(o, o, 0, 1, 2, 3, 4, 5, 6, 7) * (__builtin_amdgcn_rcpf(o[8]) * gf);
        PIN(y);
    }
    u32x4 v = (u32x4){cvtpk(y[0], y[1]), cvtpk(y[2], y[3]), cvtpk(y[4], y[5]), cvtpk(y[6], y[7])};
    if (!C && !BR) v = __builtin_elementwise_min(v, (u32x4){~1u, ~1u, ~1u, ~1u});
    BSTORE(v, a.ro, vo, 0);
}
#define OFLAG(BR, C) ((GAS u64*)(a.ws + WS_OFLAG) + (((BR) * 64 + a.g) * 2 + (C)) * 16 + (a.p ^ ((BR) * PBX)))
#define OVOFF(BR) (a.vk + (unsigned)((BR) * 64 + a.g) * OS + (a.head ^ ((BR) * 2 * PBX)) * 1024)
#define GATE() (*(const LAS float*)(a.lds + L_G + a.t * 128 + (a.lane & 31) * 4))

struct KV3 { bf16x8 k[3], v0[3], v1[3]; };
#define LDB(K, V0, V1, i) do { const int kt_ = a.g - (i); \
        const __amdgpu_buffer_rsrc_t rb_ = __builtin_amdgcn_make_buffer_rsrc(a.ws + WS_KV, 0, kt_ >= 0 ? KV_BYTES : 0u, 0x00020000); \
        LD3(K, V0, V1, rb_, (unsigned)((32 + (a.head ^ (2 * PBX))) * 64 + (kt_ >= 0 ? kt_ : 0)) * TS); } while (0)
#define LDB_FIRST(f) do { LDB(f.k[0], f.v0[0], f.v1[0], 0); SBAR(); LDB(f.k[1], f.v0[1], f.v1[1], 1); SBAR(); LDB(f.k[2], f.v0[2], f.v1[2], 2); SBAR(); } while (0)
#define LDA_FIRST(f) do { LD3(f.k[0], f.v0[0], f.v1[0], a.rkv, so0[0]); SBAR(); LD3(f.k[1], f.v0[1], f.v1[1], a.rkv, so0[1]); SBAR(); \
        LD3(f.k[2], f.v0[2], f.v1[2], a.rkv, so0[2]); SBAR(); } while (0)
__device__ __forceinline__ unsigned window_flag(const Att& a) {
    const int i = a.lane & 15, kt = a.g - (i < 9 ? i : 0);
    return kvflag(1, a.p >> 1, kt >= 0 ? kt : 0, (a.lane >> 4) & 1);
}

template <int C>
__device__ __forceinline__ void attn_full(const Att& a, f32x16 o0, f32x16 o1, f32x16 o2, f32x16 o3, f32x16 S1, unsigned so,
                                          __amdgpu_buffer_rsrc_t rwo, unsigned vwo, unsigned wi, const GAS u64* fp2, const GAS u64* fp3, KV3& nxt PROF_ARG) {
    const lds_ptr lds = a.lds; const int t = a.t, lane = a.lane;
    const f32x16 zc = f32x16{};
    bf16x8 ka = nxt.k[0], va0 = nxt.v0[0], va1 = nxt.v1[0], kb = nxt.k[1], vb0 = nxt.v0[1], vb1 = nxt.v1[1], kc = nxt.k[2], vc0 = nxt.v0[2], vc1 = nxt.v1[2], kd, vd0, vd1;
    const unsigned sBeg = a.sBeg, jb = a.g >> 2;
    unsigned ki = 3;
    vd1 = lds_frag(a.zp);
    lds_ptr pw = lds + L_PART + (4 * C + t) * PREC + lane * 16;
    lds_cptr pr0 = lds + L_PART + (4 * C + ((t - 1) & 3)) * PREC + lane * 16, pr1 = lds + L_PART + (4 * C + ((t - 2) & 3)) * PREC + lane * 16,
             pr2 = lds + L_PART + (4 * C + ((t - 3) & 3)) * PREC + lane * 16;
    unsigned ovo = OVOFF(0); float gf = 0.f;
    if (C) { BAR(); STAMP(4); }
    else { gf = GATE(); PIN(pw); PIN(pr0); PIN(pr1); PIN(pr2); PIN(ovo); PIN(gf);
           DRAIN(); if (lane == 0) __hip_atomic_store(OFLAG(1, 0), a.tag, RLX_AGENT); }
    bf16x8 q0 = lds_frag(a.qp[0]); SBAR();
    bf16x8 q1 = lds_frag(a.qp[1]), q2 = lds_frag(a.qp[2]), q3 = lds_frag(a.qp[3]);
    SBAR();
    f32x16 S0 = MFMA(ka, q0, zc);
    PIN(S0); SBAR();
    PIN(q1); PIN(q2); PIN(q3); PIN(vd1); PIN(o0); PIN(o1); PIN(o2); PIN(o3); PIN(S1);
    __builtin_amdgcn_s_waitcnt(0x0F76);
#pragma unroll
    for (int i = 0; i < 8; ++i) __builtin_amdgcn_s_setprio(C);
    STAMP(3);
    SBAR();
    unsigned wso = 0;
    u64 fv = a.tag;
#define NEXT1() asm volatile("s_add_i32 %0, %0, 1\n\ts_xor_b32 %1, %0, %2" : "+s"(ki), "=s"(so) : "s"(jb) : "scc")
#define NEXT2() asm volatile("s_and_b32 %0, %0, 15" : "+s"(so) :: "scc")
#define NEXT3() asm volatile("s_mulk_i32 %0, %2\n\ts_add_i32 %0, %0, %1" : "+s"(so) : "s"(sBeg), "i"(4 * TS) : "scc")
#define WO_OFF() do { if (C) asm volatile("s_lshl_b32 %0, %1, 4" : "=s"(wso) : "s"(wi) : "scc"); } while (0)
#define WO_DMA() do { if (C) __builtin_amdgcn_raw_ptr_buffer_load_lds(rwo, (LAS void*)(lds + L_WO + t * 16384 + wi), 16, vwo, wso, 0, 0); } while (0)
#define WO_NEXT() do { if (C) asm volatile("s_addk_i32 %0, 0x400\n\ts_and_b32 %0, %0, 0x3fff" : "+s"(wi) :: "scc"); } while (0)
#define TILE(K, V0, V1, KN, LK, LV0, LV1, NW, G) do { \
        STEP(S0, S1, o3, LV1, K, q1, zc, o0, V0, LK = BLOAD(a.rkv, a.vk, so), WO_OFF(), LV0 = BLOAD(a.rkv, a.vv0, so)); \
        STEP(S1, S0, o0, V1, K, q2, zc, o1, V0, LV1 = BLOAD(a.rkv, a.vv1, so), , WO_DMA()); \
        STEP(S0, S1, o1, V1, K, q3, zc, o2, V0, NEXT1(), NEXT2(), NEXT3()); \
        STEP(S1, S0, o2, V1, KN, q0, zc, o3, V0, __builtin_amdgcn_s_waitcnt(0x0F70 | (C ? NW : 6)), G, WO_NEXT()); } while (0)
#define PUTA(O, sl) (*(LAS f32x4*)(pw + (sl) * 2048) = __builtin_shufflevector(O, O, 0, 1, 2, 3))
#define PUTB(O, sl) (*(LAS f32x4*)(pw + (sl) * 2048 + 1024) = __builtin_shufflevector(O, O, 4, 5, 6, 7))
#define PUTL(O, sl) (*(LAS float*)(pw + 6144 + (sl) * 4) = O[8])
    f32x4 x0, y0, x1, y1, x2, y2; float l0, l1, l2;
#define GETP() do { x0 = *(const LAS f32x4*)pr0; x1 = *(const LAS f32x4*)(pr1 + 2048); x2 = *(const LAS f32x4*)(pr2 + 4096); \
        y0 = *(const LAS f32x4*)(pr0 + 1024); y1 = *(const LAS f32x4*)(pr1 + 3072); y2 = *(const LAS f32x4*)(pr2 + 5120); \
        l0 = *(const LAS float*)(pr0 + 6144); l1 = *(const LAS float*)(pr1 + 6148); l2 = *(const LAS float*)(pr2 + 6152); } while (0)
    if (C) {
#pragma nounroll
        for (int it = 0; it < 4; ++it) {
            TILE(ka, va0, va1, kb, kd, vd0, vd1, 7, );
            PIN(S0); PIN(S1); PIN(o3); wait_tags_end(fv, fp2, a.tag, nullptr, 0);
            TILE(kb, vb0, vb1, kc, ka, va0, va1, 8, );
            TILE(kc, vc0, vc1, kd, kb, vb0, vb1, 9, fv = __hip_atomic_load(fp2, RLX_AGENT));
            TILE(kd, vd0, vd1, ka, kc, vc0, vc1, 10, );
        }
        o3 = pv_hi(vd1, S1, o3, IDX);
        STAMP(6);
        const u64 f3 = __hip_atomic_load(fp3, RLX_AGENT);
        cnt_wait(lds + L_CNT + 8);
        PUTA(o0, 0); PUTB(o0, 0); PUTL(o0, 0); PUTA(o1, 1); PUTB(o1, 1); PUTL(o1, 1); PUTA(o2, 2); PUTB(o2, 2); PUTL(o2, 2);
        DRAIN(); cnt_arrive(lds + L_CNT, lane);
        wait_tags_end(f3, fp3, a.tag, nullptr, 0);
        LDB_FIRST(nxt);
        cnt_wait(lds + L_CNT);
        GETP(); gf = GATE();
    } else {
        TILE(ka, va0, va1, kb, kd, vd0, vd1, 0, );
        TILE(kb, vb0, vb1, kc, ka, va0, va1, 0, );
#pragma nounroll
        for (int it = 0; it < 3; ++it) {
            TILE(kc, vc0, vc1, kd, kb, vb0, vb1, 0, );
            TILE(kd, vd0, vd1, ka, kc, vc0, vc1, 0, );
            TILE(ka, va0, va1, kb, kd, vd0, vd1, 0, );
            TILE(kb, vb0, vb1, kc, ka, va0, va1, 0, );
        }
        STEP(S0, S1, o3, vb1, kc, q1, zc, o0, vc0, , , );
        STEP(S1, S0, o0, vc1, kc, q2, zc, o1, vc0, , , );
        STEP(S0, S1, o1, vc1, kd, q0, zc, o2, vc0, __builtin_amdgcn_s_waitcnt(0x0F73), , );
        STEP(S1, S0, o2, vc1, kd, q1, zc, o0, vd0, , , );
        STEP(S0, S1, o0, vd1, kd, q2, zc, o1, vd0, , , );
        STEP(S1, S0, o1, vd1, kc, q3, zc, o2, vd0, PUTA(o0, 0), PUTL(o0, 0), PUTB(o0, 0));
        STEP(S0, S1, o2, vd1, kd, q3, zc, o3, vc0, PUTA(o1, 1), PUTL(o1, 1), PUTB(o1, 1));
        PUTA(o2, 2); PUTB(o2, 2); SBAR();
        EX(S1, 0); EX(S1, 1); SBAR(); o3 = pv_hi(vc1, S0, o3, IDX); SBAR(); PUTL(o2, 2); __builtin_amdgcn_s_setprio(2); SBAR();
        EX(S1, 2); EX(S1, 3); EX(S1, 4); EX(S1, 5); EX(S1, 6); EX(S1, 7); SBAR(); EX(S1, 8); EX(S1, 9); SBAR();
        o3 = pv_lo(vd0, S1, o3, IDX); PIN(S1); SBAR();
        CBAR(); GETP(); SBAR();
        EX(S1, 10); EX(S1, 11); EX(S1, 12); EX(S1, 13); EX(S1, 14); EX(S1, 15); SBAR();
        o3 = pv_hi(vd1, S1, o3, IDX); SBAR();
        STAMP(6);
    }
    x0 = (x0 + x1) + x2; y0 = (y0 + y1) + y2; l0 = (l0 + l1) + l2;
    PIN(x0); PIN(y0); PIN(l0); PIN(o3);
#pragma unroll
    for (int r = 0; r < 4; ++r) { o3[r] += x0[r]; o3[4 + r] += y0[r]; }
    o3[8] += l0;
    finish<C, 0>(a, o3, gf, q3, ovo);
}

template <int C>
__device__ __forceinline__ void attn_window(const Att& a, KV3& f, const unsigned (&so0)[4] PROF_ARG) {
    const lds_ptr lds = a.lds; const int t = a.t, lane = a.lane;
    const f32x16 zc = f32x16{};
    bf16x8 k[9], v0[9], v1[9];
#pragma unroll
    for (int i = 0; i < 3; ++i) { k[i] = f.k[i]; v0[i] = f.v0[i]; v1[i] = f.v1[i]; }
#define LDW(i) LDB(k[i], v0[i], v1[i], i)
    const bf16x8 qa = lds_frag(lds + L_Q + 8192 + C * 4096 + t * 1024 + lane * 16);
    const f32x16 cd = lds_f32x16(lds + L_MDIAG + lane * 16), cf = lds_f32x16(lds + L_MFAR + lane * 16);
    f32x16 oo = lds_zero16(a.zp);
    const float gt = GATE();
    f32x16 S0 = MFMA(k[0], qa, cd), S1 = MFMA(k[1], qa, zc);
    SBAR(); LDW(3); SBAR();
#pragma unroll
    for (int r = 0; r < 10; ++r) { EX(S0, r); SBAR(); }
    oo = pv_lo(v0[0], S0, oo, IDX); SBAR();
#pragma unroll
    for (int r = 10; r < 16; ++r) EX(S0, r);
    SBAR();
    STEP(S1, S0, oo, v1[0], k[2], qa, zc, oo, v0[1], LDW(4), , );
    STEP(S0, S1, oo, v1[1], k[3], qa, zc, oo, v0[2], LDW(5), , );
    STEP(S1, S0, oo, v1[2], k[4], qa, zc, oo, v0[3], LDW(6), , );
    STEP(S0, S1, oo, v1[3], k[5], qa, zc, oo, v0[4], LDW(7), , );
    STEP(S1, S0, oo, v1[4], k[6], qa, zc, oo, v0[5], LDW(8), , );
    const GAS u64* fa = (const GAS u64*)(a.ws + kvflag(1, a.p >> 1, t, 0) + (lane & 31) * FSTR);
    u64 fv = 0;
    STEP(S0, S1, oo, v1[5], k[7], qa, zc, oo, v0[6], if (!C) fv = __hip_atomic_load(fa, RLX_AGENT), , );
    STEP(S1, S0, oo, v1[6], k[8], qa, cf, oo, v0[7], , , );
    if (C) { PIN(S0); PIN(S1); PIN(oo); DRAIN(); if (lane == 0) __hip_atomic_store(OFLAG(0, C), a.tag, RLX_AGENT); }
    STEP(S0, S1, oo, v1[7], k[8], qa, cf, oo, v0[8], , , );
    oo = pv_hi(v1[8], S0, oo, IDX);
    STAMP(7);
    if (!C) { PIN(oo); wait_tags_end(fv, fa, a.tag, nullptr, 0); LDA_FIRST(f); }
    finish<C, 1>(a, oo, 1.f - gt, qa, OVOFF(1));
}

__global__ void __launch_bounds__(512, 2)
gmix_kernel(const char* x, const char* wA, const char* woA, const char* wB, const char* woB, const char* gw, char* out, char* ws) {
    extern __shared__ __attribute__((aligned(16))) char smem[];
    const lds_ptr lds = (lds_ptr)smem;
    const unsigned lds0 = (unsigned)(uintptr_t)lds;
    const int tid = threadIdx.x, lane = tid & 63, r32 = lane & 31, hh = lane >> 5;
    const int w = __builtin_amdgcn_readfirstlane(tid >> 6);
    const int bid = blockIdx.x, xcd = bid & 7, rr = bid >> 3;
    const int p = 4 * (xcd & 3) + (rr & 3), j = 8 * (xcd >> 2) + (rr >> 2);
    const int jo = 2 * xcd + (rr >> 4), po = rr & 15;
    const int t = w & 3, c = w >> 2;
    const int e = p & 1;
    Rev stv;
    STAMP(0);
    {
        u64 pc; const unsigned ln = (((bid & ~7) | (tid >> 6)) + (bid < CODE_LINES - 256 ? (tid & 32) * 8 : 0)) * 128;
        asm volatile("s_getpc_b64 %0\n\ts_mov_b32 m0, %2\n\ts_nop 0\n\tglobal_load_lds_dword %1, %0" : "=&s"(pc) : "v"(ln), "s"(lds0 + L_SCR) : "memory");
        SBAR();
    }
    unsigned rot = (ROTEXPR) & 7;
    unsigned vx0 = (128 * j + 32 * t + (lane >> 3)) * 1024 + (((lane & 7) ^ (lane >> 4)) * 16), vx1 = (vx0 ^ 64) + 7168, vx2 = vx0 + 14336, vx3 = vx1 + 14336;
    unsigned vw0 = (16 * t + (lane >> 3)) * 3072 + 1024 + 1024 * e + 128 * ((p >> 1) ^ QSWAP) + (((lane & 7) ^ (((lane >> 4) & 1) << 1)) * 16), vw1 = vw0 + 8 * 3072 - 1024;
    unsigned mx = lds0 + t * 4096, mw = lds0 + L_W + t * 2048, ow = rot * (64 * 3072);
    asm volatile("" : "+v"(vx0), "+v"(vx1), "+v"(vx2), "+v"(vx3), "+v"(vw0), "+v"(vw1));
    asm volatile("" : "+s"(mx), "+s"(mw), "+s"(rot), "+s"(ow));
    SBAR();
    asm volatile("" : "+s"(x), "+s"(wA), "+s"(woA), "+s"(wB), "+s"(woB), "+s"(gw), "+s"(out), "+s"(ws));
#define MK_ATT(C) Att a; a.lds = lds; a.ws = ws; a.lane = lane; a.t = t; a.p = p; a.g = 4 * j + t; a.head = 2 * p + C; a.idx = 0xdddddddd; \
    a.rkv = __builtin_amdgcn_make_buffer_rsrc(ws + WS_KV, 0, KV_BYTES, 0x00020000); a.ro = __builtin_amdgcn_make_buffer_rsrc(ws + WS_O, 0, O_BYTES, 0x00020000); \
    a.vk = lane * 16; a.vv0 = r32 < 16 ? 1024 + hh * 256 + r32 * 16 : ((r32 & ~4) == 16 ? 2048 : OOB); a.vv1 = r32 < 16 ? a.vv0 + 512 : a.vv0; a.zp = lds + L_Z + lane * 16; \
    a.sBeg = (unsigned)(a.head * 64 + t) * TS; \
    _Pragma("unroll") for (int i = 0; i < 4; ++i) a.qp[i] = lds + L_Q + C * 4096 + ((t + 1 + i) & 3) * 1024 + lane * 16
#define MK_SO() unsigned so0[4]; _Pragma("unroll") for (int i = 0; i < 4; ++i) so0[i] = a.sBeg + (j ^ i) * (4 * TS)
    if (c) {
#define DMA1(s) do { const unsigned ks_ = ((s) + rot) & 7; \
        glds16x4(vx0, vx1, vx2, vx3, x + ks_ * 128, mx + ((s) & 3) * 16384); glds16x2(vw0, vw1, wA + ks_ * (64 * 3072), mw + ((s) & 3) * 8192); } while (0)
#define DMA2(s) do { const unsigned ks_ = (((s) + rot) & 7) * (64 * 3072); \
        glds16x2(vq0, vq1, wq + ks_, mq + ((s) == 14 ? L_Q14 : ((s) & 3) * 16384)); glds16x2(vw0, vw1, wy + ks_, mw + ((s) & 3) * 8192); SBAR(); } while (0)
#define LBAR(N) asm volatile("s_waitcnt vmcnt(" #N ")\n\ts_barrier" ::: "memory")
        STAMP(20);
        glds16x4(vx0, vx1, vx2, vx3, x + rot * 128, mx); glds16x2(vw0, vw1, wA + ow, mw);
        SBAR();
        const unsigned k1 = (1 + rot) & 7;
        glds16x2(vx0, vx1, x + k1 * 128, mx + 16384);
        SBAR();
        const u64 ep = *(const __attribute__((address_space(4))) u64*)(ws + WS_EPOCH);
        unsigned vq0 = (32 * (t & 1) + 16 * e + (lane >> 2)) * 3072 + 64 * (p ^ ((t >> 1) * PBX)) + (lane & 3) * 16 + 1024, vq1 = vq0 + (e ? -16 * 3072 : 16 * 3072) - 1024;
        unsigned vwo = ((t & 1) * 256 + (lane >> 2)) * 1024 + 64 * po + (lane & 3) * 16;
        MK_ATT(1);
        const GAS u64* fp = (const GAS u64*)(ws + kvflag(0, p >> 1, (j >> 3) * 32 + t, 0) + (lane & 15) * FSTR);
        const GAS u64* fp2 = (const GAS u64*)(ws + kvflag(0, p >> 1, ((j >> 3) ^ 1) * 32 + t, 0) + (lane & 15) * FSTR);
        const GAS u64* fp3 = (const GAS u64*)(ws + (hh ? WS_OFLAG + ((64 + 4 * jo + t) * 32 + (lane & 15)) * 8 : window_flag(a)));
        PIN(vq0); PIN(vq1); PIN(vwo); PIN(a.vk); PIN(a.vv0); PIN(a.vv1); PIN(a.qp[0]); PIN(a.qp[1]); PIN(a.qp[2]); PIN(a.qp[3]); PIN(a.zp); PIN(fp); PIN(fp2); PIN(fp3); PIN(a.idx);
        SBAR();
        STAMP(30);
        glds16x2(vx0, vx1, x + k1 * 128 + 16384, mx + 16384 + 2048); glds16x2(vw0, vw1, wA + k1 * (64 * 3072), mw + 8192);
        STAMP(22);
        SBAR();
#pragma nounroll
        for (int s = 2; s < 8; ++s) { LBAR(6); DMA1(s);
            if (s == 2) STAMP(1); if (s == 3) STAMP(2);
        }
        const unsigned mq = lds0 + t * 2048;
        const char* wq = ((t >> 1) ? wB : wA) - 1024;
        const char* wy = wB + 128 * ((((p >> 1) ^ QSWAP) ^ (PBX >> 1)) - ((p >> 1) ^ QSWAP));
        LBAR(6); STAMP(14); DMA2(8);
        LBAR(4); STAMP(15); DMA2(9);
        a.tag = ep * 0x9E3779B97F4A7C15ull + 0x632BE59BD9B4E019ull;
        *(LAS u64*)(lds + L_CNT + 16) = a.tag;
        asm volatile("s_waitcnt lgkmcnt(0)" ::: "memory");
        LBAR(4); STAMP(16); DMA2(10);
        const unsigned k7 = ((7 + rot) & 7) * (64 * 3072), sx = mq + L_Q15, sw = mw + 32768;
        LBAR(4); STAMP(17); glds16(vq0, wq + k7, sx); DMA2(11);
        f32x16 oo = lds_zero16(a.zp), o1 = lds_zero16(a.zp); PIN(oo); PIN(o1);
        LBAR(5); STAMP(18); glds16(vw0, wy + k7, sw); DMA2(12);
        f32x16 o2 = lds_zero16(a.zp), o3 = lds_zero16(a.zp); PIN(o2); PIN(o3);
        LBAR(5); STAMP(19); glds16(vq1, wq + k7 + 1024, sx + 1024); DMA2(13);
        f32x16 S1 = lds_zero16(a.zp); PIN(S1);
        LBAR(5); STAMP(21); glds16(vw1, wy + k7 + 1024, sw + 1024); DMA2(14);
        __amdgpu_buffer_rsrc_t rwo = __builtin_amdgcn_make_buffer_rsrc((void*)((t >> 1) ? woB : woA), 0, AB_WO, 0x00020000);
        unsigned wi0 = 4096 * (((rr >> 4) << 1) | (po & 1)) + WOX * 1024 * (xcd & 3);
        MK_SO();
        asm volatile("" : "+s"(a.rkv), "+s"(rwo), "+s"(a.sBeg), "+s"(wi0), "+s"(so0[0]), "+s"(so0[1]), "+s"(so0[2]), "+s"(so0[3]));
        __builtin_amdgcn_s_setprio(1);
        LBAR(5); STAMP(24);
        const u64 fv = __hip_atomic_load(fp, RLX_AGENT);
        LBAR(1); STAMP(29);
        wait_tags_end(fv, fp, a.tag, nullptr, lane);
        STAMP(5);
        KV3 kw;
        LDA_FIRST(kw);
        attn_full<1>(a, oo, o1, o2, o3, S1, so0[3], rwo, vwo, wi0, fp2, fp3, kw PROF_PASS);
        STAMP(23);
        attn_window<1>(a, kw, so0 PROF_PASS);
        STAMP(8);
        const int go = 4 * jo + t;
        const lds_cptr wo = lds + L_WO + (4 * hh + ((lane & 15) >> 2)) * 64 + ((lane >> 4) & 1) * 32 + (lane & 3) * 8;
        const GAS u64* of_ = (const GAS u64*)(ws + WS_OFLAG);
#define OBR(n) ((n) >> 4 == 1 ? 0 : 1)
#define OHD(n) (2 * ((n) & 15) + ((n) >= 16))
#define OLD(n) BLOAD(a.ro, a.vk, (unsigned)(OBR(n) * 64 + go) * OS + OHD(n) * 1024)
#define OWF(n) tr_frag(wo + OBR(n) * 32768 + OHD(n) * 1024, wo + OBR(n) * 32768 + OHD(n) * 1024 + 512)
#define OIN 6
        bf16x8 oe[48];
#pragma unroll
        for (int n = 0; n < 48; ++n) {
            if (n == 16) { wait_tags(of_ + go * 32 + 16 + (lane & 15), a.tag, OFLAG(1, 1), lane); STAMP(9); }
            if (n == 32) wait_tags(of_ + (64 + go) * 32 + 16 + (lane & 15), a.tag);
            oe[n] = OLD(n); SBAR();
            if (n >= OIN - 1) __builtin_amdgcn_s_waitcnt(0x0F70 | (OIN - 1));
            SBAR();
        }
        bf16x8 wn[4];
#pragma unroll
        for (int n = 0; n < 4; ++n) { wn[n] = OWF(n); PIN(wn[n]); }
        __builtin_amdgcn_s_waitcnt(0x0F70);
#define NEARLY 16
#define NMID 32
        unsigned sg[4]; lds_cptr wg[4];
#pragma unroll
        for (int q = 0; q < 4; ++q) { const int gq = ((p >> 2) + 1 + q) & 3; sg[q] = (unsigned)go * OS + gq * 8192; wg[q] = wo + gq * 8192; PIN(wg[q]); }
        unsigned vout = (32 * go + r32) * 1024 + 64 * po + 16 * hh;
        __amdgpu_buffer_rsrc_t rout = __builtin_amdgcn_make_buffer_rsrc(out, 0, 2048 * 1024, 0x00020000);
        PIN(vout); asm volatile("" : "+s"(sg[0]), "+s"(sg[1]), "+s"(sg[2]), "+s"(sg[3]), "+s"(rout));
        f32x16 ya = f32x16{};
        bf16x8 wf[16], of[16];
#define FLD(i) of[i] = BLOAD(a.ro, a.vk, sg[(i) >> 2] + ((i) & 3) * 2048)
#define GRP_BAD(q) ({ unsigned mx_ = 0; _Pragma("unroll") for (int i = 4 * (q); i < 4 * (q) + 4; ++i) { const u32x4 u_ = __builtin_bit_cast(u32x4, of[i]); \
            mx_ = __builtin_elementwise_max(__builtin_elementwise_max(mx_, u_[0]), u_[1]); mx_ = __builtin_elementwise_max(__builtin_elementwise_max(mx_, u_[2]), u_[3]); } \
            __any(mx_ == ~0u); })
#define GRP_LD(q) do { _Pragma("unroll") for (int i = 4 * (q); i < 4 * (q) + 4; ++i) FLD(i); } while (0)
        STAMP(10);
        BAR();
        BAR();
#pragma unroll
        for (int n = 0; n < 48; ++n) {
            ya = MFMA(wn[n & 3], oe[n], ya); SBAR();
            if (n >= NEARLY && n < NEARLY + 12) FLD((n - NEARLY) & 15);
            if (n >= NMID && n < NMID + 4) FLD((n - NMID + 12) & 15);
            if (n + 4 < 48) wn[n & 3] = OWF(n + 4);
            if (n >= 32) wf[n - 32] = tr_frag(wg[(n - 32) >> 2] + (n & 3) * 2048, wg[(n - 32) >> 2] + (n & 3) * 2048 + 512);
            SBAR();
        }
        STAMP(31);
        f32x16 yb = lds_zero16(a.zp), yc = lds_zero16(a.zp), yd = lds_zero16(a.zp);
        unsigned pend = 15;
#define GRP_TRY(q, Y) do { if (pend & (1u << (q))) { if (!GRP_BAD(q)) { \
            _Pragma("unroll") for (int i = 4 * (q); i < 4 * (q) + 4; ++i) Y = MFMA(wf[i], of[i], Y); pend ^= 1u << (q); } } } while (0)
        unsigned spins = SPIN_CAP;
#pragma nounroll
        do {
            GRP_TRY(0, ya); GRP_TRY(1, yb); GRP_TRY(2, yc); GRP_TRY(3, yd);
            asm volatile("s_sub_u32 %1, %1, 1\n\ts_cselect_b32 %0, 0, %0" : "+s"(pend), "+s"(spins) :: "memory", "scc");
            if (pend & 1) GRP_LD(0); if (pend & 2) GRP_LD(1); if (pend & 4) GRP_LD(2); if (pend & 8) GRP_LD(3);
        } while (pend);
        ya = (ya + yb) + (yc + yd);
#pragma unroll
        for (int s = 0; s < 2; ++s) {
            auto u = __builtin_amdgcn_permlane32_swap(cvtpk(ya[8 * s], ya[8 * s + 1]), cvtpk(ya[8 * s + 4], ya[8 * s + 5]), false, false);
            auto v = __builtin_amdgcn_permlane32_swap(cvtpk(ya[8 * s + 2], ya[8 * s + 3]), cvtpk(ya[8 * s + 6], ya[8 * s + 7]), false, false);
#define OUT_AUX 0
            __builtin_amdgcn_raw_buffer_store_b128(((u32x4){u[0], v[0], u[1], v[1]}), rout, vout + 32 * s, 0, OUT_AUX);
        }
        if (bid == 0 && tid == 256) __hip_atomic_store((GAS u64*)(ws + WS_EPOCH), a.tag, RLX_AGENT);
        STAMP(12);
    } else {
        glds16((((lane >> 3) + rot) & 7) * 128 + (lane & 7) * 16, gw, lds0 + L_GW + t * 1024);
#pragma unroll
        for (int m = 0; m < 2; ++m) {
            f32x4 mk;
#pragma unroll
            for (int i = 0; i < 4; ++i) { const bool le = (i + 8 * t + 4 * hh) <= r32; mk[i] = (le != (m != 0)) ? 0.f : -__builtin_inff(); }
            *(LAS f32x4*)(lds + L_MDIAG + m * 4096 + t * 1024 + lane * 16) = mk;
        }
        if (tid < 4) *(LAS unsigned*)(lds + L_CNT + tid * 4) = 0;
        *(LAS unsigned*)(lds + L_Z + tid * 4) = 0;
        u64 tag;
        STAMP(21);
        {
            const __amdgpu_buffer_rsrc_t rkv = __builtin_amdgcn_make_buffer_rsrc(ws + WS_KV, 0, KV_BYTES, 0x00020000);
            const __amdgpu_buffer_rsrc_t ro = __builtin_amdgcn_make_buffer_rsrc(ws + WS_O, 0, O_BYTES, 0x00020000);
            if (e) project<1>(lds, ws, rkv, ro, lane, t, p, j, tag PROF_PASS); else project<0>(lds, ws, rkv, ro, lane, t, p, j, tag PROF_PASS);
        }
        cnt_wait(lds + L_CNT + 8);
        __builtin_amdgcn_s_sleep(BSLP);
        STAMP(24);
        MK_ATT(0);
        a.tag = tag;
        wait_tags((const GAS u64*)(ws + window_flag(a)), tag);
        STAMP(25);
        KV3 kw;
        LDB_FIRST(kw);
        MK_SO();
        attn_window<0>(a, kw, so0 PROF_PASS);
        STAMP(23);
        PIN(a.idx);
        attn_full<0>(a, lds_zero16(a.zp), lds_zero16(a.zp), lds_zero16(a.zp), lds_zero16(a.zp), lds_zero16(a.zp), so0[3], a.rkv, 0, 0, nullptr, nullptr, kw PROF_PASS);
        BAR();
        STAMP(8);
    }
    DRAIN();
    STAMP(13);
    if (c == RROLE) while (__builtin_amdgcn_s_memrealtime() < stv.r0 + 6000 + stv.d) __builtin_amdgcn_s_sleep(1);
}

extern "C" void kernel_launch(void* const* d_in, const int* in_sizes, int n_in, void* d_out, int out_size, void* d_ws, size_t ws_size,
                              hipStream_t stream) {
    (void)in_sizes; (void)n_in; (void)out_size; (void)ws_size;
    (void)hipFuncSetAttribute((const void*)gmix_kernel, hipFuncAttributeMaxDynamicSharedMemorySize, L_END);
    gmix_kernel<<<256, 512, L_END, stream>>>((const char*)d_in[0], (const char*)d_in[1], (const char*)d_in[2], (const char*)d_in[4],
                                             (const char*)d_in[5], (const char*)d_in[6], (char*)d_out, (char*)d_ws);
}
```

```cpp
#define OUT_LDS
#define OUT_AUX 17
#include <hip/hip_runtime.h>
#include <hip/hip_bf16.h>

typedef __attribute__((ext_vector_type(8))) __bf16 bf16x8;
typedef __attribute__((ext_vector_type(16))) __bf16 bf16x16;
typedef __attribute__((ext_vector_type(16))) float f32x16;
typedef __attribute__((ext_vector_type(8))) float f32x8;
typedef __attribute__((ext_vector_type(4))) float f32x4;
typedef __attribute__((ext_vector_type(4))) unsigned u32x4;
typedef __attribute__((ext_vector_type(2))) unsigned u32x2;
typedef __attribute__((ext_vector_type(2))) __bf16 bf16x2;
typedef short s16x4 __attribute__((ext_vector_type(4)));
typedef unsigned long long u64;
#define GAS __attribute__((address_space(1)))
#define LAS __attribute__((address_space(3)))
typedef LAS const char* lds_cptr;
typedef LAS char* lds_ptr;

constexpr unsigned WS_EPOCH = 0;
#define FSTR 8
constexpr unsigned WS_OFLAG = 65536;
constexpr unsigned WS_KVFLAG = 131072;
constexpr unsigned WS_KV = 1u << 20;
constexpr unsigned TS = 2176;
constexpr unsigned KV_BYTES = 2u * 32u * 64u * TS;
#define OSTR 32896
constexpr unsigned WS_O = 10u << 20;
constexpr unsigned OS = OSTR;
constexpr unsigned O_BYTES = 128u * OS;
constexpr unsigned WS_STAMP = 16u << 20;
constexpr unsigned OOB = 0x7ff00000u;
constexpr unsigned L_W = 65536;
constexpr unsigned L_WO = 0, L_PART = 65536, PREC = 7168, L_Q14 = 106496, L_Q15 = 114688, L_MDIAG = 122880, L_MFAR = 126976, L_Q = 131072,
                   L_GW = 147456, L_Z = 151552, L_G = 152576, L_CNT = 153088, L_SCR = 153600, L_END = 154112;
constexpr float QSCALE = 0.25f * 1.4426950408889634f;
#define LMAXV 1.2676506e30f
constexpr float LMAX = LMAXV, LMIN = 9.094947e-13f;
constexpr unsigned SPIN_CAP = 1u << 17;

#define SBAR() __builtin_amdgcn_sched_barrier(0)
#define PIN(x) asm volatile("" : "+v"(x))
#define MFMA(a, b, c) __builtin_amdgcn_mfma_f32_32x32x16_bf16(a, b, c, 0, 0, 0)
#define BAR() asm volatile("s_barrier" ::: "memory")
#define CBAR() asm volatile("s_waitcnt lgkmcnt(0)\n\ts_barrier" ::: "memory")
#define DRAIN() asm volatile("s_waitcnt vmcnt(0)" ::: "memory")
#define PACEN 6
#define PACE() asm volatile("s_nop %0" :: "i"(PACEN) : "memory")
#define RLX_AGENT __ATOMIC_RELAXED, __HIP_MEMORY_SCOPE_AGENT
#define STAMP(i)

__device__ __forceinline__ void glds16(unsigned voff, const void* sbase, unsigned ldsaddr) {
    asm volatile("s_mov_b32 m0, %2\n\ts_nop 0\n\tglobal_load_lds_dwordx4 %0, %1" ::"v"(voff), "s"(sbase), "s"(ldsaddr) : "memory");
}
__device__ __forceinline__ void glds16x2(unsigned v0, unsigned v1, const void* sbase, unsigned ldsaddr) {
    asm volatile("s_mov_b32 m0, %3\n\ts_nop 0\n\tglobal_load_lds_dwordx4 %0, %2\n\tglobal_load_lds_dwordx4 %1, %2 offset:1024"
                 ::"v"(v0), "v"(v1), "s"(sbase), "s"(ldsaddr) : "memory");
}
__device__ __forceinline__ void glds16x4(unsigned v0, unsigned v1, unsigned v2, unsigned v3, const void* sbase, unsigned ldsaddr) {
    asm volatile("s_mov_b32 m0, %5\n\ts_nop 0\n\tglobal_load_lds_dwordx4 %0, %4\n\tglobal_load_lds_dwordx4 %1, %4 offset:1024\n\t"
                 "global_load_lds_dwordx4 %2, %4 offset:2048\n\tglobal_load_lds_dwordx4 %3, %4 offset:3072"
                 ::"v"(v0), "v"(v1), "v"(v2), "v"(v3), "s"(sbase), "s"(ldsaddr) : "memory");
}
__device__ __forceinline__ unsigned cvtpk(float lo, float hi) {
    typedef __attribute__((ext_vector_type(2))) float f32x2;
    return __builtin_bit_cast(unsigned, __builtin_convertvector((f32x2){lo, hi}, bf16x2));
}
__device__ __forceinline__ bf16x8 as_frag(u32x4 v) { return __builtin_bit_cast(bf16x8, v); }
__device__ __forceinline__ bf16x8 lds_frag(lds_cptr p) { return *(const LAS bf16x8*)p; }
__device__ __forceinline__ bf16x8 tr_frag(lds_cptr p0, lds_cptr p1) {
    s16x4 a = __builtin_amdgcn_ds_read_tr16_b64_v4i16((LAS s16x4*)p0);
    s16x4 b = __builtin_amdgcn_ds_read_tr16_b64_v4i16((LAS s16x4*)p1);
    return __builtin_bit_cast(bf16x8, __builtin_shufflevector(a, b, 0, 1, 2, 3, 4, 5, 6, 7));
}
__device__ __forceinline__ f32x16 pv_lo(bf16x8 v, const f32x16& p, f32x16 acc, int idx) {
    f32x8 h = __builtin_shufflevector(p, p, 0, 1, 2, 3, 4, 5, 6, 7);
    return __builtin_amdgcn_smfmac_f32_32x32x32_bf16(v, __builtin_bit_cast(bf16x16, h), acc, idx, 0, 0);
}
__device__ __forceinline__ f32x16 pv_hi(bf16x8 v, const f32x16& p, f32x16 acc, int idx) {
    f32x8 h = __builtin_shufflevector(p, p, 8, 9, 10, 11, 12, 13, 14, 15);
    return __builtin_amdgcn_smfmac_f32_32x32x32_bf16(v, __builtin_bit_cast(bf16x16, h), acc, idx, 0, 0);
}
#define MX3(a, b, c) __builtin_fmaxf(__builtin_fmaxf((a), (b)), (c))
__device__ __forceinline__ float max16(const f32x16& s) {
    float a = MX3(s[0], s[1], s[2]), b = MX3(s[3], s[4], s[5]);
    a = MX3(a, s[6], s[7]); b = MX3(b, s[8], s[9]); a = MX3(a, s[10], s[11]); b = MX3(b, s[12], s[13]);
    return MX3(a, b, __builtin_fmaxf(s[14], s[15]));
}
__device__ __forceinline__ float halfmax(float m) {
    auto r = __builtin_amdgcn_permlane32_swap(__float_as_uint(m), __float_as_uint(m), false, false);
    return __builtin_fmaxf(__uint_as_float(r[0]), __uint_as_float(r[1]));
}
__device__ __forceinline__ f32x16 lds_f32x16(lds_cptr p) {
    f32x4 a = *(const LAS f32x4*)p, b = *(const LAS f32x4*)(p + 1024), c = *(const LAS f32x4*)(p + 2048), d = *(const LAS f32x4*)(p + 3072);
    f32x8 lo = __builtin_shufflevector(a, b, 0, 1, 2, 3, 4, 5, 6, 7), hi = __builtin_shufflevector(c, d, 0, 1, 2, 3, 4, 5, 6, 7);
    return __builtin_shufflevector(lo, hi, 0, 1, 2, 3, 4, 5, 6, 7, 8, 9, 10, 11, 12, 13, 14, 15);
}
#define BLOAD(rs, vo, so) as_frag(__builtin_amdgcn_raw_buffer_load_b128(rs, vo, so, 16))
#define BSTORE(v, rs, vo, so) __builtin_amdgcn_raw_buffer_store_b128(v, rs, (vo) + (so), 0, 16)

#define POLLSLP 8
#define BSLP 36
__device__ __forceinline__ void wait_tags_end(u64 v, const GAS u64* p, u64 tag, GAS u64* pub, int lane) {
    if (pub) { DRAIN(); if (lane == 0) __hip_atomic_store(pub, tag, RLX_AGENT); }
    if (__builtin_expect(!__all(v == tag), 0)) {
#pragma nounroll
        for (unsigned spins = 0; spins < SPIN_CAP; ++spins) {
            __builtin_amdgcn_s_sleep(POLLSLP);
            if (__all(__hip_atomic_load(p, RLX_AGENT) == tag)) break;
        }
    }
    asm volatile("" ::: "memory");
}

__device__ __forceinline__ void wait_tags(const GAS u64* p, u64 tag, GAS u64* pub = nullptr, int lane = 0) {
    wait_tags_end(__hip_atomic_load(p, RLX_AGENT), p, tag, pub, lane);
}

__device__ __forceinline__ void cnt_arrive(lds_ptr cnt, int lane) {
    asm volatile("s_waitcnt lgkmcnt(0)" ::: "memory");
    if (lane == 0) __hip_atomic_fetch_add((LAS unsigned*)cnt, 1u, __ATOMIC_RELAXED, __HIP_MEMORY_SCOPE_WORKGROUP);
}
__device__ __forceinline__ void cnt_wait(lds_ptr cnt) {
#pragma nounroll
    for (unsigned spins = 0; spins < (1u << 22); ++spins) {
        if (__builtin_amdgcn_readfirstlane(__hip_atomic_load((LAS unsigned*)cnt, __ATOMIC_RELAXED, __HIP_MEMORY_SCOPE_WORKGROUP)) == 4) break;
        __builtin_amdgcn_s_sleep(1);
    }
    asm volatile("" ::: "memory");
}
__device__ __forceinline__ unsigned kvflag(int br, int quad, int tile, int type) {
    return WS_KVFLAG + (unsigned)(((((br * 8 + quad) * 4 + (tile & 3)) * 2 + (tile >> 5)) * 2 + type) * 8 + ((tile >> 2) & 7)) * FSTR;
}

__device__ __forceinline__ void slow_rows(__amdgpu_buffer_rsrc_t rs, unsigned sbase, int lo, int hi, int g, bool band, bf16x8 q,
                                          unsigned vk, unsigned vv0, unsigned vv1, lds_cptr lds, int lane, int idx, f32x16& acc) {
    float m = -__builtin_inff();
    for (int pass = 0; pass < 2; ++pass) {
        if (pass) { m = halfmax(m); acc = f32x16{}; }
        for (int kt = lo; kt <= hi; ++kt) {
            const unsigned so = sbase + (unsigned)kt * TS;
            const bf16x8 k = BLOAD(rs, vk, so), v0 = BLOAD(rs, vv0, so), v1 = BLOAD(rs, vv1, so);
            f32x16 c = f32x16{};
            if (band && kt == g) c = lds_f32x16(lds + L_MDIAG + lane * 16);
            if (band && kt == g - 8) c = lds_f32x16(lds + L_MFAR + lane * 16);
            f32x16 s = MFMA(k, q, c);
            if (!pass) m = __builtin_fmaxf(m, max16(s));
            else {
#pragma unroll
                for (int r = 0; r < 16; ++r) s[r] = __builtin_amdgcn_exp2f(s[r] - m);
                acc = pv_lo(v0, s, acc, idx); acc = pv_hi(v1, s, acc, idx);
            }
        }
    }
}

#define PK4(A, o) ((u32x4){cvtpk(A[o], A[o + 1]), cvtpk(A[o + 2], A[o + 3]), cvtpk(A[o + 4], A[o + 5]), cvtpk(A[o + 6], A[o + 7])})
#define PROF_ARG
#define PROF_PASS
template <int E>
__device__ __forceinline__ u32x4 pack_kv(int i, const f32x16& a) {
    const int s = i & 1;
    if (E == 0) return PK4(a, 8 * s);
    auto x = __builtin_amdgcn_permlane32_swap(cvtpk(a[8 * s], a[8 * s + 1]), cvtpk(a[8 * s + 4], a[8 * s + 5]), false, false);
    auto y = __builtin_amdgcn_permlane32_swap(cvtpk(a[8 * s + 2], a[8 * s + 3]), cvtpk(a[8 * s + 6], a[8 * s + 7]), false, false);
    return (u32x4){x[0], y[0], x[1], y[1]};
}
#define PUT_KV(i, v, vo) BSTORE(v, rkv, vo, ((i) < 2 ? 64 * TS : 0) + ((i) & 1) * (E ? 512 : 128 * TS))
#define QSWAP 1
#define PBX 4
template <int E>
__device__ __forceinline__ void project(lds_ptr lds, char* ws, __amdgpu_buffer_rsrc_t rkv, __amdgpu_buffer_rsrc_t ro, int lane, int t, int p, int j, u64& tag PROF_ARG) {
    const int r32 = lane & 31, hh = lane >> 5, qd4 = (p >> 1) ^ QSWAP;
    const unsigned q4 = (lane & 15) >> 2, wcol = ((lane >> 4) & 1) * 32 + (lane & 3) * 8;
    lds_cptr xa[4];
#pragma unroll
    for (int ks = 0; ks < 4; ++ks) xa[ks] = lds + (32 * t + r32) * 128 + (((hh ^ ((r32 >> 1) & 7)) * 16) ^ (ks * 32));
    unsigned w1o = L_W + (8 * hh + q4) * 128 + ((lane >> 4) & 1) * 64 + (q4 >> 1) * 32 + (lane & 3) * 8, w0o = w1o ^ 32;
    PIN(w0o); PIN(w1o);
    const lds_cptr w0 = lds + w0o, w1 = lds + w1o, wq = lds + (8 * hh + q4) * 64 + wcol;
#define XRD(n) lds_frag(xa[(n) & 3] + (((n) >> 2) & 3) * 16384)
#define WRD(b, n) tr_frag(b + ((n) & 15) * 2048, b + ((n) & 15) * 2048 + 512)
#define YOF(n) ((n) >= 28 ? 32768 + ((n) & 3) * 2048 : ((n) & 15) * 2048)
#define YRD(b, n) tr_frag(b + YOF(n), b + YOF(n) + 512)
#define QOF(br, n) (((n) >= 28 ? L_Q15 - L_Q14 : ((n) >= 24 ? 0 : (((n) >> 2) & 3) * 16384)) + (br) * 4096 + (((n) & 3) ^ E) * 1024)
#define QRD(br, n) tr_frag(((n) >= 24 ? wqh : wq) + QOF(br, n), ((n) >= 24 ? wqh : wq) + QOF(br, n) + 256)
#define MM(W, X, A) (E ? MFMA(X, W, A) : MFMA(W, X, A))
    bf16x8 xf[32], f0[4], f1[4];
    f32x16 a0 = f32x16{}, a1 = f32x16{};
    unsigned gwo = (lane & 15) == ((lane >> 4) & 1) ? L_GW + t * 1024 + hh * 16 : L_Z;
    PIN(gwo);
    f32x4 zg = f32x4{};
    bf16x8 gf[4];
#define RD1(n) do { xf[n] = XRD(n); f0[(n) & 3] = WRD(w0, n); f1[(n) & 3] = WRD(w1, n); SBAR(); } while (0)
#define GRD(n) gf[(n) & 3] = lds_frag(lds + gwo + (n) * 32)
#define GMM(n) zg = __builtin_amdgcn_mfma_f32_16x16x32_bf16(gf[(n) & 3], xf[n], zg, 0, 0, 0)
#define MM1(n) do { a0 = MM(f0[(n) & 3], xf[n], a0); a1 = MM(f1[(n) & 3], xf[n], a1); SBAR(); } while (0)
#pragma unroll
    for (int s = 0; s < 8; ++s) {
        if (s == 0) CBAR(); else BAR();
        SBAR();
        RD1(4 * s); RD1(4 * s + 1);
        if (s) { MM1(4 * s - 1); GMM(4 * s - 4); GMM(4 * s - 3); GMM(4 * s - 2); GMM(4 * s - 1); SBAR(); }
        RD1(4 * s + 2); MM1(4 * s); RD1(4 * s + 3); MM1(4 * s + 1);
        if (s == 0) DRAIN();
        GRD(4 * s); GRD(4 * s + 1); GRD(4 * s + 2); GRD(4 * s + 3); SBAR();
        MM1(4 * s + 2);
        SBAR();
    }
    const unsigned tile = 4 * j + t;
    unsigned vo = (unsigned)(4 * qd4 * 64 + tile) * TS + (E ? (r32 >> 4) * (128 * TS) + 1024 + hh * 256 + (r32 & 15) * 16 : lane * 16);
    unsigned wqo = L_Q14 + (8 * hh + q4) * 64 + wcol;
    PIN(vo); PIN(wqo);
    const lds_cptr wqh = lds + wqo;
    MM1(31); GMM(28); GMM(29); GMM(30); GMM(31); SBAR();
#define PRE8 2
#define PUBN 7
#define LATE0 99
    u32x4 pk[4];
#pragma unroll
    for (int i = 0; i < 4; ++i) { pk[i] = pack_kv<E>(i, i < 2 ? a0 : a1); PIN(pk[i]); if (i < PRE8) PUT_KV(i, pk[i], vo); SBAR(); }
    if (!E) BSTORE(((u32x4){0x3f803f80u, 0x3f803f80u, 0x3f803f80u, 0x3f803f80u}), rkv,
                   (unsigned)((hh * 32 + 4 * (qd4 ^ (hh * (PBX >> 1))) + (r32 >> 3)) * 64 + tile) * TS + 2048 + (lane & 7) * 16, 0);
    { LAS float* gp = (LAS float*)(lds + (lane < 16 ? L_G + t * 128 : L_SCR)) + lane;
      gp[0] = __builtin_amdgcn_rcpf(1.f + __expf(-zg[0])); gp[16] = __builtin_amdgcn_rcpf(1.f + __expf(-zg[1])); }
    SBAR();
    BAR(); SBAR();
    bf16x8 u0 = QRD(0, 0), u1 = QRD(1, 0), y0 = YRD(w0, 0), y1 = YRD(w1, 0);
    const u32x2 tg = *(const LAS u32x2*)(lds + L_CNT + 16);
    f32x16 b0 = f32x16{}, b1 = f32x16{}, aQ0 = f32x16{}, aQ1 = f32x16{};
    SBAR();
#pragma unroll
    for (int n = 0; n < 24; ++n) {
        aQ0 = MFMA(u0, xf[n], aQ0); SBAR();
        if (n == 1) { tag = ((u64)(unsigned)__builtin_amdgcn_readfirstlane(tg[1]) << 32) | (unsigned)__builtin_amdgcn_readfirstlane(tg[0]); asm volatile("" : "+s"(tag)); SBAR(); }
        if (n == PUBN) { DRAIN(); if (lane == 0) __hip_atomic_store((GAS u64*)(ws + kvflag(0, qd4, tile, E)), tag, RLX_AGENT); SBAR(); }
        if ((n & 3) == 3) {
            if (PRE8 + (n >> 2) < 2) { PUT_KV((PRE8 + (n >> 2)) & 3, pk[(PRE8 + (n >> 2)) & 3], vo); SBAR(); }
            if (n == LATE0) { PUT_KV(2, pk[2], vo); SBAR(); }
            if (n == LATE0 + 4) { PUT_KV(3, pk[3], vo); SBAR(); }
            BAR(); SBAR();
        }
        u0 = QRD(0, n + 1); SBAR();
        aQ1 = MFMA(u1, xf[n], aQ1); SBAR();
        u1 = n < 23 ? QRD(1, n + 1) : QRD(0, 25); SBAR();
        b0 = MM(y0, xf[n], b0); SBAR(); y0 = n < 23 ? YRD(w0, n + 1) : QRD(0, 26); SBAR();
        b1 = MM(y1, xf[n], b1); SBAR(); y1 = n < 23 ? YRD(w1, n + 1) : QRD(0, 27); SBAR();
    }
    aQ0 = MFMA(u0, xf[24], aQ0); SBAR(); u0 = QRD(0, 28); SBAR(); aQ0 = MFMA(u1, xf[25], aQ0); SBAR(); u1 = QRD(0, 29); SBAR();
    aQ0 = MFMA(y0, xf[26], aQ0); SBAR(); y0 = QRD(0, 30); SBAR(); aQ0 = MFMA(y1, xf[27], aQ0); SBAR(); y1 = QRD(0, 31); SBAR();
    aQ0 = MFMA(u0, xf[28], aQ0); SBAR(); aQ0 = MFMA(u1, xf[29], aQ0); SBAR(); u1 = QRD(1, 24); SBAR();
    aQ0 = MFMA(y0, xf[30], aQ0); SBAR(); y0 = YRD(w0, 24); SBAR(); aQ0 = MFMA(y1, xf[31], aQ0); SBAR(); y1 = YRD(w1, 24); SBAR();
    const lds_ptr qd = lds + L_Q + t * 1024 + lane * 16;
#pragma unroll
    for (int n = 24; n < 32; ++n) {
        aQ1 = MFMA(u1, xf[n], aQ1); SBAR();
        if (n == 24) {
#pragma unroll
            for (int r = 8; r < 16; ++r) aQ0[r] *= QSCALE;
            *(LAS u32x4*)(qd + 4096) = PK4(aQ0, 8);
            __builtin_amdgcn_s_setprio(2);
            CBAR(); STAMP(4); SBAR();
        }
        if (n < 31) u1 = QRD(1, n + 1); PACE(); SBAR();
        b0 = MM(y0, xf[n], b0); SBAR(); if (n < 31) y0 = YRD(w0, n + 1); PACE(); SBAR();
        b1 = MM(y1, xf[n], b1); SBAR(); if (n < 31) y1 = YRD(w1, n + 1); PACE(); SBAR();
    }
#pragma unroll
    for (int r = 0; r < 8; ++r) aQ0[r] *= QSCALE;
#pragma unroll
    for (int r = 0; r < 16; ++r) aQ1[r] *= QSCALE;
    *(LAS u32x4*)qd = PK4(aQ0, 0); *(LAS u32x4*)(qd + 8192) = PK4(aQ1, 0); *(LAS u32x4*)(qd + 12288) = PK4(aQ1, 8);
    cnt_arrive(lds + L_CNT + 8, lane);
    const unsigned vo2 = vo + (32 + 4 * ((qd4 ^ (PBX >> 1)) - qd4)) * 64 * TS;
#define YSLP 8
    u32x4 py[4] = {pack_kv<E>(0, b0), pack_kv<E>(1, b0), pack_kv<E>(2, b1), pack_kv<E>(3, b1)};
    PIN(py[0]); PIN(py[1]); PIN(py[2]); PIN(py[3]);
#define YSTAG 2
    if (t & 1) __builtin_amdgcn_s_sleep(YSTAG);
    if (t & 2) __builtin_amdgcn_s_sleep(2 * YSTAG);
    if (LATE0 > 23) { PUT_KV(2, pk[2], vo); __builtin_amdgcn_s_sleep(YSLP); }
    if (LATE0 > 19) { PUT_KV(3, pk[3], vo); __builtin_amdgcn_s_sleep(YSLP); }
    PUT_KV(0, py[0], vo2); __builtin_amdgcn_s_sleep(YSLP); PUT_KV(1, py[1], vo2); __builtin_amdgcn_s_sleep(YSLP);
    PUT_KV(2, py[2], vo2); __builtin_amdgcn_s_sleep(YSLP); PUT_KV(3, py[3], vo2);
    DRAIN();
    if (lane == 0) __hip_atomic_store((GAS u64*)(ws + kvflag(1, qd4, tile, E)), tag, RLX_AGENT);
    BSTORE(((u32x4){~0u, ~0u, ~0u, ~0u}), ro, lane * 16, tile * OS + 2 * p * 1024);
    STAMP(19);
}

#define AB_WO (512 * 1024)
#define ROTEXPR j + 2 * (p & 3)
#define WOX 1
#define WOIN 2
#define CODE_LINES 280

struct Att {
    lds_ptr lds; char* ws; __amdgpu_buffer_rsrc_t rkv, ro;
    int lane, t, p, g, head, idx; u64 tag; unsigned vk, vv0, vv1, sBeg; lds_cptr zp, qp[4];
};
#define IDX a.idx
#define LD3(K, V0, V1, rs, so) do { K = BLOAD(rs, a.vk, so); V0 = BLOAD(rs, a.vv0, so); V1 = BLOAD(rs, a.vv1, so); } while (0)
#define EX(X, r) X[r] = __builtin_amdgcn_exp2f(X[r])
#define STEP(X, Y, OP, VP, KN, QN, CN, O, V0, F1, F3, F2) do { \
        EX(X, 0); EX(X, 1); SBAR(); OP = pv_hi(VP, Y, OP, IDX); SBAR(); F1; SBAR(); EX(X, 2); EX(X, 3); EX(X, 4); SBAR(); Y = MFMA(KN, QN, CN); SBAR(); F3; SBAR(); \
        EX(X, 5); EX(X, 6); EX(X, 7); SBAR(); EX(X, 8); EX(X, 9); SBAR(); O = pv_lo(V0, X, O, IDX); SBAR(); F2; SBAR(); \
        EX(X, 10); EX(X, 11); EX(X, 12); EX(X, 13); EX(X, 14); EX(X, 15); SBAR(); } while (0)

__device__ __forceinline__ f32x16 lds_zero16(lds_cptr zp) {
    const volatile LAS f32x4* z = (const volatile LAS f32x4*)zp;
    const f32x4 a = *z, b = *z, c = *z, d = *z;
    f32x8 lo = __builtin_shufflevector(a, b, 0, 1, 2, 3, 4, 5, 6, 7), hi = __builtin_shufflevector(c, d, 0, 1, 2, 3, 4, 5, 6, 7);
    return __builtin_shufflevector(lo, hi, 0, 1, 2, 3, 4, 5, 6, 7, 8, 9, 10, 11, 12, 13, 14, 15);
}

template <int C, int BR>
__device__ __forceinline__ void finish(const Att& a, f32x16 o, float gf, bf16x8 q, unsigned vo) {
    f32x8 y = __builtin_shufflevector(o, o, 0, 1, 2, 3, 4, 5, 6, 7) * (__builtin_amdgcn_rcpf(o[8]) * gf);
    PIN(y);
    const float chk = __builtin_fmaf(((y[0] + y[2]) + (y[4] + y[6])) + ((y[1] + y[3]) + (y[5] + y[7])), 0.f, o[8]);
    if (__builtin_expect(__any(__builtin_amdgcn_fmed3f(chk, LMIN, LMAX) != chk), 0)) {
        if (!BR) { wait_tags((const GAS u64*)(a.ws + kvflag(!C, a.p >> 1, 0, 0) + a.lane * FSTR), a.tag); wait_tags((const GAS u64*)(a.ws + kvflag(!C, a.p >> 1, 2, 0) + a.lane * FSTR), a.tag); }
        slow_rows(a.rkv, (unsigned)((BR * 32 + (a.head ^ (BR * 2 * PBX))) * 64) * TS, BR ? (a.g >= 8 ? a.g - 8 : 0) : 0, BR ? a.g : 63, a.g, BR, q, a.vk, a.vv0, a.vv1, a.lds, a.lane, IDX, o);
        y = __builtin_shufflevector(o, o, 0, 1, 2, 3, 4, 5, 6, 7) * (__builtin_amdgcn_rcpf(o[8]) * gf);
        PIN(y);
    }
    u32x4 v = (u32x4){cvtpk(y[0], y[1]), cvtpk(y[2], y[3]), cvtpk(y[4], y[5]), cvtpk(y[6], y[7])};
    if (!C && !BR) v = __builtin_elementwise_min(v, (u32x4){~1u, ~1u, ~1u, ~1u});
    BSTORE(v, a.ro, vo, 0);
}
#define OFLAG(BR, C) ((GAS u64*)(a.ws + WS_OFLAG) + (((BR) * 64 + a.g) * 2 + (C)) * 16 + (a.p ^ ((BR) * PBX)))
#define OVOFF(BR) (a.vk + (unsigned)((BR) * 64 + a.g) * OS + (a.head ^ ((BR) * 2 * PBX)) * 1024)
#define GATE() (*(const LAS float*)(a.lds + L_G + a.t * 128 + (a.lane & 31) * 4))

struct KV3 { bf16x8 k[3], v0[3], v1[3]; };
#define LDB(K, V0, V1, i) do { const int kt_ = a.g - (i); \
        const __amdgpu_buffer_rsrc_t rb_ = __builtin_amdgcn_make_buffer_rsrc(a.ws + WS_KV, 0, kt_ >= 0 ? KV_BYTES : 0u, 0x00020000); \
        LD3(K, V0, V1, rb_, (unsigned)((32 + (a.head ^ (2 * PBX))) * 64 + (kt_ >= 0 ? kt_ : 0)) * TS); } while (0)
#define LDB_FIRST(f) do { LDB(f.k[0], f.v0[0], f.v1[0], 0); SBAR(); LDB(f.k[1], f.v0[1], f.v1[1], 1); SBAR(); LDB(f.k[2], f.v0[2], f.v1[2], 2); SBAR(); } while (0)
#define LDA_FIRST(f) do { LD3(f.k[0], f.v0[0], f.v1[0], a.rkv, so0[0]); SBAR(); LD3(f.k[1], f.v0[1], f.v1[1], a.rkv, so0[1]); SBAR(); \
        LD3(f.k[2], f.v0[2], f.v1[2], a.rkv, so0[2]); SBAR(); } while (0)
__device__ __forceinline__ unsigned window_flag(const Att& a) {
    const int i = a.lane & 15, kt = a.g - (i < 9 ? i : 0);
    return kvflag(1, a.p >> 1, kt >= 0 ? kt : 0, (a.lane >> 4) & 1);
}

template <int C>
__device__ __forceinline__ void attn_full(const Att& a, f32x16 o0, f32x16 o1, f32x16 o2, f32x16 o3, f32x16 S1, unsigned so,
                                          __amdgpu_buffer_rsrc_t rwo, unsigned vwo, unsigned wi, const GAS u64* fp2, const GAS u64* fp3, KV3& nxt PROF_ARG) {
    const lds_ptr lds = a.lds; const int t = a.t, lane = a.lane;
    const f32x16 zc = f32x16{};
    bf16x8 ka = nxt.k[0], va0 = nxt.v0[0], va1 = nxt.v1[0], kb = nxt.k[1], vb0 = nxt.v0[1], vb1 = nxt.v1[1], kc = nxt.k[2], vc0 = nxt.v0[2], vc1 = nxt.v1[2], kd, vd0, vd1;
    const unsigned sBeg = a.sBeg, jb = a.g >> 2;
    unsigned ki = 3;
    vd1 = lds_frag(a.zp);
    lds_ptr pw = lds + L_PART + (4 * C + t) * PREC + lane * 16;
    lds_cptr pr0 = lds + L_PART + (4 * C + ((t - 1) & 3)) * PREC + lane * 16, pr1 = lds + L_PART + (4 * C + ((t - 2) & 3)) * PREC + lane * 16,
             pr2 = lds + L_PART + (4 * C + ((t - 3) & 3)) * PREC + lane * 16;
    unsigned ovo = OVOFF(0); float gf = 0.f;
    if (C) { BAR(); STAMP(4); }
    else { gf = GATE(); PIN(pw); PIN(pr0); PIN(pr1); PIN(pr2); PIN(ovo); PIN(gf);
           DRAIN(); if (lane == 0) __hip_atomic_store(OFLAG(1, 0), a.tag, RLX_AGENT); }
    bf16x8 q0 = lds_frag(a.qp[0]); SBAR();
    bf16x8 q1 = lds_frag(a.qp[1]), q2 = lds_frag(a.qp[2]), q3 = lds_frag(a.qp[3]);
    SBAR();
    f32x16 S0 = MFMA(ka, q0, zc);
    PIN(S0); SBAR();
    PIN(q1); PIN(q2); PIN(q3); PIN(vd1); PIN(o0); PIN(o1); PIN(o2); PIN(o3); PIN(S1);
    __builtin_amdgcn_s_waitcnt(0x0F76);
#pragma unroll
    for (int i = 0; i < 8; ++i) __builtin_amdgcn_s_setprio(C);
    STAMP(3);
    SBAR();
    unsigned wso = 0;
    u64 fv = a.tag;
#define NEXT1() asm volatile("s_add_i32 %0, %0, 1\n\ts_xor_b32 %1, %0, %2" : "+s"(ki), "=s"(so) : "s"(jb) : "scc")
#define NEXT2() asm volatile("s_and_b32 %0, %0, 15" : "+s"(so) :: "scc")
#define NEXT3() asm volatile("s_mulk_i32 %0, %2\n\ts_add_i32 %0, %0, %1" : "+s"(so) : "s"(sBeg), "i"(4 * TS) : "scc")
#define WO_OFF() do { if (C) asm volatile("s_lshl_b32 %0, %1, 4" : "=s"(wso) : "s"(wi) : "scc"); } while (0)
#define WO_DMA() do { if (C) __builtin_amdgcn_raw_ptr_buffer_load_lds(rwo, (LAS void*)(lds + L_WO + t * 16384 + wi), 16, vwo, wso, 0, 0); } while (0)
#define WO_NEXT() do { if (C) asm volatile("s_addk_i32 %0, 0x400\n\ts_and_b32 %0, %0, 0x3fff" : "+s"(wi) :: "scc"); } while (0)
#define TILE(K, V0, V1, KN, LK, LV0, LV1, NW, G) do { \
        STEP(S0, S1, o3, LV1, K, q1, zc, o0, V0, LK = BLOAD(a.rkv, a.vk, so), WO_OFF(), LV0 = BLOAD(a.rkv, a.vv0, so)); \
        STEP(S1, S0, o0, V1, K, q2, zc, o1, V0, LV1 = BLOAD(a.rkv, a.vv1, so), , WO_DMA()); \
        STEP(S0, S1, o1, V1, K, q3, zc, o2, V0, NEXT1(), NEXT2(), NEXT3()); \
        STEP(S1, S0, o2, V1, KN, q0, zc, o3, V0, __builtin_amdgcn_s_waitcnt(0x0F70 | (C ? NW : 6)), G, WO_NEXT()); } while (0)
#define PUTA(O, sl) (*(LAS f32x4*)(pw + (sl) * 2048) = __builtin_shufflevector(O, O, 0, 1, 2, 3))
#define PUTB(O, sl) (*(LAS f32x4*)(pw + (sl) * 2048 + 1024) = __builtin_shufflevector(O, O, 4, 5, 6, 7))
#define PUTL(O, sl) (*(LAS float*)(pw + 6144 + (sl) * 4) = O[8])
    f32x4 x0, y0, x1, y1, x2, y2; float l0, l1, l2;
#define GETP() do { x0 = *(const LAS f32x4*)pr0; x1 = *(const LAS f32x4*)(pr1 + 2048); x2 = *(const LAS f32x4*)(pr2 + 4096); \
        y0 = *(const LAS f32x4*)(pr0 + 1024); y1 = *(const LAS f32x4*)(pr1 + 3072); y2 = *(const LAS f32x4*)(pr2 + 5120); \
        l0 = *(const LAS float*)(pr0 + 6144); l1 = *(const LAS float*)(pr1 + 6148); l2 = *(const LAS float*)(pr2 + 6152); } while (0)
    if (C) {
#pragma nounroll
        for (int it = 0; it < 4; ++it) {
            TILE(ka, va0, va1, kb, kd, vd0, vd1, 7, );
            PIN(S0); PIN(S1); PIN(o3); wait_tags_end(fv, fp2, a.tag, nullptr, 0);
            TILE(kb, vb0, vb1, kc, ka, va0, va1, 8, );
            TILE(kc, vc0, vc1, kd, kb, vb0, vb1, 9, fv = __hip_atomic_load(fp2, RLX_AGENT));
            TILE(kd, vd0, vd1, ka, kc, vc0, vc1, 10, );
        }
        o3 = pv_hi(vd1, S1, o3, IDX);
        STAMP(6);
        const u64 f3 = __hip_atomic_load(fp3, RLX_AGENT);
        cnt_wait(lds + L_CNT + 8);
        PUTA(o0, 0); PUTB(o0, 0); PUTL(o0, 0); PUTA(o1, 1); PUTB(o1, 1); PUTL(o1, 1); PUTA(o2, 2); PUTB(o2, 2); PUTL(o2, 2);
        DRAIN(); cnt_arrive(lds + L_CNT, lane);
        wait_tags_end(f3, fp3, a.tag, nullptr, 0);
        LDB_FIRST(nxt);
        cnt_wait(lds + L_CNT);
        GETP(); gf = GATE();
    } else {
        TILE(ka, va0, va1, kb, kd, vd0, vd1, 0, );
        TILE(kb, vb0, vb1, kc, ka, va0, va1, 0, );
#pragma nounroll
        for (int it = 0; it < 3; ++it) {
            TILE(kc, vc0, vc1, kd, kb, vb0, vb1, 0, );
            TILE(kd, vd0, vd1, ka, kc, vc0, vc1, 0, );
            TILE(ka, va0, va1, kb, kd, vd0, vd1, 0, );
            TILE(kb, vb0, vb1, kc, ka, va0, va1, 0, );
        }
        STEP(S0, S1, o3, vb1, kc, q1, zc, o0, vc0, , , );
        STEP(S1, S0, o0, vc1, kc, q2, zc, o1, vc0, , , );
        STEP(S0, S1, o1, vc1, kd, q0, zc, o2, vc0, __builtin_amdgcn_s_waitcnt(0x0F73), , );
        STEP(S1, S0, o2, vc1, kd, q1, zc, o0, vd0, , , );
        STEP(S0, S1, o0, vd1, kd, q2, zc, o1, vd0, , , );
        STEP(S1, S0, o1, vd1, kc, q3, zc, o2, vd0, PUTA(o0, 0), PUTL(o0, 0), PUTB(o0, 0));
        STEP(S0, S1, o2, vd1, kd, q3, zc, o3, vc0, PUTA(o1, 1), PUTL(o1, 1), PUTB(o1, 1));
        PUTA(o2, 2); PUTB(o2, 2); SBAR();
        EX(S1, 0); EX(S1, 1); SBAR(); o3 = pv_hi(vc1, S0, o3, IDX); SBAR(); PUTL(o2, 2); __builtin_amdgcn_s_setprio(2); SBAR();
        EX(S1, 2); EX(S1, 3); EX(S1, 4); EX(S1, 5); EX(S1, 6); EX(S1, 7); SBAR(); EX(S1, 8); EX(S1, 9); SBAR();
        o3 = pv_lo(vd0, S1, o3, IDX); PIN(S1); SBAR();
        CBAR(); GETP(); SBAR();
        EX(S1, 10); EX(S1, 11); EX(S1, 12); EX(S1, 13); EX(S1, 14); EX(S1, 15); SBAR();
        o3 = pv_hi(vd1, S1, o3, IDX); SBAR();
        STAMP(6);
    }
    x0 = (x0 + x1) + x2; y0 = (y0 + y1) + y2; l0 = (l0 + l1) + l2;
    PIN(x0); PIN(y0); PIN(l0); PIN(o3);
#pragma unroll
    for (int r = 0; r < 4; ++r) { o3[r] += x0[r]; o3[4 + r] += y0[r]; }
    o3[8] += l0;
    finish<C, 0>(a, o3, gf, q3, ovo);
}

template <int C>
__device__ __forceinline__ void attn_window(const Att& a, KV3& f, const unsigned (&so0)[4] PROF_ARG) {
    const lds_ptr lds = a.lds; const int t = a.t, lane = a.lane;
    const f32x16 zc = f32x16{};
    bf16x8 k[9], v0[9], v1[9];
#pragma unroll
    for (int i = 0; i < 3; ++i) { k[i] = f.k[i]; v0[i] = f.v0[i]; v1[i] = f.v1[i]; }
#define LDW(i) LDB(k[i], v0[i], v1[i], i)
    const bf16x8 qa = lds_frag(lds + L_Q + 8192 + C * 4096 + t * 1024 + lane * 16);
    const f32x16 cd = lds_f32x16(lds + L_MDIAG + lane * 16), cf = lds_f32x16(lds + L_MFAR + lane * 16);
    f32x16 oo = lds_zero16(a.zp);
    const float gt = GATE();
    f32x16 S0 = MFMA(k[0], qa, cd), S1 = MFMA(k[1], qa, zc);
    SBAR(); LDW(3); SBAR();
#pragma unroll
    for (int r = 0; r < 10; ++r) { EX(S0, r); SBAR(); }
    oo = pv_lo(v0[0], S0, oo, IDX); SBAR();
#pragma unroll
    for (int r = 10; r < 16; ++r) EX(S0, r);
    SBAR();
    STEP(S1, S0, oo, v1[0], k[2], qa, zc, oo, v0[1], LDW(4), , );
    STEP(S0, S1, oo, v1[1], k[3], qa, zc, oo, v0[2], LDW(5), , );
    STEP(S1, S0, oo, v1[2], k[4], qa, zc, oo, v0[3], LDW(6), , );
    STEP(S0, S1, oo, v1[3], k[5], qa, zc, oo, v0[4], LDW(7), , );
    STEP(S1, S0, oo, v1[4], k[6], qa, zc, oo, v0[5], LDW(8), , );
    const GAS u64* fa = (const GAS u64*)(a.ws + kvflag(1, a.p >> 1, t, 0) + (lane & 31) * FSTR);
    u64 fv = 0;
    STEP(S0, S1, oo, v1[5], k[7], qa, zc, oo, v0[6], if (!C) fv = __hip_atomic_load(fa, RLX_AGENT), , );
    STEP(S1, S0, oo, v1[6], k[8], qa, cf, oo, v0[7], , , );
    if (C) { PIN(S0); PIN(S1); PIN(oo); DRAIN(); if (lane == 0) __hip_atomic_store(OFLAG(0, C), a.tag, RLX_AGENT); }
    STEP(S0, S1, oo, v1[7], k[8], qa, cf, oo, v0[8], , , );
    oo = pv_hi(v1[8], S0, oo, IDX);
    STAMP(7);
    if (!C) { PIN(oo); wait_tags_end(fv, fa, a.tag, nullptr, 0); LDA_FIRST(f); }
    finish<C, 1>(a, oo, 1.f - gt, qa, OVOFF(1));
}

__global__ void __launch_bounds__(512, 2)
gmix_kernel(const char* x, const char* wA, const char* woA, const char* wB, const char* woB, const char* gw, char* out, char* ws) {
    extern __shared__ __attribute__((aligned(16))) char smem[];
    const lds_ptr lds = (lds_ptr)smem;
    const unsigned lds0 = (unsigned)(uintptr_t)lds;
    const int tid = threadIdx.x, lane = tid & 63, r32 = lane & 31, hh = lane >> 5;
    const int w = __builtin_amdgcn_readfirstlane(tid >> 6);
    const int bid = blockIdx.x, xcd = bid & 7, rr = bid >> 3;
    const int p = 4 * (xcd & 3) + (rr & 3), j = 8 * (xcd >> 2) + (rr >> 2);
    const int jo = 2 * xcd + (rr >> 4), po = rr & 15;
    const int t = w & 3, c = w >> 2;
    const int e = p & 1;
    STAMP(0);
    {
        u64 pc; const unsigned ln = (((bid & ~7) | (tid >> 6)) + (bid < CODE_LINES - 256 ? (tid & 32) * 8 : 0)) * 128;
        asm volatile("s_getpc_b64 %0\n\ts_mov_b32 m0, %2\n\ts_nop 0\n\tglobal_load_lds_dword %1, %0" : "=&s"(pc) : "v"(ln), "s"(lds0 + L_SCR) : "memory");
        SBAR();
    }
    unsigned rot = (ROTEXPR) & 7;
    unsigned vx0 = (128 * j + 32 * t + (lane >> 3)) * 1024 + (((lane & 7) ^ (lane >> 4)) * 16), vx1 = (vx0 ^ 64) + 7168, vx2 = vx0 + 14336, vx3 = vx1 + 14336;
    unsigned vw0 = (16 * t + (lane >> 3)) * 3072 + 1024 + 1024 * e + 128 * ((p >> 1) ^ QSWAP) + (((lane & 7) ^ (((lane >> 4) & 1) << 1)) * 16), vw1 = vw0 + 8 * 3072 - 1024;
    unsigned mx = lds0 + t * 4096, mw = lds0 + L_W + t * 2048, ow = rot * (64 * 3072);
    asm volatile("" : "+v"(vx0), "+v"(vx1), "+v"(vx2), "+v"(vx3), "+v"(vw0), "+v"(vw1));
    asm volatile("" : "+s"(mx), "+s"(mw), "+s"(rot), "+s"(ow));
    SBAR();
    asm volatile("" : "+s"(x), "+s"(wA), "+s"(woA), "+s"(wB), "+s"(woB), "+s"(gw), "+s"(out), "+s"(ws));
#define MK_ATT(C) Att a; a.lds = lds; a.ws = ws; a.lane = lane; a.t = t; a.p = p; a.g = 4 * j + t; a.head = 2 * p + C; a.idx = 0xdddddddd; \
    a.rkv = __builtin_amdgcn_make_buffer_rsrc(ws + WS_KV, 0, KV_BYTES, 0x00020000); a.ro = __builtin_amdgcn_make_buffer_rsrc(ws + WS_O, 0, O_BYTES, 0x00020000); \
    a.vk = lane * 16; a.vv0 = r32 < 16 ? 1024 + hh * 256 + r32 * 16 : ((r32 & ~4) == 16 ? 2048 : OOB); a.vv1 = r32 < 16 ? a.vv0 + 512 : a.vv0; a.zp = lds + L_Z + lane * 16; \
    a.sBeg = (unsigned)(a.head * 64 + t) * TS; \
    _Pragma("unroll") for (int i = 0; i < 4; ++i) a.qp[i] = lds + L_Q + C * 4096 + ((t + 1 + i) & 3) * 1024 + lane * 16
#define MK_SO() unsigned so0[4]; _Pragma("unroll") for (int i = 0; i < 4; ++i) so0[i] = a.sBeg + (j ^ i) * (4 * TS)
    if (c) {
#define DMA1(s) do { const unsigned ks_ = ((s) + rot) & 7; \
        glds16x4(vx0, vx1, vx2, vx3, x + ks_ * 128, mx + ((s) & 3) * 16384); glds16x2(vw0, vw1, wA + ks_ * (64 * 3072), mw + ((s) & 3) * 8192); } while (0)
#define DMA2(s) do { const unsigned ks_ = (((s) + rot) & 7) * (64 * 3072); \
        glds16x2(vq0, vq1, wq + ks_, mq + ((s) == 14 ? L_Q14 : ((s) & 3) * 16384)); glds16x2(vw0, vw1, wy + ks_, mw + ((s) & 3) * 8192); SBAR(); } while (0)
#define LBAR(N) asm volatile("s_waitcnt vmcnt(" #N ")\n\ts_barrier" ::: "memory")
        STAMP(20);
        glds16x4(vx0, vx1, vx2, vx3, x + rot * 128, mx); glds16x2(vw0, vw1, wA + ow, mw);
        SBAR();
        const unsigned k1 = (1 + rot) & 7;
        glds16x2(vx0, vx1, x + k1 * 128, mx + 16384);
        SBAR();
        const u64 ep = *(const __attribute__((address_space(4))) u64*)(ws + WS_EPOCH);
        unsigned vq0 = (32 * (t & 1) + 16 * e + (lane >> 2)) * 3072 + 64 * (p ^ ((t >> 1) * PBX)) + (lane & 3) * 16 + 1024, vq1 = vq0 + (e ? -16 * 3072 : 16 * 3072) - 1024;
        unsigned vwo = ((t & 1) * 256 + (lane >> 2)) * 1024 + 64 * po + (lane & 3) * 16;
        MK_ATT(1);
        const GAS u64* fp = (const GAS u64*)(ws + kvflag(0, p >> 1, (j >> 3) * 32 + t, 0) + (lane & 15) * FSTR);
        const GAS u64* fp2 = (const GAS u64*)(ws + kvflag(0, p >> 1, ((j >> 3) ^ 1) * 32 + t, 0) + (lane & 15) * FSTR);
        const GAS u64* fp3 = (const GAS u64*)(ws + (hh ? WS_OFLAG + ((64 + 4 * jo + t) * 32 + (lane & 15)) * 8 : window_flag(a)));
        PIN(vq0); PIN(vq1); PIN(vwo); PIN(a.vk); PIN(a.vv0); PIN(a.vv1); PIN(a.qp[0]); PIN(a.qp[1]); PIN(a.qp[2]); PIN(a.qp[3]); PIN(a.zp); PIN(fp); PIN(fp2); PIN(fp3); PIN(a.idx);
        SBAR();
        STAMP(30);
        glds16x2(vx0, vx1, x + k1 * 128 + 16384, mx + 16384 + 2048); glds16x2(vw0, vw1, wA + k1 * (64 * 3072), mw + 8192);
        STAMP(22);
        SBAR();
#pragma nounroll
        for (int s = 2; s < 8; ++s) { LBAR(6); DMA1(s);
        }
        const unsigned mq = lds0 + t * 2048;
        const char* wq = ((t >> 1) ? wB : wA) - 1024;
        const char* wy = wB + 128 * ((((p >> 1) ^ QSWAP) ^ (PBX >> 1)) - ((p >> 1) ^ QSWAP));
        LBAR(6); STAMP(14); DMA2(8);
        LBAR(4); STAMP(15); DMA2(9);
        a.tag = ep * 0x9E3779B97F4A7C15ull + 0x632BE59BD9B4E019ull;
        *(LAS u64*)(lds + L_CNT + 16) = a.tag;
        asm volatile("s_waitcnt lgkmcnt(0)" ::: "memory");
        LBAR(4); STAMP(16); DMA2(10);
        const unsigned k7 = ((7 + rot) & 7) * (64 * 3072), sx = mq + L_Q15, sw = mw + 32768;
        LBAR(4); STAMP(17); glds16(vq0, wq + k7, sx); DMA2(11);
        f32x16 oo = lds_zero16(a.zp), o1 = lds_zero16(a.zp); PIN(oo); PIN(o1);
        LBAR(5); STAMP(18); glds16(vw0, wy + k7, sw); DMA2(12);
        f32x16 o2 = lds_zero16(a.zp), o3 = lds_zero16(a.zp); PIN(o2); PIN(o3);
        LBAR(5); STAMP(19); glds16(vq1, wq + k7 + 1024, sx + 1024); DMA2(13);
        f32x16 S1 = lds_zero16(a.zp); PIN(S1);
        LBAR(5); STAMP(21); glds16(vw1, wy + k7 + 1024, sw + 1024); DMA2(14);
        __amdgpu_buffer_rsrc_t rwo = __builtin_amdgcn_make_buffer_rsrc((void*)((t >> 1) ? woB : woA), 0, AB_WO, 0x00020000);
        unsigned wi0 = 4096 * (((rr >> 4) << 1) | (po & 1)) + WOX * 1024 * (xcd & 3);
        MK_SO();
        asm volatile("" : "+s"(a.rkv), "+s"(rwo), "+s"(a.sBeg), "+s"(wi0), "+s"(so0[0]), "+s"(so0[1]), "+s"(so0[2]), "+s"(so0[3]));
        __builtin_amdgcn_s_setprio(1);
        LBAR(5); STAMP(24);
        const u64 fv = __hip_atomic_load(fp, RLX_AGENT);
        LBAR(1); STAMP(29);
        wait_tags_end(fv, fp, a.tag, nullptr, lane);
        STAMP(5);
        KV3 kw;
        LDA_FIRST(kw);
        attn_full<1>(a, oo, o1, o2, o3, S1, so0[3], rwo, vwo, wi0, fp2, fp3, kw PROF_PASS);
        STAMP(23);
        attn_window<1>(a, kw, so0 PROF_PASS);
        STAMP(8);
        const int go = 4 * jo + t;
        const lds_cptr wo = lds + L_WO + (4 * hh + ((lane & 15) >> 2)) * 64 + ((lane >> 4) & 1) * 32 + (lane & 3) * 8;
        const GAS u64* of_ = (const GAS u64*)(ws + WS_OFLAG);
#define OBR(n) ((n) >> 4 == 1 ? 0 : 1)
#define OHD(n) (2 * ((n) & 15) + ((n) >= 16))
#define OLD(n) BLOAD(a.ro, a.vk, (unsigned)(OBR(n) * 64 + go) * OS + OHD(n) * 1024)
#define OWF(n) tr_frag(wo + OBR(n) * 32768 + OHD(n) * 1024, wo + OBR(n) * 32768 + OHD(n) * 1024 + 512)
#define OIN 6
        bf16x8 oe[48];
#pragma unroll
        for (int n = 0; n < 48; ++n) {
            if (n == 16) { wait_tags(of_ + go * 32 + 16 + (lane & 15), a.tag, OFLAG(1, 1), lane); STAMP(9); }
            if (n == 32) wait_tags(of_ + (64 + go) * 32 + 16 + (lane & 15), a.tag);
            oe[n] = OLD(n); SBAR();
            if (n >= OIN - 1) __builtin_amdgcn_s_waitcnt(0x0F70 | (OIN - 1));
            SBAR();
        }
        bf16x8 wn[4];
#pragma unroll
        for (int n = 0; n < 4; ++n) { wn[n] = OWF(n); PIN(wn[n]); }
        __builtin_amdgcn_s_waitcnt(0x0F70);
#define NEARLY 16
#define NMID 32
        unsigned sg[4]; lds_cptr wg[4];
#pragma unroll
        for (int q = 0; q < 4; ++q) { const int gq = ((p >> 2) + 1 + q) & 3; sg[q] = (unsigned)go * OS + gq * 8192; wg[q] = wo + gq * 8192; PIN(wg[q]); }
        unsigned vout = (32 * go + (lane >> 2)) * 1024 + 64 * po + (lane & 3) * 16;
        lds_ptr ow = lds + L_PART + (4 + t) * PREC + r32 * 80 + hh * 8;
        lds_cptr orr = lds + L_PART + (4 + t) * PREC + (lane >> 2) * 80 + (lane & 3) * 16;
        PIN(ow); PIN(orr);
        __amdgpu_buffer_rsrc_t rout = __builtin_amdgcn_make_buffer_rsrc(out, 0, 2048 * 1024, 0x00020000);
        PIN(vout); asm volatile("" : "+s"(sg[0]), "+s"(sg[1]), "+s"(sg[2]), "+s"(sg[3]), "+s"(rout));
        f32x16 ya = f32x16{};
        bf16x8 wf[16], of[16];
#define FLD(i) of[i] = BLOAD(a.ro, a.vk, sg[(i) >> 2] + ((i) & 3) * 2048)
#define GRP_BAD(q) ({ unsigned mx_ = 0; _Pragma("unroll") for (int i = 4 * (q); i < 4 * (q) + 4; ++i) { const u32x4 u_ = __builtin_bit_cast(u32x4, of[i]); \
            mx_ = __builtin_elementwise_max(__builtin_elementwise_max(mx_, u_[0]), u_[1]); mx_ = __builtin_elementwise_max(__builtin_elementwise_max(mx_, u_[2]), u_[3]); } \
            __any(mx_ == ~0u); })
#define GRP_LD(q) do { _Pragma("unroll") for (int i = 4 * (q); i < 4 * (q) + 4; ++i) FLD(i); } while (0)
        STAMP(10);
        BAR();
        BAR();
#pragma unroll
        for (int n = 0; n < 48; ++n) {
            ya = MFMA(wn[n & 3], oe[n], ya); SBAR();
            if (n >= NEARLY && n < NEARLY + 12) FLD((n - NEARLY) & 15);
            if (n >= NMID && n < NMID + 4) FLD((n - NMID + 12) & 15);
            if (n + 4 < 48) wn[n & 3] = OWF(n + 4);
            if (n >= 32) wf[n - 32] = tr_frag(wg[(n - 32) >> 2] + (n & 3) * 2048, wg[(n - 32) >> 2] + (n & 3) * 2048 + 512);
            SBAR();
        }
        STAMP(31);
        f32x16 yb = lds_zero16(a.zp), yc = lds_zero16(a.zp), yd = lds_zero16(a.zp);
        unsigned pend = 15;
#define GRP_TRY(q, Y) do { if (pend & (1u << (q))) { if (!GRP_BAD(q)) { \
            _Pragma("unroll") for (int i = 4 * (q); i < 4 * (q) + 4; ++i) Y = MFMA(wf[i], of[i], Y); pend ^= 1u << (q); } } } while (0)
        unsigned spins = SPIN_CAP;
#pragma nounroll
        do {
            GRP_TRY(0, ya); GRP_TRY(1, yb); GRP_TRY(2, yc); GRP_TRY(3, yd);
            asm volatile("s_sub_u32 %1, %1, 1\n\ts_cselect_b32 %0, 0, %0" : "+s"(pend), "+s"(spins) :: "memory", "scc");
            if (pend & 1) GRP_LD(0); if (pend & 2) GRP_LD(1); if (pend & 4) GRP_LD(2); if (pend & 8) GRP_LD(3);
        } while (pend);
        ya = (ya + yb) + (yc + yd);
#pragma unroll
        for (int q = 0; q < 4; ++q) *(LAS u32x2*)(ow + q * 16) = (u32x2){cvtpk(ya[4 * q], ya[4 * q + 1]), cvtpk(ya[4 * q + 2], ya[4 * q + 3])};
        asm volatile("" ::: "memory");
#pragma unroll
        for (int s = 0; s < 2; ++s) __builtin_amdgcn_raw_buffer_store_b128(*(const LAS u32x4*)(orr + s * 1280), rout, vout + s * 16384, 0, OUT_AUX);
        if (bid == 0 && tid == 256) __hip_atomic_store((GAS u64*)(ws + WS_EPOCH), a.tag, RLX_AGENT);
        STAMP(12);
    } else {
        glds16((((lane >> 3) + rot) & 7) * 128 + (lane & 7) * 16, gw, lds0 + L_GW + t * 1024);
#pragma unroll
        for (int m = 0; m < 2; ++m) {
            f32x4 mk;
#pragma unroll
            for (int i = 0; i < 4; ++i) { const bool le = (i + 8 * t + 4 * hh) <= r32; mk[i] = (le != (m != 0)) ? 0.f : -__builtin_inff(); }
            *(LAS f32x4*)(lds + L_MDIAG + m * 4096 + t * 1024 + lane * 16) = mk;
        }
        if (tid < 4) *(LAS unsigned*)(lds + L_CNT + tid * 4) = 0;
        *(LAS unsigned*)(lds + L_Z + tid * 4) = 0;
        u64 tag;
        STAMP(21);
        {
            const __amdgpu_buffer_rsrc_t rkv = __builtin_amdgcn_make_buffer_rsrc(ws + WS_KV, 0, KV_BYTES, 0x00020000);
            const __amdgpu_buffer_rsrc_t ro = __builtin_amdgcn_make_buffer_rsrc(ws + WS_O, 0, O_BYTES, 0x00020000);
            if (e) project<1>(lds, ws, rkv, ro, lane, t, p, j, tag PROF_PASS); else project<0>(lds, ws, rkv, ro, lane, t, p, j, tag PROF_PASS);
        }
        cnt_wait(lds + L_CNT + 8);
        __builtin_amdgcn_s_sleep(BSLP);
        STAMP(24);
        MK_ATT(0);
        a.tag = tag;
        wait_tags((const GAS u64*)(ws + window_flag(a)), tag);
        STAMP(25);
        KV3 kw;
        LDB_FIRST(kw);
        MK_SO();
        attn_window<0>(a, kw, so0 PROF_PASS);
        STAMP(23);
        PIN(a.idx);
        attn_full<0>(a, lds_zero16(a.zp), lds_zero16(a.zp), lds_zero16(a.zp), lds_zero16(a.zp), lds_zero16(a.zp), so0[3], a.rkv, 0, 0, nullptr, nullptr, kw PROF_PASS);
        BAR();
        STAMP(8);
    }
}

extern "C" void kernel_launch(void* const* d_in, const int* in_sizes, int n_in, void* d_out, int out_size, void* d_ws, size_t ws_size,
                              hipStream_t stream) {
    (void)in_sizes; (void)n_in; (void)out_size; (void)ws_size;
    (void)hipFuncSetAttribute((const void*)gmix_kernel, hipFuncAttributeMaxDynamicSharedMemorySize, L_END);
    gmix_kernel<<<256, 512, L_END, stream>>>((const char*)d_in[0], (const char*)d_in[1], (const char*)d_in[2], (const char*)d_in[4],
                                             (const char*)d_in[5], (const char*)d_in[6], (char*)d_out, (char*)d_ws);
}
```

```cpp
#define OUT_AUX 19
#include <hip/hip_runtime.h>
#include <hip/hip_bf16.h>
#define OUT_LDS

typedef __attribute__((ext_vector_type(8))) __bf16 bf16x8;
typedef __attribute__((ext_vector_type(16))) __bf16 bf16x16;
typedef __attribute__((ext_vector_type(16))) float f32x16;
typedef __attribute__((ext_vector_type(8))) float f32x8;
typedef __attribute__((ext_vector_type(4))) float f32x4;
typedef __attribute__((ext_vector_type(4))) unsigned u32x4;
typedef __attribute__((ext_vector_type(2))) unsigned u32x2;
typedef __attribute__((ext_vector_type(2))) __bf16 bf16x2;
typedef short s16x4 __attribute__((ext_vector_type(4)));
typedef unsigned long long u64;
#define GAS __attribute__((address_space(1)))
#define LAS __attribute__((address_space(3)))
typedef LAS const char* lds_cptr;
typedef LAS char* lds_ptr;

constexpr unsigned WS_EPOCH = 0;
#define FSTR 8
constexpr unsigned WS_OFLAG = 65536;
constexpr unsigned WS_KVFLAG = 131072;
constexpr unsigned WS_KV = 1u << 20;
constexpr unsigned TS = 2176;
constexpr unsigned KV_BYTES = 2u * 32u * 64u * TS;
#define OSTR 32896
constexpr unsigned WS_O = 10u << 20;
constexpr unsigned OS = OSTR;
constexpr unsigned O_BYTES = 128u * OS;
constexpr unsigned WS_STAMP = 16u << 20;
constexpr unsigned OOB = 0x7ff00000u;
constexpr unsigned L_W = 65536;
constexpr unsigned L_WO = 0, L_PART = 65536, PREC = 7168, L_Q14 = 106496, L_Q15 = 114688, L_MDIAG = 122880, L_MFAR = 126976, L_Q = 131072,
                   L_GW = 147456, L_Z = 151552, L_G = 152576, L_CNT = 153088, L_SCR = 153600, L_END = 154112;
constexpr float QSCALE = 0.25f * 1.4426950408889634f;
#define LMAXV 1.2676506e30f
constexpr float LMAX = LMAXV, LMIN = 9.094947e-13f;
constexpr unsigned SPIN_CAP = 1u << 17;

#define SBAR() __builtin_amdgcn_sched_barrier(0)
#define PIN(x) asm volatile("" : "+v"(x))
#define MFMA(a, b, c) __builtin_amdgcn_mfma_f32_32x32x16_bf16(a, b, c, 0, 0, 0)
#define BAR() asm volatile("s_barrier" ::: "memory")
#define CBAR() asm volatile("s_waitcnt lgkmcnt(0)\n\ts_barrier" ::: "memory")
#define DRAIN() asm volatile("s_waitcnt vmcnt(0)" ::: "memory")
#define PACEN 6
#define PACE() asm volatile("s_nop %0" :: "i"(PACEN) : "memory")
#define RLX_AGENT __ATOMIC_RELAXED, __HIP_MEMORY_SCOPE_AGENT
#define FL_SCOPE __HIP_MEMORY_SCOPE_AGENT
#define RLX_ST __ATOMIC_RELAXED, FL_SCOPE
#define STAMP(i)

__device__ __forceinline__ void glds16(unsigned voff, const void* sbase, unsigned ldsaddr) {
    asm volatile("s_mov_b32 m0, %2\n\ts_nop 0\n\tglobal_load_lds_dwordx4 %0, %1" ::"v"(voff), "s"(sbase), "s"(ldsaddr) : "memory");
}
__device__ __forceinline__ void glds16x2(unsigned v0, unsigned v1, const void* sbase, unsigned ldsaddr) {
    asm volatile("s_mov_b32 m0, %3\n\ts_nop 0\n\tglobal_load_lds_dwordx4 %0, %2\n\tglobal_load_lds_dwordx4 %1, %2 offset:1024"
                 ::"v"(v0), "v"(v1), "s"(sbase), "s"(ldsaddr) : "memory");
}
__device__ __forceinline__ void glds16x4(unsigned v0, unsigned v1, unsigned v2, unsigned v3, const void* sbase, unsigned ldsaddr) {
    asm volatile("s_mov_b32 m0, %5\n\ts_nop 0\n\tglobal_load_lds_dwordx4 %0, %4\n\tglobal_load_lds_dwordx4 %1, %4 offset:1024\n\t"
                 "global_load_lds_dwordx4 %2, %4 offset:2048\n\tglobal_load_lds_dwordx4 %3, %4 offset:3072"
                 ::"v"(v0), "v"(v1), "v"(v2), "v"(v3), "s"(sbase), "s"(ldsaddr) : "memory");
}
__device__ __forceinline__ unsigned cvtpk(float lo, float hi) {
    typedef __attribute__((ext_vector_type(2))) float f32x2;
    return __builtin_bit_cast(unsigned, __builtin_convertvector((f32x2){lo, hi}, bf16x2));
}
__device__ __forceinline__ bf16x8 as_frag(u32x4 v) { return __builtin_bit_cast(bf16x8, v); }
__device__ __forceinline__ bf16x8 lds_frag(lds_cptr p) { return *(const LAS bf16x8*)p; }
__device__ __forceinline__ bf16x8 tr_frag(lds_cptr p0, lds_cptr p1) {
    s16x4 a = __builtin_amdgcn_ds_read_tr16_b64_v4i16((LAS s16x4*)p0);
    s16x4 b = __builtin_amdgcn_ds_read_tr16_b64_v4i16((LAS s16x4*)p1);
    return __builtin_bit_cast(bf16x8, __builtin_shufflevector(a, b, 0, 1, 2, 3, 4, 5, 6, 7));
}
__device__ __forceinline__ f32x16 pv_lo(bf16x8 v, const f32x16& p, f32x16 acc, int idx) {
    f32x8 h = __builtin_shufflevector(p, p, 0, 1, 2, 3, 4, 5, 6, 7);
    return __builtin_amdgcn_smfmac_f32_32x32x32_bf16(v, __builtin_bit_cast(bf16x16, h), acc, idx, 0, 0);
}
__device__ __forceinline__ f32x16 pv_hi(bf16x8 v, const f32x16& p, f32x16 acc, int idx) {
    f32x8 h = __builtin_shufflevector(p, p, 8, 9, 10, 11, 12, 13, 14, 15);
    return __builtin_amdgcn_smfmac_f32_32x32x32_bf16(v, __builtin_bit_cast(bf16x16, h), acc, idx, 0, 0);
}
#define MX3(a, b, c) __builtin_fmaxf(__builtin_fmaxf((a), (b)), (c))
__device__ __forceinline__ float max16(const f32x16& s) {
    float a = MX3(s[0], s[1], s[2]), b = MX3(s[3], s[4], s[5]);
    a = MX3(a, s[6], s[7]); b = MX3(b, s[8], s[9]); a = MX3(a, s[10], s[11]); b = MX3(b, s[12], s[13]);
    return MX3(a, b, __builtin_fmaxf(s[14], s[15]));
}
__device__ __forceinline__ float halfmax(float m) {
    auto r = __builtin_amdgcn_permlane32_swap(__float_as_uint(m), __float_as_uint(m), false, false);
    return __builtin_fmaxf(__uint_as_float(r[0]), __uint_as_float(r[1]));
}
__device__ __forceinline__ f32x16 lds_f32x16(lds_cptr p) {
    f32x4 a = *(const LAS f32x4*)p, b = *(const LAS f32x4*)(p + 1024), c = *(const LAS f32x4*)(p + 2048), d = *(const LAS f32x4*)(p + 3072);
    f32x8 lo = __builtin_shufflevector(a, b, 0, 1, 2, 3, 4, 5, 6, 7), hi = __builtin_shufflevector(c, d, 0, 1, 2, 3, 4, 5, 6, 7);
    return __builtin_shufflevector(lo, hi, 0, 1, 2, 3, 4, 5, 6, 7, 8, 9, 10, 11, 12, 13, 14, 15);
}
#define ST_AUX 16
#define LD_AUX 16
#define BLOAD(rs, vo, so) as_frag(__builtin_amdgcn_raw_buffer_load_b128(rs, vo, so, LD_AUX))
#define BSTORE(v, rs, vo, so) __builtin_amdgcn_raw_buffer_store_b128(v, rs, (vo) + (so), 0, ST_AUX)

#define POLLSLP 8
#define BSLP 36
__device__ __forceinline__ void wait_tags_end(u64 v, const GAS u64* p, u64 tag, GAS u64* pub, int lane) {
    if (pub) { DRAIN(); if (lane == 0) __hip_atomic_store(pub, tag, RLX_ST); }
    if (__builtin_expect(!__all(v == tag), 0)) {
#pragma nounroll
        for (unsigned spins = 0; spins < SPIN_CAP; ++spins) {
            __builtin_amdgcn_s_sleep(POLLSLP);
            if (__all(__hip_atomic_load(p, RLX_AGENT) == tag)) break;
        }
    }
    asm volatile("" ::: "memory");
}

__device__ __forceinline__ void wait_tags(const GAS u64* p, u64 tag, GAS u64* pub = nullptr, int lane = 0) {
    wait_tags_end(__hip_atomic_load(p, RLX_AGENT), p, tag, pub, lane);
}

__device__ __forceinline__ void cnt_arrive(lds_ptr cnt, int lane) {
    asm volatile("s_waitcnt lgkmcnt(0)" ::: "memory");
    if (lane == 0) __hip_atomic_fetch_add((LAS unsigned*)cnt, 1u, __ATOMIC_RELAXED, __HIP_MEMORY_SCOPE_WORKGROUP);
}
__device__ __forceinline__ void cnt_wait(lds_ptr cnt) {
#pragma nounroll
    for (unsigned spins = 0; spins < (1u << 22); ++spins) {
        if (__builtin_amdgcn_readfirstlane(__hip_atomic_load((LAS unsigned*)cnt, __ATOMIC_RELAXED, __HIP_MEMORY_SCOPE_WORKGROUP)) == 4) break;
        __builtin_amdgcn_s_sleep(1);
    }
    asm volatile("" ::: "memory");
}
__device__ __forceinline__ unsigned kvflag(int br, int quad, int tile, int type) {
    return WS_KVFLAG + (unsigned)(((((br * 8 + quad) * 4 + (tile & 3)) * 2 + (tile >> 5)) * 2 + type) * 8 + ((tile >> 2) & 7)) * FSTR;
}

__device__ __forceinline__ void slow_rows(__amdgpu_buffer_rsrc_t rs, unsigned sbase, int lo, int hi, int g, bool band, bf16x8 q,
                                          unsigned vk, unsigned vv0, unsigned vv1, lds_cptr lds, int lane, int idx, f32x16& acc) {
    float m = -__builtin_inff();
    for (int pass = 0; pass < 2; ++pass) {
        if (pass) { m = halfmax(m); acc = f32x16{}; }
        for (int kt = lo; kt <= hi; ++kt) {
            const unsigned so = sbase + (unsigned)kt * TS;
            const bf16x8 k = BLOAD(rs, vk, so), v0 = BLOAD(rs, vv0, so), v1 = BLOAD(rs, vv1, so);
            f32x16 c = f32x16{};
            if (band && kt == g) c = lds_f32x16(lds + L_MDIAG + lane * 16);
            if (band && kt == g - 8) c = lds_f32x16(lds + L_MFAR + lane * 16);
            f32x16 s = MFMA(k, q, c);
            if (!pass) m = __builtin_fmaxf(m, max16(s));
            else {
#pragma unroll
                for (int r = 0; r < 16; ++r) s[r] = __builtin_amdgcn_exp2f(s[r] - m);
                acc = pv_lo(v0, s, acc, idx); acc = pv_hi(v1, s, acc, idx);
            }
        }
    }
}

#define PK4(A, o) ((u32x4){cvtpk(A[o], A[o + 1]), cvtpk(A[o + 2], A[o + 3]), cvtpk(A[o + 4], A[o + 5]), cvtpk(A[o + 6], A[o + 7])})
#define PROF_ARG
#define PROF_PASS
template <int E>
__device__ __forceinline__ u32x4 pack_kv(int i, const f32x16& a) {
    const int s = i & 1;
    if (E == 0) return PK4(a, 8 * s);
    auto x = __builtin_amdgcn_permlane32_swap(cvtpk(a[8 * s], a[8 * s + 1]), cvtpk(a[8 * s + 4], a[8 * s + 5]), false, false);
    auto y = __builtin_amdgcn_permlane32_swap(cvtpk(a[8 * s + 2], a[8 * s + 3]), cvtpk(a[8 * s + 6], a[8 * s + 7]), false, false);
    return (u32x4){x[0], y[0], x[1], y[1]};
}
#define PUT_KV(i, v, vo) BSTORE(v, rkv, vo, ((i) < 2 ? 64 * TS : 0) + ((i) & 1) * (E ? 512 : 128 * TS))
#define QSWAP 1
#define PBX 4
template <int E>
__device__ __forceinline__ void project(lds_ptr lds, char* ws, __amdgpu_buffer_rsrc_t rkv, __amdgpu_buffer_rsrc_t ro, int lane, int t, int p, int j, u64& tag PROF_ARG) {
    const int r32 = lane & 31, hh = lane >> 5, qd4 = (p >> 1) ^ QSWAP;
    const unsigned q4 = (lane & 15) >> 2, wcol = ((lane >> 4) & 1) * 32 + (lane & 3) * 8;
    lds_cptr xa[4];
#pragma unroll
    for (int ks = 0; ks < 4; ++ks) xa[ks] = lds + (32 * t + r32) * 128 + (((hh ^ ((r32 >> 1) & 7)) * 16) ^ (ks * 32));
    unsigned w1o = L_W + (8 * hh + q4) * 128 + ((lane >> 4) & 1) * 64 + (q4 >> 1) * 32 + (lane & 3) * 8, w0o = w1o ^ 32;
    PIN(w0o); PIN(w1o);
    const lds_cptr w0 = lds + w0o, w1 = lds + w1o, wq = lds + (8 * hh + q4) * 64 + wcol;
#define XRD(n) lds_frag(xa[(n) & 3] + (((n) >> 2) & 3) * 16384)
#define WRD(b, n) tr_frag(b + ((n) & 15) * 2048, b + ((n) & 15) * 2048 + 512)
#define YOF(n) ((n) >= 28 ? 32768 + ((n) & 3) * 2048 : ((n) & 15) * 2048)
#define YRD(b, n) tr_frag(b + YOF(n), b + YOF(n) + 512)
#define QOF(br, n) (((n) >= 28 ? L_Q15 - L_Q14 : ((n) >= 24 ? 0 : (((n) >> 2) & 3) * 16384)) + (br) * 4096 + (((n) & 3) ^ E) * 1024)
#define QRD(br, n) tr_frag(((n) >= 24 ? wqh : wq) + QOF(br, n), ((n) >= 24 ? wqh : wq) + QOF(br, n) + 256)
#define MM(W, X, A) (E ? MFMA(X, W, A) : MFMA(W, X, A))
    bf16x8 xf[32], f0[4], f1[4];
    f32x16 a0 = f32x16{}, a1 = f32x16{};
    unsigned gwo = (lane & 15) == ((lane >> 4) & 1) ? L_GW + t * 1024 + hh * 16 : L_Z;
    PIN(gwo);
    f32x4 zg = f32x4{};
    bf16x8 gf[4];
#define RD1(n) do { xf[n] = XRD(n); f0[(n) & 3] = WRD(w0, n); f1[(n) & 3] = WRD(w1, n); SBAR(); } while (0)
#define GRD(n) gf[(n) & 3] = lds_frag(lds + gwo + (n) * 32)
#define GMM(n) zg = __builtin_amdgcn_mfma_f32_16x16x32_bf16(gf[(n) & 3], xf[n], zg, 0, 0, 0)
#define MM1(n) do { a0 = MM(f0[(n) & 3], xf[n], a0); a1 = MM(f1[(n) & 3], xf[n], a1); SBAR(); } while (0)
#pragma unroll
    for (int s = 0; s < 8; ++s) {
        if (s == 0) CBAR(); else BAR();
        SBAR();
        RD1(4 * s); RD1(4 * s + 1);
        if (s) { MM1(4 * s - 1); GMM(4 * s - 4); GMM(4 * s - 3); GMM(4 * s - 2); GMM(4 * s - 1); SBAR(); }
        RD1(4 * s + 2); MM1(4 * s); RD1(4 * s + 3); MM1(4 * s + 1);
        if (s == 0) DRAIN();
        GRD(4 * s); GRD(4 * s + 1); GRD(4 * s + 2); GRD(4 * s + 3); SBAR();
        MM1(4 * s + 2);
        SBAR();
    }
    const unsigned tile = 4 * j + t;
    unsigned vo = (unsigned)(4 * qd4 * 64 + tile) * TS + (E ? (r32 >> 4) * (128 * TS) + 1024 + hh * 256 + (r32 & 15) * 16 : lane * 16);
    unsigned wqo = L_Q14 + (8 * hh + q4) * 64 + wcol;
    PIN(vo); PIN(wqo);
    const lds_cptr wqh = lds + wqo;
    MM1(31); GMM(28); GMM(29); GMM(30); GMM(31); SBAR();
#define PRE8 2
#define PUBN 7
#define LATE0 99
    u32x4 pk[4];
#pragma unroll
    for (int i = 0; i < 4; ++i) { pk[i] = pack_kv<E>(i, i < 2 ? a0 : a1); PIN(pk[i]); if (i < PRE8) PUT_KV(i, pk[i], vo); SBAR(); }
    if (!E) BSTORE(((u32x4){0x3f803f80u, 0x3f803f80u, 0x3f803f80u, 0x3f803f80u}), rkv,
                   (unsigned)((hh * 32 + 4 * (qd4 ^ (hh * (PBX >> 1))) + (r32 >> 3)) * 64 + tile) * TS + 2048 + (lane & 7) * 16, 0);
    { LAS float* gp = (LAS float*)(lds + (lane < 16 ? L_G + t * 128 : L_SCR)) + lane;
      gp[0] = __builtin_amdgcn_rcpf(1.f + __expf(-zg[0])); gp[16] = __builtin_amdgcn_rcpf(1.f + __expf(-zg[1])); }
    SBAR();
    BAR(); SBAR();
    bf16x8 u0 = QRD(0, 0), u1 = QRD(1, 0), y0 = YRD(w0, 0), y1 = YRD(w1, 0);
    const u32x2 tg = *(const LAS u32x2*)(lds + L_CNT + 16);
    f32x16 b0 = f32x16{}, b1 = f32x16{}, aQ0 = f32x16{}, aQ1 = f32x16{};
    SBAR();
#pragma unroll
    for (int n = 0; n < 24; ++n) {
        aQ0 = MFMA(u0, xf[n], aQ0); SBAR();
        if (n == 1) { tag = ((u64)(unsigned)__builtin_amdgcn_readfirstlane(tg[1]) << 32) | (unsigned)__builtin_amdgcn_readfirstlane(tg[0]); asm volatile("" : "+s"(tag)); SBAR(); }
        if (n == PUBN) { DRAIN(); if (lane == 0) __hip_atomic_store((GAS u64*)(ws + kvflag(0, qd4, tile, E)), tag, RLX_ST); SBAR(); }
        if ((n & 3) == 3) {
            if (PRE8 + (n >> 2) < 2) { PUT_KV((PRE8 + (n >> 2)) & 3, pk[(PRE8 + (n >> 2)) & 3], vo); SBAR(); }
            if (n == LATE0) { PUT_KV(2, pk[2], vo); SBAR(); }
            if (n == LATE0 + 4) { PUT_KV(3, pk[3], vo); SBAR(); }
            BAR(); SBAR();
        }
        u0 = QRD(0, n + 1); SBAR();
        aQ1 = MFMA(u1, xf[n], aQ1); SBAR();
        u1 = n < 23 ? QRD(1, n + 1) : QRD(0, 25); SBAR();
        b0 = MM(y0, xf[n], b0); SBAR(); y0 = n < 23 ? YRD(w0, n + 1) : QRD(0, 26); SBAR();
        b1 = MM(y1, xf[n], b1); SBAR(); y1 = n < 23 ? YRD(w1, n + 1) : QRD(0, 27); SBAR();
    }
    aQ0 = MFMA(u0, xf[24], aQ0); SBAR(); u0 = QRD(0, 28); SBAR(); aQ0 = MFMA(u1, xf[25], aQ0); SBAR(); u1 = QRD(0, 29); SBAR();
    aQ0 = MFMA(y0, xf[26], aQ0); SBAR(); y0 = QRD(0, 30); SBAR(); aQ0 = MFMA(y1, xf[27], aQ0); SBAR(); y1 = QRD(0, 31); SBAR();
    aQ0 = MFMA(u0, xf[28], aQ0); SBAR(); aQ0 = MFMA(u1, xf[29], aQ0); SBAR(); u1 = QRD(1, 24); SBAR();
    aQ0 = MFMA(y0, xf[30], aQ0); SBAR(); y0 = YRD(w0, 24); SBAR(); aQ0 = MFMA(y1, xf[31], aQ0); SBAR(); y1 = YRD(w1, 24); SBAR();
    const lds_ptr qd = lds + L_Q + t * 1024 + lane * 16;
#pragma unroll
    for (int n = 24; n < 32; ++n) {
        aQ1 = MFMA(u1, xf[n], aQ1); SBAR();
        if (n == 24) {
#pragma unroll
            for (int r = 8; r < 16; ++r) aQ0[r] *= QSCALE;
            *(LAS u32x4*)(qd + 4096) = PK4(aQ0, 8);
            __builtin_amdgcn_s_setprio(2);
            CBAR(); STAMP(4); SBAR();
        }
        if (n < 31) u1 = QRD(1, n + 1); PACE(); SBAR();
        b0 = MM(y0, xf[n], b0); SBAR(); if (n < 31) y0 = YRD(w0, n + 1); PACE(); SBAR();
        b1 = MM(y1, xf[n], b1); SBAR(); if (n < 31) y1 = YRD(w1, n + 1); PACE(); SBAR();
    }
#pragma unroll
    for (int r = 0; r < 8; ++r) aQ0[r] *= QSCALE;
#pragma unroll
    for (int r = 0; r < 16; ++r) aQ1[r] *= QSCALE;
    *(LAS u32x4*)qd = PK4(aQ0, 0); *(LAS u32x4*)(qd + 8192) = PK4(aQ1, 0); *(LAS u32x4*)(qd + 12288) = PK4(aQ1, 8);
    cnt_arrive(lds + L_CNT + 8, lane);
    const unsigned vo2 = vo + (32 + 4 * ((qd4 ^ (PBX >> 1)) - qd4)) * 64 * TS;
#define YSLP 8
    u32x4 py[4] = {pack_kv<E>(0, b0), pack_kv<E>(1, b0), pack_kv<E>(2, b1), pack_kv<E>(3, b1)};
    PIN(py[0]); PIN(py[1]); PIN(py[2]); PIN(py[3]);
#define YSTAG 2
    if (t & 1) __builtin_amdgcn_s_sleep(YSTAG);
    if (t & 2) __builtin_amdgcn_s_sleep(2 * YSTAG);
    if (LATE0 > 23) { PUT_KV(2, pk[2], vo); __builtin_amdgcn_s_sleep(YSLP); }
    if (LATE0 > 19) { PUT_KV(3, pk[3], vo); __builtin_amdgcn_s_sleep(YSLP); }
    PUT_KV(0, py[0], vo2); __builtin_amdgcn_s_sleep(YSLP); PUT_KV(1, py[1], vo2); __builtin_amdgcn_s_sleep(YSLP);
    PUT_KV(2, py[2], vo2); __builtin_amdgcn_s_sleep(YSLP); PUT_KV(3, py[3], vo2);
    DRAIN();
    if (lane == 0) __hip_atomic_store((GAS u64*)(ws + kvflag(1, qd4, tile, E)), tag, RLX_ST);
    BSTORE(((u32x4){~0u, ~0u, ~0u, ~0u}), ro, lane * 16, tile * OS + 2 * p * 1024);
    STAMP(19);
}

#define AB_WO (512 * 1024)
#define ROTEXPR j + 2 * (p & 3)
#define WOX 1
#define WOIN 2
#define CODE_LINES 280

struct Att {
    lds_ptr lds; char* ws; __amdgpu_buffer_rsrc_t rkv, ro;
    int lane, t, p, g, head, idx; u64 tag; unsigned vk, vv0, vv1, sBeg; lds_cptr zp, qp[4];
};
#define IDX a.idx
#define LD3(K, V0, V1, rs, so) do { K = BLOAD(rs, a.vk, so); V0 = BLOAD(rs, a.vv0, so); V1 = BLOAD(rs, a.vv1, so); } while (0)
#define EX(X, r) X[r] = __builtin_amdgcn_exp2f(X[r])
#define STEP(X, Y, OP, VP, KN, QN, CN, O, V0, F1, F3, F2) do { \
        EX(X, 0); EX(X, 1); SBAR(); OP = pv_hi(VP, Y, OP, IDX); SBAR(); F1; SBAR(); EX(X, 2); EX(X, 3); EX(X, 4); SBAR(); Y = MFMA(KN, QN, CN); SBAR(); F3; SBAR(); \
        EX(X, 5); EX(X, 6); EX(X, 7); SBAR(); EX(X, 8); EX(X, 9); SBAR(); O = pv_lo(V0, X, O, IDX); SBAR(); F2; SBAR(); \
        EX(X, 10); EX(X, 11); EX(X, 12); EX(X, 13); EX(X, 14); EX(X, 15); SBAR(); } while (0)

__device__ __forceinline__ f32x16 lds_zero16(lds_cptr zp) {
    const volatile LAS f32x4* z = (const volatile LAS f32x4*)zp;
    const f32x4 a = *z, b = *z, c = *z, d = *z;
    f32x8 lo = __builtin_shufflevector(a, b, 0, 1, 2, 3, 4, 5, 6, 7), hi = __builtin_shufflevector(c, d, 0, 1, 2, 3, 4, 5, 6, 7);
    return __builtin_shufflevector(lo, hi, 0, 1, 2, 3, 4, 5, 6, 7, 8, 9, 10, 11, 12, 13, 14, 15);
}

template <int C, int BR>
__device__ __forceinline__ void finish(const Att& a, f32x16 o, float gf, bf16x8 q, unsigned vo) {
    f32x8 y = __builtin_shufflevector(o, o, 0, 1, 2, 3, 4, 5, 6, 7) * (__builtin_amdgcn_rcpf(o[8]) * gf);
    PIN(y);
    const float chk = __builtin_fmaf(((y[0] + y[2]) + (y[4] + y[6])) + ((y[1] + y[3]) + (y[5] + y[7])), 0.f, o[8]);
    if (__builtin_expect(__any(__builtin_amdgcn_fmed3f(chk, LMIN, LMAX) != chk), 0)) {
        if (!BR) { wait_tags((const GAS u64*)(a.ws + kvflag(!C, a.p >> 1, 0, 0) + a.lane * FSTR), a.tag); wait_tags((const GAS u64*)(a.ws + kvflag(!C, a.p >> 1, 2, 0) + a.lane * FSTR), a.tag); }
        slow_rows(a.rkv, (unsigned)((BR * 32 + (a.head ^ (BR * 2 * PBX))) * 64) * TS, BR ? (a.g >= 8 ? a.g - 8 : 0) : 0, BR ? a.g : 63, a.g, BR, q, a.vk, a.vv0, a.vv1, a.lds, a.lane, IDX, o);
        y = __builtin_shufflevector(o, o, 0, 1, 2, 3, 4, 5, 6, 7) * (__builtin_amdgcn_rcpf(o[8]) * gf);
        PIN(y);
    }
    u32x4 v = (u32x4){cvtpk(y[0], y[1]), cvtpk(y[2], y[3]), cvtpk(y[4], y[5]), cvtpk(y[6], y[7])};
    if (!C && !BR) v = __builtin_elementwise_min(v, (u32x4){~1u, ~1u, ~1u, ~1u});
    BSTORE(v, a.ro, vo, 0);
}
#define OFLAG(BR, C) ((GAS u64*)(a.ws + WS_OFLAG) + (((BR) * 64 + a.g) * 2 + (C)) * 16 + (a.p ^ ((BR) * PBX)))
#define OVOFF(BR) (a.vk + (unsigned)((BR) * 64 + a.g) * OS + (a.head ^ ((BR) * 2 * PBX)) * 1024)
#define GATE() (*(const LAS float*)(a.lds + L_G + a.t * 128 + (a.lane & 31) * 4))

struct KV3 { bf16x8 k[3], v0[3], v1[3]; };
#define LDB(K, V0, V1, i) do { const int kt_ = a.g - (i); \
        const __amdgpu_buffer_rsrc_t rb_ = __builtin_amdgcn_make_buffer_rsrc(a.ws + WS_KV, 0, kt_ >= 0 ? KV_BYTES : 0u, 0x00020000); \
        LD3(K, V0, V1, rb_, (unsigned)((32 + (a.head ^ (2 * PBX))) * 64 + (kt_ >= 0 ? kt_ : 0)) * TS); } while (0)
#define LDB_FIRST(f) do { LDB(f.k[0], f.v0[0], f.v1[0], 0); SBAR(); LDB(f.k[1], f.v0[1], f.v1[1], 1); SBAR(); LDB(f.k[2], f.v0[2], f.v1[2], 2); SBAR(); } while (0)
#define LDA_FIRST(f) do { LD3(f.k[0], f.v0[0], f.v1[0], a.rkv, so0[0]); SBAR(); LD3(f.k[1], f.v0[1], f.v1[1], a.rkv, so0[1]); SBAR(); \
        LD3(f.k[2], f.v0[2], f.v1[2], a.rkv, so0[2]); SBAR(); } while (0)
__device__ __forceinline__ unsigned window_flag(const Att& a) {
    const int i = a.lane & 15, kt = a.g - (i < 9 ? i : 0);
    return kvflag(1, a.p >> 1, kt >= 0 ? kt : 0, (a.lane >> 4) & 1);
}

template <int C>
__device__ __forceinline__ void attn_full(const Att& a, f32x16 o0, f32x16 o1, f32x16 o2, f32x16 o3, f32x16 S1, unsigned so,
                                          __amdgpu_buffer_rsrc_t rwo, unsigned vwo, unsigned wi, const GAS u64* fp2, const GAS u64* fp3, KV3& nxt PROF_ARG) {
    const lds_ptr lds = a.lds; const int t = a.t, lane = a.lane;
    const f32x16 zc = f32x16{};
    bf16x8 ka = nxt.k[0], va0 = nxt.v0[0], va1 = nxt.v1[0], kb = nxt.k[1], vb0 = nxt.v0[1], vb1 = nxt.v1[1], kc = nxt.k[2], vc0 = nxt.v0[2], vc1 = nxt.v1[2], kd, vd0, vd1;
    const unsigned sBeg = a.sBeg, jb = a.g >> 2;
    unsigned ki = 3;
    vd1 = lds_frag(a.zp);
    lds_ptr pw = lds + L_PART + (4 * C + t) * PREC + lane * 16;
    lds_cptr pr0 = lds + L_PART + (4 * C + ((t - 1) & 3)) * PREC + lane * 16, pr1 = lds + L_PART + (4 * C + ((t - 2) & 3)) * PREC + lane * 16,
             pr2 = lds + L_PART + (4 * C + ((t - 3) & 3)) * PREC + lane * 16;
    unsigned ovo = OVOFF(0); float gf = 0.f;
    if (C) { BAR(); STAMP(4); }
    else { gf = GATE(); PIN(pw); PIN(pr0); PIN(pr1); PIN(pr2); PIN(ovo); PIN(gf);
           DRAIN(); if (lane == 0) __hip_atomic_store(OFLAG(1, 0), a.tag, RLX_ST); }
    bf16x8 q0 = lds_frag(a.qp[0]); SBAR();
    bf16x8 q1 = lds_frag(a.qp[1]), q2 = lds_frag(a.qp[2]), q3 = lds_frag(a.qp[3]);
    SBAR();
    f32x16 S0 = MFMA(ka, q0, zc);
    PIN(S0); SBAR();
    PIN(q1); PIN(q2); PIN(q3); PIN(vd1); PIN(o0); PIN(o1); PIN(o2); PIN(o3); PIN(S1);
    __builtin_amdgcn_s_waitcnt(0x0F76);
#pragma unroll
    for (int i = 0; i < 8; ++i) __builtin_amdgcn_s_setprio(C);
    STAMP(3);
    SBAR();
    unsigned wso = 0;
    u64 fv = a.tag;
#define NEXT1() asm volatile("s_add_i32 %0, %0, 1\n\ts_xor_b32 %1, %0, %2" : "+s"(ki), "=s"(so) : "s"(jb) : "scc")
#define NEXT2() asm volatile("s_and_b32 %0, %0, 15" : "+s"(so) :: "scc")
#define NEXT3() asm volatile("s_mulk_i32 %0, %2\n\ts_add_i32 %0, %0, %1" : "+s"(so) : "s"(sBeg), "i"(4 * TS) : "scc")
#define WO_OFF() do { if (C) asm volatile("s_lshl_b32 %0, %1, 4" : "=s"(wso) : "s"(wi) : "scc"); } while (0)
#define WO_DMA() do { if (C) __builtin_amdgcn_raw_ptr_buffer_load_lds(rwo, (LAS void*)(lds + L_WO + t * 16384 + wi), 16, vwo, wso, 0, 0); } while (0)
#define WO_NEXT() do { if (C) asm volatile("s_addk_i32 %0, 0x400\n\ts_and_b32 %0, %0, 0x3fff" : "+s"(wi) :: "scc"); } while (0)
#define TILE(K, V0, V1, KN, LK, LV0, LV1, NW, G) do { \
        STEP(S0, S1, o3, LV1, K, q1, zc, o0, V0, LK = BLOAD(a.rkv, a.vk, so), WO_OFF(), LV0 = BLOAD(a.rkv, a.vv0, so)); \
        STEP(S1, S0, o0, V1, K, q2, zc, o1, V0, LV1 = BLOAD(a.rkv, a.vv1, so), , WO_DMA()); \
        STEP(S0, S1, o1, V1, K, q3, zc, o2, V0, NEXT1(), NEXT2(), NEXT3()); \
        STEP(S1, S0, o2, V1, KN, q0, zc, o3, V0, __builtin_amdgcn_s_waitcnt(0x0F70 | (C ? NW : 6)), G, WO_NEXT()); } while (0)
#define PUTA(O, sl) (*(LAS f32x4*)(pw + (sl) * 2048) = __builtin_shufflevector(O, O, 0, 1, 2, 3))
#define PUTB(O, sl) (*(LAS f32x4*)(pw + (sl) * 2048 + 1024) = __builtin_shufflevector(O, O, 4, 5, 6, 7))
#define PUTL(O, sl) (*(LAS float*)(pw + 6144 + (sl) * 4) = O[8])
    f32x4 x0, y0, x1, y1, x2, y2; float l0, l1, l2;
#define GETP() do { x0 = *(const LAS f32x4*)pr0; x1 = *(const LAS f32x4*)(pr1 + 2048); x2 = *(const LAS f32x4*)(pr2 + 4096); \
        y0 = *(const LAS f32x4*)(pr0 + 1024); y1 = *(const LAS f32x4*)(pr1 + 3072); y2 = *(const LAS f32x4*)(pr2 + 5120); \
        l0 = *(const LAS float*)(pr0 + 6144); l1 = *(const LAS float*)(pr1 + 6148); l2 = *(const LAS float*)(pr2 + 6152); } while (0)
    if (C) {
#pragma nounroll
        for (int it = 0; it < 4; ++it) {
            TILE(ka, va0, va1, kb, kd, vd0, vd1, 7, );
            PIN(S0); PIN(S1); PIN(o3); wait_tags_end(fv, fp2, a.tag, nullptr, 0);
            TILE(kb, vb0, vb1, kc, ka, va0, va1, 8, );
            TILE(kc, vc0, vc1, kd, kb, vb0, vb1, 9, fv = __hip_atomic_load(fp2, RLX_AGENT));
            TILE(kd, vd0, vd1, ka, kc, vc0, vc1, 10, );
        }
        o3 = pv_hi(vd1, S1, o3, IDX);
        STAMP(6);
        const u64 f3 = __hip_atomic_load(fp3, RLX_AGENT);
        cnt_wait(lds + L_CNT + 8);
        PUTA(o0, 0); PUTB(o0, 0); PUTL(o0, 0); PUTA(o1, 1); PUTB(o1, 1); PUTL(o1, 1); PUTA(o2, 2); PUTB(o2, 2); PUTL(o2, 2);
        DRAIN(); cnt_arrive(lds + L_CNT, lane);
        wait_tags_end(f3, fp3, a.tag, nullptr, 0);
        LDB_FIRST(nxt);
        cnt_wait(lds + L_CNT);
        GETP(); gf = GATE();
    } else {
        TILE(ka, va0, va1, kb, kd, vd0, vd1, 0, );
        TILE(kb, vb0, vb1, kc, ka, va0, va1, 0, );
#pragma nounroll
        for (int it = 0; it < 3; ++it) {
            TILE(kc, vc0, vc1, kd, kb, vb0, vb1, 0, );
            TILE(kd, vd0, vd1, ka, kc, vc0, vc1, 0, );
            TILE(ka, va0, va1, kb, kd, vd0, vd1, 0, );
            TILE(kb, vb0, vb1, kc, ka, va0, va1, 0, );
        }
        STEP(S0, S1, o3, vb1, kc, q1, zc, o0, vc0, , , );
        STEP(S1, S0, o0, vc1, kc, q2, zc, o1, vc0, , , );
        STEP(S0, S1, o1, vc1, kd, q0, zc, o2, vc0, __builtin_amdgcn_s_waitcnt(0x0F73), , );
        STEP(S1, S0, o2, vc1, kd, q1, zc, o0, vd0, , , );
        STEP(S0, S1, o0, vd1, kd, q2, zc, o1, vd0, , , );
        STEP(S1, S0, o1, vd1, kc, q3, zc, o2, vd0, PUTA(o0, 0), PUTL(o0, 0), PUTB(o0, 0));
        STEP(S0, S1, o2, vd1, kd, q3, zc, o3, vc0, PUTA(o1, 1), PUTL(o1, 1), PUTB(o1, 1));
        PUTA(o2, 2); PUTB(o2, 2); SBAR();
        EX(S1, 0); EX(S1, 1); SBAR(); o3 = pv_hi(vc1, S0, o3, IDX); SBAR(); PUTL(o2, 2); __builtin_amdgcn_s_setprio(2); SBAR();
        EX(S1, 2); EX(S1, 3); EX(S1, 4); EX(S1, 5); EX(S1, 6); EX(S1, 7); SBAR(); EX(S1, 8); EX(S1, 9); SBAR();
        o3 = pv_lo(vd0, S1, o3, IDX); PIN(S1); SBAR();
        CBAR(); GETP(); SBAR();
        EX(S1, 10); EX(S1, 11); EX(S1, 12); EX(S1, 13); EX(S1, 14); EX(S1, 15); SBAR();
        o3 = pv_hi(vd1, S1, o3, IDX); SBAR();
        STAMP(6);
    }
    x0 = (x0 + x1) + x2; y0 = (y0 + y1) + y2; l0 = (l0 + l1) + l2;
    PIN(x0); PIN(y0); PIN(l0); PIN(o3);
#pragma unroll
    for (int r = 0; r < 4; ++r) { o3[r] += x0[r]; o3[4 + r] += y0[r]; }
    o3[8] += l0;
    finish<C, 0>(a, o3, gf, q3, ovo);
}

template <int C>
__device__ __forceinline__ void attn_window(const Att& a, KV3& f, const unsigned (&so0)[4] PROF_ARG) {
    const lds_ptr lds = a.lds; const int t = a.t, lane = a.lane;
    const f32x16 zc = f32x16{};
    bf16x8 k[9], v0[9], v1[9];
#pragma unroll
    for (int i = 0; i < 3; ++i) { k[i] = f.k[i]; v0[i] = f.v0[i]; v1[i] = f.v1[i]; }
#define LDW(i) LDB(k[i], v0[i], v1[i], i)
    const bf16x8 qa = lds_frag(lds + L_Q + 8192 + C * 4096 + t * 1024 + lane * 16);
    const f32x16 cd = lds_f32x16(lds + L_MDIAG + lane * 16), cf = lds_f32x16(lds + L_MFAR + lane * 16);
    f32x16 oo = lds_zero16(a.zp);
    const float gt = GATE();
    f32x16 S0 = MFMA(k[0], qa, cd), S1 = MFMA(k[1], qa, zc);
    SBAR(); LDW(3); SBAR();
#pragma unroll
    for (int r = 0; r < 10; ++r) { EX(S0, r); SBAR(); }
    oo = pv_lo(v0[0], S0, oo, IDX); SBAR();
#pragma unroll
    for (int r = 10; r < 16; ++r) EX(S0, r);
    SBAR();
    STEP(S1, S0, oo, v1[0], k[2], qa, zc, oo, v0[1], LDW(4), , );
    STEP(S0, S1, oo, v1[1], k[3], qa, zc, oo, v0[2], LDW(5), , );
    STEP(S1, S0, oo, v1[2], k[4], qa, zc, oo, v0[3], LDW(6), , );
    STEP(S0, S1, oo, v1[3], k[5], qa, zc, oo, v0[4], LDW(7), , );
    STEP(S1, S0, oo, v1[4], k[6], qa, zc, oo, v0[5], LDW(8), , );
    const GAS u64* fa = (const GAS u64*)(a.ws + kvflag(1, a.p >> 1, t, 0) + (lane & 31) * FSTR);
    u64 fv = 0;
    STEP(S0, S1, oo, v1[5], k[7], qa, zc, oo, v0[6], if (!C) fv = __hip_atomic_load(fa, RLX_AGENT), , );
    STEP(S1, S0, oo, v1[6], k[8], qa, cf, oo, v0[7], , , );
    if (C) { PIN(S0); PIN(S1); PIN(oo); DRAIN(); if (lane == 0) __hip_atomic_store(OFLAG(0, C), a.tag, RLX_ST); }
    STEP(S0, S1, oo, v1[7], k[8], qa, cf, oo, v0[8], , , );
    oo = pv_hi(v1[8], S0, oo, IDX);
    STAMP(7);
    if (!C) { PIN(oo); wait_tags_end(fv, fa, a.tag, nullptr, 0); LDA_FIRST(f); }
    finish<C, 1>(a, oo, 1.f - gt, qa, OVOFF(1));
}

__global__ void __launch_bounds__(512, 2)
gmix_kernel(const char* x, const char* wA, const char* woA, const char* wB, const char* woB, const char* gw, char* out, char* ws) {
    extern __shared__ __attribute__((aligned(16))) char smem[];
    const lds_ptr lds = (lds_ptr)smem;
    const unsigned lds0 = (unsigned)(uintptr_t)lds;
    const int tid = threadIdx.x, lane = tid & 63, r32 = lane & 31, hh = lane >> 5;
    const int w = __builtin_amdgcn_readfirstlane(tid >> 6);
    const int bid = blockIdx.x, xcd = bid & 7, rr = bid >> 3;
    const int p = 4 * (xcd & 3) + (rr & 3), j = 8 * (xcd >> 2) + (rr >> 2);
    const int jo = 2 * xcd + (rr >> 4), po = rr & 15;
    const int t = w & 3, c = w >> 2;
    const int e = p & 1;
    STAMP(0);
    {
        u64 pc; const unsigned ln = (((bid & ~7) | (tid >> 6)) + (bid < CODE_LINES - 256 ? (tid & 32) * 8 : 0)) * 128;
        asm volatile("s_getpc_b64 %0\n\ts_mov_b32 m0, %2\n\ts_nop 0\n\tglobal_load_lds_dword %1, %0" : "=&s"(pc) : "v"(ln), "s"(lds0 + L_SCR) : "memory");
        SBAR();
    }
    unsigned rot = (ROTEXPR) & 7;
    unsigned vx0 = (128 * j + 32 * t + (lane >> 3)) * 1024 + (((lane & 7) ^ (lane >> 4)) * 16), vx1 = (vx0 ^ 64) + 7168, vx2 = vx0 + 14336, vx3 = vx1 + 14336;
    unsigned vw0 = (16 * t + (lane >> 3)) * 3072 + 1024 + 1024 * e + 128 * ((p >> 1) ^ QSWAP) + (((lane & 7) ^ (((lane >> 4) & 1) << 1)) * 16), vw1 = vw0 + 8 * 3072 - 1024;
    unsigned mx = lds0 + t * 4096, mw = lds0 + L_W + t * 2048, ow = rot * (64 * 3072);
    asm volatile("" : "+v"(vx0), "+v"(vx1), "+v"(vx2), "+v"(vx3), "+v"(vw0), "+v"(vw1));
    asm volatile("" : "+s"(mx), "+s"(mw), "+s"(rot), "+s"(ow));
    SBAR();
    asm volatile("" : "+s"(x), "+s"(wA), "+s"(woA), "+s"(wB), "+s"(woB), "+s"(gw), "+s"(out), "+s"(ws));
#define MK_ATT(C) Att a; a.lds = lds; a.ws = ws; a.lane = lane; a.t = t; a.p = p; a.g = 4 * j + t; a.head = 2 * p + C; a.idx = 0xdddddddd; \
    a.rkv = __builtin_amdgcn_make_buffer_rsrc(ws + WS_KV, 0, KV_BYTES, 0x00020000); a.ro = __builtin_amdgcn_make_buffer_rsrc(ws + WS_O, 0, O_BYTES, 0x00020000); \
    a.vk = lane * 16; a.vv0 = r32 < 16 ? 1024 + hh * 256 + r32 * 16 : ((r32 & ~4) == 16 ? 2048 : OOB); a.vv1 = r32 < 16 ? a.vv0 + 512 : a.vv0; a.zp = lds + L_Z + lane * 16; \
    a.sBeg = (unsigned)(a.head * 64 + t) * TS; \
    _Pragma("unroll") for (int i = 0; i < 4; ++i) a.qp[i] = lds + L_Q + C * 4096 + ((t + 1 + i) & 3) * 1024 + lane * 16
#define MK_SO() unsigned so0[4]; _Pragma("unroll") for (int i = 0; i < 4; ++i) so0[i] = a.sBeg + (j ^ i) * (4 * TS)
    if (c) {
#define DMA1(s) do { const unsigned ks_ = ((s) + rot) & 7; \
        glds16x4(vx0, vx1, vx2, vx3, x + ks_ * 128, mx + ((s) & 3) * 16384); glds16x2(vw0, vw1, wA + ks_ * (64 * 3072), mw + ((s) & 3) * 8192); } while (0)
#define DMA2(s) do { const unsigned ks_ = (((s) + rot) & 7) * (64 * 3072); \
        glds16x2(vq0, vq1, wq + ks_, mq + ((s) == 14 ? L_Q14 : ((s) & 3) * 16384)); glds16x2(vw0, vw1, wy + ks_, mw + ((s) & 3) * 8192); SBAR(); } while (0)
#define LBAR(N) asm volatile("s_waitcnt vmcnt(" #N ")\n\ts_barrier" ::: "memory")
        STAMP(20);
        glds16x4(vx0, vx1, vx2, vx3, x + rot * 128, mx); glds16x2(vw0, vw1, wA + ow, mw);
        SBAR();
        const unsigned k1 = (1 + rot) & 7;
        glds16x2(vx0, vx1, x + k1 * 128, mx + 16384);
        SBAR();
        const u64 ep = *(const __attribute__((address_space(4))) u64*)(ws + WS_EPOCH);
        unsigned vq0 = (32 * (t & 1) + 16 * e + (lane >> 2)) * 3072 + 64 * (p ^ ((t >> 1) * PBX)) + (lane & 3) * 16 + 1024, vq1 = vq0 + (e ? -16 * 3072 : 16 * 3072) - 1024;
        unsigned vwo = ((t & 1) * 256 + (lane >> 2)) * 1024 + 64 * po + (lane & 3) * 16;
        MK_ATT(1);
        const GAS u64* fp = (const GAS u64*)(ws + kvflag(0, p >> 1, (j >> 3) * 32 + t, 0) + (lane & 15) * FSTR);
        const GAS u64* fp2 = (const GAS u64*)(ws + kvflag(0, p >> 1, ((j >> 3) ^ 1) * 32 + t, 0) + (lane & 15) * FSTR);
        const GAS u64* fp3 = (const GAS u64*)(ws + (hh ? WS_OFLAG + ((64 + 4 * jo + t) * 32 + (lane & 15)) * 8 : window_flag(a)));
        PIN(vq0); PIN(vq1); PIN(vwo); PIN(a.vk); PIN(a.vv0); PIN(a.vv1); PIN(a.qp[0]); PIN(a.qp[1]); PIN(a.qp[2]); PIN(a.qp[3]); PIN(a.zp); PIN(fp); PIN(fp2); PIN(fp3); PIN(a.idx);
        SBAR();
        STAMP(30);
        glds16x2(vx0, vx1, x + k1 * 128 + 16384, mx + 16384 + 2048); glds16x2(vw0, vw1, wA + k1 * (64 * 3072), mw + 8192);
        STAMP(22);
        SBAR();
#pragma nounroll
        for (int s = 2; s < 8; ++s) { LBAR(6); DMA1(s);
        }
        const unsigned mq = lds0 + t * 2048;
        const char* wq = ((t >> 1) ? wB : wA) - 1024;
        const char* wy = wB + 128 * ((((p >> 1) ^ QSWAP) ^ (PBX >> 1)) - ((p >> 1) ^ QSWAP));
        LBAR(6); STAMP(14); DMA2(8);
        LBAR(4); STAMP(15); DMA2(9);
        a.tag = ep * 0x9E3779B97F4A7C15ull + 0x632BE59BD9B4E019ull;
        *(LAS u64*)(lds + L_CNT + 16) = a.tag;
        asm volatile("s_waitcnt lgkmcnt(0)" ::: "memory");
        LBAR(4); STAMP(16); DMA2(10);
        const unsigned k7 = ((7 + rot) & 7) * (64 * 3072), sx = mq + L_Q15, sw = mw + 32768;
        LBAR(4); STAMP(17); glds16(vq0, wq + k7, sx); DMA2(11);
        f32x16 oo = lds_zero16(a.zp), o1 = lds_zero16(a.zp); PIN(oo); PIN(o1);
        LBAR(5); STAMP(18); glds16(vw0, wy + k7, sw); DMA2(12);
        f32x16 o2 = lds_zero16(a.zp), o3 = lds_zero16(a.zp); PIN(o2); PIN(o3);
        LBAR(5); STAMP(19); glds16(vq1, wq + k7 + 1024, sx + 1024); DMA2(13);
        f32x16 S1 = lds_zero16(a.zp); PIN(S1);
        LBAR(5); STAMP(21); glds16(vw1, wy + k7 + 1024, sw + 1024); DMA2(14);
        __amdgpu_buffer_rsrc_t rwo = __builtin_amdgcn_make_buffer_rsrc((void*)((t >> 1) ? woB : woA), 0, AB_WO, 0x00020000);
        unsigned wi0 = 4096 * (((rr >> 4) << 1) | (po & 1)) + WOX * 1024 * (xcd & 3);
        MK_SO();
        asm volatile("" : "+s"(a.rkv), "+s"(rwo), "+s"(a.sBeg), "+s"(wi0), "+s"(so0[0]), "+s"(so0[1]), "+s"(so0[2]), "+s"(so0[3]));
        __builtin_amdgcn_s_setprio(1);
        LBAR(5); STAMP(24);
        const u64 fv = __hip_atomic_load(fp, RLX_AGENT);
        LBAR(1); STAMP(29);
        wait_tags_end(fv, fp, a.tag, nullptr, lane);
        STAMP(5);
        KV3 kw;
        LDA_FIRST(kw);
        attn_full<1>(a, oo, o1, o2, o3, S1, so0[3], rwo, vwo, wi0, fp2, fp3, kw PROF_PASS);
        STAMP(23);
        attn_window<1>(a, kw, so0 PROF_PASS);
        STAMP(8);
        const int go = 4 * jo + t;
        const lds_cptr wo = lds + L_WO + (4 * hh + ((lane & 15) >> 2)) * 64 + ((lane >> 4) & 1) * 32 + (lane & 3) * 8;
        const GAS u64* of_ = (const GAS u64*)(ws + WS_OFLAG);
#define OBR(n) ((n) >> 4 == 1 ? 0 : 1)
#define OHD(n) (2 * ((n) & 15) + ((n) >= 16))
#define OLD(n) BLOAD(a.ro, a.vk, (unsigned)(OBR(n) * 64 + go) * OS + OHD(n) * 1024)
#define OWF(n) tr_frag(wo + OBR(n) * 32768 + OHD(n) * 1024, wo + OBR(n) * 32768 + OHD(n) * 1024 + 512)
#define OIN 6
        bf16x8 oe[48];
#pragma unroll
        for (int n = 0; n < 48; ++n) {
            if (n == 16) { wait_tags(of_ + go * 32 + 16 + (lane & 15), a.tag, OFLAG(1, 1), lane); STAMP(9); }
            if (n == 32) wait_tags(of_ + (64 + go) * 32 + 16 + (lane & 15), a.tag);
            oe[n] = OLD(n); SBAR();
            if (n >= OIN - 1) __builtin_amdgcn_s_waitcnt(0x0F70 | (OIN - 1));
            SBAR();
        }
        bf16x8 wn[4];
#pragma unroll
        for (int n = 0; n < 4; ++n) { wn[n] = OWF(n); PIN(wn[n]); }
        __builtin_amdgcn_s_waitcnt(0x0F70);
#define NEARLY 16
#define NMID 32
        unsigned sg[4]; lds_cptr wg[4];
#pragma unroll
        for (int q = 0; q < 4; ++q) { const int gq = ((p >> 2) + 1 + q) & 3; sg[q] = (unsigned)go * OS + gq * 8192; wg[q] = wo + gq * 8192; PIN(wg[q]); }
        unsigned vout = (32 * go + (lane >> 2)) * 1024 + 64 * po + (lane & 3) * 16;
        lds_ptr ow = lds + L_PART + (4 + t) * PREC + r32 * 80 + hh * 8;
        lds_cptr orr = lds + L_PART + (4 + t) * PREC + (lane >> 2) * 80 + (lane & 3) * 16;
        PIN(ow); PIN(orr);
        __amdgpu_buffer_rsrc_t rout = __builtin_amdgcn_make_buffer_rsrc(out, 0, 2048 * 1024, 0x00020000);
        PIN(vout); asm volatile("" : "+s"(sg[0]), "+s"(sg[1]), "+s"(sg[2]), "+s"(sg[3]), "+s"(rout));
        f32x16 ya = f32x16{};
        bf16x8 wf[16], of[16];
#define FLD(i) of[i] = BLOAD(a.ro, a.vk, sg[(i) >> 2] + ((i) & 3) * 2048)
#define GRP_BAD(q) ({ unsigned mx_ = 0; _Pragma("unroll") for (int i = 4 * (q); i < 4 * (q) + 4; ++i) { const u32x4 u_ = __builtin_bit_cast(u32x4, of[i]); \
            mx_ = __builtin_elementwise_max(__builtin_elementwise_max(mx_, u_[0]), u_[1]); mx_ = __builtin_elementwise_max(__builtin_elementwise_max(mx_, u_[2]), u_[3]); } \
            __any(mx_ == ~0u); })
#define GRP_LD(q) do { _Pragma("unroll") for (int i = 4 * (q); i < 4 * (q) + 4; ++i) FLD(i); } while (0)
        STAMP(10);
        BAR();
        BAR();
#pragma unroll
        for (int n = 0; n < 48; ++n) {
            ya = MFMA(wn[n & 3], oe[n], ya); SBAR();
            if (n >= NEARLY && n < NEARLY + 12) FLD((n - NEARLY) & 15);
            if (n >= NMID && n < NMID + 4) FLD((n - NMID + 12) & 15);
            if (n + 4 < 48) wn[n & 3] = OWF(n + 4);
            if (n >= 32) wf[n - 32] = tr_frag(wg[(n - 32) >> 2] + (n & 3) * 2048, wg[(n - 32) >> 2] + (n & 3) * 2048 + 512);
            SBAR();
        }
        STAMP(31);
        f32x16 yb = lds_zero16(a.zp), yc = lds_zero16(a.zp), yd = lds_zero16(a.zp);
        unsigned pend = 15;
#define GRP_TRY(q, Y) do { if (pend & (1u << (q))) { if (!GRP_BAD(q)) { \
            _Pragma("unroll") for (int i = 4 * (q); i < 4 * (q) + 4; ++i) Y = MFMA(wf[i], of[i], Y); pend ^= 1u << (q); } } } while (0)
        unsigned spins = SPIN_CAP;
#pragma nounroll
        do {
            GRP_TRY(0, ya); GRP_TRY(1, yb); GRP_TRY(2, yc); GRP_TRY(3, yd);
            asm volatile("s_sub_u32 %1, %1, 1\n\ts_cselect_b32 %0, 0, %0" : "+s"(pend), "+s"(spins) :: "memory", "scc");
            if (pend & 1) GRP_LD(0); if (pend & 2) GRP_LD(1); if (pend & 4) GRP_LD(2); if (pend & 8) GRP_LD(3);
        } while (pend);
        ya = (ya + yb) + (yc + yd);
#pragma unroll
        for (int q = 0; q < 4; ++q) *(LAS u32x2*)(ow + q * 16) = (u32x2){cvtpk(ya[4 * q], ya[4 * q + 1]), cvtpk(ya[4 * q + 2], ya[4 * q + 3])};
        asm volatile("" ::: "memory");
#pragma unroll
        for (int s = 0; s < 2; ++s) __builtin_amdgcn_raw_buffer_store_b128(*(const LAS u32x4*)(orr + s * 1280), rout, vout + s * 16384, 0, OUT_AUX);
        if (bid == 0 && tid == 256) __hip_atomic_store((GAS u64*)(ws + WS_EPOCH), a.tag, RLX_ST);
        STAMP(12);
    } else {
        glds16((((lane >> 3) + rot) & 7) * 128 + (lane & 7) * 16, gw, lds0 + L_GW + t * 1024);
#pragma unroll
        for (int m = 0; m < 2; ++m) {
            f32x4 mk;
#pragma unroll
            for (int i = 0; i < 4; ++i) { const bool le = (i + 8 * t + 4 * hh) <= r32; mk[i] = (le != (m != 0)) ? 0.f : -__builtin_inff(); }
            *(LAS f32x4*)(lds + L_MDIAG + m * 4096 + t * 1024 + lane * 16) = mk;
        }
        if (tid < 4) *(LAS unsigned*)(lds + L_CNT + tid * 4) = 0;
        *(LAS unsigned*)(lds + L_Z + tid * 4) = 0;
        u64 tag;
        STAMP(21);
        {
            const __amdgpu_buffer_rsrc_t rkv = __builtin_amdgcn_make_buffer_rsrc(ws + WS_KV, 0, KV_BYTES, 0x00020000);
            const __amdgpu_buffer_rsrc_t ro = __builtin_amdgcn_make_buffer_rsrc(ws + WS_O, 0, O_BYTES, 0x00020000);
            if (e) project<1>(lds, ws, rkv, ro, lane, t, p, j, tag PROF_PASS); else project<0>(lds, ws, rkv, ro, lane, t, p, j, tag PROF_PASS);
        }
        cnt_wait(lds + L_CNT + 8);
        __builtin_amdgcn_s_sleep(BSLP);
        STAMP(24);
        MK_ATT(0);
        a.tag = tag;
        wait_tags((const GAS u64*)(ws + window_flag(a)), tag);
        STAMP(25);
        KV3 kw;
        LDB_FIRST(kw);
        MK_SO();
        attn_window<0>(a, kw, so0 PROF_PASS);
        STAMP(23);
        PIN(a.idx);
        attn_full<0>(a, lds_zero16(a.zp), lds_zero16(a.zp), lds_zero16(a.zp), lds_zero16(a.zp), lds_zero16(a.zp), so0[3], a.rkv, 0, 0, nullptr, nullptr, kw PROF_PASS);
        BAR();
        STAMP(8);
    }
}

extern "C" void kernel_launch(void* const* d_in, const int* in_sizes, int n_in, void* d_out, int out_size, void* d_ws, size_t ws_size,
                              hipStream_t stream) {
    (void)in_sizes; (void)n_in; (void)out_size; (void)ws_size;
    (void)hipFuncSetAttribute((const void*)gmix_kernel, hipFuncAttributeMaxDynamicSharedMemorySize, L_END);
    gmix_kernel<<<256, 512, L_END, stream>>>((const char*)d_in[0], (const char*)d_in[1], (const char*)d_in[2], (const char*)d_in[4],
                                             (const char*)d_in[5], (const char*)d_in[6], (char*)d_out, (char*)d_ws);
}
```

```cpp
#include <hip/hip_runtime.h>
#include <hip/hip_bf16.h>
#define OUT_LDS

typedef __attribute__((ext_vector_type(8))) __bf16 bf16x8;
typedef __attribute__((ext_vector_type(16))) __bf16 bf16x16;
typedef __attribute__((ext_vector_type(16))) float f32x16;
typedef __attribute__((ext_vector_type(8))) float f32x8;
typedef __attribute__((ext_vector_type(4))) float f32x4;
typedef __attribute__((ext_vector_type(4))) unsigned u32x4;
typedef __attribute__((ext_vector_type(2))) unsigned u32x2;
typedef __attribute__((ext_vector_type(2))) __bf16 bf16x2;
typedef short s16x4 __attribute__((ext_vector_type(4)));
typedef unsigned long long u64;
#define GAS __attribute__((address_space(1)))
#define LAS __attribute__((address_space(3)))
typedef LAS const char* lds_cptr;
typedef LAS char* lds_ptr;

constexpr unsigned WS_EPOCH = 0;
#define FSTR 8
constexpr unsigned WS_OFLAG = 65536;
constexpr unsigned WS_KVFLAG = 131072;
constexpr unsigned WS_KV = 1u << 20;
constexpr unsigned TS = 2176;
constexpr unsigned KV_BYTES = 2u * 32u * 64u * TS;
#define OSTR 32896
constexpr unsigned WS_O = 10u << 20;
constexpr unsigned OS = OSTR;
constexpr unsigned O_BYTES = 128u * OS;
constexpr unsigned WS_STAMP = 16u << 20;
constexpr unsigned OOB = 0x7ff00000u;
constexpr unsigned L_W = 65536;
constexpr unsigned L_WO = 0, L_PART = 65536, PREC = 7168, L_Q14 = 106496, L_Q15 = 114688, L_MDIAG = 122880, L_MFAR = 126976, L_Q = 131072,
                   L_GW = 147456, L_Z = 151552, L_G = 152576, L_CNT = 153088, L_EP = 153344, L_SCR = 153600, L_END = 154112;
constexpr float QSCALE = 0.25f * 1.4426950408889634f;
#define LMAXV 1.2676506e30f
constexpr float LMAX = LMAXV, LMIN = 9.094947e-13f;
constexpr unsigned SPIN_CAP = 1u << 17;

#define SBAR() __builtin_amdgcn_sched_barrier(0)
#define PIN(x) asm volatile("" : "+v"(x))
#define MFMA(a, b, c) __builtin_amdgcn_mfma_f32_32x32x16_bf16(a, b, c, 0, 0, 0)
#define BAR() asm volatile("s_barrier" ::: "memory")
#define CBAR() asm volatile("s_waitcnt lgkmcnt(0)\n\ts_barrier" ::: "memory")
#define DRAIN() asm volatile("s_waitcnt vmcnt(0)" ::: "memory")
#define PACEN 6
#define PACE() asm volatile("s_nop %0" :: "i"(PACEN) : "memory")
#define RLX_AGENT __ATOMIC_RELAXED, __HIP_MEMORY_SCOPE_AGENT
#define FL_SCOPE __HIP_MEMORY_SCOPE_AGENT
#define RLX_ST __ATOMIC_RELAXED, FL_SCOPE
#define STAMP(i)

#define POL2 ""
#define POL4 ""
__device__ __forceinline__ void glds16(unsigned voff, const void* sbase, unsigned ldsaddr) {
    asm volatile("s_mov_b32 m0, %2\n\ts_nop 0\n\tglobal_load_lds_dwordx4 %0, %1" POL2 ::"v"(voff), "s"(sbase), "s"(ldsaddr) : "memory");
}
__device__ __forceinline__ void glds16x2(unsigned v0, unsigned v1, const void* sbase, unsigned ldsaddr) {
    asm volatile("s_mov_b32 m0, %3\n\ts_nop 0\n\tglobal_load_lds_dwordx4 %0, %2" POL2 "\n\tglobal_load_lds_dwordx4 %1, %2 offset:1024" POL2
                 ::"v"(v0), "v"(v1), "s"(sbase), "s"(ldsaddr) : "memory");
}
__device__ __forceinline__ void glds16x4(unsigned v0, unsigned v1, unsigned v2, unsigned v3, const void* sbase, unsigned ldsaddr) {
    asm volatile("s_mov_b32 m0, %5\n\ts_nop 0\n\tglobal_load_lds_dwordx4 %0, %4" POL4 "\n\tglobal_load_lds_dwordx4 %1, %4 offset:1024" POL4 "\n\t"
                 "global_load_lds_dwordx4 %2, %4 offset:2048" POL4 "\n\tglobal_load_lds_dwordx4 %3, %4 offset:3072" POL4
                 ::"v"(v0), "v"(v1), "v"(v2), "v"(v3), "s"(sbase), "s"(ldsaddr) : "memory");
}
__device__ __forceinline__ unsigned cvtpk(float lo, float hi) {
    typedef __attribute__((ext_vector_type(2))) float f32x2;
    return __builtin_bit_cast(unsigned, __builtin_convertvector((f32x2){lo, hi}, bf16x2));
}
__device__ __forceinline__ bf16x8 as_frag(u32x4 v) { return __builtin_bit_cast(bf16x8, v); }
__device__ __forceinline__ bf16x8 lds_frag(lds_cptr p) { return *(const LAS bf16x8*)p; }
__device__ __forceinline__ bf16x8 tr_frag(lds_cptr p0, lds_cptr p1) {
    s16x4 a = __builtin_amdgcn_ds_read_tr16_b64_v4i16((LAS s16x4*)p0);
    s16x4 b = __builtin_amdgcn_ds_read_tr16_b64_v4i16((LAS s16x4*)p1);
    return __builtin_bit_cast(bf16x8, __builtin_shufflevector(a, b, 0, 1, 2, 3, 4, 5, 6, 7));
}
__device__ __forceinline__ f32x16 pv_lo(bf16x8 v, const f32x16& p, f32x16 acc, int idx) {
    f32x8 h = __builtin_shufflevector(p, p, 0, 1, 2, 3, 4, 5, 6, 7);
    return __builtin_amdgcn_smfmac_f32_32x32x32_bf16(v, __builtin_bit_cast(bf16x16, h), acc, idx, 0, 0);
}
__device__ __forceinline__ f32x16 pv_hi(bf16x8 v, const f32x16& p, f32x16 acc, int idx) {
    f32x8 h = __builtin_shufflevector(p, p, 8, 9, 10, 11, 12, 13, 14, 15);
    return __builtin_amdgcn_smfmac_f32_32x32x32_bf16(v, __builtin_bit_cast(bf16x16, h), acc, idx, 0, 0);
}
#define MX3(a, b, c) __builtin_fmaxf(__builtin_fmaxf((a), (b)), (c))
__device__ __forceinline__ float max16(const f32x16& s) {
    float a = MX3(s[0], s[1], s[2]), b = MX3(s[3], s[4], s[5]);
    a = MX3(a, s[6], s[7]); b = MX3(b, s[8], s[9]); a = MX3(a, s[10], s[11]); b = MX3(b, s[12], s[13]);
    return MX3(a, b, __builtin_fmaxf(s[14], s[15]));
}
__device__ __forceinline__ float halfmax(float m) {
    auto r = __builtin_amdgcn_permlane32_swap(__float_as_uint(m), __float_as_uint(m), false, false);
    return __builtin_fmaxf(__uint_as_float(r[0]), __uint_as_float(r[1]));
}
__device__ __forceinline__ f32x16 lds_f32x16(lds_cptr p) {
    f32x4 a = *(const LAS f32x4*)p, b = *(const LAS f32x4*)(p + 1024), c = *(const LAS f32x4*)(p + 2048), d = *(const LAS f32x4*)(p + 3072);
    f32x8 lo = __builtin_shufflevector(a, b, 0, 1, 2, 3, 4, 5, 6, 7), hi = __builtin_shufflevector(c, d, 0, 1, 2, 3, 4, 5, 6, 7);
    return __builtin_shufflevector(lo, hi, 0, 1, 2, 3, 4, 5, 6, 7, 8, 9, 10, 11, 12, 13, 14, 15);
}
#define ST_AUX 16
#define LD_AUX 16
#define BLOAD(rs, vo, so) as_frag(__builtin_amdgcn_raw_buffer_load_b128(rs, vo, so, LD_AUX))
#define BSTORE(v, rs, vo, so) __builtin_amdgcn_raw_buffer_store_b128(v, rs, (vo) + (so), 0, ST_AUX)

#define POLLSLP 8
#define BSLP 36
__device__ __forceinline__ void wait_tags_end(u64 v, const GAS u64* p, u64 tag, GAS u64* pub, int lane) {
    if (pub) { DRAIN(); if (lane == 0) __hip_atomic_store(pub, tag, RLX_ST); }
    if (__builtin_expect(!__all(v == tag), 0)) {
#pragma nounroll
        for (unsigned spins = 0; spins < SPIN_CAP; ++spins) {
            __builtin_amdgcn_s_sleep(POLLSLP);
            if (__all(__hip_atomic_load(p, RLX_AGENT) == tag)) break;
        }
    }
    asm volatile("" ::: "memory");
}

__device__ __forceinline__ void wait_tags(const GAS u64* p, u64 tag, GAS u64* pub = nullptr, int lane = 0) {
    wait_tags_end(__hip_atomic_load(p, RLX_AGENT), p, tag, pub, lane);
}

__device__ __forceinline__ void cnt_arrive(lds_ptr cnt, int lane) {
    asm volatile("s_waitcnt lgkmcnt(0)" ::: "memory");
    if (lane == 0) __hip_atomic_fetch_add((LAS unsigned*)cnt, 1u, __ATOMIC_RELAXED, __HIP_MEMORY_SCOPE_WORKGROUP);
}
__device__ __forceinline__ void cnt_wait(lds_ptr cnt) {
#pragma nounroll
    for (unsigned spins = 0; spins < (1u << 22); ++spins) {
        if (__builtin_amdgcn_readfirstlane(__hip_atomic_load((LAS unsigned*)cnt, __ATOMIC_RELAXED, __HIP_MEMORY_SCOPE_WORKGROUP)) == 4) break;
        __builtin_amdgcn_s_sleep(1);
    }
    asm volatile("" ::: "memory");
}
__device__ __forceinline__ unsigned kvflag(int br, int quad, int tile, int type) {
    return WS_KVFLAG + (unsigned)(((((br * 8 + quad) * 4 + (tile & 3)) * 2 + (tile >> 5)) * 2 + type) * 8 + ((tile >> 2) & 7)) * FSTR;
}

__device__ __forceinline__ void slow_rows(__amdgpu_buffer_rsrc_t rs, unsigned sbase, int lo, int hi, int g, bool band, bf16x8 q,
                                          unsigned vk, unsigned vv0, unsigned vv1, lds_cptr lds, int lane, int idx, f32x16& acc) {
    float m = -__builtin_inff();
    for (int pass = 0; pass < 2; ++pass) {
        if (pass) { m = halfmax(m); acc = f32x16{}; }
        for (int kt = lo; kt <= hi; ++kt) {
            const unsigned so = sbase + (unsigned)kt * TS;
            const bf16x8 k = BLOAD(rs, vk, so), v0 = BLOAD(rs, vv0, so), v1 = BLOAD(rs, vv1, so);
            f32x16 c = f32x16{};
            if (band && kt == g) c = lds_f32x16(lds + L_MDIAG + lane * 16);
            if (band && kt == g - 8) c = lds_f32x16(lds + L_MFAR + lane * 16);
            f32x16 s = MFMA(k, q, c);
            if (!pass) m = __builtin_fmaxf(m, max16(s));
            else {
#pragma unroll
                for (int r = 0; r < 16; ++r) s[r] = __builtin_amdgcn_exp2f(s[r] - m);
                acc = pv_lo(v0, s, acc, idx); acc = pv_hi(v1, s, acc, idx);
            }
        }
    }
}

#define PK4(A, o) ((u32x4){cvtpk(A[o], A[o + 1]), cvtpk(A[o + 2], A[o + 3]), cvtpk(A[o + 4], A[o + 5]), cvtpk(A[o + 6], A[o + 7])})
#define PROF_ARG
#define PROF_PASS
__device__ __forceinline__ u64 make_tag(u32x2 e) {
    return (((u64)(unsigned)__builtin_amdgcn_readfirstlane(e[1]) << 32) | (unsigned)__builtin_amdgcn_readfirstlane(e[0])) * 0x9E3779B97F4A7C15ull + 0x632BE59BD9B4E019ull;
}
template <int E>
__device__ __forceinline__ u32x4 pack_kv(int i, const f32x16& a) {
    const int s = i & 1;
    if (E == 0) return PK4(a, 8 * s);
    auto x = __builtin_amdgcn_permlane32_swap(cvtpk(a[8 * s], a[8 * s + 1]), cvtpk(a[8 * s + 4], a[8 * s + 5]), false, false);
    auto y = __builtin_amdgcn_permlane32_swap(cvtpk(a[8 * s + 2], a[8 * s + 3]), cvtpk(a[8 * s + 6], a[8 * s + 7]), false, false);
    return (u32x4){x[0], y[0], x[1], y[1]};
}
#define PUT_KV(i, v, vo) BSTORE(v, rkv, vo, ((i) < 2 ? 64 * TS : 0) + ((i) & 1) * (E ? 512 : 128 * TS))
#define QSWAP 1
#define PBX 4
template <int E>
__device__ __forceinline__ void project(lds_ptr lds, char* ws, __amdgpu_buffer_rsrc_t rkv, __amdgpu_buffer_rsrc_t ro, int lane, int t, int p, int j, u64& tag PROF_ARG) {
    const int r32 = lane & 31, hh = lane >> 5, qd4 = (p >> 1) ^ QSWAP;
    const unsigned q4 = (lane & 15) >> 2, wcol = ((lane >> 4) & 1) * 32 + (lane & 3) * 8;
    lds_cptr xa[4];
#pragma unroll
    for (int ks = 0; ks < 4; ++ks) xa[ks] = lds + (32 * t + r32) * 128 + (((hh ^ ((r32 >> 1) & 7)) * 16) ^ (ks * 32));
    unsigned w1o = L_W + (8 * hh + q4) * 128 + ((lane >> 4) & 1) * 64 + (q4 >> 1) * 32 + (lane & 3) * 8, w0o = w1o ^ 32;
    PIN(w0o); PIN(w1o);
    const lds_cptr w0 = lds + w0o, w1 = lds + w1o, wq = lds + (8 * hh + q4) * 64 + wcol;
#define XRD(n) lds_frag(xa[(n) & 3] + (((n) >> 2) & 3) * 16384)
#define WRD(b, n) tr_frag(b + ((n) & 15) * 2048, b + ((n) & 15) * 2048 + 512)
#define YOF(n) ((n) >= 28 ? 32768 + ((n) & 3) * 2048 : ((n) & 15) * 2048)
#define YRD(b, n) tr_frag(b + YOF(n), b + YOF(n) + 512)
#define QOF(br, n) (((n) >= 28 ? L_Q15 - L_Q14 : ((n) >= 24 ? 0 : (((n) >> 2) & 3) * 16384)) + (br) * 4096 + (((n) & 3) ^ E) * 1024)
#define QRD(br, n) tr_frag(((n) >= 24 ? wqh : wq) + QOF(br, n), ((n) >= 24 ? wqh : wq) + QOF(br, n) + 256)
#define MM(W, X, A) (E ? MFMA(X, W, A) : MFMA(W, X, A))
    bf16x8 xf[32], f0[4], f1[4];
    f32x16 a0 = f32x16{}, a1 = f32x16{};
    unsigned gwo = (lane & 15) == ((lane >> 4) & 1) ? L_GW + t * 1024 + hh * 16 : L_Z;
    PIN(gwo);
    f32x4 zg = f32x4{};
    bf16x8 gf[4];
#define RD1(n) do { xf[n] = XRD(n); f0[(n) & 3] = WRD(w0, n); f1[(n) & 3] = WRD(w1, n); SBAR(); } while (0)
#define GRD(n) gf[(n) & 3] = lds_frag(lds + gwo + (n) * 32)
#define GMM(n) zg = __builtin_amdgcn_mfma_f32_16x16x32_bf16(gf[(n) & 3], xf[n], zg, 0, 0, 0)
#define MM1(n) do { a0 = MM(f0[(n) & 3], xf[n], a0); a1 = MM(f1[(n) & 3], xf[n], a1); SBAR(); } while (0)
#pragma unroll
    for (int s = 0; s < 8; ++s) {
        if (s == 0) CBAR(); else BAR();
        SBAR();
        RD1(4 * s); RD1(4 * s + 1);
        if (s) { MM1(4 * s - 1); GMM(4 * s - 4); GMM(4 * s - 3); GMM(4 * s - 2); GMM(4 * s - 1); SBAR(); }
        RD1(4 * s + 2); MM1(4 * s); RD1(4 * s + 3); MM1(4 * s + 1);
        if (s == 0) DRAIN();
        GRD(4 * s); GRD(4 * s + 1); GRD(4 * s + 2); GRD(4 * s + 3); SBAR();
        MM1(4 * s + 2);
        SBAR();
    }
    const unsigned tile = 4 * j + t;
    unsigned vo = (unsigned)(4 * qd4 * 64 + tile) * TS + (E ? (r32 >> 4) * (128 * TS) + 1024 + hh * 256 + (r32 & 15) * 16 : lane * 16);
    unsigned wqo = L_Q14 + (8 * hh + q4) * 64 + wcol;
    PIN(vo); PIN(wqo);
    const lds_cptr wqh = lds + wqo;
    MM1(31); GMM(28); GMM(29); GMM(30); GMM(31); SBAR();
#define PRE8 2
#define PUBN 7
#define LATE0 99
    u32x4 pk[4];
#pragma unroll
    for (int i = 0; i < 4; ++i) { pk[i] = pack_kv<E>(i, i < 2 ? a0 : a1); PIN(pk[i]); if (i < PRE8) PUT_KV(i, pk[i], vo); SBAR(); }
    if (!E) BSTORE(((u32x4){0x3f803f80u, 0x3f803f80u, 0x3f803f80u, 0x3f803f80u}), rkv,
                   (unsigned)((hh * 32 + 4 * (qd4 ^ (hh * (PBX >> 1))) + (r32 >> 3)) * 64 + tile) * TS + 2048 + (lane & 7) * 16, 0);
    { LAS float* gp = (LAS float*)(lds + (lane < 16 ? L_G + t * 128 : L_SCR)) + lane;
      gp[0] = __builtin_amdgcn_rcpf(1.f + __expf(-zg[0])); gp[16] = __builtin_amdgcn_rcpf(1.f + __expf(-zg[1])); }
    SBAR();
    BAR(); SBAR();
    bf16x8 u0 = QRD(0, 0), u1 = QRD(1, 0), y0 = YRD(w0, 0), y1 = YRD(w1, 0);
    const u32x2 tg = *(const LAS u32x2*)(lds + L_EP);
    f32x16 b0 = f32x16{}, b1 = f32x16{}, aQ0 = f32x16{}, aQ1 = f32x16{};
    SBAR();
#pragma unroll
    for (int n = 0; n < 24; ++n) {
        aQ0 = MFMA(u0, xf[n], aQ0); SBAR();
        if (n == 1) { tag = make_tag(tg); asm volatile("" : "+s"(tag)); SBAR(); }
        if (n == PUBN) { DRAIN(); if (lane == 0) __hip_atomic_store((GAS u64*)(ws + kvflag(0, qd4, tile, E)), tag, RLX_ST); SBAR(); }
        if ((n & 3) == 3) {
            if (PRE8 + (n >> 2) < 2) { PUT_KV((PRE8 + (n >> 2)) & 3, pk[(PRE8 + (n >> 2)) & 3], vo); SBAR(); }
            if (n == LATE0) { PUT_KV(2, pk[2], vo); SBAR(); }
            if (n == LATE0 + 4) { PUT_KV(3, pk[3], vo); SBAR(); }
            BAR(); SBAR();
        }
        u0 = QRD(0, n + 1); SBAR();
        aQ1 = MFMA(u1, xf[n], aQ1); SBAR();
        u1 = n < 23 ? QRD(1, n + 1) : QRD(0, 25); SBAR();
        b0 = MM(y0, xf[n], b0); SBAR(); y0 = n < 23 ? YRD(w0, n + 1) : QRD(0, 26); SBAR();
        b1 = MM(y1, xf[n], b1); SBAR(); y1 = n < 23 ? YRD(w1, n + 1) : QRD(0, 27); SBAR();
    }
    aQ0 = MFMA(u0, xf[24], aQ0); SBAR(); u0 = QRD(0, 28); SBAR(); aQ0 = MFMA(u1, xf[25], aQ0); SBAR(); u1 = QRD(0, 29); SBAR();
    aQ0 = MFMA(y0, xf[26], aQ0); SBAR(); y0 = QRD(0, 30); SBAR(); aQ0 = MFMA(y1, xf[27], aQ0); SBAR(); y1 = QRD(0, 31); SBAR();
    aQ0 = MFMA(u0, xf[28], aQ0); SBAR(); aQ0 = MFMA(u1, xf[29], aQ0); SBAR(); u1 = QRD(1, 24); SBAR();
    aQ0 = MFMA(y0, xf[30], aQ0); SBAR(); y0 = YRD(w0, 24); SBAR(); aQ0 = MFMA(y1, xf[31], aQ0); SBAR(); y1 = YRD(w1, 24); SBAR();
    const lds_ptr qd = lds + L_Q + t * 1024 + lane * 16;
#define QBAR 4
#define NFREE 3
    f32x8 qs; u32x4 qk;
#define QEPI(m) do { if ((m) == 1) { PIN(aQ0); qs = __builtin_shufflevector(aQ0, aQ0, 8, 9, 10, 11, 12, 13, 14, 15) * QSCALE; PIN(qs); } \
        if ((m) == 2) { qk = PK4(qs, 0); *(LAS u32x4*)(qd + 4096) = qk; } \
        if ((m) == QBAR) { __builtin_amdgcn_s_setprio(2); __builtin_amdgcn_s_waitcnt(0xC07F | ((2 * (QBAR - 2)) << 8)); BAR(); STAMP(4); } \
        if ((m) > QBAR + NFREE) PACE(); SBAR(); } while (0)
#pragma unroll
    for (int n = 24; n < 32; ++n) {
        aQ1 = MFMA(u1, xf[n], aQ1); SBAR(); if (n < 31) u1 = QRD(1, n + 1); SBAR(); QEPI(3 * (n - 24));
        b0 = MM(y0, xf[n], b0); SBAR(); if (n < 31) y0 = YRD(w0, n + 1); SBAR(); QEPI(3 * (n - 24) + 1);
        b1 = MM(y1, xf[n], b1); SBAR(); if (n < 31) y1 = YRD(w1, n + 1); SBAR(); QEPI(3 * (n - 24) + 2);
    }
#pragma unroll
    for (int r = 0; r < 8; ++r) aQ0[r] *= QSCALE;
#pragma unroll
    for (int r = 0; r < 16; ++r) aQ1[r] *= QSCALE;
    *(LAS u32x4*)qd = PK4(aQ0, 0); *(LAS u32x4*)(qd + 8192) = PK4(aQ1, 0); *(LAS u32x4*)(qd + 12288) = PK4(aQ1, 8);
    cnt_arrive(lds + L_CNT + 8, lane);
    const unsigned vo2 = vo + (32 + 4 * ((qd4 ^ (PBX >> 1)) - qd4)) * 64 * TS;
#define YSLP 8
    u32x4 py[4] = {pack_kv<E>(0, b0), pack_kv<E>(1, b0), pack_kv<E>(2, b1), pack_kv<E>(3, b1)};
    PIN(py[0]); PIN(py[1]); PIN(py[2]); PIN(py[3]);
#define YSTAG 2
    if (t & 1) __builtin_amdgcn_s_sleep(YSTAG);
    if (t & 2) __builtin_amdgcn_s_sleep(2 * YSTAG);
    if (LATE0 > 23) { PUT_KV(2, pk[2], vo); __builtin_amdgcn_s_sleep(YSLP); }
    if (LATE0 > 19) { PUT_KV(3, pk[3], vo); __builtin_amdgcn_s_sleep(YSLP); }
    PUT_KV(0, py[0], vo2); __builtin_amdgcn_s_sleep(YSLP); PUT_KV(1, py[1], vo2); __builtin_amdgcn_s_sleep(YSLP);
    PUT_KV(2, py[2], vo2); __builtin_amdgcn_s_sleep(YSLP); PUT_KV(3, py[3], vo2);
    DRAIN();
    if (lane == 0) __hip_atomic_store((GAS u64*)(ws + kvflag(1, qd4, tile, E)), tag, RLX_ST);
    BSTORE(((u32x4){~0u, ~0u, ~0u, ~0u}), ro, lane * 16, tile * OS + 2 * p * 1024);
    STAMP(19);
}

#define AB_WO (512 * 1024)
#define ROTEXPR j + 2 * (p & 3)
#define WOX 1
#define WOIN 2
#define CODE_LINES 280

struct Att {
    lds_ptr lds; char* ws; __amdgpu_buffer_rsrc_t rkv, ro;
    int lane, t, p, g, head, idx; u64 tag; unsigned vk, vv0, vv1, sBeg; lds_cptr zp, qp[4];
};
#define IDX a.idx
#define LD3(K, V0, V1, rs, so) do { K = BLOAD(rs, a.vk, so); V0 = BLOAD(rs, a.vv0, so); V1 = BLOAD(rs, a.vv1, so); } while (0)
#define EX(X, r) X[r] = __builtin_amdgcn_exp2f(X[r])
#define STEP(X, Y, OP, VP, KN, QN, CN, O, V0, F1, F3, F2) do { \
        EX(X, 0); EX(X, 1); SBAR(); OP = pv_hi(VP, Y, OP, IDX); SBAR(); F1; SBAR(); EX(X, 2); EX(X, 3); EX(X, 4); SBAR(); Y = MFMA(KN, QN, CN); SBAR(); F3; SBAR(); \
        EX(X, 5); EX(X, 6); EX(X, 7); SBAR(); EX(X, 8); EX(X, 9); SBAR(); O = pv_lo(V0, X, O, IDX); SBAR(); F2; SBAR(); \
        EX(X, 10); EX(X, 11); EX(X, 12); EX(X, 13); EX(X, 14); EX(X, 15); SBAR(); } while (0)

__device__ __forceinline__ f32x16 lds_zero16(lds_cptr zp) {
    const volatile LAS f32x4* z = (const volatile LAS f32x4*)zp;
    const f32x4 a = *z, b = *z, c = *z, d = *z;
    f32x8 lo = __builtin_shufflevector(a, b, 0, 1, 2, 3, 4, 5, 6, 7), hi = __builtin_shufflevector(c, d, 0, 1, 2, 3, 4, 5, 6, 7);
    return __builtin_shufflevector(lo, hi, 0, 1, 2, 3, 4, 5, 6, 7, 8, 9, 10, 11, 12, 13, 14, 15);
}

template <int C, int BR>
__device__ __forceinline__ void finish(const Att& a, f32x16 o, float gf, bf16x8 q, unsigned vo) {
    f32x8 y = __builtin_shufflevector(o, o, 0, 1, 2, 3, 4, 5, 6, 7) * (__builtin_amdgcn_rcpf(o[8]) * gf);
    PIN(y);
    const float chk = __builtin_fmaf(((y[0] + y[2]) + (y[4] + y[6])) + ((y[1] + y[3]) + (y[5] + y[7])), 0.f, o[8]);
    if (__builtin_expect(__any(__builtin_amdgcn_fmed3f(chk, LMIN, LMAX) != chk), 0)) {
        if (!BR) { wait_tags((const GAS u64*)(a.ws + kvflag(!C, a.p >> 1, 0, 0) + a.lane * FSTR), a.tag); wait_tags((const GAS u64*)(a.ws + kvflag(!C, a.p >> 1, 2, 0) + a.lane * FSTR), a.tag); }
        slow_rows(a.rkv, (unsigned)((BR * 32 + (a.head ^ (BR * 2 * PBX))) * 64) * TS, BR ? (a.g >= 8 ? a.g - 8 : 0) : 0, BR ? a.g : 63, a.g, BR, q, a.vk, a.vv0, a.vv1, a.lds, a.lane, IDX, o);
        y = __builtin_shufflevector(o, o, 0, 1, 2, 3, 4, 5, 6, 7) * (__builtin_amdgcn_rcpf(o[8]) * gf);
        PIN(y);
    }
    u32x4 v = (u32x4){cvtpk(y[0], y[1]), cvtpk(y[2], y[3]), cvtpk(y[4], y[5]), cvtpk(y[6], y[7])};
    if (!C && !BR) v = __builtin_elementwise_min(v, (u32x4){~1u, ~1u, ~1u, ~1u});
    BSTORE(v, a.ro, vo, 0);
}
#define OFLAG(BR, C) ((GAS u64*)(a.ws + WS_OFLAG) + (((BR) * 64 + a.g) * 2 + (C)) * 16 + (a.p ^ ((BR) * PBX)))
#define OVOFF(BR) (a.vk + (unsigned)((BR) * 64 + a.g) * OS + (a.head ^ ((BR) * 2 * PBX)) * 1024)
#define GATE() (*(const LAS float*)(a.lds + L_G + a.t * 128 + (a.lane & 31) * 4))

struct KV3 { bf16x8 k[3], v0[3], v1[3]; };
#define LDB(K, V0, V1, i) do { const int kt_ = a.g - (i); \
        const __amdgpu_buffer_rsrc_t rb_ = __builtin_amdgcn_make_buffer_rsrc(a.ws + WS_KV, 0, kt_ >= 0 ? KV_BYTES : 0u, 0x00020000); \
        LD3(K, V0, V1, rb_, (unsigned)((32 + (a.head ^ (2 * PBX))) * 64 + (kt_ >= 0 ? kt_ : 0)) * TS); } while (0)
#define LDB_FIRST(f) do { LDB(f.k[0], f.v0[0], f.v1[0], 0); SBAR(); LDB(f.k[1], f.v0[1], f.v1[1], 1); SBAR(); LDB(f.k[2], f.v0[2], f.v1[2], 2); SBAR(); } while (0)
#define LDA_FIRST(f) do { LD3(f.k[0], f.v0[0], f.v1[0], a.rkv, so0[0]); SBAR(); LD3(f.k[1], f.v0[1], f.v1[1], a.rkv, so0[1]); SBAR(); \
        LD3(f.k[2], f.v0[2], f.v1[2], a.rkv, so0[2]); SBAR(); } while (0)
__device__ __forceinline__ unsigned window_flag(const Att& a) {
    const int i = a.lane & 15, kt = a.g - (i < 9 ? i : 0);
    return kvflag(1, a.p >> 1, kt >= 0 ? kt : 0, (a.lane >> 4) & 1);
}

template <int C>
__device__ __forceinline__ void attn_full(const Att& a, f32x16 o0, f32x16 o1, f32x16 o2, f32x16 o3, f32x16 S1, unsigned so,
                                          __amdgpu_buffer_rsrc_t rwo, unsigned vwo, unsigned wi, const GAS u64* fp2, const GAS u64* fp3, KV3& nxt PROF_ARG) {
    const lds_ptr lds = a.lds; const int t = a.t, lane = a.lane;
    const f32x16 zc = f32x16{};
    bf16x8 ka = nxt.k[0], va0 = nxt.v0[0], va1 = nxt.v1[0], kb = nxt.k[1], vb0 = nxt.v0[1], vb1 = nxt.v1[1], kc = nxt.k[2], vc0 = nxt.v0[2], vc1 = nxt.v1[2], kd, vd0, vd1;
    const unsigned sBeg = a.sBeg, jb = a.g >> 2;
    unsigned ki = 3;
    vd1 = lds_frag(a.zp);
    lds_ptr pw = lds + L_PART + (4 * C + t) * PREC + lane * 16;
    lds_cptr pr0 = lds + L_PART + (4 * C + ((t - 1) & 3)) * PREC + lane * 16, pr1 = lds + L_PART + (4 * C + ((t - 2) & 3)) * PREC + lane * 16,
             pr2 = lds + L_PART + (4 * C + ((t - 3) & 3)) * PREC + lane * 16;
    unsigned ovo = OVOFF(0); float gf = 0.f;
    if (C) { BAR(); STAMP(4); }
    else { gf = GATE(); PIN(pw); PIN(pr0); PIN(pr1); PIN(pr2); PIN(ovo); PIN(gf);
           DRAIN(); if (lane == 0) __hip_atomic_store(OFLAG(1, 0), a.tag, RLX_ST); }
    bf16x8 q0 = lds_frag(a.qp[0]); SBAR();
    bf16x8 q1 = lds_frag(a.qp[1]), q2 = lds_frag(a.qp[2]), q3 = lds_frag(a.qp[3]);
    SBAR();
    f32x16 S0 = MFMA(ka, q0, zc);
    PIN(S0); SBAR();
    PIN(q1); PIN(q2); PIN(q3); PIN(vd1); PIN(o0); PIN(o1); PIN(o2); PIN(o3); PIN(S1);
    __builtin_amdgcn_s_waitcnt(0x0F76);
#pragma unroll
    for (int i = 0; i < 8; ++i) __builtin_amdgcn_s_setprio(C);
    STAMP(3);
    SBAR();
    unsigned wso = 0;
    u64 fv = a.tag;
#define NEXT1() asm volatile("s_add_i32 %0, %0, 1\n\ts_xor_b32 %1, %0, %2" : "+s"(ki), "=s"(so) : "s"(jb) : "scc")
#define NEXT2() asm volatile("s_and_b32 %0, %0, 15" : "+s"(so) :: "scc")
#define NEXT3() asm volatile("s_mulk_i32 %0, %2\n\ts_add_i32 %0, %0, %1" : "+s"(so) : "s"(sBeg), "i"(4 * TS) : "scc")
#define WO_AUX 0
#define WOC 1
#define WO_OFF() do { if (C == WOC) asm volatile("s_lshl_b32 %0, %1, 4" : "=s"(wso) : "s"(wi) : "scc"); } while (0)
#define WO_DMA() do { if (C == WOC) __builtin_amdgcn_raw_ptr_buffer_load_lds(rwo, (LAS void*)(lds + L_WO + t * 16384 + wi), 16, vwo, wso, 0, WO_AUX); } while (0)
#define WO_NEXT() do { if (C == WOC) asm volatile("s_addk_i32 %0, 0x400\n\ts_and_b32 %0, %0, 0x3fff" : "+s"(wi) :: "scc"); } while (0)
#define TILE(K, V0, V1, KN, LK, LV0, LV1, NW, G) do { \
        STEP(S0, S1, o3, LV1, K, q1, zc, o0, V0, LK = BLOAD(a.rkv, a.vk, so), WO_OFF(), LV0 = BLOAD(a.rkv, a.vv0, so)); \
        STEP(S1, S0, o0, V1, K, q2, zc, o1, V0, LV1 = BLOAD(a.rkv, a.vv1, so), , WO_DMA()); \
        STEP(S0, S1, o1, V1, K, q3, zc, o2, V0, NEXT1(), NEXT2(), NEXT3()); \
        STEP(S1, S0, o2, V1, KN, q0, zc, o3, V0, __builtin_amdgcn_s_waitcnt(0x0F70 | (NW)), G, WO_NEXT()); } while (0)
#define PUTA(O, sl) (*(LAS f32x4*)(pw + (sl) * 2048) = __builtin_shufflevector(O, O, 0, 1, 2, 3))
#define PUTB(O, sl) (*(LAS f32x4*)(pw + (sl) * 2048 + 1024) = __builtin_shufflevector(O, O, 4, 5, 6, 7))
#define PUTL(O, sl) (*(LAS float*)(pw + 6144 + (sl) * 4) = O[8])
    f32x4 x0, y0, x1, y1, x2, y2; float l0, l1, l2;
#define GETP() do { x0 = *(const LAS f32x4*)pr0; x1 = *(const LAS f32x4*)(pr1 + 2048); x2 = *(const LAS f32x4*)(pr2 + 4096); \
        y0 = *(const LAS f32x4*)(pr0 + 1024); y1 = *(const LAS f32x4*)(pr1 + 3072); y2 = *(const LAS f32x4*)(pr2 + 5120); \
        l0 = *(const LAS float*)(pr0 + 6144); l1 = *(const LAS float*)(pr1 + 6148); l2 = *(const LAS float*)(pr2 + 6152); } while (0)
    if (C) {
#pragma nounroll
        for (int it = 0; it < 4; ++it) {
            TILE(ka, va0, va1, kb, kd, vd0, vd1, WOC ? 7 : 6, );
            PIN(S0); PIN(S1); PIN(o3); wait_tags_end(fv, fp2, a.tag, nullptr, 0);
            TILE(kb, vb0, vb1, kc, ka, va0, va1, WOC ? 8 : 6, );
            TILE(kc, vc0, vc1, kd, kb, vb0, vb1, WOC ? 9 : 6, fv = __hip_atomic_load(fp2, RLX_AGENT));
            TILE(kd, vd0, vd1, ka, kc, vc0, vc1, WOC ? 10 : 7, );
        }
        o3 = pv_hi(vd1, S1, o3, IDX);
        STAMP(6);
        const u64 f3 = __hip_atomic_load(fp3, RLX_AGENT);
        cnt_wait(lds + L_CNT + 8);
        PUTA(o0, 0); PUTB(o0, 0); PUTL(o0, 0); PUTA(o1, 1); PUTB(o1, 1); PUTL(o1, 1); PUTA(o2, 2); PUTB(o2, 2); PUTL(o2, 2);
        DRAIN(); cnt_arrive(lds + L_CNT, lane);
        wait_tags_end(f3, fp3, a.tag, nullptr, 0);
        LDB_FIRST(nxt);
        cnt_wait(lds + L_CNT);
        GETP(); gf = GATE();
    } else {
        WO_OFF(); WO_DMA(); WO_NEXT(); WO_OFF(); WO_DMA(); WO_NEXT(); SBAR();
        TILE(ka, va0, va1, kb, kd, vd0, vd1, WOC ? 6 : 9, );
        TILE(kb, vb0, vb1, kc, ka, va0, va1, WOC ? 6 : 9, );
#pragma nounroll
        for (int it = 0; it < 3; ++it) {
            TILE(kc, vc0, vc1, kd, kb, vb0, vb1, WOC ? 6 : 9, );
            TILE(kd, vd0, vd1, ka, kc, vc0, vc1, WOC ? 6 : 9, );
            TILE(ka, va0, va1, kb, kd, vd0, vd1, WOC ? 6 : 9, );
            TILE(kb, vb0, vb1, kc, ka, va0, va1, WOC ? 6 : 9, );
        }
        STEP(S0, S1, o3, vb1, kc, q1, zc, o0, vc0, , , );
        STEP(S1, S0, o0, vc1, kc, q2, zc, o1, vc0, , , );
        STEP(S0, S1, o1, vc1, kd, q0, zc, o2, vc0, __builtin_amdgcn_s_waitcnt(WOC ? 0x0F73 : 0x0F75), , );
        STEP(S1, S0, o2, vc1, kd, q1, zc, o0, vd0, , , );
        STEP(S0, S1, o0, vd1, kd, q2, zc, o1, vd0, , , );
        STEP(S1, S0, o1, vd1, kc, q3, zc, o2, vd0, PUTA(o0, 0), PUTL(o0, 0), PUTB(o0, 0));
        STEP(S0, S1, o2, vd1, kd, q3, zc, o3, vc0, PUTA(o1, 1), PUTL(o1, 1), PUTB(o1, 1));
        PUTA(o2, 2); PUTB(o2, 2); asm volatile("" :: "v"(o0), "v"(o1), "v"(o2)); SBAR();
        EX(S1, 0); EX(S1, 1); SBAR(); o3 = pv_hi(vc1, S0, o3, IDX); SBAR(); PUTL(o2, 2); __builtin_amdgcn_s_setprio(2); SBAR();
        EX(S1, 2); EX(S1, 3); EX(S1, 4); EX(S1, 5); EX(S1, 6); EX(S1, 7); SBAR(); EX(S1, 8); EX(S1, 9); SBAR();
        o3 = pv_lo(vd0, S1, o3, IDX); PIN(S1); SBAR();
        if (!WOC) DRAIN();
        CBAR(); GETP(); SBAR();
        EX(S1, 10); EX(S1, 11); EX(S1, 12); EX(S1, 13); EX(S1, 14); EX(S1, 15); SBAR();
        o3 = pv_hi(vd1, S1, o3, IDX); SBAR();
        STAMP(6);
    }
    x0 = (x0 + x1) + x2; y0 = (y0 + y1) + y2; l0 = (l0 + l1) + l2;
    PIN(x0); PIN(y0); PIN(l0); PIN(o3);
#pragma unroll
    for (int r = 0; r < 4; ++r) { o3[r] += x0[r]; o3[4 + r] += y0[r]; }
    o3[8] += l0;
    finish<C, 0>(a, o3, gf, q3, ovo);
}

template <int C>
__device__ __forceinline__ void attn_window(const Att& a, KV3& f, const unsigned (&so0)[4] PROF_ARG) {
    const lds_ptr lds = a.lds; const int t = a.t, lane = a.lane;
    const f32x16 zc = f32x16{};
    bf16x8 k[9], v0[9], v1[9];
#pragma unroll
    for (int i = 0; i < 3; ++i) { k[i] = f.k[i]; v0[i] = f.v0[i]; v1[i] = f.v1[i]; }
#define LDW(i) LDB(k[i], v0[i], v1[i], i)
    const bf16x8 qa = lds_frag(lds + L_Q + 8192 + C * 4096 + t * 1024 + lane * 16);
    const f32x16 cd = lds_f32x16(lds + L_MDIAG + lane * 16), cf = lds_f32x16(lds + L_MFAR + lane * 16);
    f32x16 oo = lds_zero16(a.zp);
    const float gt = GATE();
    f32x16 S0 = MFMA(k[0], qa, cd), S1 = MFMA(k[1], qa, zc);
    SBAR(); LDW(3); SBAR();
#pragma unroll
    for (int r = 0; r < 10; ++r) { EX(S0, r); SBAR(); }
    oo = pv_lo(v0[0], S0, oo, IDX); SBAR();
#pragma unroll
    for (int r = 10; r < 16; ++r) EX(S0, r);
    SBAR();
    STEP(S1, S0, oo, v1[0], k[2], qa, zc, oo, v0[1], LDW(4), , );
    STEP(S0, S1, oo, v1[1], k[3], qa, zc, oo, v0[2], LDW(5), , );
    STEP(S1, S0, oo, v1[2], k[4], qa, zc, oo, v0[3], LDW(6), , );
    STEP(S0, S1, oo, v1[3], k[5], qa, zc, oo, v0[4], LDW(7), , );
    STEP(S1, S0, oo, v1[4], k[6], qa, zc, oo, v0[5], LDW(8), , );
    const GAS u64* fa = (const GAS u64*)(a.ws + kvflag(1, a.p >> 1, t, 0) + (lane & 31) * FSTR);
    u64 fv = 0;
    STEP(S0, S1, oo, v1[5], k[7], qa, zc, oo, v0[6], if (!C) fv = __hip_atomic_load(fa, RLX_AGENT), , );
    STEP(S1, S0, oo, v1[6], k[8], qa, cf, oo, v0[7], , , );
    if (C) { PIN(S0); PIN(S1); PIN(oo); DRAIN(); if (lane == 0) __hip_atomic_store(OFLAG(0, C), a.tag, RLX_ST); }
    STEP(S0, S1, oo, v1[7], k[8], qa, cf, oo, v0[8], , , );
    oo = pv_hi(v1[8], S0, oo, IDX);
    STAMP(7);
    if (!C) { PIN(oo); wait_tags_end(fv, fa, a.tag, nullptr, 0); LDA_FIRST(f); }
    finish<C, 1>(a, oo, 1.f - gt, qa, OVOFF(1));
}

__global__ void __launch_bounds__(512, 2)
gmix_kernel(const char* x, const char* wA, const char* woA, const char* wB, const char* woB, const char* gw, char* out, char* ws) {
    extern __shared__ __attribute__((aligned(16))) char smem[];
    const lds_ptr lds = (lds_ptr)smem;
    const unsigned lds0 = (unsigned)(uintptr_t)lds;
    const int tid = threadIdx.x, lane = tid & 63, r32 = lane & 31, hh = lane >> 5;
    const int w = __builtin_amdgcn_readfirstlane(tid >> 6);
    const int bid = blockIdx.x, xcd = bid & 7, rr = bid >> 3;
    const int p = 4 * (xcd & 3) + (rr & 3), j = 8 * (xcd >> 2) + (rr >> 2);
    const int jo = 2 * xcd + (rr >> 4), po = rr & 15;
    const int t = w & 3, c = w >> 2;
    const int e = p & 1;
    STAMP(0);
    {
        u64 pc; const unsigned ln = (((bid & ~7) | (tid >> 6)) + (bid < CODE_LINES - 256 ? (tid & 32) * 8 : 0)) * 128;
        asm volatile("s_getpc_b64 %0\n\ts_mov_b32 m0, %2\n\ts_nop 0\n\tglobal_load_lds_dword %1, %0" : "=&s"(pc) : "v"(ln), "s"(lds0 + L_SCR) : "memory");
        SBAR();
    }
    unsigned rot = (ROTEXPR) & 7;
    unsigned vx0 = (128 * j + 32 * t + (lane >> 3)) * 1024 + (((lane & 7) ^ (lane >> 4)) * 16), vx1 = (vx0 ^ 64) + 7168, vx2 = vx0 + 14336, vx3 = vx1 + 14336;
    unsigned vw0 = (16 * t + (lane >> 3)) * 3072 + 1024 + 1024 * e + 128 * ((p >> 1) ^ QSWAP) + (((lane & 7) ^ (((lane >> 4) & 1) << 1)) * 16), vw1 = vw0 + 8 * 3072 - 1024;
    unsigned mx = lds0 + t * 4096, mw = lds0 + L_W + t * 2048, ow = rot * (64 * 3072);
    asm volatile("" : "+v"(vx0), "+v"(vx1), "+v"(vx2), "+v"(vx3), "+v"(vw0), "+v"(vw1));
    asm volatile("" : "+s"(mx), "+s"(mw), "+s"(rot), "+s"(ow));
    SBAR();
    asm volatile("" : "+s"(x), "+s"(wA), "+s"(woA), "+s"(wB), "+s"(woB), "+s"(gw), "+s"(out), "+s"(ws));
#define MK_ATT(C) Att a; a.lds = lds; a.ws = ws; a.lane = lane; a.t = t; a.p = p; a.g = 4 * j + t; a.head = 2 * p + C; a.idx = 0xdddddddd; \
    a.rkv = __builtin_amdgcn_make_buffer_rsrc(ws + WS_KV, 0, KV_BYTES, 0x00020000); a.ro = __builtin_amdgcn_make_buffer_rsrc(ws + WS_O, 0, O_BYTES, 0x00020000); \
    a.vk = lane * 16; a.vv0 = r32 < 16 ? 1024 + hh * 256 + r32 * 16 : ((r32 & ~4) == 16 ? 2048 : OOB); a.vv1 = r32 < 16 ? a.vv0 + 512 : a.vv0; a.zp = lds + L_Z + lane * 16; \
    a.sBeg = (unsigned)(a.head * 64 + t) * TS; \
    _Pragma("unroll") for (int i = 0; i < 4; ++i) a.qp[i] = lds + L_Q + C * 4096 + ((t + 1 + i) & 3) * 1024 + lane * 16
#define RWO __builtin_amdgcn_make_buffer_rsrc((void*)((t >> 1) ? woB : woA), 0, AB_WO, 0x00020000)
#define VWO (((t & 1) * 256 + (lane >> 2)) * 1024 + 64 * po + (lane & 3) * 16)
#define WI0 (4096 * (((rr >> 4) << 1) | (po & 1)) + WOX * 1024 * (xcd & 3))
#define MK_SO() unsigned so0[4]; _Pragma("unroll") for (int i = 0; i < 4; ++i) so0[i] = a.sBeg + (j ^ i) * (4 * TS)
    if (c) {
#define DMA1(s) do { const unsigned ks_ = ((s) + rot) & 7; \
        glds16x4(vx0, vx1, vx2, vx3, x + ks_ * 128, mx + ((s) & 3) * 16384); glds16x2(vw0, vw1, wA + ks_ * (64 * 3072), mw + ((s) & 3) * 8192); } while (0)
#define DMA2(s) do { const unsigned ks_ = (((s) + rot) & 7) * (64 * 3072); \
        glds16x2(vq0, vq1, wq + ks_, mq + ((s) == 14 ? L_Q14 : ((s) & 3) * 16384)); glds16x2(vw0, vw1, wy + ks_, mw + ((s) & 3) * 8192); SBAR(); } while (0)
#define LBAR(N) asm volatile("s_waitcnt vmcnt(" #N ")\n\ts_barrier" ::: "memory")
        STAMP(20);
        glds16x4(vx0, vx1, vx2, vx3, x + rot * 128, mx); glds16x2(vw0, vw1, wA + ow, mw);
        SBAR();
        const unsigned k1 = (1 + rot) & 7;
        glds16x2(vx0, vx1, x + k1 * 128, mx + 16384);
        SBAR();
        if (t == 0) asm volatile("s_mov_b32 m0, %2\n\ts_nop 0\n\tglobal_load_lds_dword %0, %1 sc1" :: "v"((lane & 1) * 4), "s"(ws + WS_EPOCH), "s"(lds0 + L_EP) : "memory");
        unsigned vq0 = (32 * (t & 1) + 16 * e + (lane >> 2)) * 3072 + 64 * (p ^ ((t >> 1) * PBX)) + (lane & 3) * 16 + 1024, vq1 = vq0 + (e ? -16 * 3072 : 16 * 3072) - 1024;
        unsigned vwo = VWO;
        MK_ATT(1);
        const GAS u64* fp = (const GAS u64*)(ws + kvflag(0, p >> 1, (j >> 3) * 32 + t, 0) + (lane & 15) * FSTR);
        const GAS u64* fp2 = (const GAS u64*)(ws + kvflag(0, p >> 1, ((j >> 3) ^ 1) * 32 + t, 0) + (lane & 15) * FSTR);
        const GAS u64* fp3 = (const GAS u64*)(ws + (hh ? WS_OFLAG + ((64 + 4 * jo + t) * 32 + (lane & 15)) * 8 : window_flag(a)));
        PIN(vq0); PIN(vq1); PIN(vwo); PIN(a.vk); PIN(a.vv0); PIN(a.vv1); PIN(a.qp[0]); PIN(a.qp[1]); PIN(a.qp[2]); PIN(a.qp[3]); PIN(a.zp); PIN(fp); PIN(fp2); PIN(fp3); PIN(a.idx);
        SBAR();
        STAMP(30);
        glds16x2(vx0, vx1, x + k1 * 128 + 16384, mx + 16384 + 2048); glds16x2(vw0, vw1, wA + k1 * (64 * 3072), mw + 8192);
        STAMP(22);
        SBAR();
#pragma nounroll
        for (int s = 2; s < 8; ++s) { LBAR(6); DMA1(s);
        }
        const unsigned mq = lds0 + t * 2048;
        const char* wq = ((t >> 1) ? wB : wA) - 1024;
        const char* wy = wB + 128 * ((((p >> 1) ^ QSWAP) ^ (PBX >> 1)) - ((p >> 1) ^ QSWAP));
        LBAR(6); STAMP(14); DMA2(8);
        LBAR(4); STAMP(15); DMA2(9);
        a.tag = make_tag(*(const LAS u32x2*)(lds + L_EP));
        asm volatile("" : "+s"(a.tag));
        LBAR(4); STAMP(16); DMA2(10);
        const unsigned k7 = ((7 + rot) & 7) * (64 * 3072), sx = mq + L_Q15, sw = mw + 32768;
        LBAR(4); STAMP(17); glds16(vq0, wq + k7, sx); DMA2(11);
        f32x16 oo = lds_zero16(a.zp), o1 = lds_zero16(a.zp); PIN(oo); PIN(o1);
        LBAR(5); STAMP(18); glds16(vw0, wy + k7, sw); DMA2(12);
        f32x16 o2 = lds_zero16(a.zp), o3 = lds_zero16(a.zp); PIN(o2); PIN(o3);
        LBAR(5); STAMP(19); glds16(vq1, wq + k7 + 1024, sx + 1024); DMA2(13);
        f32x16 S1 = lds_zero16(a.zp); PIN(S1);
        LBAR(5); STAMP(21); glds16(vw1, wy + k7 + 1024, sw + 1024); DMA2(14);
        __amdgpu_buffer_rsrc_t rwo = RWO; unsigned wi0 = WI0;
        MK_SO();
        asm volatile("" : "+s"(a.rkv), "+s"(rwo), "+s"(a.sBeg), "+s"(wi0), "+s"(so0[0]), "+s"(so0[1]), "+s"(so0[2]), "+s"(so0[3]));
        __builtin_amdgcn_s_setprio(1);
        LBAR(5); STAMP(24);
        const u64 fv = __hip_atomic_load(fp, RLX_AGENT);
        LBAR(1); STAMP(29);
        wait_tags_end(fv, fp, a.tag, nullptr, lane);
        STAMP(5);
        KV3 kw;
        LDA_FIRST(kw);
        attn_full<1>(a, oo, o1, o2, o3, S1, so0[3], rwo, vwo, wi0, fp2, fp3, kw PROF_PASS);
        STAMP(23);
        attn_window<1>(a, kw, so0 PROF_PASS);
        STAMP(8);
        const int go = 4 * jo + t;
        lds_cptr wo = lds + L_WO + (4 * hh + ((lane & 15) >> 2)) * 64 + ((lane >> 4) & 1) * 32 + (lane & 3) * 8;
        const GAS u64* of_ = (const GAS u64*)(ws + WS_OFLAG);
#define OBR(n) ((n) >> 4 == 1 ? 0 : 1)
#define OHD(n) (2 * ((n) & 15) + ((n) >= 16))
#define OLD(n) BLOAD(a.ro, a.vk, (unsigned)(OBR(n) * 64 + go) * OS + OHD(n) * 1024)
#define OWF(n) tr_frag(wo + OBR(n) * 32768 + OHD(n) * 1024, wo + OBR(n) * 32768 + OHD(n) * 1024 + 512)
#define OIN 5
        bf16x8 oe[48];
#pragma unroll
        for (int n = 0; n < 48; ++n) {
            if (n == 16) { wait_tags(of_ + go * 32 + 16 + (lane & 15), a.tag, OFLAG(1, 1), lane); STAMP(9); }
            if (n == 32) wait_tags(of_ + (64 + go) * 32 + 16 + (lane & 15), a.tag);
            oe[n] = OLD(n); SBAR();
            if (n >= OIN - 1) __builtin_amdgcn_s_waitcnt(0x0F70 | (OIN - 1));
            SBAR();
        }
        bf16x8 wn[4];
        __builtin_amdgcn_s_waitcnt(0x0F70);
#define NEARLY 16
#define NMID 32
        unsigned sg[4]; lds_cptr wg[4];
#pragma unroll
        for (int q = 0; q < 4; ++q) { const int gq = ((p >> 2) + 1 + q) & 3; sg[q] = (unsigned)go * OS + gq * 8192; wg[q] = wo + gq * 8192; PIN(wg[q]); }
        unsigned vout = (32 * go + (lane >> 2)) * 1024 + 64 * po + (lane & 3) * 16;
        lds_ptr ow = lds + L_PART + (4 + t) * PREC + r32 * 80 + hh * 8;
        lds_cptr orr = lds + L_PART + (4 + t) * PREC + (lane >> 2) * 80 + (lane & 3) * 16;
        PIN(ow); PIN(orr);
        __amdgpu_buffer_rsrc_t rout = __builtin_amdgcn_make_buffer_rsrc(out, 0, 2048 * 1024, 0x00020000);
        PIN(vout); PIN(wo); asm volatile("" : "+s"(sg[0]), "+s"(sg[1]), "+s"(sg[2]), "+s"(sg[3]), "+s"(rout));
        f32x16 ya = f32x16{};
        bf16x8 wf[16], of[16];
#define FLD(i) of[i] = BLOAD(a.ro, a.vk, sg[(i) >> 2] + ((i) & 3) * 2048)
#define GRP_BAD(q) ({ unsigned mx_ = 0; _Pragma("unroll") for (int i = 4 * (q); i < 4 * (q) + 4; ++i) { const u32x4 u_ = __builtin_bit_cast(u32x4, of[i]); \
            mx_ = __builtin_elementwise_max(__builtin_elementwise_max(mx_, u_[0]), u_[1]); mx_ = __builtin_elementwise_max(__builtin_elementwise_max(mx_, u_[2]), u_[3]); } \
            __any(mx_ == ~0u); })
#define GRP_LD(q) do { _Pragma("unroll") for (int i = 4 * (q); i < 4 * (q) + 4; ++i) FLD(i); } while (0)
#define WF0 28
        f32x16 yb, yc, yd;
        unsigned pend = 15;
        STAMP(10);
        BAR();
#pragma unroll
        for (int n = 0; n < 4; ++n) { wn[n] = OWF(n); PIN(wn[n]); }
        BAR();
#pragma unroll
        for (int n = 0; n < 48; ++n) {
            ya = MFMA(wn[n & 3], oe[n], ya); SBAR();
            if (n >= NEARLY && n < NEARLY + 12) FLD((n - NEARLY) & 15);
            if (n >= NMID && n < NMID + 4) FLD((n - NMID + 12) & 15);
            if (n + 4 < 48) wn[n & 3] = OWF(n + 4);
            if (n >= WF0 && n < WF0 + 16) wf[(n - WF0) & 15] = tr_frag(wg[((n - WF0) & 15) >> 2] + ((n - WF0) & 3) * 2048, wg[((n - WF0) & 15) >> 2] + ((n - WF0) & 3) * 2048 + 512);
            if (n == 44) { yb = lds_zero16(a.zp); } if (n == 45) { yc = lds_zero16(a.zp); } if (n == 46) { yd = lds_zero16(a.zp); }
            SBAR();
        }
        STAMP(31);
#define GRP_TRY(q, Y) do { if (pend & (1u << (q))) { if (!GRP_BAD(q)) { \
            _Pragma("unroll") for (int i = 4 * (q); i < 4 * (q) + 4; ++i) Y = MFMA(wf[i], of[i], Y); pend ^= 1u << (q); } } } while (0)
        unsigned spins = SPIN_CAP;
#pragma nounroll
        do {
            GRP_TRY(0, ya); GRP_TRY(1, yb); GRP_TRY(2, yc); GRP_TRY(3, yd);
            asm volatile("s_sub_u32 %1, %1, 1\n\ts_cselect_b32 %0, 0, %0" : "+s"(pend), "+s"(spins) :: "memory", "scc");
            if (pend & 1) GRP_LD(0); if (pend & 2) GRP_LD(1); if (pend & 4) GRP_LD(2); if (pend & 8) GRP_LD(3);
        } while (pend);
        ya = (ya + yb) + (yc + yd);
#define OUT_AUX 16
#pragma unroll
        for (int q = 0; q < 4; ++q) *(LAS u32x2*)(ow + q * 16) = (u32x2){cvtpk(ya[4 * q], ya[4 * q + 1]), cvtpk(ya[4 * q + 2], ya[4 * q + 3])};
        asm volatile("" ::: "memory");
#pragma unroll
        for (int s = 0; s < 2; ++s) __builtin_amdgcn_raw_buffer_store_b128(*(const LAS u32x4*)(orr + s * 1280), rout, vout + s * 16384, 0, OUT_AUX);
        if (bid == 0 && tid == 256) __hip_atomic_store((GAS u64*)(ws + WS_EPOCH), a.tag, RLX_ST);
        STAMP(12);
    } else {
        glds16((((lane >> 3) + rot) & 7) * 128 + (lane & 7) * 16, gw, lds0 + L_GW + t * 1024);
#pragma unroll
        for (int m = 0; m < 2; ++m) {
            f32x4 mk;
#pragma unroll
            for (int i = 0; i < 4; ++i) { const bool le = (i + 8 * t + 4 * hh) <= r32; mk[i] = (le != (m != 0)) ? 0.f : -__builtin_inff(); }
            *(LAS f32x4*)(lds + L_MDIAG + m * 4096 + t * 1024 + lane * 16) = mk;
        }
        if (tid < 4) *(LAS unsigned*)(lds + L_CNT + tid * 4) = 0;
        *(LAS unsigned*)(lds + L_Z + tid * 4) = 0;
        u64 tag;
        STAMP(21);
        {
            const __amdgpu_buffer_rsrc_t rkv = __builtin_amdgcn_make_buffer_rsrc(ws + WS_KV, 0, KV_BYTES, 0x00020000);
            const __amdgpu_buffer_rsrc_t ro = __builtin_amdgcn_make_buffer_rsrc(ws + WS_O, 0, O_BYTES, 0x00020000);
            if (e) project<1>(lds, ws, rkv, ro, lane, t, p, j, tag PROF_PASS); else project<0>(lds, ws, rkv, ro, lane, t, p, j, tag PROF_PASS);
        }
        cnt_wait(lds + L_CNT + 8);
        __builtin_amdgcn_s_sleep(BSLP);
        STAMP(24);
        MK_ATT(0);
        a.tag = tag;
        wait_tags((const GAS u64*)(ws + window_flag(a)), tag);
        STAMP(25);
        KV3 kw;
        LDB_FIRST(kw);
        MK_SO();
        attn_window<0>(a, kw, so0 PROF_PASS);
        STAMP(23);
        PIN(a.idx);
        attn_full<0>(a, lds_zero16(a.zp), lds_zero16(a.zp), lds_zero16(a.zp), lds_zero16(a.zp), lds_zero16(a.zp), so0[3], RWO, VWO, WI0, nullptr, nullptr, kw PROF_PASS);
        BAR();
        STAMP(8);
    }
}

extern "C" void kernel_launch(void* const* d_in, const int* in_sizes, int n_in, void* d_out, int out_size, void* d_ws, size_t ws_size,
                              hipStream_t stream) {
    (void)in_sizes; (void)n_in; (void)out_size; (void)ws_size;
    (void)hipFuncSetAttribute((const void*)gmix_kernel, hipFuncAttributeMaxDynamicSharedMemorySize, L_END);
    gmix_kernel<<<256, 512, L_END, stream>>>((const char*)d_in[0], (const char*)d_in[1], (const char*)d_in[2], (const char*)d_in[4],
                                             (const char*)d_in[5], (const char*)d_in[6], (char*)d_out, (char*)d_ws);
}
```

```cpp
#include <hip/hip_runtime.h>
#include <hip/hip_bf16.h>

typedef __attribute__((ext_vector_type(8))) __bf16 bf16x8;
typedef __attribute__((ext_vector_type(16))) __bf16 bf16x16;
typedef __attribute__((ext_vector_type(16))) float f32x16;
typedef __attribute__((ext_vector_type(8))) float f32x8;
typedef __attribute__((ext_vector_type(4))) float f32x4;
typedef __attribute__((ext_vector_type(4))) unsigned u32x4;
typedef __attribute__((ext_vector_type(2))) unsigned u32x2;
typedef __attribute__((ext_vector_type(2))) __bf16 bf16x2;
typedef short s16x4 __attribute__((ext_vector_type(4)));
typedef unsigned long long u64;
#define GAS __attribute__((address_space(1)))
#define LAS __attribute__((address_space(3)))
typedef LAS const char* lds_cptr;
typedef LAS char* lds_ptr;

constexpr unsigned WS_EPOCH = 0;
constexpr unsigned FSTR = 8;
constexpr unsigned WS_OFLAG = 65536;
constexpr unsigned WS_KVFLAG = 131072;
constexpr unsigned WS_KV = 1u << 20;
constexpr unsigned TS = 2176;
constexpr unsigned KV_BYTES = 2u * 32u * 64u * TS;
constexpr unsigned WS_O = 10u << 20;
constexpr unsigned OS = 32896;
constexpr unsigned O_BYTES = 128u * OS;
constexpr unsigned OOB = 0x7ff00000u;
constexpr unsigned L_W = 65536;
constexpr unsigned L_WO = 0, L_PART = 65536, PREC = 7168, L_Q14 = 106496, L_Q15 = 114688, L_MDIAG = 122880, L_MFAR = 126976, L_Q = 131072,
                   L_GW = 147456, L_Z = 151552, L_G = 152576, L_CNT = 153088, L_EP = 153344, L_SCR = 153600, L_END = 154112;
constexpr float QSCALE = 0.25f * 1.4426950408889634f;
constexpr float LMAX = 1.2676506e30f, LMIN = 9.094947e-13f;
constexpr unsigned SPIN_CAP = 1u << 17;

#define SBAR() __builtin_amdgcn_sched_barrier(0)
#define PIN(x) asm volatile("" : "+v"(x))
#define MFMA(a, b, c) __builtin_amdgcn_mfma_f32_32x32x16_bf16(a, b, c, 0, 0, 0)
#define BAR() asm volatile("s_barrier" ::: "memory")
#define CBAR() asm volatile("s_waitcnt lgkmcnt(0)\n\ts_barrier" ::: "memory")
#define DRAIN() asm volatile("s_waitcnt vmcnt(0)" ::: "memory")
#define PACE() asm volatile("s_nop 6" ::: "memory")
#define RLX_AGENT __ATOMIC_RELAXED, __HIP_MEMORY_SCOPE_AGENT

__device__ __forceinline__ void glds16(unsigned voff, const void* sbase, unsigned ldsaddr) {
    asm volatile("s_mov_b32 m0, %2\n\ts_nop 0\n\tglobal_load_lds_dwordx4 %0, %1" ::"v"(voff), "s"(sbase), "s"(ldsaddr) : "memory");
}
__device__ __forceinline__ void glds16x2(unsigned v0, unsigned v1, const void* sbase, unsigned ldsaddr) {
    asm volatile("s_mov_b32 m0, %3\n\ts_nop 0\n\tglobal_load_lds_dwordx4 %0, %2\n\tglobal_load_lds_dwordx4 %1, %2 offset:1024"
                 ::"v"(v0), "v"(v1), "s"(sbase), "s"(ldsaddr) : "memory");
}
__device__ __forceinline__ void glds16x4(unsigned v0, unsigned v1, unsigned v2, unsigned v3, const void* sbase, unsigned ldsaddr) {
    asm volatile("s_mov_b32 m0, %5\n\ts_nop 0\n\tglobal_load_lds_dwordx4 %0, %4\n\tglobal_load_lds_dwordx4 %1, %4 offset:1024\n\t"
                 "global_load_lds_dwordx4 %2, %4 offset:2048\n\tglobal_load_lds_dwordx4 %3, %4 offset:3072"
                 ::"v"(v0), "v"(v1), "v"(v2), "v"(v3), "s"(sbase), "s"(ldsaddr) : "memory");
}
__device__ __forceinline__ unsigned cvtpk(float lo, float hi) {
    typedef __attribute__((ext_vector_type(2))) float f32x2;
    return __builtin_bit_cast(unsigned, __builtin_convertvector((f32x2){lo, hi}, bf16x2));
}
__device__ __forceinline__ bf16x8 as_frag(u32x4 v) { return __builtin_bit_cast(bf16x8, v); }
__device__ __forceinline__ bf16x8 lds_frag(lds_cptr p) { return *(const LAS bf16x8*)p; }
__device__ __forceinline__ bf16x8 tr_frag(lds_cptr p0, lds_cptr p1) {
    s16x4 a = __builtin_amdgcn_ds_read_tr16_b64_v4i16((LAS s16x4*)p0);
    s16x4 b = __builtin_amdgcn_ds_read_tr16_b64_v4i16((LAS s16x4*)p1);
    return __builtin_bit_cast(bf16x8, __builtin_shufflevector(a, b, 0, 1, 2, 3, 4, 5, 6, 7));
}
__device__ __forceinline__ f32x16 pv_lo(bf16x8 v, const f32x16& p, f32x16 acc, int idx) {
    f32x8 h = __builtin_shufflevector(p, p, 0, 1, 2, 3, 4, 5, 6, 7);
    return __builtin_amdgcn_smfmac_f32_32x32x32_bf16(v, __builtin_bit_cast(bf16x16, h), acc, idx, 0, 0);
}
__device__ __forceinline__ f32x16 pv_hi(bf16x8 v, const f32x16& p, f32x16 acc, int idx) {
    f32x8 h = __builtin_shufflevector(p, p, 8, 9, 10, 11, 12, 13, 14, 15);
    return __builtin_amdgcn_smfmac_f32_32x32x32_bf16(v, __builtin_bit_cast(bf16x16, h), acc, idx, 0, 0);
}
#define MX3(a, b, c) __builtin_fmaxf(__builtin_fmaxf((a), (b)), (c))
__device__ __forceinline__ float max16(const f32x16& s) {
    float a = MX3(s[0], s[1], s[2]), b = MX3(s[3], s[4], s[5]);
    a = MX3(a, s[6], s[7]); b = MX3(b, s[8], s[9]); a = MX3(a, s[10], s[11]); b = MX3(b, s[12], s[13]);
    return MX3(a, b, __builtin_fmaxf(s[14], s[15]));
}
__device__ __forceinline__ float halfmax(float m) {
    auto r = __builtin_amdgcn_permlane32_swap(__float_as_uint(m), __float_as_uint(m), false, false);
    return __builtin_fmaxf(__uint_as_float(r[0]), __uint_as_float(r[1]));
}
__device__ __forceinline__ f32x16 lds_f32x16(lds_cptr p) {
    f32x4 a = *(const LAS f32x4*)p, b = *(const LAS f32x4*)(p + 1024), c = *(const LAS f32x4*)(p + 2048), d = *(const LAS f32x4*)(p + 3072);
    f32x8 lo = __builtin_shufflevector(a, b, 0, 1, 2, 3, 4, 5, 6, 7), hi = __builtin_shufflevector(c, d, 0, 1, 2, 3, 4, 5, 6, 7);
    return __builtin_shufflevector(lo, hi, 0, 1, 2, 3, 4, 5, 6, 7, 8, 9, 10, 11, 12, 13, 14, 15);
}
constexpr int SC1 = 16;
#define BLOAD(rs, vo, so) as_frag(__builtin_amdgcn_raw_buffer_load_b128(rs, vo, so, SC1))
#define BSTORE(v, rs, vo, so) __builtin_amdgcn_raw_buffer_store_b128(v, rs, (vo) + (so), 0, SC1)

__device__ __forceinline__ void wait_tags_end(u64 v, const GAS u64* p, u64 tag, GAS u64* pub, int lane) {
    if (pub) { DRAIN(); if (lane == 0) __hip_atomic_store(pub, tag, RLX_AGENT); }
    if (__builtin_expect(!__all(v == tag), 0)) {
#pragma nounroll
        for (unsigned spins = 0; spins < SPIN_CAP; ++spins) {
            __builtin_amdgcn_s_sleep(8);
            if (__all(__hip_atomic_load(p, RLX_AGENT) == tag)) break;
        }
    }
    asm volatile("" ::: "memory");
}

__device__ __forceinline__ void wait_tags(const GAS u64* p, u64 tag, GAS u64* pub = nullptr, int lane = 0) {
    wait_tags_end(__hip_atomic_load(p, RLX_AGENT), p, tag, pub, lane);
}

__device__ __forceinline__ void cnt_arrive(lds_ptr cnt, int lane) {
    asm volatile("s_waitcnt lgkmcnt(0)" ::: "memory");
    if (lane == 0) __hip_atomic_fetch_add((LAS unsigned*)cnt, 1u, __ATOMIC_RELAXED, __HIP_MEMORY_SCOPE_WORKGROUP);
}
__device__ __forceinline__ void cnt_wait(lds_ptr cnt) {
#pragma nounroll
    for (unsigned spins = 0; spins < (1u << 22); ++spins) {
        if (__builtin_amdgcn_readfirstlane(__hip_atomic_load((LAS unsigned*)cnt, __ATOMIC_RELAXED, __HIP_MEMORY_SCOPE_WORKGROUP)) == 4) break;
        __builtin_amdgcn_s_sleep(1);
    }
    asm volatile("" ::: "memory");
}
__device__ __forceinline__ unsigned kvflag(int br, int quad, int tile, int type) {
    return WS_KVFLAG + (unsigned)(((((br * 8 + quad) * 4 + (tile & 3)) * 2 + (tile >> 5)) * 2 + type) * 8 + ((tile >> 2) & 7)) * FSTR;
}

__device__ __forceinline__ void slow_rows(__amdgpu_buffer_rsrc_t rs, unsigned sbase, int lo, int hi, int g, bool band, bf16x8 q,
                                          unsigned vk, unsigned vv0, unsigned vv1, lds_cptr lds, int lane, int idx, f32x16& acc) {
    float m = -__builtin_inff();
    for (int pass = 0; pass < 2; ++pass) {
        if (pass) { m = halfmax(m); acc = f32x16{}; }
        for (int kt = lo; kt <= hi; ++kt) {
            const unsigned so = sbase + (unsigned)kt * TS;
            const bf16x8 k = BLOAD(rs, vk, so), v0 = BLOAD(rs, vv0, so), v1 = BLOAD(rs, vv1, so);
            f32x16 c = f32x16{};
            if (band && kt == g) c = lds_f32x16(lds + L_MDIAG + lane * 16);
            if (band && kt == g - 8) c = lds_f32x16(lds + L_MFAR + lane * 16);
            f32x16 s = MFMA(k, q, c);
            if (!pass) m = __builtin_fmaxf(m, max16(s));
            else {
#pragma unroll
                for (int r = 0; r < 16; ++r) s[r] = __builtin_amdgcn_exp2f(s[r] - m);
                acc = pv_lo(v0, s, acc, idx); acc = pv_hi(v1, s, acc, idx);
            }
        }
    }
}

#define PK4(A, o) ((u32x4){cvtpk(A[o], A[o + 1]), cvtpk(A[o + 2], A[o + 3]), cvtpk(A[o + 4], A[o + 5]), cvtpk(A[o + 6], A[o + 7])})
__device__ __forceinline__ u64 make_tag(u32x2 e) {
    return (((u64)(unsigned)__builtin_amdgcn_readfirstlane(e[1]) << 32) | (unsigned)__builtin_amdgcn_readfirstlane(e[0])) * 0x9E3779B97F4A7C15ull + 0x632BE59BD9B4E019ull;
}
template <int E>
__device__ __forceinline__ u32x4 pack_kv(int i, const f32x16& a) {
    const int s = i & 1;
    if (E == 0) return PK4(a, 8 * s);
    auto x = __builtin_amdgcn_permlane32_swap(cvtpk(a[8 * s], a[8 * s + 1]), cvtpk(a[8 * s + 4], a[8 * s + 5]), false, false);
    auto y = __builtin_amdgcn_permlane32_swap(cvtpk(a[8 * s + 2], a[8 * s + 3]), cvtpk(a[8 * s + 6], a[8 * s + 7]), false, false);
    return (u32x4){x[0], y[0], x[1], y[1]};
}
#define PUT_KV(i, v, vo) BSTORE(v, rkv, vo, ((i) < 2 ? 64 * TS : 0) + ((i) & 1) * (E ? 512 : 128 * TS))
constexpr int QSWAP = 1;
constexpr int PBX = 4;
template <int E>
__device__ __forceinline__ void project(lds_ptr lds, char* ws, __amdgpu_buffer_rsrc_t rkv, __amdgpu_buffer_rsrc_t ro, int lane, int t, int p, int j, u64& tag) {
    const int r32 = lane & 31, hh = lane >> 5, qd4 = (p >> 1) ^ QSWAP;
    const unsigned q4 = (lane & 15) >> 2, wcol = ((lane >> 4) & 1) * 32 + (lane & 3) * 8;
    lds_cptr xa[4];
#pragma unroll
    for (int ks = 0; ks < 4; ++ks) xa[ks] = lds + (32 * t + r32) * 128 + (((hh ^ ((r32 >> 1) & 7)) * 16) ^ (ks * 32));
    unsigned w1o = L_W + (8 * hh + q4) * 128 + ((lane >> 4) & 1) * 64 + (q4 >> 1) * 32 + (lane & 3) * 8, w0o = w1o ^ 32;
    PIN(w0o); PIN(w1o);
    const lds_cptr w0 = lds + w0o, w1 = lds + w1o, wq = lds + (8 * hh + q4) * 64 + wcol;
#define XRD(n) lds_frag(xa[(n) & 3] + (((n) >> 2) & 3) * 16384)
#define WRD(b, n) tr_frag(b + ((n) & 15) * 2048, b + ((n) & 15) * 2048 + 512)
#define YOF(n) ((n) >= 28 ? 32768 + ((n) & 3) * 2048 : ((n) & 15) * 2048)
#define YRD(b, n) tr_frag(b + YOF(n), b + YOF(n) + 512)
#define QOF(br, n) (((n) >= 28 ? L_Q15 - L_Q14 : ((n) >= 24 ? 0 : (((n) >> 2) & 3) * 16384)) + (br) * 4096 + (((n) & 3) ^ E) * 1024)
#define QRD(br, n) tr_frag(((n) >= 24 ? wqh : wq) + QOF(br, n), ((n) >= 24 ? wqh : wq) + QOF(br, n) + 256)
#define MM(W, X, A) (E ? MFMA(X, W, A) : MFMA(W, X, A))
    bf16x8 xf[32], f0[4], f1[4];
    f32x16 a0 = f32x16{}, a1 = f32x16{};
    unsigned gwo = (lane & 15) == ((lane >> 4) & 1) ? L_GW + t * 1024 + hh * 16 : L_Z;
    PIN(gwo);
    f32x4 zg = f32x4{};
    bf16x8 gf[4];
#define RD1(n) do { xf[n] = XRD(n); f0[(n) & 3] = WRD(w0, n); f1[(n) & 3] = WRD(w1, n); SBAR(); } while (0)
#define GRD(n) gf[(n) & 3] = lds_frag(lds + gwo + (n) * 32)
#define GMM(n) zg = __builtin_amdgcn_mfma_f32_16x16x32_bf16(gf[(n) & 3], xf[n], zg, 0, 0, 0)
#define MM1(n) do { a0 = MM(f0[(n) & 3], xf[n], a0); a1 = MM(f1[(n) & 3], xf[n], a1); SBAR(); } while (0)
#pragma unroll
    for (int s = 0; s < 8; ++s) {
        if (s == 0) CBAR(); else BAR();
        SBAR();
        RD1(4 * s); RD1(4 * s + 1);
        if (s) { MM1(4 * s - 1); GMM(4 * s - 4); GMM(4 * s - 3); GMM(4 * s - 2); GMM(4 * s - 1); SBAR(); }
        RD1(4 * s + 2); MM1(4 * s); RD1(4 * s + 3); MM1(4 * s + 1);
        if (s == 0) DRAIN();
        GRD(4 * s); GRD(4 * s + 1); GRD(4 * s + 2); GRD(4 * s + 3); SBAR();
        MM1(4 * s + 2);
        SBAR();
    }
    const unsigned tile = 4 * j + t;
    unsigned vo = (unsigned)(4 * qd4 * 64 + tile) * TS + (E ? (r32 >> 4) * (128 * TS) + 1024 + hh * 256 + (r32 & 15) * 16 : lane * 16);
    unsigned wqo = L_Q14 + (8 * hh + q4) * 64 + wcol;
    PIN(vo); PIN(wqo);
    const lds_cptr wqh = lds + wqo;
    MM1(31); GMM(28); GMM(29); GMM(30); GMM(31); SBAR();
    u32x4 pk[4];
#pragma unroll
    for (int i = 0; i < 4; ++i) { pk[i] = pack_kv<E>(i, i < 2 ? a0 : a1); PIN(pk[i]); if (i < 2) PUT_KV(i, pk[i], vo); SBAR(); }
    if (!E) BSTORE(((u32x4){0x3f803f80u, 0x3f803f80u, 0x3f803f80u, 0x3f803f80u}), rkv,
                   (unsigned)((hh * 32 + 4 * (qd4 ^ (hh * (PBX >> 1))) + (r32 >> 3)) * 64 + tile) * TS + 2048 + (lane & 7) * 16, 0);
    { LAS float* gp = (LAS float*)(lds + (lane < 16 ? L_G + t * 128 : L_SCR)) + lane;
      gp[0] = __builtin_amdgcn_rcpf(1.f + __expf(-zg[0])); gp[16] = __builtin_amdgcn_rcpf(1.f + __expf(-zg[1])); }
    SBAR();
    BAR(); SBAR();
    bf16x8 u0 = QRD(0, 0), u1 = QRD(1, 0), y0 = YRD(w0, 0), y1 = YRD(w1, 0);
    const u32x2 tg = *(const LAS u32x2*)(lds + L_EP);
    f32x16 b0 = f32x16{}, b1 = f32x16{}, aQ0 = f32x16{}, aQ1 = f32x16{};
    SBAR();
#pragma unroll
    for (int n = 0; n < 24; ++n) {
        aQ0 = MFMA(u0, xf[n], aQ0); SBAR();
        if (n == 1) { tag = make_tag(tg); asm volatile("" : "+s"(tag)); SBAR(); }
        if (n == 7) { DRAIN(); if (lane == 0) __hip_atomic_store((GAS u64*)(ws + kvflag(0, qd4, tile, E)), tag, RLX_AGENT); SBAR(); }
        if ((n & 3) == 3) { BAR(); SBAR(); }
        u0 = QRD(0, n + 1); SBAR();
        aQ1 = MFMA(u1, xf[n], aQ1); SBAR();
        u1 = n < 23 ? QRD(1, n + 1) : QRD(0, 25); SBAR();
        b0 = MM(y0, xf[n], b0); SBAR(); y0 = n < 23 ? YRD(w0, n + 1) : QRD(0, 26); SBAR();
        b1 = MM(y1, xf[n], b1); SBAR(); y1 = n < 23 ? YRD(w1, n + 1) : QRD(0, 27); SBAR();
    }
    aQ0 = MFMA(u0, xf[24], aQ0); SBAR(); u0 = QRD(0, 28); SBAR(); aQ0 = MFMA(u1, xf[25], aQ0); SBAR(); u1 = QRD(0, 29); SBAR();
    aQ0 = MFMA(y0, xf[26], aQ0); SBAR(); y0 = QRD(0, 30); SBAR(); aQ0 = MFMA(y1, xf[27], aQ0); SBAR(); y1 = QRD(0, 31); SBAR();
    aQ0 = MFMA(u0, xf[28], aQ0); SBAR(); aQ0 = MFMA(u1, xf[29], aQ0); SBAR(); u1 = QRD(1, 24); SBAR();
    aQ0 = MFMA(y0, xf[30], aQ0); SBAR(); y0 = YRD(w0, 24); SBAR(); aQ0 = MFMA(y1, xf[31], aQ0); SBAR(); y1 = YRD(w1, 24); SBAR();
    const lds_ptr qd = lds + L_Q + t * 1024 + lane * 16;
    constexpr int QBAR = 4;
    constexpr int NFREE = 3;
    f32x8 qs; u32x4 qk;
#define QEPI(m) do { if ((m) == 1) { PIN(aQ0); qs = __builtin_shufflevector(aQ0, aQ0, 8, 9, 10, 11, 12, 13, 14, 15) * QSCALE; PIN(qs); } \
        if ((m) == 2) { qk = PK4(qs, 0); *(LAS u32x4*)(qd + 4096) = qk; } \
        if ((m) == QBAR) { __builtin_amdgcn_s_setprio(2); __builtin_amdgcn_s_waitcnt(0xC07F | ((2 * (QBAR - 2)) << 8)); BAR(); } \
        if ((m) > QBAR + NFREE) PACE(); SBAR(); } while (0)
#pragma unroll
    for (int n = 24; n < 32; ++n) {
        aQ1 = MFMA(u1, xf[n], aQ1); SBAR(); if (n < 31) u1 = QRD(1, n + 1); SBAR(); QEPI(3 * (n - 24));
        b0 = MM(y0, xf[n], b0); SBAR(); if (n < 31) y0 = YRD(w0, n + 1); SBAR(); QEPI(3 * (n - 24) + 1);
        b1 = MM(y1, xf[n], b1); SBAR(); if (n < 31) y1 = YRD(w1, n + 1); SBAR(); QEPI(3 * (n - 24) + 2);
    }
#pragma unroll
    for (int r = 0; r < 8; ++r) aQ0[r] *= QSCALE;
#pragma unroll
    for (int r = 0; r < 16; ++r) aQ1[r] *= QSCALE;
    *(LAS u32x4*)qd = PK4(aQ0, 0); *(LAS u32x4*)(qd + 8192) = PK4(aQ1, 0); *(LAS u32x4*)(qd + 12288) = PK4(aQ1, 8);
    cnt_arrive(lds + L_CNT + 8, lane);
    const unsigned vo2 = vo + (32 + 4 * ((qd4 ^ (PBX >> 1)) - qd4)) * 64 * TS;
    u32x4 py[4] = {pack_kv<E>(0, b0), pack_kv<E>(1, b0), pack_kv<E>(2, b1), pack_kv<E>(3, b1)};
    PIN(py[0]); PIN(py[1]); PIN(py[2]); PIN(py[3]);
    constexpr int YSLP = 8, YSTAG = 2;
    if (t & 1) __builtin_amdgcn_s_sleep(YSTAG);
    if (t & 2) __builtin_amdgcn_s_sleep(2 * YSTAG);
    PUT_KV(2, pk[2], vo); __builtin_amdgcn_s_sleep(YSLP); PUT_KV(3, pk[3], vo); __builtin_amdgcn_s_sleep(YSLP);
    PUT_KV(0, py[0], vo2); __builtin_amdgcn_s_sleep(YSLP); PUT_KV(1, py[1], vo2); __builtin_amdgcn_s_sleep(YSLP);
    PUT_KV(2, py[2], vo2); __builtin_amdgcn_s_sleep(YSLP); PUT_KV(3, py[3], vo2);
    DRAIN();
    if (lane == 0) __hip_atomic_store((GAS u64*)(ws + kvflag(1, qd4, tile, E)), tag, RLX_AGENT);
    BSTORE(((u32x4){~0u, ~0u, ~0u, ~0u}), ro, lane * 16, tile * OS + 2 * p * 1024);
}

constexpr int CODE_LINES = 280;

struct Att {
    lds_ptr lds; char* ws; __amdgpu_buffer_rsrc_t rkv, ro;
    int lane, t, p, g, head, idx; u64 tag; unsigned vk, vv0, vv1, sBeg; lds_cptr zp, qp[4];
};
#define IDX a.idx
#define LD3(K, V0, V1, rs, so) do { K = BLOAD(rs, a.vk, so); V0 = BLOAD(rs, a.vv0, so); V1 = BLOAD(rs, a.vv1, so); } while (0)
#define EX(X, r) X[r] = __builtin_amdgcn_exp2f(X[r])
#define STEP(X, Y, OP, VP, KN, QN, CN, O, V0, F1, F3, F2) do { \
        EX(X, 0); EX(X, 1); SBAR(); OP = pv_hi(VP, Y, OP, IDX); SBAR(); F1; SBAR(); EX(X, 2); EX(X, 3); EX(X, 4); SBAR(); Y = MFMA(KN, QN, CN); SBAR(); F3; SBAR(); \
        EX(X, 5); EX(X, 6); EX(X, 7); SBAR(); EX(X, 8); EX(X, 9); SBAR(); O = pv_lo(V0, X, O, IDX); SBAR(); F2; SBAR(); \
        EX(X, 10); EX(X, 11); EX(X, 12); EX(X, 13); EX(X, 14); EX(X, 15); SBAR(); } while (0)

__device__ __forceinline__ f32x16 lds_zero16(lds_cptr zp) {
    const volatile LAS f32x4* z = (const volatile LAS f32x4*)zp;
    const f32x4 a = *z, b = *z, c = *z, d = *z;
    f32x8 lo = __builtin_shufflevector(a, b, 0, 1, 2, 3, 4, 5, 6, 7), hi = __builtin_shufflevector(c, d, 0, 1, 2, 3, 4, 5, 6, 7);
    return __builtin_shufflevector(lo, hi, 0, 1, 2, 3, 4, 5, 6, 7, 8, 9, 10, 11, 12, 13, 14, 15);
}

template <int C, int BR>
__device__ __forceinline__ void finish(const Att& a, f32x16 o, float gf, bf16x8 q, unsigned vo) {
    f32x8 y = __builtin_shufflevector(o, o, 0, 1, 2, 3, 4, 5, 6, 7) * (__builtin_amdgcn_rcpf(o[8]) * gf);
    PIN(y);
    const float chk = __builtin_fmaf(((y[0] + y[2]) + (y[4] + y[6])) + ((y[1] + y[3]) + (y[5] + y[7])), 0.f, o[8]);
    if (__builtin_expect(__any(__builtin_amdgcn_fmed3f(chk, LMIN, LMAX) != chk), 0)) {
        if (!BR) { wait_tags((const GAS u64*)(a.ws + kvflag(!C, a.p >> 1, 0, 0) + a.lane * FSTR), a.tag); wait_tags((const GAS u64*)(a.ws + kvflag(!C, a.p >> 1, 2, 0) + a.lane * FSTR), a.tag); }
        slow_rows(a.rkv, (unsigned)((BR * 32 + (a.head ^ (BR * 2 * PBX))) * 64) * TS, BR ? (a.g >= 8 ? a.g - 8 : 0) : 0, BR ? a.g : 63, a.g, BR, q, a.vk, a.vv0, a.vv1, a.lds, a.lane, IDX, o);
        y = __builtin_shufflevector(o, o, 0, 1, 2, 3, 4, 5, 6, 7) * (__builtin_amdgcn_rcpf(o[8]) * gf);
        PIN(y);
    }
    u32x4 v = (u32x4){cvtpk(y[0], y[1]), cvtpk(y[2], y[3]), cvtpk(y[4], y[5]), cvtpk(y[6], y[7])};
    if (!C && !BR) v = __builtin_elementwise_min(v, (u32x4){~1u, ~1u, ~1u, ~1u});
    BSTORE(v, a.ro, vo, 0);
}
#define OFLAG(BR, C) ((GAS u64*)(a.ws + WS_OFLAG) + (((BR) * 64 + a.g) * 2 + (C)) * 16 + (a.p ^ ((BR) * PBX)))
#define OVOFF(BR) (a.vk + (unsigned)((BR) * 64 + a.g) * OS + (a.head ^ ((BR) * 2 * PBX)) * 1024)
#define GATE() (*(const LAS float*)(a.lds + L_G + a.t * 128 + (a.lane & 31) * 4))

struct KV3 { bf16x8 k[3], v0[3], v1[3]; };
#define LDB(K, V0, V1, i) do { const int kt_ = a.g - (i); \
        const __amdgpu_buffer_rsrc_t rb_ = __builtin_amdgcn_make_buffer_rsrc(a.ws + WS_KV, 0, kt_ >= 0 ? KV_BYTES : 0u, 0x00020000); \
        LD3(K, V0, V1, rb_, (unsigned)((32 + (a.head ^ (2 * PBX))) * 64 + (kt_ >= 0 ? kt_ : 0)) * TS); } while (0)
#define LDB_FIRST(f) do { LDB(f.k[0], f.v0[0], f.v1[0], 0); SBAR(); LDB(f.k[1], f.v0[1], f.v1[1], 1); SBAR(); LDB(f.k[2], f.v0[2], f.v1[2], 2); SBAR(); } while (0)
#define LDA_FIRST(f) do { LD3(f.k[0], f.v0[0], f.v1[0], a.rkv, so0[0]); SBAR(); LD3(f.k[1], f.v0[1], f.v1[1], a.rkv, so0[1]); SBAR(); \
        LD3(f.k[2], f.v0[2], f.v1[2], a.rkv, so0[2]); SBAR(); } while (0)
__device__ __forceinline__ unsigned window_flag(const Att& a) {
    const int i = a.lane & 15, kt = a.g - (i < 9 ? i : 0);
    return kvflag(1, a.p >> 1, kt >= 0 ? kt : 0, (a.lane >> 4) & 1);
}

template <int C>
__device__ __forceinline__ void attn_full(const Att& a, f32x16 o0, f32x16 o1, f32x16 o2, f32x16 o3, f32x16 S1, unsigned so,
                                          __amdgpu_buffer_rsrc_t rwo, unsigned vwo, unsigned wi, const GAS u64* fp2, const GAS u64* fp3, KV3& nxt) {
    const lds_ptr lds = a.lds; const int t = a.t, lane = a.lane;
    const f32x16 zc = f32x16{};
    bf16x8 ka = nxt.k[0], va0 = nxt.v0[0], va1 = nxt.v1[0], kb = nxt.k[1], vb0 = nxt.v0[1], vb1 = nxt.v1[1], kc = nxt.k[2], vc0 = nxt.v0[2], vc1 = nxt.v1[2], kd, vd0, vd1;
    const unsigned sBeg = a.sBeg, jb = a.g >> 2;
    unsigned ki = 3;
    vd1 = lds_frag(a.zp);
    lds_ptr pw = lds + L_PART + (4 * C + t) * PREC + lane * 16;
    lds_cptr pr0 = lds + L_PART + (4 * C + ((t - 1) & 3)) * PREC + lane * 16, pr1 = lds + L_PART + (4 * C + ((t - 2) & 3)) * PREC + lane * 16,
             pr2 = lds + L_PART + (4 * C + ((t - 3) & 3)) * PREC + lane * 16;
    unsigned ovo = OVOFF(0); float gf = 0.f;
    if (C) { BAR(); }
    else { gf = GATE(); PIN(pw); PIN(pr0); PIN(pr1); PIN(pr2); PIN(ovo); PIN(gf);
           DRAIN(); if (lane == 0) __hip_atomic_store(OFLAG(1, 0), a.tag, RLX_AGENT); }
    bf16x8 q0 = lds_frag(a.qp[0]); SBAR();
    bf16x8 q1 = lds_frag(a.qp[1]), q2 = lds_frag(a.qp[2]), q3 = lds_frag(a.qp[3]);
    SBAR();
    f32x16 S0 = MFMA(ka, q0, zc);
    PIN(S0); SBAR();
    PIN(q1); PIN(q2); PIN(q3); PIN(vd1); PIN(o0); PIN(o1); PIN(o2); PIN(o3); PIN(S1);
    __builtin_amdgcn_s_waitcnt(0x0F76);
#pragma unroll
    for (int i = 0; i < 8; ++i) __builtin_amdgcn_s_setprio(C);
    SBAR();
    unsigned wso = 0;
    u64 fv = a.tag;
#define NEXT1() asm volatile("s_add_i32 %0, %0, 1\n\ts_xor_b32 %1, %0, %2" : "+s"(ki), "=s"(so) : "s"(jb) : "scc")
#define NEXT2() asm volatile("s_and_b32 %0, %0, 15" : "+s"(so) :: "scc")
#define NEXT3() asm volatile("s_mulk_i32 %0, %2\n\ts_add_i32 %0, %0, %1" : "+s"(so) : "s"(sBeg), "i"(4 * TS) : "scc")
#define WO_OFF() do { if (C) asm volatile("s_lshl_b32 %0, %1, 4" : "=s"(wso) : "s"(wi) : "scc"); } while (0)
#define WO_DMA() do { if (C) __builtin_amdgcn_raw_ptr_buffer_load_lds(rwo, (LAS void*)(lds + L_WO + t * 16384 + wi), 16, vwo, wso, 0, 0); } while (0)
#define WO_NEXT() do { if (C) asm volatile("s_addk_i32 %0, 0x400\n\ts_and_b32 %0, %0, 0x3fff" : "+s"(wi) :: "scc"); } while (0)
#define TILE(K, V0, V1, KN, LK, LV0, LV1, NW, G) do { \
        STEP(S0, S1, o3, LV1, K, q1, zc, o0, V0, LK = BLOAD(a.rkv, a.vk, so), WO_OFF(), LV0 = BLOAD(a.rkv, a.vv0, so)); \
        STEP(S1, S0, o0, V1, K, q2, zc, o1, V0, LV1 = BLOAD(a.rkv, a.vv1, so), , WO_DMA()); \
        STEP(S0, S1, o1, V1, K, q3, zc, o2, V0, NEXT1(), NEXT2(), NEXT3()); \
        STEP(S1, S0, o2, V1, KN, q0, zc, o3, V0, __builtin_amdgcn_s_waitcnt(0x0F70 | (NW)), G, WO_NEXT()); } while (0)
#define PUTA(O, sl) (*(LAS f32x4*)(pw + (sl) * 2048) = __builtin_shufflevector(O, O, 0, 1, 2, 3))
#define PUTB(O, sl) (*(LAS f32x4*)(pw + (sl) * 2048 + 1024) = __builtin_shufflevector(O, O, 4, 5, 6, 7))
#define PUTL(O, sl) (*(LAS float*)(pw + 6144 + (sl) * 4) = O[8])
    f32x4 x0, y0, x1, y1, x2, y2; float l0, l1, l2;
#define GETP() do { x0 = *(const LAS f32x4*)pr0; x1 = *(const LAS f32x4*)(pr1 + 2048); x2 = *(const LAS f32x4*)(pr2 + 4096); \
        y0 = *(const LAS f32x4*)(pr0 + 1024); y1 = *(const LAS f32x4*)(pr1 + 3072); y2 = *(const LAS f32x4*)(pr2 + 5120); \
        l0 = *(const LAS float*)(pr0 + 6144); l1 = *(const LAS float*)(pr1 + 6148); l2 = *(const LAS float*)(pr2 + 6152); } while (0)
    if (C) {
#pragma nounroll
        for (int it = 0; it < 4; ++it) {
            TILE(ka, va0, va1, kb, kd, vd0, vd1, 7, );
            PIN(S0); PIN(S1); PIN(o3); wait_tags_end(fv, fp2, a.tag, nullptr, 0);
            TILE(kb, vb0, vb1, kc, ka, va0, va1, 8, );
            TILE(kc, vc0, vc1, kd, kb, vb0, vb1, 9, fv = __hip_atomic_load(fp2, RLX_AGENT));
            TILE(kd, vd0, vd1, ka, kc, vc0, vc1, 10, );
        }
        o3 = pv_hi(vd1, S1, o3, IDX);
        const u64 f3 = __hip_atomic_load(fp3, RLX_AGENT);
        cnt_wait(lds + L_CNT + 8);
        PUTA(o0, 0); PUTB(o0, 0); PUTL(o0, 0); PUTA(o1, 1); PUTB(o1, 1); PUTL(o1, 1); PUTA(o2, 2); PUTB(o2, 2); PUTL(o2, 2);
        DRAIN(); cnt_arrive(lds + L_CNT, lane);
        wait_tags_end(f3, fp3, a.tag, nullptr, 0);
        LDB_FIRST(nxt);
        cnt_wait(lds + L_CNT);
        GETP(); gf = GATE();
    } else {
        SBAR();
        TILE(ka, va0, va1, kb, kd, vd0, vd1, 6, );
        TILE(kb, vb0, vb1, kc, ka, va0, va1, 6, );
#pragma nounroll
        for (int it = 0; it < 3; ++it) {
            TILE(kc, vc0, vc1, kd, kb, vb0, vb1, 6, );
            TILE(kd, vd0, vd1, ka, kc, vc0, vc1, 6, );
            TILE(ka, va0, va1, kb, kd, vd0, vd1, 6, );
            TILE(kb, vb0, vb1, kc, ka, va0, va1, 6, );
        }
        STEP(S0, S1, o3, vb1, kc, q1, zc, o0, vc0, , , );
        STEP(S1, S0, o0, vc1, kc, q2, zc, o1, vc0, , , );
        STEP(S0, S1, o1, vc1, kd, q0, zc, o2, vc0, __builtin_amdgcn_s_waitcnt(0x0F73), , );
        STEP(S1, S0, o2, vc1, kd, q1, zc, o0, vd0, , , );
        STEP(S0, S1, o0, vd1, kd, q2, zc, o1, vd0, , , );
        STEP(S1, S0, o1, vd1, kc, q3, zc, o2, vd0, PUTA(o0, 0), PUTL(o0, 0), PUTB(o0, 0));
        STEP(S0, S1, o2, vd1, kd, q3, zc, o3, vc0, PUTA(o1, 1), PUTL(o1, 1), PUTB(o1, 1));
        PUTA(o2, 2); PUTB(o2, 2); asm volatile("" :: "v"(o0), "v"(o1), "v"(o2)); SBAR();
        EX(S1, 0); EX(S1, 1); SBAR(); o3 = pv_hi(vc1, S0, o3, IDX); SBAR(); PUTL(o2, 2); __builtin_amdgcn_s_setprio(2); SBAR();
        EX(S1, 2); EX(S1, 3); EX(S1, 4); EX(S1, 5); EX(S1, 6); EX(S1, 7); SBAR(); EX(S1, 8); EX(S1, 9); SBAR();
        o3 = pv_lo(vd0, S1, o3, IDX); PIN(S1); SBAR();
        CBAR(); GETP(); SBAR();
        EX(S1, 10); EX(S1, 11); EX(S1, 12); EX(S1, 13); EX(S1, 14); EX(S1, 15); SBAR();
        o3 = pv_hi(vd1, S1, o3, IDX); SBAR();
    }
    x0 = (x0 + x1) + x2; y0 = (y0 + y1) + y2; l0 = (l0 + l1) + l2;
    PIN(x0); PIN(y0); PIN(l0); PIN(o3);
#pragma unroll
    for (int r = 0; r < 4; ++r) { o3[r] += x0[r]; o3[4 + r] += y0[r]; }
    o3[8] += l0;
    finish<C, 0>(a, o3, gf, q3, ovo);
}

template <int C>
__device__ __forceinline__ void attn_window(const Att& a, KV3& f, const unsigned (&so0)[4]) {
    const lds_ptr lds = a.lds; const int t = a.t, lane = a.lane;
    const f32x16 zc = f32x16{};
    bf16x8 k[9], v0[9], v1[9];
#pragma unroll
    for (int i = 0; i < 3; ++i) { k[i] = f.k[i]; v0[i] = f.v0[i]; v1[i] = f.v1[i]; }
#define LDW(i) LDB(k[i], v0[i], v1[i], i)
    const bf16x8 qa = lds_frag(lds + L_Q + 8192 + C * 4096 + t * 1024 + lane * 16);
    const f32x16 cd = lds_f32x16(lds + L_MDIAG + lane * 16), cf = lds_f32x16(lds + L_MFAR + lane * 16);
    f32x16 oo = lds_zero16(a.zp);
    const float gt = GATE();
    f32x16 S0 = MFMA(k[0], qa, cd), S1 = MFMA(k[1], qa, zc);
    SBAR(); LDW(3); SBAR();
#pragma unroll
    for (int r = 0; r < 10; ++r) { EX(S0, r); SBAR(); }
    oo = pv_lo(v0[0], S0, oo, IDX); SBAR();
#pragma unroll
    for (int r = 10; r < 16; ++r) EX(S0, r);
    SBAR();
    STEP(S1, S0, oo, v1[0], k[2], qa, zc, oo, v0[1], LDW(4), , );
    STEP(S0, S1, oo, v1[1], k[3], qa, zc, oo, v0[2], LDW(5), , );
    STEP(S1, S0, oo, v1[2], k[4], qa, zc, oo, v0[3], LDW(6), , );
    STEP(S0, S1, oo, v1[3], k[5], qa, zc, oo, v0[4], LDW(7), , );
    STEP(S1, S0, oo, v1[4], k[6], qa, zc, oo, v0[5], LDW(8), , );
    const GAS u64* fa = (const GAS u64*)(a.ws + kvflag(1, a.p >> 1, t, 0) + (lane & 31) * FSTR);
    u64 fv = 0;
    STEP(S0, S1, oo, v1[5], k[7], qa, zc, oo, v0[6], if (!C) fv = __hip_atomic_load(fa, RLX_AGENT), , );
    STEP(S1, S0, oo, v1[6], k[8], qa, cf, oo, v0[7], , , );
    if (C) { PIN(S0); PIN(S1); PIN(oo); DRAIN(); if (lane == 0) __hip_atomic_store(OFLAG(0, C), a.tag, RLX_AGENT); }
    STEP(S0, S1, oo, v1[7], k[8], qa, cf, oo, v0[8], , , );
    oo = pv_hi(v1[8], S0, oo, IDX);
    if (!C) { PIN(oo); wait_tags_end(fv, fa, a.tag, nullptr, 0); LDA_FIRST(f); }
    finish<C, 1>(a, oo, 1.f - gt, qa, OVOFF(1));
}

__global__ void __launch_bounds__(512, 2)
gmix_kernel(const char* x, const char* wA, const char* woA, const char* wB, const char* woB, const char* gw, char* out, char* ws) {
    extern __shared__ __attribute__((aligned(16))) char smem[];
    const lds_ptr lds = (lds_ptr)smem;
    const unsigned lds0 = (unsigned)(uintptr_t)lds;
    const int tid = threadIdx.x, lane = tid & 63, r32 = lane & 31, hh = lane >> 5;
    const int w = __builtin_amdgcn_readfirstlane(tid >> 6);
    const int bid = blockIdx.x, xcd = bid & 7, rr = bid >> 3;
    const int p = 4 * (xcd & 3) + (rr & 3), j = 8 * (xcd >> 2) + (rr >> 2);
    const int jo = 2 * xcd + (rr >> 4), po = rr & 15;
    const int t = w & 3, c = w >> 2;
    const int e = p & 1;
    {
        u64 pc; const unsigned ln = (((bid & ~7) | (tid >> 6)) + (bid < CODE_LINES - 256 ? (tid & 32) * 8 : 0)) * 128;
        asm volatile("s_getpc_b64 %0\n\ts_mov_b32 m0, %2\n\ts_nop 0\n\tglobal_load_lds_dword %1, %0" : "=&s"(pc) : "v"(ln), "s"(lds0 + L_SCR) : "memory");
        SBAR();
    }
    unsigned rot = (j + 4 * p) & 7;
    unsigned vx0 = (128 * j + 32 * t + (lane >> 3)) * 1024 + (((lane & 7) ^ (lane >> 4)) * 16), vx1 = (vx0 ^ 64) + 7168, vx2 = vx0 + 14336, vx3 = vx1 + 14336;
    unsigned vw0 = (16 * t + (lane >> 3)) * 3072 + 1024 + 1024 * e + 128 * ((p >> 1) ^ QSWAP) + (((lane & 7) ^ (((lane >> 4) & 1) << 1)) * 16), vw1 = vw0 + 8 * 3072 - 1024;
    unsigned mx = lds0 + t * 4096, mw = lds0 + L_W + t * 2048, ow = rot * (64 * 3072);
    asm volatile("" : "+v"(vx0), "+v"(vx1), "+v"(vx2), "+v"(vx3), "+v"(vw0), "+v"(vw1));
    asm volatile("" : "+s"(mx), "+s"(mw), "+s"(rot), "+s"(ow));
    SBAR();
    asm volatile("" : "+s"(x), "+s"(wA), "+s"(woA), "+s"(wB), "+s"(woB), "+s"(gw), "+s"(out), "+s"(ws));
#define MK_ATT(C) Att a; a.lds = lds; a.ws = ws; a.lane = lane; a.t = t; a.p = p; a.g = 4 * j + t; a.head = 2 * p + C; a.idx = 0xdddddddd; \
    a.rkv = __builtin_amdgcn_make_buffer_rsrc(ws + WS_KV, 0, KV_BYTES, 0x00020000); a.ro = __builtin_amdgcn_make_buffer_rsrc(ws + WS_O, 0, O_BYTES, 0x00020000); \
    a.vk = lane * 16; a.vv0 = r32 < 16 ? 1024 + hh * 256 + r32 * 16 : ((r32 & ~4) == 16 ? 2048 : OOB); a.vv1 = r32 < 16 ? a.vv0 + 512 : a.vv0; a.zp = lds + L_Z + lane * 16; \
    a.sBeg = (unsigned)(a.head * 64 + t) * TS; \
    _Pragma("unroll") for (int i = 0; i < 4; ++i) a.qp[i] = lds + L_Q + C * 4096 + ((t + 1 + i) & 3) * 1024 + lane * 16
#define MK_SO() unsigned so0[4]; _Pragma("unroll") for (int i = 0; i < 4; ++i) so0[i] = a.sBeg + (j ^ i) * (4 * TS)
    if (c) {
#define DMA1(s) do { const unsigned ks_ = ((s) + rot) & 7; \
        glds16x4(vx0, vx1, vx2, vx3, x + ks_ * 128, mx + ((s) & 3) * 16384); glds16x2(vw0, vw1, wA + ks_ * (64 * 3072), mw + ((s) & 3) * 8192); } while (0)
#define DMA2(s) do { const unsigned ks_ = (((s) + rot) & 7) * (64 * 3072); \
        glds16x2(vq0, vq1, wq + ks_, mq + ((s) == 14 ? L_Q14 : ((s) & 3) * 16384)); glds16x2(vw0, vw1, wy + ks_, mw + ((s) & 3) * 8192); SBAR(); } while (0)
#define LBAR(N) asm volatile("s_waitcnt vmcnt(" #N ")\n\ts_barrier" ::: "memory")
        glds16x4(vx0, vx1, vx2, vx3, x + rot * 128, mx); glds16x2(vw0, vw1, wA + ow, mw);
        SBAR();
        const unsigned k1 = (1 + rot) & 7;
        glds16x2(vx0, vx1, x + k1 * 128, mx + 16384);
        SBAR();
        if (t == 0)
            asm volatile("s_mov_b32 m0, %2\n\ts_nop 0\n\tglobal_load_lds_dword %0, %1 sc1" :: "v"((lane & 1) * 4), "s"(ws + WS_EPOCH), "s"(lds0 + L_EP) : "memory");
        unsigned vq0 = (32 * (t & 1) + 16 * e + (lane >> 2)) * 3072 + 64 * (p ^ ((t >> 1) * PBX)) + (lane & 3) * 16 + 1024, vq1 = vq0 + (e ? -16 * 3072 : 16 * 3072) - 1024;
        unsigned vwo = ((t & 1) * 256 + (lane >> 2)) * 1024 + 64 * po + (lane & 3) * 16;
        MK_ATT(1);
        const GAS u64* fp = (const GAS u64*)(ws + kvflag(0, p >> 1, (j >> 3) * 32 + t, 0) + (lane & 15) * FSTR);
        const GAS u64* fp2 = (const GAS u64*)(ws + kvflag(0, p >> 1, ((j >> 3) ^ 1) * 32 + t, 0) + (lane & 15) * FSTR);
        const GAS u64* fp3 = (const GAS u64*)(ws + (hh ? WS_OFLAG + ((64 + 4 * jo + t) * 32 + (lane & 15)) * 8 : window_flag(a)));
        PIN(vq0); PIN(vq1); PIN(vwo); PIN(a.vk); PIN(a.vv0); PIN(a.vv1); PIN(a.qp[0]); PIN(a.qp[1]); PIN(a.qp[2]); PIN(a.qp[3]); PIN(a.zp); PIN(fp); PIN(fp2); PIN(fp3); PIN(a.idx);
        SBAR();
        glds16x2(vx0, vx1, x + k1 * 128 + 16384, mx + 16384 + 2048); glds16x2(vw0, vw1, wA + k1 * (64 * 3072), mw + 8192);
        SBAR();
#pragma nounroll
        for (int s = 2; s < 8; ++s) { LBAR(6); DMA1(s); }
        const unsigned mq = lds0 + t * 2048;
        const char* wq = ((t >> 1) ? wB : wA) - 1024;
        const char* wy = wB + 128 * ((((p >> 1) ^ QSWAP) ^ (PBX >> 1)) - ((p >> 1) ^ QSWAP));
        LBAR(6); DMA2(8);
        LBAR(4); DMA2(9);
        a.tag = make_tag(*(const LAS u32x2*)(lds + L_EP));
        asm volatile("" : "+s"(a.tag));
        LBAR(4); DMA2(10);
        const unsigned k7 = ((7 + rot) & 7) * (64 * 3072), sx = mq + L_Q15, sw = mw + 32768;
        LBAR(4); glds16(vq0, wq + k7, sx); DMA2(11);
        f32x16 oo = lds_zero16(a.zp), o1 = lds_zero16(a.zp); PIN(oo); PIN(o1);
        LBAR(5); glds16(vw0, wy + k7, sw); DMA2(12);
        f32x16 o2 = lds_zero16(a.zp), o3 = lds_zero16(a.zp); PIN(o2); PIN(o3);
        LBAR(5); glds16(vq1, wq + k7 + 1024, sx + 1024); DMA2(13);
        f32x16 S1 = lds_zero16(a.zp); PIN(S1);
        LBAR(5); glds16(vw1, wy + k7 + 1024, sw + 1024); DMA2(14);
        __amdgpu_buffer_rsrc_t rwo = __builtin_amdgcn_make_buffer_rsrc((void*)((t >> 1) ? woB : woA), 0, 512 * 1024, 0x00020000);
        unsigned wi0 = 4096 * (((rr >> 4) << 1) | (po & 1)) + 1024 * (xcd & 3);
        MK_SO();
        asm volatile("" : "+s"(a.rkv), "+s"(rwo), "+s"(a.sBeg), "+s"(wi0), "+s"(so0[0]), "+s"(so0[1]), "+s"(so0[2]), "+s"(so0[3]));
        __builtin_amdgcn_s_setprio(1);
        LBAR(5);
        const u64 fv = __hip_atomic_load(fp, RLX_AGENT);
        LBAR(1);
        wait_tags_end(fv, fp, a.tag, nullptr, lane);
        KV3 kw;
        LDA_FIRST(kw);
        attn_full<1>(a, oo, o1, o2, o3, S1, so0[3], rwo, vwo, wi0, fp2, fp3, kw);
        attn_window<1>(a, kw, so0);
        const int go = 4 * jo + t;
        lds_cptr wo = lds + L_WO + (4 * hh + ((lane & 15) >> 2)) * 64 + ((lane >> 4) & 1) * 32 + (lane & 3) * 8;
        const GAS u64* of_ = (const GAS u64*)(ws + WS_OFLAG);
#define OBR(n) ((n) >> 4 == 1 ? 0 : 1)
#define OHD(n) (2 * ((n) & 15) + ((n) >= 16))
#define OLD(n) BLOAD(a.ro, a.vk, (unsigned)(OBR(n) * 64 + go) * OS + OHD(n) * 1024)
#define OWF(n) tr_frag(wo + OBR(n) * 32768 + OHD(n) * 1024, wo + OBR(n) * 32768 + OHD(n) * 1024 + 512)
        constexpr int OIN = 5;
        bf16x8 oe[48];
#pragma unroll
        for (int n = 0; n < 48; ++n) {
            if (n == 16) { wait_tags(of_ + go * 32 + 16 + (lane & 15), a.tag, OFLAG(1, 1), lane); }
            if (n == 32) wait_tags(of_ + (64 + go) * 32 + 16 + (lane & 15), a.tag);
            oe[n] = OLD(n); SBAR();
            if (n >= OIN - 1) __builtin_amdgcn_s_waitcnt(0x0F70 | (OIN - 1));
            SBAR();
        }
        bf16x8 wn[4];
        __builtin_amdgcn_s_waitcnt(0x0F70);
        constexpr int NEARLY = 10;
        constexpr int NMID = 34;
        unsigned sg[4]; lds_cptr wg[4];
#pragma unroll
        for (int q = 0; q < 4; ++q) { const int gq = ((p >> 2) + 1 + q) & 3; sg[q] = (unsigned)go * OS + gq * 8192; wg[q] = wo + gq * 8192; PIN(wg[q]); }
        unsigned vout = (32 * go + (lane >> 2)) * 1024 + 64 * po + (lane & 3) * 16;
        lds_ptr ow = lds + L_PART + (4 + t) * PREC + r32 * 80 + hh * 8;
        lds_cptr orr = lds + L_PART + (4 + t) * PREC + (lane >> 2) * 80 + (lane & 3) * 16;
        PIN(ow); PIN(orr);
        __amdgpu_buffer_rsrc_t rout = __builtin_amdgcn_make_buffer_rsrc(out, 0, 2048 * 1024, 0x00020000);
        PIN(vout); PIN(wo); asm volatile("" : "+s"(sg[0]), "+s"(sg[1]), "+s"(sg[2]), "+s"(sg[3]), "+s"(rout));
        f32x16 ya = f32x16{};
        bf16x8 wf[16], of[16];
#define FLD(i) of[i] = BLOAD(a.ro, a.vk, sg[(i) >> 2] + ((i) & 3) * 2048)
#define GRP_BAD(q) ({ unsigned mx_ = 0; _Pragma("unroll") for (int i = 4 * (q); i < 4 * (q) + 4; ++i) { const u32x4 u_ = __builtin_bit_cast(u32x4, of[i]); \
            mx_ = __builtin_elementwise_max(__builtin_elementwise_max(mx_, u_[0]), u_[1]); mx_ = __builtin_elementwise_max(__builtin_elementwise_max(mx_, u_[2]), u_[3]); } \
            __any(mx_ == ~0u); })
#define GRP_LD(q) do { _Pragma("unroll") for (int i = 4 * (q); i < 4 * (q) + 4; ++i) FLD(i); } while (0)
        constexpr int ESTEP = 2, MSTEP = 2, WF0 = 28;
        f32x16 yb, yc, yd;
        unsigned pend = 15;
        BAR();
#pragma unroll
        for (int n = 0; n < 4; ++n) { wn[n] = OWF(n); PIN(wn[n]); }
        BAR();
#pragma unroll
        for (int n = 0; n < 48; ++n) {
            ya = MFMA(wn[n & 3], oe[n], ya); SBAR();
            if (n >= NEARLY && n < NEARLY + 12 * ESTEP && (n - NEARLY) % ESTEP == 0) FLD(((n - NEARLY) / ESTEP) & 15);
            if (n >= NMID && n < NMID + 4 * MSTEP && (n - NMID) % MSTEP == 0) FLD(((n - NMID) / MSTEP + 12) & 15);
            if (n + 4 < 48) wn[n & 3] = OWF(n + 4);
            if (n >= WF0 && n < WF0 + 16) wf[(n - WF0) & 15] = tr_frag(wg[((n - WF0) & 15) >> 2] + ((n - WF0) & 3) * 2048, wg[((n - WF0) & 15) >> 2] + ((n - WF0) & 3) * 2048 + 512);
            if (n == 44) { yb = lds_zero16(a.zp); } if (n == 45) { yc = lds_zero16(a.zp); } if (n == 46) { yd = lds_zero16(a.zp); }
            SBAR();
        }
#define GRP_TRY(q, Y) do { if (pend & (1u << (q))) { if (!GRP_BAD(q)) { \
            _Pragma("unroll") for (int i = 4 * (q); i < 4 * (q) + 4; ++i) Y = MFMA(wf[i], of[i], Y); pend ^= 1u << (q); } } } while (0)
        unsigned spins = SPIN_CAP;
#pragma nounroll
        do {
            GRP_TRY(0, ya); GRP_TRY(1, yb); GRP_TRY(2, yc); GRP_TRY(3, yd);
            asm volatile("s_sub_u32 %1, %1, 1\n\ts_cselect_b32 %0, 0, %0" : "+s"(pend), "+s"(spins) :: "memory", "scc");
            if (pend & 1) GRP_LD(0); if (pend & 2) GRP_LD(1); if (pend & 4) GRP_LD(2); if (pend & 8) GRP_LD(3);
        } while (pend);
        ya = (ya + yb) + (yc + yd);
#pragma unroll
        for (int q = 0; q < 4; ++q) *(LAS u32x2*)(ow + q * 16) = (u32x2){cvtpk(ya[4 * q], ya[4 * q + 1]), cvtpk(ya[4 * q + 2], ya[4 * q + 3])};
        asm volatile("" ::: "memory");
#pragma unroll
        for (int s = 0; s < 2; ++s) __builtin_amdgcn_raw_buffer_store_b128(*(const LAS u32x4*)(orr + s * 1280), rout, vout + s * 16384, 0, SC1);
        if (bid == 0 && tid == 256) __hip_atomic_store((GAS u64*)(ws + WS_EPOCH), a.tag, RLX_AGENT);
    } else {
        glds16((((lane >> 3) + rot) & 7) * 128 + (lane & 7) * 16, gw, lds0 + L_GW + t * 1024);
#pragma unroll
        for (int m = 0; m < 2; ++m) {
            f32x4 mk;
#pragma unroll
            for (int i = 0; i < 4; ++i) { const bool le = (i + 8 * t + 4 * hh) <= r32; mk[i] = (le != (m != 0)) ? 0.f : -__builtin_inff(); }
            *(LAS f32x4*)(lds + L_MDIAG + m * 4096 + t * 1024 + lane * 16) = mk;
        }
        if (tid < 4) *(LAS unsigned*)(lds + L_CNT + tid * 4) = 0;
        *(LAS unsigned*)(lds + L_Z + tid * 4) = 0;
        u64 tag;
        {
            const __amdgpu_buffer_rsrc_t rkv = __builtin_amdgcn_make_buffer_rsrc(ws + WS_KV, 0, KV_BYTES, 0x00020000);
            const __amdgpu_buffer_rsrc_t ro = __builtin_amdgcn_make_buffer_rsrc(ws + WS_O, 0, O_BYTES, 0x00020000);
            if (e) project<1>(lds, ws, rkv, ro, lane, t, p, j, tag); else project<0>(lds, ws, rkv, ro, lane, t, p, j, tag);
        }
        cnt_wait(lds + L_CNT + 8);
        __builtin_amdgcn_s_sleep(36);
        MK_ATT(0);
        a.tag = tag;
        wait_tags((const GAS u64*)(ws + window_flag(a)), tag);
        KV3 kw;
        LDB_FIRST(kw);
        MK_SO();
        attn_window<0>(a, kw, so0);
        PIN(a.idx);
        attn_full<0>(a, lds_zero16(a.zp), lds_zero16(a.zp), lds_zero16(a.zp), lds_zero16(a.zp), lds_zero16(a.zp), so0[3], a.rkv, 0, 0, nullptr, nullptr, kw);
        BAR();
    }
}

extern "C" void kernel_launch(void* const* d_in, const int* in_sizes, int n_in, void* d_out, int out_size, void* d_ws, size_t ws_size,
                              hipStream_t stream) {
    (void)in_sizes; (void)n_in; (void)out_size; (void)ws_size;
    (void)hipFuncSetAttribute((const void*)gmix_kernel, hipFuncAttributeMaxDynamicSharedMemorySize, L_END);
    gmix_kernel<<<256, 512, L_END, stream>>>((const char*)d_in[0], (const char*)d_in[1], (const char*)d_in[2], (const char*)d_in[4],
                                             (const char*)d_in[5], (const char*)d_in[6], (char*)d_out, (char*)d_ws);
}
```

```cpp
#define REVEAL 99
#define RROLE 1
#define RCYC 0
#include <hip/hip_runtime.h>
#include <hip/hip_bf16.h>
#define OUT_LDS

typedef __attribute__((ext_vector_type(8))) __bf16 bf16x8;
typedef __attribute__((ext_vector_type(16))) __bf16 bf16x16;
typedef __attribute__((ext_vector_type(16))) float f32x16;
typedef __attribute__((ext_vector_type(8))) float f32x8;
typedef __attribute__((ext_vector_type(4))) float f32x4;
typedef __attribute__((ext_vector_type(4))) unsigned u32x4;
typedef __attribute__((ext_vector_type(2))) unsigned u32x2;
typedef __attribute__((ext_vector_type(2))) __bf16 bf16x2;
typedef short s16x4 __attribute__((ext_vector_type(4)));
typedef unsigned long long u64;
#define GAS __attribute__((address_space(1)))
#define LAS __attribute__((address_space(3)))
typedef LAS const char* lds_cptr;
typedef LAS char* lds_ptr;

constexpr unsigned WS_EPOCH = 0;
#define FSTR 8
constexpr unsigned WS_OFLAG = 65536;
constexpr unsigned WS_KVFLAG = 131072;
constexpr unsigned WS_KV = 1u << 20;
constexpr unsigned TS = 2176;
constexpr unsigned KV_BYTES = 2u * 32u * 64u * TS;
#define OSTR 32896
constexpr unsigned WS_O = 10u << 20;
constexpr unsigned OS = OSTR;
constexpr unsigned O_BYTES = 128u * OS;
constexpr unsigned WS_STAMP = 16u << 20;
constexpr unsigned OOB = 0x7ff00000u;
constexpr unsigned L_W = 65536;
constexpr unsigned L_WO = 0, L_PART = 65536, PREC = 7168, L_Q14 = 106496, L_Q15 = 114688, L_MDIAG = 122880, L_MFAR = 126976, L_Q = 131072,
                   L_GW = 147456, L_Z = 151552, L_G = 152576, L_CNT = 153088, L_EP = 153344, L_SCR = 153600, L_END = 154112;
constexpr float QSCALE = 0.25f * 1.4426950408889634f;
#define LMAXV 1.2676506e30f
constexpr float LMAX = LMAXV, LMIN = 9.094947e-13f;
constexpr unsigned SPIN_CAP = 1u << 17;

#define SBAR() __builtin_amdgcn_sched_barrier(0)
#define PIN(x) asm volatile("" : "+v"(x))
#define MFMA(a, b, c) __builtin_amdgcn_mfma_f32_32x32x16_bf16(a, b, c, 0, 0, 0)
#define BAR() asm volatile("s_barrier" ::: "memory")
#define CBAR() asm volatile("s_waitcnt lgkmcnt(0)\n\ts_barrier" ::: "memory")
#define DRAIN() asm volatile("s_waitcnt vmcnt(0)" ::: "memory")
#define PACEN 6
#define PACE() asm volatile("s_nop %0" :: "i"(PACEN) : "memory")
#define RLX_AGENT __ATOMIC_RELAXED, __HIP_MEMORY_SCOPE_AGENT
#define FL_SCOPE __HIP_MEMORY_SCOPE_AGENT
#define RLX_ST __ATOMIC_RELAXED, FL_SCOPE
#define STAMP(i) do { if ((i) == 0 || (i) == REVEAL) { u64 a_, b_; SBAR(); asm volatile("s_memrealtime %0\n\ts_memtime %1\n\ts_waitcnt lgkmcnt(0)" : "=s"(a_), "=s"(b_) :: "memory"); \
    if ((i) == 0) { stv.r0 = a_; stv.c0 = b_; stv.d = 0; } else stv.d = RCYC ? (unsigned)(b_ - stv.c0) / 20u : (unsigned)(a_ - stv.r0); SBAR(); } } while (0)
struct Rev { u64 r0, c0; unsigned d; };

__device__ __forceinline__ void glds16(unsigned voff, const void* sbase, unsigned ldsaddr) {
    asm volatile("s_mov_b32 m0, %2\n\ts_nop 0\n\tglobal_load_lds_dwordx4 %0, %1" ::"v"(voff), "s"(sbase), "s"(ldsaddr) : "memory");
}
__device__ __forceinline__ void glds16x2(unsigned v0, unsigned v1, const void* sbase, unsigned ldsaddr) {
    asm volatile("s_mov_b32 m0, %3\n\ts_nop 0\n\tglobal_load_lds_dwordx4 %0, %2\n\tglobal_load_lds_dwordx4 %1, %2 offset:1024"
                 ::"v"(v0), "v"(v1), "s"(sbase), "s"(ldsaddr) : "memory");
}
__device__ __forceinline__ void glds16x4(unsigned v0, unsigned v1, unsigned v2, unsigned v3, const void* sbase, unsigned ldsaddr) {
    asm volatile("s_mov_b32 m0, %5\n\ts_nop 0\n\tglobal_load_lds_dwordx4 %0, %4\n\tglobal_load_lds_dwordx4 %1, %4 offset:1024\n\t"
                 "global_load_lds_dwordx4 %2, %4 offset:2048\n\tglobal_load_lds_dwordx4 %3, %4 offset:3072"
                 ::"v"(v0), "v"(v1), "v"(v2), "v"(v3), "s"(sbase), "s"(ldsaddr) : "memory");
}
__device__ __forceinline__ unsigned cvtpk(float lo, float hi) {
    typedef __attribute__((ext_vector_type(2))) float f32x2;
    return __builtin_bit_cast(unsigned, __builtin_convertvector((f32x2){lo, hi}, bf16x2));
}
__device__ __forceinline__ bf16x8 as_frag(u32x4 v) { return __builtin_bit_cast(bf16x8, v); }
__device__ __forceinline__ bf16x8 lds_frag(lds_cptr p) { return *(const LAS bf16x8*)p; }
__device__ __forceinline__ bf16x8 tr_frag(lds_cptr p0, lds_cptr p1) {
    s16x4 a = __builtin_amdgcn_ds_read_tr16_b64_v4i16((LAS s16x4*)p0);
    s16x4 b = __builtin_amdgcn_ds_read_tr16_b64_v4i16((LAS s16x4*)p1);
    return __builtin_bit_cast(bf16x8, __builtin_shufflevector(a, b, 0, 1, 2, 3, 4, 5, 6, 7));
}
__device__ __forceinline__ f32x16 pv_lo(bf16x8 v, const f32x16& p, f32x16 acc, int idx) {
    f32x8 h = __builtin_shufflevector(p, p, 0, 1, 2, 3, 4, 5, 6, 7);
    return __builtin_amdgcn_smfmac_f32_32x32x32_bf16(v, __builtin_bit_cast(bf16x16, h), acc, idx, 0, 0);
}
__device__ __forceinline__ f32x16 pv_hi(bf16x8 v, const f32x16& p, f32x16 acc, int idx) {
    f32x8 h = __builtin_shufflevector(p, p, 8, 9, 10, 11, 12, 13, 14, 15);
    return __builtin_amdgcn_smfmac_f32_32x32x32_bf16(v, __builtin_bit_cast(bf16x16, h), acc, idx, 0, 0);
}
#define MX3(a, b, c) __builtin_fmaxf(__builtin_fmaxf((a), (b)), (c))
__device__ __forceinline__ float max16(const f32x16& s) {
    float a = MX3(s[0], s[1], s[2]), b = MX3(s[3], s[4], s[5]);
    a = MX3(a, s[6], s[7]); b = MX3(b, s[8], s[9]); a = MX3(a, s[10], s[11]); b = MX3(b, s[12], s[13]);
    return MX3(a, b, __builtin_fmaxf(s[14], s[15]));
}
__device__ __forceinline__ float halfmax(float m) {
    auto r = __builtin_amdgcn_permlane32_swap(__float_as_uint(m), __float_as_uint(m), false, false);
    return __builtin_fmaxf(__uint_as_float(r[0]), __uint_as_float(r[1]));
}
__device__ __forceinline__ f32x16 lds_f32x16(lds_cptr p) {
    f32x4 a = *(const LAS f32x4*)p, b = *(const LAS f32x4*)(p + 1024), c = *(const LAS f32x4*)(p + 2048), d = *(const LAS f32x4*)(p + 3072);
    f32x8 lo = __builtin_shufflevector(a, b, 0, 1, 2, 3, 4, 5, 6, 7), hi = __builtin_shufflevector(c, d, 0, 1, 2, 3, 4, 5, 6, 7);
    return __builtin_shufflevector(lo, hi, 0, 1, 2, 3, 4, 5, 6, 7, 8, 9, 10, 11, 12, 13, 14, 15);
}
#define ST_AUX 16
#define LD_AUX 16
#define BLOAD(rs, vo, so) as_frag(__builtin_amdgcn_raw_buffer_load_b128(rs, vo, so, LD_AUX))
#define BSTORE(v, rs, vo, so) __builtin_amdgcn_raw_buffer_store_b128(v, rs, (vo) + (so), 0, ST_AUX)

#define POLLSLP 8
#define BSLP 36
__device__ __forceinline__ void wait_tags_end(u64 v, const GAS u64* p, u64 tag, GAS u64* pub, int lane) {
    if (pub) { DRAIN(); if (lane == 0) __hip_atomic_store(pub, tag, RLX_ST); }
    if (__builtin_expect(!__all(v == tag), 0)) {
#pragma nounroll
        for (unsigned spins = 0; spins < SPIN_CAP; ++spins) {
            __builtin_amdgcn_s_sleep(POLLSLP);
            if (__all(__hip_atomic_load(p, RLX_AGENT) == tag)) break;
        }
    }
    asm volatile("" ::: "memory");
}

__device__ __forceinline__ void wait_tags(const GAS u64* p, u64 tag, GAS u64* pub = nullptr, int lane = 0) {
    wait_tags_end(__hip_atomic_load(p, RLX_AGENT), p, tag, pub, lane);
}

__device__ __forceinline__ void cnt_arrive(lds_ptr cnt, int lane) {
    asm volatile("s_waitcnt lgkmcnt(0)" ::: "memory");
    if (lane == 0) __hip_atomic_fetch_add((LAS unsigned*)cnt, 1u, __ATOMIC_RELAXED, __HIP_MEMORY_SCOPE_WORKGROUP);
}
__device__ __forceinline__ void cnt_wait(lds_ptr cnt) {
#pragma nounroll
    for (unsigned spins = 0; spins < (1u << 22); ++spins) {
        if (__builtin_amdgcn_readfirstlane(__hip_atomic_load((LAS unsigned*)cnt, __ATOMIC_RELAXED, __HIP_MEMORY_SCOPE_WORKGROUP)) == 4) break;
        __builtin_amdgcn_s_sleep(1);
    }
    asm volatile("" ::: "memory");
}
__device__ __forceinline__ unsigned kvflag(int br, int quad, int tile, int type) {
    return WS_KVFLAG + (unsigned)(((((br * 8 + quad) * 4 + (tile & 3)) * 2 + (tile >> 5)) * 2 + type) * 8 + ((tile >> 2) & 7)) * FSTR;
}

__device__ __forceinline__ void slow_rows(__amdgpu_buffer_rsrc_t rs, unsigned sbase, int lo, int hi, int g, bool band, bf16x8 q,
                                          unsigned vk, unsigned vv0, unsigned vv1, lds_cptr lds, int lane, int idx, f32x16& acc) {
    float m = -__builtin_inff();
    for (int pass = 0; pass < 2; ++pass) {
        if (pass) { m = halfmax(m); acc = f32x16{}; }
        for (int kt = lo; kt <= hi; ++kt) {
            const unsigned so = sbase + (unsigned)kt * TS;
            const bf16x8 k = BLOAD(rs, vk, so), v0 = BLOAD(rs, vv0, so), v1 = BLOAD(rs, vv1, so);
            f32x16 c = f32x16{};
            if (band && kt == g) c = lds_f32x16(lds + L_MDIAG + lane * 16);
            if (band && kt == g - 8) c = lds_f32x16(lds + L_MFAR + lane * 16);
            f32x16 s = MFMA(k, q, c);
            if (!pass) m = __builtin_fmaxf(m, max16(s));
            else {
#pragma unroll
                for (int r = 0; r < 16; ++r) s[r] = __builtin_amdgcn_exp2f(s[r] - m);
                acc = pv_lo(v0, s, acc, idx); acc = pv_hi(v1, s, acc, idx);
            }
        }
    }
}

#define PK4(A, o) ((u32x4){cvtpk(A[o], A[o + 1]), cvtpk(A[o + 2], A[o + 3]), cvtpk(A[o + 4], A[o + 5]), cvtpk(A[o + 6], A[o + 7])})
#define PROF_ARG , Rev& stv
#define PROF_PASS , stv
__device__ __forceinline__ u64 make_tag(u32x2 e) {
    return (((u64)(unsigned)__builtin_amdgcn_readfirstlane(e[1]) << 32) | (unsigned)__builtin_amdgcn_readfirstlane(e[0])) * 0x9E3779B97F4A7C15ull + 0x632BE59BD9B4E019ull;
}
template <int E>
__device__ __forceinline__ u32x4 pack_kv(int i, const f32x16& a) {
    const int s = i & 1;
    if (E == 0) return PK4(a, 8 * s);
    auto x = __builtin_amdgcn_permlane32_swap(cvtpk(a[8 * s], a[8 * s + 1]), cvtpk(a[8 * s + 4], a[8 * s + 5]), false, false);
    auto y = __builtin_amdgcn_permlane32_swap(cvtpk(a[8 * s + 2], a[8 * s + 3]), cvtpk(a[8 * s + 6], a[8 * s + 7]), false, false);
    return (u32x4){x[0], y[0], x[1], y[1]};
}
#define PUT_KV(i, v, vo) BSTORE(v, rkv, vo, ((i) < 2 ? 64 * TS : 0) + ((i) & 1) * (E ? 512 : 128 * TS))
#define QSWAP 1
#define PBX 4
template <int E>
__device__ __forceinline__ void project(lds_ptr lds, char* ws, __amdgpu_buffer_rsrc_t rkv, __amdgpu_buffer_rsrc_t ro, int lane, int t, int p, int j, u64& tag PROF_ARG) {
    const int r32 = lane & 31, hh = lane >> 5, qd4 = (p >> 1) ^ QSWAP;
    const unsigned q4 = (lane & 15) >> 2, wcol = ((lane >> 4) & 1) * 32 + (lane & 3) * 8;
    lds_cptr xa[4];
#pragma unroll
    for (int ks = 0; ks < 4; ++ks) xa[ks] = lds + (32 * t + r32) * 128 + (((hh ^ ((r32 >> 1) & 7)) * 16) ^ (ks * 32));
    unsigned w1o = L_W + (8 * hh + q4) * 128 + ((lane >> 4) & 1) * 64 + (q4 >> 1) * 32 + (lane & 3) * 8, w0o = w1o ^ 32;
    PIN(w0o); PIN(w1o);
    const lds_cptr w0 = lds + w0o, w1 = lds + w1o, wq = lds + (8 * hh + q4) * 64 + wcol;
#define XRD(n) lds_frag(xa[(n) & 3] + (((n) >> 2) & 3) * 16384)
#define WRD(b, n) tr_frag(b + ((n) & 15) * 2048, b + ((n) & 15) * 2048 + 512)
#define YOF(n) ((n) >= 28 ? 32768 + ((n) & 3) * 2048 : ((n) & 15) * 2048)
#define YRD(b, n) tr_frag(b + YOF(n), b + YOF(n) + 512)
#define QOF(br, n) (((n) >= 28 ? L_Q15 - L_Q14 : ((n) >= 24 ? 0 : (((n) >> 2) & 3) * 16384)) + (br) * 4096 + (((n) & 3) ^ E) * 1024)
#define QRD(br, n) tr_frag(((n) >= 24 ? wqh : wq) + QOF(br, n), ((n) >= 24 ? wqh : wq) + QOF(br, n) + 256)
#define MM(W, X, A) (E ? MFMA(X, W, A) : MFMA(W, X, A))
    bf16x8 xf[32], f0[4], f1[4];
    f32x16 a0 = f32x16{}, a1 = f32x16{};
    unsigned gwo = (lane & 15) == ((lane >> 4) & 1) ? L_GW + t * 1024 + hh * 16 : L_Z;
    PIN(gwo);
    f32x4 zg = f32x4{};
    bf16x8 gf[4];
#define RD1(n) do { xf[n] = XRD(n); f0[(n) & 3] = WRD(w0, n); f1[(n) & 3] = WRD(w1, n); SBAR(); } while (0)
#define GRD(n) gf[(n) & 3] = lds_frag(lds + gwo + (n) * 32)
#define GMM(n) zg = __builtin_amdgcn_mfma_f32_16x16x32_bf16(gf[(n) & 3], xf[n], zg, 0, 0, 0)
#define MM1(n) do { a0 = MM(f0[(n) & 3], xf[n], a0); a1 = MM(f1[(n) & 3], xf[n], a1); SBAR(); } while (0)
#pragma unroll
    for (int s = 0; s < 8; ++s) {
        if (s == 0) CBAR(); else BAR();
        SBAR();
        RD1(4 * s); RD1(4 * s + 1);
        if (s) { MM1(4 * s - 1); GMM(4 * s - 4); GMM(4 * s - 3); GMM(4 * s - 2); GMM(4 * s - 1); SBAR(); }
        RD1(4 * s + 2); MM1(4 * s); RD1(4 * s + 3); MM1(4 * s + 1);
        if (s == 0) DRAIN();
        GRD(4 * s); GRD(4 * s + 1); GRD(4 * s + 2); GRD(4 * s + 3); SBAR();
        MM1(4 * s + 2);
        SBAR();
    }
    const unsigned tile = 4 * j + t;
    unsigned vo = (unsigned)(4 * qd4 * 64 + tile) * TS + (E ? (r32 >> 4) * (128 * TS) + 1024 + hh * 256 + (r32 & 15) * 16 : lane * 16);
    unsigned wqo = L_Q14 + (8 * hh + q4) * 64 + wcol;
    PIN(vo); PIN(wqo);
    const lds_cptr wqh = lds + wqo;
    MM1(31); GMM(28); GMM(29); GMM(30); GMM(31); SBAR();
#define PRE8 2
#define PUBN 7
#define LATE0 99
    u32x4 pk[4];
#pragma unroll
    for (int i = 0; i < 4; ++i) { pk[i] = pack_kv<E>(i, i < 2 ? a0 : a1); PIN(pk[i]); if (i < PRE8) PUT_KV(i, pk[i], vo); SBAR(); }
    if (!E) BSTORE(((u32x4){0x3f803f80u, 0x3f803f80u, 0x3f803f80u, 0x3f803f80u}), rkv,
                   (unsigned)((hh * 32 + 4 * (qd4 ^ (hh * (PBX >> 1))) + (r32 >> 3)) * 64 + tile) * TS + 2048 + (lane & 7) * 16, 0);
    { LAS float* gp = (LAS float*)(lds + (lane < 16 ? L_G + t * 128 : L_SCR)) + lane;
      gp[0] = __builtin_amdgcn_rcpf(1.f + __expf(-zg[0])); gp[16] = __builtin_amdgcn_rcpf(1.f + __expf(-zg[1])); }
    SBAR();
    BAR(); SBAR();
    bf16x8 u0 = QRD(0, 0), u1 = QRD(1, 0), y0 = YRD(w0, 0), y1 = YRD(w1, 0);
    const u32x2 tg = *(const LAS u32x2*)(lds + L_EP);
    f32x16 b0 = f32x16{}, b1 = f32x16{}, aQ0 = f32x16{}, aQ1 = f32x16{};
    SBAR();
#pragma unroll
    for (int n = 0; n < 24; ++n) {
        aQ0 = MFMA(u0, xf[n], aQ0); SBAR();
        if (n == 1) { tag = make_tag(tg); asm volatile("" : "+s"(tag)); SBAR(); }
        if (n == PUBN) { DRAIN(); if (lane == 0) __hip_atomic_store((GAS u64*)(ws + kvflag(0, qd4, tile, E)), tag, RLX_ST); SBAR(); }
        if ((n & 3) == 3) {
            if (PRE8 + (n >> 2) < 2) { PUT_KV((PRE8 + (n >> 2)) & 3, pk[(PRE8 + (n >> 2)) & 3], vo); SBAR(); }
            if (n == LATE0) { PUT_KV(2, pk[2], vo); SBAR(); }
            if (n == LATE0 + 4) { PUT_KV(3, pk[3], vo); SBAR(); }
            BAR(); SBAR();
        }
        u0 = QRD(0, n + 1); SBAR();
        aQ1 = MFMA(u1, xf[n], aQ1); SBAR();
        u1 = n < 23 ? QRD(1, n + 1) : QRD(0, 25); SBAR();
        b0 = MM(y0, xf[n], b0); SBAR(); y0 = n < 23 ? YRD(w0, n + 1) : QRD(0, 26); SBAR();
        b1 = MM(y1, xf[n], b1); SBAR(); y1 = n < 23 ? YRD(w1, n + 1) : QRD(0, 27); SBAR();
    }
    aQ0 = MFMA(u0, xf[24], aQ0); SBAR(); u0 = QRD(0, 28); SBAR(); aQ0 = MFMA(u1, xf[25], aQ0); SBAR(); u1 = QRD(0, 29); SBAR();
    aQ0 = MFMA(y0, xf[26], aQ0); SBAR(); y0 = QRD(0, 30); SBAR(); aQ0 = MFMA(y1, xf[27], aQ0); SBAR(); y1 = QRD(0, 31); SBAR();
    aQ0 = MFMA(u0, xf[28], aQ0); SBAR(); aQ0 = MFMA(u1, xf[29], aQ0); SBAR(); u1 = QRD(1, 24); SBAR();
    aQ0 = MFMA(y0, xf[30], aQ0); SBAR(); y0 = YRD(w0, 24); SBAR(); aQ0 = MFMA(y1, xf[31], aQ0); SBAR(); y1 = YRD(w1, 24); SBAR();
    const lds_ptr qd = lds + L_Q + t * 1024 + lane * 16;
#define QBAR 4
#define NFREE 3
    f32x8 qs; u32x4 qk;
#define QEPI(m) do { if ((m) == 1) { PIN(aQ0); qs = __builtin_shufflevector(aQ0, aQ0, 8, 9, 10, 11, 12, 13, 14, 15) * QSCALE; PIN(qs); } \
        if ((m) == 2) { qk = PK4(qs, 0); *(LAS u32x4*)(qd + 4096) = qk; } \
        if ((m) == QBAR) { __builtin_amdgcn_s_setprio(2); __builtin_amdgcn_s_waitcnt(0xC07F | ((2 * (QBAR - 2)) << 8)); BAR(); STAMP(4); } \
        if ((m) > QBAR + NFREE) PACE(); SBAR(); } while (0)
#pragma unroll
    for (int n = 24; n < 32; ++n) {
        aQ1 = MFMA(u1, xf[n], aQ1); SBAR(); if (n < 31) u1 = QRD(1, n + 1); SBAR(); QEPI(3 * (n - 24));
        b0 = MM(y0, xf[n], b0); SBAR(); if (n < 31) y0 = YRD(w0, n + 1); SBAR(); QEPI(3 * (n - 24) + 1);
        b1 = MM(y1, xf[n], b1); SBAR(); if (n < 31) y1 = YRD(w1, n + 1); SBAR(); QEPI(3 * (n - 24) + 2);
    }
#pragma unroll
    for (int r = 0; r < 8; ++r) aQ0[r] *= QSCALE;
#pragma unroll
    for (int r = 0; r < 16; ++r) aQ1[r] *= QSCALE;
    *(LAS u32x4*)qd = PK4(aQ0, 0); *(LAS u32x4*)(qd + 8192) = PK4(aQ1, 0); *(LAS u32x4*)(qd + 12288) = PK4(aQ1, 8);
    cnt_arrive(lds + L_CNT + 8, lane);
    const unsigned vo2 = vo + (32 + 4 * ((qd4 ^ (PBX >> 1)) - qd4)) * 64 * TS;
#define YSLP 8
    u32x4 py[4] = {pack_kv<E>(0, b0), pack_kv<E>(1, b0), pack_kv<E>(2, b1), pack_kv<E>(3, b1)};
    PIN(py[0]); PIN(py[1]); PIN(py[2]); PIN(py[3]);
#define YSTAG 2
    if (t & 1) __builtin_amdgcn_s_sleep(YSTAG);
    if (t & 2) __builtin_amdgcn_s_sleep(2 * YSTAG);
    if (LATE0 > 23) { PUT_KV(2, pk[2], vo); __builtin_amdgcn_s_sleep(YSLP); }
    if (LATE0 > 19) { PUT_KV(3, pk[3], vo); __builtin_amdgcn_s_sleep(YSLP); }
    PUT_KV(0, py[0], vo2); __builtin_amdgcn_s_sleep(YSLP); PUT_KV(1, py[1], vo2); __builtin_amdgcn_s_sleep(YSLP);
    PUT_KV(2, py[2], vo2); __builtin_amdgcn_s_sleep(YSLP); PUT_KV(3, py[3], vo2);
    DRAIN();
    if (lane == 0) __hip_atomic_store((GAS u64*)(ws + kvflag(1, qd4, tile, E)), tag, RLX_ST);
    BSTORE(((u32x4){~0u, ~0u, ~0u, ~0u}), ro, lane * 16, tile * OS + 2 * p * 1024);
    STAMP(19);
}

#define AB_WO (512 * 1024)
#define ROTEXPR j + 4 * p
#define WOX 1
#define WOIN 2
#define CODE_LINES 280

struct Att {
    lds_ptr lds; char* ws; __amdgpu_buffer_rsrc_t rkv, ro;
    int lane, t, p, g, head, idx; u64 tag; unsigned vk, vv0, vv1, sBeg; lds_cptr zp, qp[4];
};
#define IDX a.idx
#define LD3(K, V0, V1, rs, so) do { K = BLOAD(rs, a.vk, so); V0 = BLOAD(rs, a.vv0, so); V1 = BLOAD(rs, a.vv1, so); } while (0)
#define EX(X, r) X[r] = __builtin_amdgcn_exp2f(X[r])
#define STEP(X, Y, OP, VP, KN, QN, CN, O, V0, F1, F3, F2) do { \
        EX(X, 0); EX(X, 1); SBAR(); OP = pv_hi(VP, Y, OP, IDX); SBAR(); F1; SBAR(); EX(X, 2); EX(X, 3); EX(X, 4); SBAR(); Y = MFMA(KN, QN, CN); SBAR(); F3; SBAR(); \
        EX(X, 5); EX(X, 6); EX(X, 7); SBAR(); EX(X, 8); EX(X, 9); SBAR(); O = pv_lo(V0, X, O, IDX); SBAR(); F2; SBAR(); \
        EX(X, 10); EX(X, 11); EX(X, 12); EX(X, 13); EX(X, 14); EX(X, 15); SBAR(); } while (0)

__device__ __forceinline__ f32x16 lds_zero16(lds_cptr zp) {
    const volatile LAS f32x4* z = (const volatile LAS f32x4*)zp;
    const f32x4 a = *z, b = *z, c = *z, d = *z;
    f32x8 lo = __builtin_shufflevector(a, b, 0, 1, 2, 3, 4, 5, 6, 7), hi = __builtin_shufflevector(c, d, 0, 1, 2, 3, 4, 5, 6, 7);
    return __builtin_shufflevector(lo, hi, 0, 1, 2, 3, 4, 5, 6, 7, 8, 9, 10, 11, 12, 13, 14, 15);
}

template <int C, int BR>
__device__ __forceinline__ void finish(const Att& a, f32x16 o, float gf, bf16x8 q, unsigned vo) {
    f32x8 y = __builtin_shufflevector(o, o, 0, 1, 2, 3, 4, 5, 6, 7) * (__builtin_amdgcn_rcpf(o[8]) * gf);
    PIN(y);
    const float chk = __builtin_fmaf(((y[0] + y[2]) + (y[4] + y[6])) + ((y[1] + y[3]) + (y[5] + y[7])), 0.f, o[8]);
    if (__builtin_expect(__any(__builtin_amdgcn_fmed3f(chk, LMIN, LMAX) != chk), 0)) {
        if (!BR) { wait_tags((const GAS u64*)(a.ws + kvflag(!C, a.p >> 1, 0, 0) + a.lane * FSTR), a.tag); wait_tags((const GAS u64*)(a.ws + kvflag(!C, a.p >> 1, 2, 0) + a.lane * FSTR), a.tag); }
        slow_rows(a.rkv, (unsigned)((BR * 32 + (a.head ^ (BR * 2 * PBX))) * 64) * TS, BR ? (a.g >= 8 ? a.g - 8 : 0) : 0, BR ? a.g : 63, a.g, BR, q, a.vk, a.vv0, a.vv1, a.lds, a.lane, IDX, o);
        y = __builtin_shufflevector(o, o, 0, 1, 2, 3, 4, 5, 6, 7) * (__builtin_amdgcn_rcpf(o[8]) * gf);
        PIN(y);
    }
    u32x4 v = (u32x4){cvtpk(y[0], y[1]), cvtpk(y[2], y[3]), cvtpk(y[4], y[5]), cvtpk(y[6], y[7])};
    if (!C && !BR) v = __builtin_elementwise_min(v, (u32x4){~1u, ~1u, ~1u, ~1u});
    BSTORE(v, a.ro, vo, 0);
}
#define OFLAG(BR, C) ((GAS u64*)(a.ws + WS_OFLAG) + (((BR) * 64 + a.g) * 2 + (C)) * 16 + (a.p ^ ((BR) * PBX)))
#define OVOFF(BR) (a.vk + (unsigned)((BR) * 64 + a.g) * OS + (a.head ^ ((BR) * 2 * PBX)) * 1024)
#define GATE() (*(const LAS float*)(a.lds + L_G + a.t * 128 + (a.lane & 31) * 4))

struct KV3 { bf16x8 k[3], v0[3], v1[3]; };
#define LDB(K, V0, V1, i) do { const int kt_ = a.g - (i); \
        const __amdgpu_buffer_rsrc_t rb_ = __builtin_amdgcn_make_buffer_rsrc(a.ws + WS_KV, 0, kt_ >= 0 ? KV_BYTES : 0u, 0x00020000); \
        LD3(K, V0, V1, rb_, (unsigned)((32 + (a.head ^ (2 * PBX))) * 64 + (kt_ >= 0 ? kt_ : 0)) * TS); } while (0)
#define LDB_FIRST(f) do { LDB(f.k[0], f.v0[0], f.v1[0], 0); SBAR(); LDB(f.k[1], f.v0[1], f.v1[1], 1); SBAR(); LDB(f.k[2], f.v0[2], f.v1[2], 2); SBAR(); } while (0)
#define LDA_FIRST(f) do { LD3(f.k[0], f.v0[0], f.v1[0], a.rkv, so0[0]); SBAR(); LD3(f.k[1], f.v0[1], f.v1[1], a.rkv, so0[1]); SBAR(); \
        LD3(f.k[2], f.v0[2], f.v1[2], a.rkv, so0[2]); SBAR(); } while (0)
__device__ __forceinline__ unsigned window_flag(const Att& a) {
    const int i = a.lane & 15, kt = a.g - (i < 9 ? i : 0);
    return kvflag(1, a.p >> 1, kt >= 0 ? kt : 0, (a.lane >> 4) & 1);
}

template <int C>
__device__ __forceinline__ void attn_full(const Att& a, f32x16 o0, f32x16 o1, f32x16 o2, f32x16 o3, f32x16 S1, unsigned so,
                                          __amdgpu_buffer_rsrc_t rwo, unsigned vwo, unsigned wi, const GAS u64* fp2, const GAS u64* fp3, KV3& nxt PROF_ARG) {
    const lds_ptr lds = a.lds; const int t = a.t, lane = a.lane;
    const f32x16 zc = f32x16{};
    bf16x8 ka = nxt.k[0], va0 = nxt.v0[0], va1 = nxt.v1[0], kb = nxt.k[1], vb0 = nxt.v0[1], vb1 = nxt.v1[1], kc = nxt.k[2], vc0 = nxt.v0[2], vc1 = nxt.v1[2], kd, vd0, vd1;
    const unsigned sBeg = a.sBeg, jb = a.g >> 2;
    unsigned ki = 3;
    vd1 = lds_frag(a.zp);
    lds_ptr pw = lds + L_PART + (4 * C + t) * PREC + lane * 16;
    lds_cptr pr0 = lds + L_PART + (4 * C + ((t - 1) & 3)) * PREC + lane * 16, pr1 = lds + L_PART + (4 * C + ((t - 2) & 3)) * PREC + lane * 16,
             pr2 = lds + L_PART + (4 * C + ((t - 3) & 3)) * PREC + lane * 16;
    unsigned ovo = OVOFF(0); float gf = 0.f;
    if (C) { BAR(); STAMP(4); }
    else { gf = GATE(); PIN(pw); PIN(pr0); PIN(pr1); PIN(pr2); PIN(ovo); PIN(gf);
           DRAIN(); if (lane == 0) __hip_atomic_store(OFLAG(1, 0), a.tag, RLX_ST); }
    bf16x8 q0 = lds_frag(a.qp[0]); SBAR();
    bf16x8 q1 = lds_frag(a.qp[1]), q2 = lds_frag(a.qp[2]), q3 = lds_frag(a.qp[3]);
    SBAR();
    f32x16 S0 = MFMA(ka, q0, zc);
    PIN(S0); SBAR();
    PIN(q1); PIN(q2); PIN(q3); PIN(vd1); PIN(o0); PIN(o1); PIN(o2); PIN(o3); PIN(S1);
    __builtin_amdgcn_s_waitcnt(0x0F76);
#pragma unroll
    for (int i = 0; i < 8; ++i) __builtin_amdgcn_s_setprio(C);
    STAMP(3);
    SBAR();
    unsigned wso = 0;
    u64 fv = a.tag;
#define NEXT1() asm volatile("s_add_i32 %0, %0, 1\n\ts_xor_b32 %1, %0, %2" : "+s"(ki), "=s"(so) : "s"(jb) : "scc")
#define NEXT2() asm volatile("s_and_b32 %0, %0, 15" : "+s"(so) :: "scc")
#define NEXT3() asm volatile("s_mulk_i32 %0, %2\n\ts_add_i32 %0, %0, %1" : "+s"(so) : "s"(sBeg), "i"(4 * TS) : "scc")
#define WOC 1
#define WO_OFF() do { if (C == WOC) asm volatile("s_lshl_b32 %0, %1, 4" : "=s"(wso) : "s"(wi) : "scc"); } while (0)
#define WO_DMA() do { if (C == WOC) __builtin_amdgcn_raw_ptr_buffer_load_lds(rwo, (LAS void*)(lds + L_WO + t * 16384 + wi), 16, vwo, wso, 0, 0); } while (0)
#define WO_NEXT() do { if (C == WOC) asm volatile("s_addk_i32 %0, 0x400\n\ts_and_b32 %0, %0, 0x3fff" : "+s"(wi) :: "scc"); } while (0)
#define TILE(K, V0, V1, KN, LK, LV0, LV1, NW, G) do { \
        STEP(S0, S1, o3, LV1, K, q1, zc, o0, V0, LK = BLOAD(a.rkv, a.vk, so), WO_OFF(), LV0 = BLOAD(a.rkv, a.vv0, so)); \
        STEP(S1, S0, o0, V1, K, q2, zc, o1, V0, LV1 = BLOAD(a.rkv, a.vv1, so), , WO_DMA()); \
        STEP(S0, S1, o1, V1, K, q3, zc, o2, V0, NEXT1(), NEXT2(), NEXT3()); \
        STEP(S1, S0, o2, V1, KN, q0, zc, o3, V0, __builtin_amdgcn_s_waitcnt(0x0F70 | (NW)), G, WO_NEXT()); } while (0)
#define PUTA(O, sl) (*(LAS f32x4*)(pw + (sl) * 2048) = __builtin_shufflevector(O, O, 0, 1, 2, 3))
#define PUTB(O, sl) (*(LAS f32x4*)(pw + (sl) * 2048 + 1024) = __builtin_shufflevector(O, O, 4, 5, 6, 7))
#define PUTL(O, sl) (*(LAS float*)(pw + 6144 + (sl) * 4) = O[8])
    f32x4 x0, y0, x1, y1, x2, y2; float l0, l1, l2;
#define GETP() do { x0 = *(const LAS f32x4*)pr0; x1 = *(const LAS f32x4*)(pr1 + 2048); x2 = *(const LAS f32x4*)(pr2 + 4096); \
        y0 = *(const LAS f32x4*)(pr0 + 1024); y1 = *(const LAS f32x4*)(pr1 + 3072); y2 = *(const LAS f32x4*)(pr2 + 5120); \
        l0 = *(const LAS float*)(pr0 + 6144); l1 = *(const LAS float*)(pr1 + 6148); l2 = *(const LAS float*)(pr2 + 6152); } while (0)
    if (C) {
#pragma nounroll
        for (int it = 0; it < 4; ++it) {
            TILE(ka, va0, va1, kb, kd, vd0, vd1, WOC ? 7 : 6, );
            PIN(S0); PIN(S1); PIN(o3); wait_tags_end(fv, fp2, a.tag, nullptr, 0);
            TILE(kb, vb0, vb1, kc, ka, va0, va1, WOC ? 8 : 6, );
            TILE(kc, vc0, vc1, kd, kb, vb0, vb1, WOC ? 9 : 6, fv = __hip_atomic_load(fp2, RLX_AGENT));
            TILE(kd, vd0, vd1, ka, kc, vc0, vc1, WOC ? 10 : 7, );
        }
        o3 = pv_hi(vd1, S1, o3, IDX);
        STAMP(6);
        const u64 f3 = __hip_atomic_load(fp3, RLX_AGENT);
        cnt_wait(lds + L_CNT + 8);
        PUTA(o0, 0); PUTB(o0, 0); PUTL(o0, 0); PUTA(o1, 1); PUTB(o1, 1); PUTL(o1, 1); PUTA(o2, 2); PUTB(o2, 2); PUTL(o2, 2);
        DRAIN(); cnt_arrive(lds + L_CNT, lane);
        wait_tags_end(f3, fp3, a.tag, nullptr, 0);
        LDB_FIRST(nxt);
        cnt_wait(lds + L_CNT);
        GETP(); gf = GATE();
    } else {
        WO_OFF(); WO_DMA(); WO_NEXT(); WO_OFF(); WO_DMA(); WO_NEXT(); SBAR();
        TILE(ka, va0, va1, kb, kd, vd0, vd1, WOC ? 6 : 9, );
        TILE(kb, vb0, vb1, kc, ka, va0, va1, WOC ? 6 : 9, );
#pragma nounroll
        for (int it = 0; it < 3; ++it) {
            TILE(kc, vc0, vc1, kd, kb, vb0, vb1, WOC ? 6 : 9, );
            TILE(kd, vd0, vd1, ka, kc, vc0, vc1, WOC ? 6 : 9, );
            TILE(ka, va0, va1, kb, kd, vd0, vd1, WOC ? 6 : 9, );
            TILE(kb, vb0, vb1, kc, ka, va0, va1, WOC ? 6 : 9, );
        }
        STEP(S0, S1, o3, vb1, kc, q1, zc, o0, vc0, , , );
        STEP(S1, S0, o0, vc1, kc, q2, zc, o1, vc0, , , );
        STEP(S0, S1, o1, vc1, kd, q0, zc, o2, vc0, __builtin_amdgcn_s_waitcnt(WOC ? 0x0F73 : 0x0F75), , );
        STEP(S1, S0, o2, vc1, kd, q1, zc, o0, vd0, , , );
        STEP(S0, S1, o0, vd1, kd, q2, zc, o1, vd0, , , );
        STEP(S1, S0, o1, vd1, kc, q3, zc, o2, vd0, PUTA(o0, 0), PUTL(o0, 0), PUTB(o0, 0));
        STEP(S0, S1, o2, vd1, kd, q3, zc, o3, vc0, PUTA(o1, 1), PUTL(o1, 1), PUTB(o1, 1));
        PUTA(o2, 2); PUTB(o2, 2); asm volatile("" :: "v"(o0), "v"(o1), "v"(o2)); SBAR();
        EX(S1, 0); EX(S1, 1); SBAR(); o3 = pv_hi(vc1, S0, o3, IDX); SBAR(); PUTL(o2, 2); __builtin_amdgcn_s_setprio(2); SBAR();
        EX(S1, 2); EX(S1, 3); EX(S1, 4); EX(S1, 5); EX(S1, 6); EX(S1, 7); SBAR(); EX(S1, 8); EX(S1, 9); SBAR();
        o3 = pv_lo(vd0, S1, o3, IDX); PIN(S1); SBAR();
        if (!WOC) DRAIN();
        CBAR(); GETP(); SBAR();
        EX(S1, 10); EX(S1, 11); EX(S1, 12); EX(S1, 13); EX(S1, 14); EX(S1, 15); SBAR();
        o3 = pv_hi(vd1, S1, o3, IDX); SBAR();
        STAMP(6);
    }
    x0 = (x0 + x1) + x2; y0 = (y0 + y1) + y2; l0 = (l0 + l1) + l2;
    PIN(x0); PIN(y0); PIN(l0); PIN(o3);
#pragma unroll
    for (int r = 0; r < 4; ++r) { o3[r] += x0[r]; o3[4 + r] += y0[r]; }
    o3[8] += l0;
    finish<C, 0>(a, o3, gf, q3, ovo);
}

template <int C>
__device__ __forceinline__ void attn_window(const Att& a, KV3& f, const unsigned (&so0)[4] PROF_ARG) {
    const lds_ptr lds = a.lds; const int t = a.t, lane = a.lane;
    const f32x16 zc = f32x16{};
    bf16x8 k[9], v0[9], v1[9];
#pragma unroll
    for (int i = 0; i < 3; ++i) { k[i] = f.k[i]; v0[i] = f.v0[i]; v1[i] = f.v1[i]; }
#define LDW(i) LDB(k[i], v0[i], v1[i], i)
    const bf16x8 qa = lds_frag(lds + L_Q + 8192 + C * 4096 + t * 1024 + lane * 16);
    const f32x16 cd = lds_f32x16(lds + L_MDIAG + lane * 16), cf = lds_f32x16(lds + L_MFAR + lane * 16);
    f32x16 oo = lds_zero16(a.zp);
    const float gt = GATE();
    f32x16 S0 = MFMA(k[0], qa, cd), S1 = MFMA(k[1], qa, zc);
    SBAR(); LDW(3); SBAR();
#pragma unroll
    for (int r = 0; r < 10; ++r) { EX(S0, r); SBAR(); }
    oo = pv_lo(v0[0], S0, oo, IDX); SBAR();
#pragma unroll
    for (int r = 10; r < 16; ++r) EX(S0, r);
    SBAR();
    STEP(S1, S0, oo, v1[0], k[2], qa, zc, oo, v0[1], LDW(4), , );
    STEP(S0, S1, oo, v1[1], k[3], qa, zc, oo, v0[2], LDW(5), , );
    STEP(S1, S0, oo, v1[2], k[4], qa, zc, oo, v0[3], LDW(6), , );
    STEP(S0, S1, oo, v1[3], k[5], qa, zc, oo, v0[4], LDW(7), , );
    STEP(S1, S0, oo, v1[4], k[6], qa, zc, oo, v0[5], LDW(8), , );
    const GAS u64* fa = (const GAS u64*)(a.ws + kvflag(1, a.p >> 1, t, 0) + (lane & 31) * FSTR);
    u64 fv = 0;
    STEP(S0, S1, oo, v1[5], k[7], qa, zc, oo, v0[6], if (!C) fv = __hip_atomic_load(fa, RLX_AGENT), , );
    STEP(S1, S0, oo, v1[6], k[8], qa, cf, oo, v0[7], , , );
    if (C) { PIN(S0); PIN(S1); PIN(oo); DRAIN(); if (lane == 0) __hip_atomic_store(OFLAG(0, C), a.tag, RLX_ST); }
    STEP(S0, S1, oo, v1[7], k[8], qa, cf, oo, v0[8], , , );
    oo = pv_hi(v1[8], S0, oo, IDX);
    STAMP(7);
    if (!C) { PIN(oo); wait_tags_end(fv, fa, a.tag, nullptr, 0); LDA_FIRST(f); }
    finish<C, 1>(a, oo, 1.f - gt, qa, OVOFF(1));
}

__global__ void __launch_bounds__(512, 2)
gmix_kernel(const char* x, const char* wA, const char* woA, const char* wB, const char* woB, const char* gw, char* out, char* ws) {
    extern __shared__ __attribute__((aligned(16))) char smem[];
    const lds_ptr lds = (lds_ptr)smem;
    const unsigned lds0 = (unsigned)(uintptr_t)lds;
    const int tid = threadIdx.x, lane = tid & 63, r32 = lane & 31, hh = lane >> 5;
    const int w = __builtin_amdgcn_readfirstlane(tid >> 6);
    const int bid = blockIdx.x, xcd = bid & 7, rr = bid >> 3;
    const int p = 4 * (xcd & 3) + (rr & 3), j = 8 * (xcd >> 2) + (rr >> 2);
    const int jo = 2 * xcd + (rr >> 4), po = rr & 15;
    const int t = w & 3, c = w >> 2;
    const int e = p & 1;
    Rev stv;
    STAMP(0);
    {
        u64 pc; const unsigned ln = (((bid & ~7) | (tid >> 6)) + (bid < CODE_LINES - 256 ? (tid & 32) * 8 : 0)) * 128;
        asm volatile("s_getpc_b64 %0\n\ts_mov_b32 m0, %2\n\ts_nop 0\n\tglobal_load_lds_dword %1, %0" : "=&s"(pc) : "v"(ln), "s"(lds0 + L_SCR) : "memory");
        SBAR();
    }
    unsigned rot = (ROTEXPR) & 7;
    unsigned vx0 = (128 * j + 32 * t + (lane >> 3)) * 1024 + (((lane & 7) ^ (lane >> 4)) * 16), vx1 = (vx0 ^ 64) + 7168, vx2 = vx0 + 14336, vx3 = vx1 + 14336;
    unsigned vw0 = (16 * t + (lane >> 3)) * 3072 + 1024 + 1024 * e + 128 * ((p >> 1) ^ QSWAP) + (((lane & 7) ^ (((lane >> 4) & 1) << 1)) * 16), vw1 = vw0 + 8 * 3072 - 1024;
    unsigned mx = lds0 + t * 4096, mw = lds0 + L_W + t * 2048, ow = rot * (64 * 3072);
    asm volatile("" : "+v"(vx0), "+v"(vx1), "+v"(vx2), "+v"(vx3), "+v"(vw0), "+v"(vw1));
    asm volatile("" : "+s"(mx), "+s"(mw), "+s"(rot), "+s"(ow));
    SBAR();
    asm volatile("" : "+s"(x), "+s"(wA), "+s"(woA), "+s"(wB), "+s"(woB), "+s"(gw), "+s"(out), "+s"(ws));
#define MK_ATT(C) Att a; a.lds = lds; a.ws = ws; a.lane = lane; a.t = t; a.p = p; a.g = 4 * j + t; a.head = 2 * p + C; a.idx = 0xdddddddd; \
    a.rkv = __builtin_amdgcn_make_buffer_rsrc(ws + WS_KV, 0, KV_BYTES, 0x00020000); a.ro = __builtin_amdgcn_make_buffer_rsrc(ws + WS_O, 0, O_BYTES, 0x00020000); \
    a.vk = lane * 16; a.vv0 = r32 < 16 ? 1024 + hh * 256 + r32 * 16 : ((r32 & ~4) == 16 ? 2048 : OOB); a.vv1 = r32 < 16 ? a.vv0 + 512 : a.vv0; a.zp = lds + L_Z + lane * 16; \
    a.sBeg = (unsigned)(a.head * 64 + t) * TS; \
    _Pragma("unroll") for (int i = 0; i < 4; ++i) a.qp[i] = lds + L_Q + C * 4096 + ((t + 1 + i) & 3) * 1024 + lane * 16
#define RWO __builtin_amdgcn_make_buffer_rsrc((void*)((t >> 1) ? woB : woA), 0, AB_WO, 0x00020000)
#define VWO (((t & 1) * 256 + (lane >> 2)) * 1024 + 64 * po + (lane & 3) * 16)
#define WI0 (4096 * (((rr >> 4) << 1) | (po & 1)) + WOX * 1024 * (xcd & 3))
#define MK_SO() unsigned so0[4]; _Pragma("unroll") for (int i = 0; i < 4; ++i) so0[i] = a.sBeg + (j ^ i) * (4 * TS)
    if (c) {
#define DMA1(s) do { const unsigned ks_ = ((s) + rot) & 7; \
        glds16x4(vx0, vx1, vx2, vx3, x + ks_ * 128, mx + ((s) & 3) * 16384); glds16x2(vw0, vw1, wA + ks_ * (64 * 3072), mw + ((s) & 3) * 8192); } while (0)
#define DMA2(s) do { const unsigned ks_ = (((s) + rot) & 7) * (64 * 3072); \
        glds16x2(vq0, vq1, wq + ks_, mq + ((s) == 14 ? L_Q14 : ((s) & 3) * 16384)); glds16x2(vw0, vw1, wy + ks_, mw + ((s) & 3) * 8192); SBAR(); } while (0)
#define LBAR(N) asm volatile("s_waitcnt vmcnt(" #N ")\n\ts_barrier" ::: "memory")
        STAMP(20);
        glds16x4(vx0, vx1, vx2, vx3, x + rot * 128, mx); glds16x2(vw0, vw1, wA + ow, mw);
        SBAR();
        const unsigned k1 = (1 + rot) & 7;
        glds16x2(vx0, vx1, x + k1 * 128, mx + 16384);
        SBAR();
        if (t == 0) asm volatile("s_mov_b32 m0, %2\n\ts_nop 0\n\tglobal_load_lds_dword %0, %1 sc1" :: "v"((lane & 1) * 4), "s"(ws + WS_EPOCH), "s"(lds0 + L_EP) : "memory");
        unsigned vq0 = (32 * (t & 1) + 16 * e + (lane >> 2)) * 3072 + 64 * (p ^ ((t >> 1) * PBX)) + (lane & 3) * 16 + 1024, vq1 = vq0 + (e ? -16 * 3072 : 16 * 3072) - 1024;
        unsigned vwo = VWO;
        MK_ATT(1);
        const GAS u64* fp = (const GAS u64*)(ws + kvflag(0, p >> 1, (j >> 3) * 32 + t, 0) + (lane & 15) * FSTR);
        const GAS u64* fp2 = (const GAS u64*)(ws + kvflag(0, p >> 1, ((j >> 3) ^ 1) * 32 + t, 0) + (lane & 15) * FSTR);
        const GAS u64* fp3 = (const GAS u64*)(ws + (hh ? WS_OFLAG + ((64 + 4 * jo + t) * 32 + (lane & 15)) * 8 : window_flag(a)));
        PIN(vq0); PIN(vq1); PIN(vwo); PIN(a.vk); PIN(a.vv0); PIN(a.vv1); PIN(a.qp[0]); PIN(a.qp[1]); PIN(a.qp[2]); PIN(a.qp[3]); PIN(a.zp); PIN(fp); PIN(fp2); PIN(fp3); PIN(a.idx);
        SBAR();
        STAMP(30);
        glds16x2(vx0, vx1, x + k1 * 128 + 16384, mx + 16384 + 2048); glds16x2(vw0, vw1, wA + k1 * (64 * 3072), mw + 8192);
        STAMP(22);
        SBAR();
#pragma nounroll
        for (int s = 2; s < 8; ++s) { LBAR(6); DMA1(s);
            if (s == 2) STAMP(1); if (s == 3) STAMP(2);
        }
        const unsigned mq = lds0 + t * 2048;
        const char* wq = ((t >> 1) ? wB : wA) - 1024;
        const char* wy = wB + 128 * ((((p >> 1) ^ QSWAP) ^ (PBX >> 1)) - ((p >> 1) ^ QSWAP));
        LBAR(6); STAMP(14); DMA2(8);
        LBAR(4); STAMP(15); DMA2(9);
        a.tag = make_tag(*(const LAS u32x2*)(lds + L_EP));
        asm volatile("" : "+s"(a.tag));
        LBAR(4); STAMP(16); DMA2(10);
        const unsigned k7 = ((7 + rot) & 7) * (64 * 3072), sx = mq + L_Q15, sw = mw + 32768;
        LBAR(4); STAMP(17); glds16(vq0, wq + k7, sx); DMA2(11);
        f32x16 oo = lds_zero16(a.zp), o1 = lds_zero16(a.zp); PIN(oo); PIN(o1);
        LBAR(5); STAMP(18); glds16(vw0, wy + k7, sw); DMA2(12);
        f32x16 o2 = lds_zero16(a.zp), o3 = lds_zero16(a.zp); PIN(o2); PIN(o3);
        LBAR(5); STAMP(19); glds16(vq1, wq + k7 + 1024, sx + 1024); DMA2(13);
        f32x16 S1 = lds_zero16(a.zp); PIN(S1);
        LBAR(5); STAMP(21); glds16(vw1, wy + k7 + 1024, sw + 1024); DMA2(14);
        __amdgpu_buffer_rsrc_t rwo = RWO; unsigned wi0 = WI0;
        MK_SO();
        asm volatile("" : "+s"(a.rkv), "+s"(rwo), "+s"(a.sBeg), "+s"(wi0), "+s"(so0[0]), "+s"(so0[1]), "+s"(so0[2]), "+s"(so0[3]));
        __builtin_amdgcn_s_setprio(1);
        LBAR(5); STAMP(24);
        const u64 fv = __hip_atomic_load(fp, RLX_AGENT);
        LBAR(1); STAMP(29);
        wait_tags_end(fv, fp, a.tag, nullptr, lane);
        STAMP(5);
        KV3 kw;
        LDA_FIRST(kw);
        attn_full<1>(a, oo, o1, o2, o3, S1, so0[3], rwo, vwo, wi0, fp2, fp3, kw PROF_PASS);
        STAMP(23);
        attn_window<1>(a, kw, so0 PROF_PASS);
        STAMP(8);
        const int go = 4 * jo + t;
        lds_cptr wo = lds + L_WO + (4 * hh + ((lane & 15) >> 2)) * 64 + ((lane >> 4) & 1) * 32 + (lane & 3) * 8;
        const GAS u64* of_ = (const GAS u64*)(ws + WS_OFLAG);
#define OBR(n) ((n) >> 4 == 1 ? 0 : 1)
#define OHD(n) (2 * ((n) & 15) + ((n) >= 16))
#define OLD(n) BLOAD(a.ro, a.vk, (unsigned)(OBR(n) * 64 + go) * OS + OHD(n) * 1024)
#define OWF(n) tr_frag(wo + OBR(n) * 32768 + OHD(n) * 1024, wo + OBR(n) * 32768 + OHD(n) * 1024 + 512)
#define OIN 5
        bf16x8 oe[48];
#pragma unroll
        for (int n = 0; n < 48; ++n) {
            if (n == 16) { wait_tags(of_ + go * 32 + 16 + (lane & 15), a.tag, OFLAG(1, 1), lane); STAMP(9); }
            if (n == 32) wait_tags(of_ + (64 + go) * 32 + 16 + (lane & 15), a.tag);
            oe[n] = OLD(n); SBAR();
            if (n >= OIN - 1) __builtin_amdgcn_s_waitcnt(0x0F70 | (OIN - 1));
            SBAR();
        }
        bf16x8 wn[4];
        __builtin_amdgcn_s_waitcnt(0x0F70);
#define NEARLY 10
#define NMID 34
        unsigned sg[4]; lds_cptr wg[4];
#pragma unroll
        for (int q = 0; q < 4; ++q) { const int gq = ((p >> 2) + 1 + q) & 3; sg[q] = (unsigned)go * OS + gq * 8192; wg[q] = wo + gq * 8192; PIN(wg[q]); }
        unsigned vout = (32 * go + (lane >> 2)) * 1024 + 64 * po + (lane & 3) * 16;
        lds_ptr ow = lds + L_PART + (4 + t) * PREC + r32 * 80 + hh * 8;
        lds_cptr orr = lds + L_PART + (4 + t) * PREC + (lane >> 2) * 80 + (lane & 3) * 16;
        PIN(ow); PIN(orr);
        __amdgpu_buffer_rsrc_t rout = __builtin_amdgcn_make_buffer_rsrc(out, 0, 2048 * 1024, 0x00020000);
        PIN(vout); PIN(wo); asm volatile("" : "+s"(sg[0]), "+s"(sg[1]), "+s"(sg[2]), "+s"(sg[3]), "+s"(rout));
        f32x16 ya = f32x16{};
        bf16x8 wf[16], of[16];
#define FLD(i) of[i] = BLOAD(a.ro, a.vk, sg[(i) >> 2] + ((i) & 3) * 2048)
#define GRP_BAD(q) ({ unsigned mx_ = 0; _Pragma("unroll") for (int i = 4 * (q); i < 4 * (q) + 4; ++i) { const u32x4 u_ = __builtin_bit_cast(u32x4, of[i]); \
            mx_ = __builtin_elementwise_max(__builtin_elementwise_max(mx_, u_[0]), u_[1]); mx_ = __builtin_elementwise_max(__builtin_elementwise_max(mx_, u_[2]), u_[3]); } \
            __any(mx_ == ~0u); })
#define GRP_LD(q) do { _Pragma("unroll") for (int i = 4 * (q); i < 4 * (q) + 4; ++i) FLD(i); } while (0)
#define ESTEP 2
#define MSTEP 2
#define WF0 28
        f32x16 yb, yc, yd;
        unsigned pend = 15, mx3 = 0;
        STAMP(10);
        BAR();
#pragma unroll
        for (int n = 0; n < 4; ++n) { wn[n] = OWF(n); PIN(wn[n]); }
        BAR();
#pragma unroll
        for (int n = 0; n < 48; ++n) {
            ya = MFMA(wn[n & 3], oe[n], ya); SBAR();
            if (n >= NEARLY && n < NEARLY + 12 * ESTEP && (n - NEARLY) % ESTEP == 0) FLD(((n - NEARLY) / ESTEP) & 15);
            if (n >= NMID && n < NMID + 4 * MSTEP && (n - NMID) % MSTEP == 0) FLD(((n - NMID) / MSTEP + 12) & 15);
            if (n + 4 < 48) wn[n & 3] = OWF(n + 4);
            if (n >= WF0 && n < WF0 + 16) wf[(n - WF0) & 15] = tr_frag(wg[((n - WF0) & 15) >> 2] + ((n - WF0) & 3) * 2048, wg[((n - WF0) & 15) >> 2] + ((n - WF0) & 3) * 2048 + 512);
            if (n == 44) { yb = lds_zero16(a.zp); } if (n == 45) { yc = lds_zero16(a.zp); } if (n == 46) { yd = lds_zero16(a.zp); }
            SBAR();
        }
        STAMP(31);
#define GRP_TRY(q, Y) do { if (pend & (1u << (q))) { if (!GRP_BAD(q)) { \
            _Pragma("unroll") for (int i = 4 * (q); i < 4 * (q) + 4; ++i) Y = MFMA(wf[i], of[i], Y); pend ^= 1u << (q); } } } while (0)
        unsigned spins = SPIN_CAP;
#pragma nounroll
        do {
            GRP_TRY(0, ya); GRP_TRY(1, yb); GRP_TRY(2, yc); GRP_TRY(3, yd);
            asm volatile("s_sub_u32 %1, %1, 1\n\ts_cselect_b32 %0, 0, %0" : "+s"(pend), "+s"(spins) :: "memory", "scc");
            if (pend & 1) GRP_LD(0); if (pend & 2) GRP_LD(1); if (pend & 4) GRP_LD(2); if (pend & 8) GRP_LD(3);
        } while (pend);
        ya = (ya + yb) + (yc + yd);
#define OUT_AUX 16
#pragma unroll
        for (int q = 0; q < 4; ++q) *(LAS u32x2*)(ow + q * 16) = (u32x2){cvtpk(ya[4 * q], ya[4 * q + 1]), cvtpk(ya[4 * q + 2], ya[4 * q + 3])};
        asm volatile("" ::: "memory");
#pragma unroll
        for (int s = 0; s < 2; ++s) __builtin_amdgcn_raw_buffer_store_b128(*(const LAS u32x4*)(orr + s * 1280), rout, vout + s * 16384, 0, OUT_AUX);
        if (bid == 0 && tid == 256) __hip_atomic_store((GAS u64*)(ws + WS_EPOCH), a.tag, RLX_ST);
        STAMP(12);
    } else {
        glds16((((lane >> 3) + rot) & 7) * 128 + (lane & 7) * 16, gw, lds0 + L_GW + t * 1024);
#pragma unroll
        for (int m = 0; m < 2; ++m) {
            f32x4 mk;
#pragma unroll
            for (int i = 0; i < 4; ++i) { const bool le = (i + 8 * t + 4 * hh) <= r32; mk[i] = (le != (m != 0)) ? 0.f : -__builtin_inff(); }
            *(LAS f32x4*)(lds + L_MDIAG + m * 4096 + t * 1024 + lane * 16) = mk;
        }
        if (tid < 4) *(LAS unsigned*)(lds + L_CNT + tid * 4) = 0;
        *(LAS unsigned*)(lds + L_Z + tid * 4) = 0;
        u64 tag;
        STAMP(21);
        {
            const __amdgpu_buffer_rsrc_t rkv = __builtin_amdgcn_make_buffer_rsrc(ws + WS_KV, 0, KV_BYTES, 0x00020000);
            const __amdgpu_buffer_rsrc_t ro = __builtin_amdgcn_make_buffer_rsrc(ws + WS_O, 0, O_BYTES, 0x00020000);
            if (e) project<1>(lds, ws, rkv, ro, lane, t, p, j, tag PROF_PASS); else project<0>(lds, ws, rkv, ro, lane, t, p, j, tag PROF_PASS);
        }
        cnt_wait(lds + L_CNT + 8);
        __builtin_amdgcn_s_sleep(BSLP);
        STAMP(24);
        MK_ATT(0);
        a.tag = tag;
        wait_tags((const GAS u64*)(ws + window_flag(a)), tag);
        STAMP(25);
        KV3 kw;
        LDB_FIRST(kw);
        MK_SO();
        attn_window<0>(a, kw, so0 PROF_PASS);
        STAMP(23);
        PIN(a.idx);
        attn_full<0>(a, lds_zero16(a.zp), lds_zero16(a.zp), lds_zero16(a.zp), lds_zero16(a.zp), lds_zero16(a.zp), so0[3], RWO, VWO, WI0, nullptr, nullptr, kw PROF_PASS);
        BAR();
        STAMP(8);
    }
    DRAIN();
    STAMP(13);
    if (c == RROLE) while (__builtin_amdgcn_s_memrealtime() < stv.r0 + 3500 + stv.d) __builtin_amdgcn_s_sleep(1);
}

extern "C" void kernel_launch(void* const* d_in, const int* in_sizes, int n_in, void* d_out, int out_size, void* d_ws, size_t ws_size,
                              hipStream_t stream) {
    (void)in_sizes; (void)n_in; (void)out_size; (void)ws_size;
    (void)hipFuncSetAttribute((const void*)gmix_kernel, hipFuncAttributeMaxDynamicSharedMemorySize, L_END);
    gmix_kernel<<<256, 512, L_END, stream>>>((const char*)d_in[0], (const char*)d_in[1], (const char*)d_in[2], (const char*)d_in[4],
                                             (const char*)d_in[5], (const char*)d_in[6], (char*)d_out, (char*)d_ws);
}
```

```cpp
#include <hip/hip_runtime.h>
#include <hip/hip_bf16.h>

typedef __attribute__((ext_vector_type(8))) __bf16 bf16x8;
typedef __attribute__((ext_vector_type(16))) __bf16 bf16x16;
typedef __attribute__((ext_vector_type(16))) float f32x16;
typedef __attribute__((ext_vector_type(8))) float f32x8;
typedef __attribute__((ext_vector_type(4))) float f32x4;
typedef __attribute__((ext_vector_type(4))) unsigned u32x4;
typedef __attribute__((ext_vector_type(2))) unsigned u32x2;
typedef __attribute__((ext_vector_type(2))) __bf16 bf16x2;
typedef short s16x4 __attribute__((ext_vector_type(4)));
typedef unsigned long long u64;
#define GAS __attribute__((address_space(1)))
#define LAS __attribute__((address_space(3)))
typedef LAS const char* lds_cptr;
typedef LAS char* lds_ptr;

constexpr unsigned WS_EPOCH = 0;
constexpr unsigned FSTR = 8;
constexpr unsigned WS_OFLAG = 65536;
constexpr unsigned WS_KVFLAG = 131072;
constexpr unsigned WS_KV = 1u << 20;
constexpr unsigned TS = 2176;
constexpr unsigned KV_BYTES = 2u * 32u * 64u * TS;
constexpr unsigned WS_O = 10u << 20;
constexpr unsigned OS = 32896;
constexpr unsigned O_BYTES = 128u * OS;
constexpr unsigned OOB = 0x7ff00000u;
constexpr unsigned L_W = 65536;
constexpr unsigned L_WO = 0, L_PART = 65536, PREC = 7168, L_Q14 = 106496, L_Q15 = 114688, L_MDIAG = 122880, L_MFAR = 126976, L_Q = 131072,
                   L_GW = 147456, L_Z = 151552, L_G = 152576, L_CNT = 153088, L_EP = 153344, L_SCR = 153600, L_END = 154112;
constexpr float QSCALE = 0.25f * 1.4426950408889634f;
constexpr float LMAX = 1.2676506e30f, LMIN = 9.094947e-13f;
constexpr unsigned SPIN_CAP = 1u << 17;

#define SBAR() __builtin_amdgcn_sched_barrier(0)
#define PIN(x) asm volatile("" : "+v"(x))
#define MFMA(a, b, c) __builtin_amdgcn_mfma_f32_32x32x16_bf16(a, b, c, 0, 0, 0)
#define BAR() asm volatile("s_barrier" ::: "memory")
#define CBAR() asm volatile("s_waitcnt lgkmcnt(0)\n\ts_barrier" ::: "memory")
#define DRAIN() asm volatile("s_waitcnt vmcnt(0)" ::: "memory")
#define PACE() asm volatile("s_nop 6" ::: "memory")
#define RLX_AGENT __ATOMIC_RELAXED, __HIP_MEMORY_SCOPE_AGENT

__device__ __forceinline__ void glds16(unsigned voff, const void* sbase, unsigned ldsaddr) {
    asm volatile("s_mov_b32 m0, %2\n\ts_nop 0\n\tglobal_load_lds_dwordx4 %0, %1" ::"v"(voff), "s"(sbase), "s"(ldsaddr) : "memory");
}
__device__ __forceinline__ void glds16x2(unsigned v0, unsigned v1, const void* sbase, unsigned ldsaddr) {
    asm volatile("s_mov_b32 m0, %3\n\ts_nop 0\n\tglobal_load_lds_dwordx4 %0, %2\n\tglobal_load_lds_dwordx4 %1, %2 offset:1024"
                 ::"v"(v0), "v"(v1), "s"(sbase), "s"(ldsaddr) : "memory");
}
__device__ __forceinline__ void glds16x4(unsigned v0, unsigned v1, unsigned v2, unsigned v3, const void* sbase, unsigned ldsaddr) {
    asm volatile("s_mov_b32 m0, %5\n\ts_nop 0\n\tglobal_load_lds_dwordx4 %0, %4\n\tglobal_load_lds_dwordx4 %1, %4 offset:1024\n\t"
                 "global_load_lds_dwordx4 %2, %4 offset:2048\n\tglobal_load_lds_dwordx4 %3, %4 offset:3072"
                 ::"v"(v0), "v"(v1), "v"(v2), "v"(v3), "s"(sbase), "s"(ldsaddr) : "memory");
}
__device__ __forceinline__ unsigned cvtpk(float lo, float hi) {
    typedef __attribute__((ext_vector_type(2))) float f32x2;
    return __builtin_bit_cast(unsigned, __builtin_convertvector((f32x2){lo, hi}, bf16x2));
}
__device__ __forceinline__ bf16x8 as_frag(u32x4 v) { return __builtin_bit_cast(bf16x8, v); }
__device__ __forceinline__ bf16x8 lds_frag(lds_cptr p) { return *(const LAS bf16x8*)p; }
__device__ __forceinline__ bf16x8 tr_frag(lds_cptr p0, lds_cptr p1) {
    s16x4 a = __builtin_amdgcn_ds_read_tr16_b64_v4i16((LAS s16x4*)p0);
    s16x4 b = __builtin_amdgcn_ds_read_tr16_b64_v4i16((LAS s16x4*)p1);
    return __builtin_bit_cast(bf16x8, __builtin_shufflevector(a, b, 0, 1, 2, 3, 4, 5, 6, 7));
}
__device__ __forceinline__ f32x16 pv_lo(bf16x8 v, const f32x16& p, f32x16 acc, int idx) {
    f32x8 h = __builtin_shufflevector(p, p, 0, 1, 2, 3, 4, 5, 6, 7);
    return __builtin_amdgcn_smfmac_f32_32x32x32_bf16(v, __builtin_bit_cast(bf16x16, h), acc, idx, 0, 0);
}
__device__ __forceinline__ f32x16 pv_hi(bf16x8 v, const f32x16& p, f32x16 acc, int idx) {
    f32x8 h = __builtin_shufflevector(p, p, 8, 9, 10, 11, 12, 13, 14, 15);
    return __builtin_amdgcn_smfmac_f32_32x32x32_bf16(v, __builtin_bit_cast(bf16x16, h), acc, idx, 0, 0);
}
#define MX3(a, b, c) __builtin_fmaxf(__builtin_fmaxf((a), (b)), (c))
__device__ __forceinline__ float max16(const f32x16& s) {
    float a = MX3(s[0], s[1], s[2]), b = MX3(s[3], s[4], s[5]);
    a = MX3(a, s[6], s[7]); b = MX3(b, s[8], s[9]); a = MX3(a, s[10], s[11]); b = MX3(b, s[12], s[13]);
    return MX3(a, b, __builtin_fmaxf(s[14], s[15]));
}
__device__ __forceinline__ float halfmax(float m) {
    auto r = __builtin_amdgcn_permlane32_swap(__float_as_uint(m), __float_as_uint(m), false, false);
    return __builtin_fmaxf(__uint_as_float(r[0]), __uint_as_float(r[1]));
}
__device__ __forceinline__ f32x16 lds_f32x16(lds_cptr p) {
    f32x4 a = *(const LAS f32x4*)p, b = *(const LAS f32x4*)(p + 1024), c = *(const LAS f32x4*)(p + 2048), d = *(const LAS f32x4*)(p + 3072);
    f32x8 lo = __builtin_shufflevector(a, b, 0, 1, 2, 3, 4, 5, 6, 7), hi = __builtin_shufflevector(c, d, 0, 1, 2, 3, 4, 5, 6, 7);
    return __builtin_shufflevector(lo, hi, 0, 1, 2, 3, 4, 5, 6, 7, 8, 9, 10, 11, 12, 13, 14, 15);
}
constexpr int SC1 = 16;
#define BLOAD(rs, vo, so) as_frag(__builtin_amdgcn_raw_buffer_load_b128(rs, vo, so, SC1))
#define BSTORE(v, rs, vo, so) __builtin_amdgcn_raw_buffer_store_b128(v, rs, (vo) + (so), 0, SC1)

struct Tags { __amdgpu_buffer_rsrc_t rs; u64 tag; u32x2 vtag; unsigned v0; };
__device__ __forceinline__ u64 tag_load(const Tags& f, unsigned vo, unsigned so) {
    return __builtin_bit_cast(u64, __builtin_amdgcn_raw_buffer_load_b64(f.rs, vo, so, SC1));
}
__device__ __forceinline__ void tag_raise(const Tags& f, unsigned so) {
    DRAIN();
    __builtin_amdgcn_raw_buffer_store_b64(f.vtag, f.rs, f.v0, so, SC1);
}
__device__ __forceinline__ void tag_wait(const Tags& f, u64 v, unsigned vo, unsigned so) {
    if (__builtin_expect(!__all(v == f.tag), 0)) {
#pragma nounroll
        for (unsigned spins = 0; spins < SPIN_CAP; ++spins) {
            __builtin_amdgcn_s_sleep(8);
            asm volatile("" ::: "memory");
            if (__all(tag_load(f, vo, so) == f.tag)) break;
        }
    }
    asm volatile("" ::: "memory");
}
__device__ __forceinline__ void tag_wait(const Tags& f, unsigned vo, unsigned so) { tag_wait(f, tag_load(f, vo, so), vo, so); }

__device__ __forceinline__ void cnt_arrive(lds_ptr cnt, int lane) {
    asm volatile("s_waitcnt lgkmcnt(0)" ::: "memory");
    if (lane == 0) __hip_atomic_fetch_add((LAS unsigned*)cnt, 1u, __ATOMIC_RELAXED, __HIP_MEMORY_SCOPE_WORKGROUP);
}
__device__ __forceinline__ void cnt_wait(lds_ptr cnt) {
#pragma nounroll
    for (unsigned spins = 0; spins < (1u << 22); ++spins) {
        if (__builtin_amdgcn_readfirstlane(__hip_atomic_load((LAS unsigned*)cnt, __ATOMIC_RELAXED, __HIP_MEMORY_SCOPE_WORKGROUP)) == 4) break;
        __builtin_amdgcn_s_sleep(1);
    }
    asm volatile("" ::: "memory");
}
__device__ __forceinline__ unsigned kvflag(int br, int quad, int tile, int type) {
    return WS_KVFLAG + (unsigned)(((((br * 8 + quad) * 4 + (tile & 3)) * 2 + (tile >> 5)) * 2 + type) * 8 + ((tile >> 2) & 7)) * FSTR;
}

__device__ __forceinline__ void slow_rows(__amdgpu_buffer_rsrc_t rs, unsigned sbase, int lo, int hi, int g, bool band, bf16x8 q,
                                          unsigned vk, unsigned vv0, unsigned vv1, lds_cptr lds, int lane, int idx, f32x16& acc) {
    float m = -__builtin_inff();
    for (int pass = 0; pass < 2; ++pass) {
        if (pass) { m = halfmax(m); acc = f32x16{}; }
        for (int kt = lo; kt <= hi; ++kt) {
            const unsigned so = sbase + (unsigned)kt * TS;
            const bf16x8 k = BLOAD(rs, vk, so), v0 = BLOAD(rs, vv0, so), v1 = BLOAD(rs, vv1, so);
            f32x16 c = f32x16{};
            if (band && kt == g) c = lds_f32x16(lds + L_MDIAG + lane * 16);
            if (band && kt == g - 8) c = lds_f32x16(lds + L_MFAR + lane * 16);
            f32x16 s = MFMA(k, q, c);
            if (!pass) m = __builtin_fmaxf(m, max16(s));
            else {
#pragma unroll
                for (int r = 0; r < 16; ++r) s[r] = __builtin_amdgcn_exp2f(s[r] - m);
                acc = pv_lo(v0, s, acc, idx); acc = pv_hi(v1, s, acc, idx);
            }
        }
    }
}

#define PK4(A, o) ((u32x4){cvtpk(A[o], A[o + 1]), cvtpk(A[o + 2], A[o + 3]), cvtpk(A[o + 4], A[o + 5]), cvtpk(A[o + 6], A[o + 7])})
__device__ __forceinline__ void make_tag(Tags& f, u32x2 e) {
    f.tag = (((u64)(unsigned)__builtin_amdgcn_readfirstlane(e[1]) << 32) | (unsigned)__builtin_amdgcn_readfirstlane(e[0])) * 0x9E3779B97F4A7C15ull + 0x632BE59BD9B4E019ull;
    asm volatile("" : "+s"(f.tag));
    f.vtag = __builtin_bit_cast(u32x2, f.tag);
    PIN(f.vtag);
}
template <int E>
__device__ __forceinline__ u32x4 pack_kv(int i, const f32x16& a) {
    const int s = i & 1;
    if (E == 0) return PK4(a, 8 * s);
    auto x = __builtin_amdgcn_permlane32_swap(cvtpk(a[8 * s], a[8 * s + 1]), cvtpk(a[8 * s + 4], a[8 * s + 5]), false, false);
    auto y = __builtin_amdgcn_permlane32_swap(cvtpk(a[8 * s + 2], a[8 * s + 3]), cvtpk(a[8 * s + 6], a[8 * s + 7]), false, false);
    return (u32x4){x[0], y[0], x[1], y[1]};
}
#define PUT_KV(i, v, vo) BSTORE(v, rkv, vo, ((i) < 2 ? 64 * TS : 0) + ((i) & 1) * (E ? 512 : 128 * TS))
constexpr int QSWAP = 1;
constexpr int PBX = 4;
template <int E>
__device__ __forceinline__ void project(lds_ptr lds, __amdgpu_buffer_rsrc_t rkv, __amdgpu_buffer_rsrc_t ro, int lane, int t, int p, int j, Tags& fl) {
    const int r32 = lane & 31, hh = lane >> 5, qd4 = (p >> 1) ^ QSWAP;
    const unsigned q4 = (lane & 15) >> 2, wcol = ((lane >> 4) & 1) * 32 + (lane & 3) * 8;
    lds_cptr xa[4];
#pragma unroll
    for (int ks = 0; ks < 4; ++ks) xa[ks] = lds + (32 * t + r32) * 128 + (((hh ^ ((r32 >> 1) & 7)) * 16) ^ (ks * 32));
    unsigned w1o = L_W + (8 * hh + q4) * 128 + ((lane >> 4) & 1) * 64 + (q4 >> 1) * 32 + (lane & 3) * 8, w0o = w1o ^ 32;
    PIN(w0o); PIN(w1o);
    const lds_cptr w0 = lds + w0o, w1 = lds + w1o, wq = lds + (8 * hh + q4) * 64 + wcol;
#define XRD(n) lds_frag(xa[(n) & 3] + (((n) >> 2) & 3) * 16384)
#define WRD(b, n) tr_frag(b + ((n) & 15) * 2048, b + ((n) & 15) * 2048 + 512)
#define YOF(n) ((n) >= 28 ? 32768 + ((n) & 3) * 2048 : ((n) & 15) * 2048)
#define YRD(b, n) tr_frag(b + YOF(n), b + YOF(n) + 512)
#define QOF(br, n) (((n) >= 28 ? L_Q15 - L_Q14 : ((n) >= 24 ? 0 : (((n) >> 2) & 3) * 16384)) + (br) * 4096 + (((n) & 3) ^ E) * 1024)
#define QRD(br, n) tr_frag(((n) >= 24 ? wqh : wq) + QOF(br, n), ((n) >= 24 ? wqh : wq) + QOF(br, n) + 256)
#define MM(W, X, A) (E ? MFMA(X, W, A) : MFMA(W, X, A))
    bf16x8 xf[32], f0[4], f1[4];
    f32x16 a0 = f32x16{}, a1 = f32x16{};
    unsigned gwo = (lane & 15) == ((lane >> 4) & 1) ? L_GW + t * 1024 + hh * 16 : L_Z;
    PIN(gwo);
    f32x4 zg = f32x4{};
    bf16x8 gf[4];
#define RD1(n) do { xf[n] = XRD(n); f0[(n) & 3] = WRD(w0, n); f1[(n) & 3] = WRD(w1, n); SBAR(); } while (0)
#define GRD(n) gf[(n) & 3] = lds_frag(lds + gwo + (n) * 32)
#define GMM(n) zg = __builtin_amdgcn_mfma_f32_16x16x32_bf16(gf[(n) & 3], xf[n], zg, 0, 0, 0)
#define MM1(n) do { a0 = MM(f0[(n) & 3], xf[n], a0); a1 = MM(f1[(n) & 3], xf[n], a1); SBAR(); } while (0)
#pragma unroll
    for (int s = 0; s < 8; ++s) {
        if (s == 0) CBAR(); else BAR();
        SBAR();
        RD1(4 * s); RD1(4 * s + 1);
        if (s) { MM1(4 * s - 1); GMM(4 * s - 4); GMM(4 * s - 3); GMM(4 * s - 2); GMM(4 * s - 1); SBAR(); }
        RD1(4 * s + 2); MM1(4 * s); RD1(4 * s + 3); MM1(4 * s + 1);
        if (s == 0) DRAIN();
        GRD(4 * s); GRD(4 * s + 1); GRD(4 * s + 2); GRD(4 * s + 3); SBAR();
        MM1(4 * s + 2);
        SBAR();
    }
    const unsigned tile = 4 * j + t;
    unsigned vo = (unsigned)(4 * qd4 * 64 + tile) * TS + (E ? (r32 >> 4) * (128 * TS) + 1024 + hh * 256 + (r32 & 15) * 16 : lane * 16);
    unsigned wqo = L_Q14 + (8 * hh + q4) * 64 + wcol;
    PIN(vo); PIN(wqo);
    const lds_cptr wqh = lds + wqo;
    MM1(31); GMM(28); GMM(29); GMM(30); GMM(31); SBAR();
    u32x4 pk[4];
#pragma unroll
    for (int i = 0; i < 4; ++i) { pk[i] = pack_kv<E>(i, i < 2 ? a0 : a1); PIN(pk[i]); if (i < 2) PUT_KV(i, pk[i], vo); SBAR(); }
    if (!E) BSTORE(((u32x4){0x3f803f80u, 0x3f803f80u, 0x3f803f80u, 0x3f803f80u}), rkv,
                   (unsigned)((hh * 32 + 4 * (qd4 ^ (hh * (PBX >> 1))) + (r32 >> 3)) * 64 + tile) * TS + 2048 + (lane & 7) * 16, 0);
    { LAS float* gp = (LAS float*)(lds + (lane < 16 ? L_G + t * 128 : L_SCR)) + lane;
      gp[0] = __builtin_amdgcn_rcpf(1.f + __expf(-zg[0])); gp[16] = __builtin_amdgcn_rcpf(1.f + __expf(-zg[1])); }
    SBAR();
    BAR(); SBAR();
    bf16x8 u0 = QRD(0, 0), u1 = QRD(1, 0), y0 = YRD(w0, 0), y1 = YRD(w1, 0);
    const u32x2 tg = *(const LAS u32x2*)(lds + L_EP);
    f32x16 b0 = f32x16{}, b1 = f32x16{}, aQ0 = f32x16{}, aQ1 = f32x16{};
    SBAR();
#pragma unroll
    for (int n = 0; n < 24; ++n) {
        aQ0 = MFMA(u0, xf[n], aQ0); SBAR();
        if (n == 1) { make_tag(fl, tg); SBAR(); }
        if (n == 7) { tag_raise(fl, kvflag(0, qd4, tile, E)); SBAR(); }
        if ((n & 3) == 3) { BAR(); SBAR(); }
        u0 = QRD(0, n + 1); SBAR();
        aQ1 = MFMA(u1, xf[n], aQ1); SBAR();
        u1 = n < 23 ? QRD(1, n + 1) : QRD(0, 25); SBAR();
        b0 = MM(y0, xf[n], b0); SBAR(); y0 = n < 23 ? YRD(w0, n + 1) : QRD(0, 26); SBAR();
        b1 = MM(y1, xf[n], b1); SBAR(); y1 = n < 23 ? YRD(w1, n + 1) : QRD(0, 27); SBAR();
    }
    aQ0 = MFMA(u0, xf[24], aQ0); SBAR(); u0 = QRD(0, 28); SBAR(); aQ0 = MFMA(u1, xf[25], aQ0); SBAR(); u1 = QRD(0, 29); SBAR();
    aQ0 = MFMA(y0, xf[26], aQ0); SBAR(); y0 = QRD(0, 30); SBAR(); aQ0 = MFMA(y1, xf[27], aQ0); SBAR(); y1 = QRD(0, 31); SBAR();
    aQ0 = MFMA(u0, xf[28], aQ0); SBAR(); aQ0 = MFMA(u1, xf[29], aQ0); SBAR(); u1 = QRD(1, 24); SBAR();
    aQ0 = MFMA(y0, xf[30], aQ0); SBAR(); y0 = YRD(w0, 24); SBAR(); aQ0 = MFMA(y1, xf[31], aQ0); SBAR(); y1 = YRD(w1, 24); SBAR();
    const lds_ptr qd = lds + L_Q + t * 1024 + lane * 16;
    constexpr int QBAR = 4;
    constexpr int NFREE = 3;
    f32x8 qs; u32x4 qk;
#define QEPI(m) do { if ((m) == 1) { PIN(aQ0); qs = __builtin_shufflevector(aQ0, aQ0, 8, 9, 10, 11, 12, 13, 14, 15) * QSCALE; PIN(qs); } \
        if ((m) == 2) { qk = PK4(qs, 0); *(LAS u32x4*)(qd + 4096) = qk; } \
        if ((m) == QBAR) { __builtin_amdgcn_s_setprio(2); __builtin_amdgcn_s_waitcnt(0xC07F | ((2 * (QBAR - 2)) << 8)); BAR(); } \
        if ((m) > QBAR + NFREE) PACE(); SBAR(); } while (0)
#pragma unroll
    for (int n = 24; n < 32; ++n) {
        aQ1 = MFMA(u1, xf[n], aQ1); SBAR(); if (n < 31) u1 = QRD(1, n + 1); SBAR(); QEPI(3 * (n - 24));
        b0 = MM(y0, xf[n], b0); SBAR(); if (n < 31) y0 = YRD(w0, n + 1); SBAR(); QEPI(3 * (n - 24) + 1);
        b1 = MM(y1, xf[n], b1); SBAR(); if (n < 31) y1 = YRD(w1, n + 1); SBAR(); QEPI(3 * (n - 24) + 2);
    }
#pragma unroll
    for (int r = 0; r < 8; ++r) aQ0[r] *= QSCALE;
#pragma unroll
    for (int r = 0; r < 16; ++r) aQ1[r] *= QSCALE;
    *(LAS u32x4*)qd = PK4(aQ0, 0); *(LAS u32x4*)(qd + 8192) = PK4(aQ1, 0); *(LAS u32x4*)(qd + 12288) = PK4(aQ1, 8);
    cnt_arrive(lds + L_CNT + 8, lane);
    const unsigned vo2 = vo + (32 + 4 * ((qd4 ^ (PBX >> 1)) - qd4)) * 64 * TS;
    u32x4 py[4] = {pack_kv<E>(0, b0), pack_kv<E>(1, b0), pack_kv<E>(2, b1), pack_kv<E>(3, b1)};
    PIN(py[0]); PIN(py[1]); PIN(py[2]); PIN(py[3]);
    constexpr int YSLP = 8, YSTAG = 2;
    if (t & 1) __builtin_amdgcn_s_sleep(YSTAG);
    if (t & 2) __builtin_amdgcn_s_sleep(2 * YSTAG);
    PUT_KV(2, pk[2], vo); __builtin_amdgcn_s_sleep(YSLP); PUT_KV(3, pk[3], vo); __builtin_amdgcn_s_sleep(YSLP);
    PUT_KV(0, py[0], vo2); __builtin_amdgcn_s_sleep(YSLP); PUT_KV(1, py[1], vo2); __builtin_amdgcn_s_sleep(YSLP);
    PUT_KV(2, py[2], vo2); __builtin_amdgcn_s_sleep(YSLP); PUT_KV(3, py[3], vo2);
    tag_raise(fl, kvflag(1, qd4, tile, E));
    BSTORE(((u32x4){~0u, ~0u, ~0u, ~0u}), ro, lane * 16, tile * OS + 2 * p * 1024);
}

constexpr int CODE_LINES = 280;

struct Att {
    lds_ptr lds; char* ws; __amdgpu_buffer_rsrc_t rkv, ro, rka;
    Tags fl; int lane, t, p, g, head, idx; unsigned vk, vv0, vv1; lds_cptr zp, qp[4];
};
#define IDX a.idx
#define LD3(K, V0, V1, rs, so) do { K = BLOAD(rs, a.vk, so); V0 = BLOAD(rs, a.vv0, so); V1 = BLOAD(rs, a.vv1, so); } while (0)
#define EX(X, r) X[r] = __builtin_amdgcn_exp2f(X[r])
#define STEP(X, Y, OP, VP, KN, QN, CN, O, V0, F1, F3, F2) do { \
        EX(X, 0); EX(X, 1); SBAR(); OP = pv_hi(VP, Y, OP, IDX); SBAR(); F1; SBAR(); EX(X, 2); EX(X, 3); EX(X, 4); SBAR(); Y = MFMA(KN, QN, CN); SBAR(); F3; SBAR(); \
        EX(X, 5); EX(X, 6); EX(X, 7); SBAR(); EX(X, 8); EX(X, 9); SBAR(); O = pv_lo(V0, X, O, IDX); SBAR(); F2; SBAR(); \
        EX(X, 10); EX(X, 11); EX(X, 12); EX(X, 13); EX(X, 14); EX(X, 15); SBAR(); } while (0)

__device__ __forceinline__ f32x16 lds_zero16(lds_cptr zp) {
    const volatile LAS f32x4* z = (const volatile LAS f32x4*)zp;
    const f32x4 a = *z, b = *z, c = *z, d = *z;
    f32x8 lo = __builtin_shufflevector(a, b, 0, 1, 2, 3, 4, 5, 6, 7), hi = __builtin_shufflevector(c, d, 0, 1, 2, 3, 4, 5, 6, 7);
    return __builtin_shufflevector(lo, hi, 0, 1, 2, 3, 4, 5, 6, 7, 8, 9, 10, 11, 12, 13, 14, 15);
}

template <int C, int BR>
__device__ __forceinline__ void finish(const Att& a, f32x16 o, float gf, bf16x8 q, unsigned vo) {
    f32x8 y = __builtin_shufflevector(o, o, 0, 1, 2, 3, 4, 5, 6, 7) * (__builtin_amdgcn_rcpf(o[8]) * gf);
    PIN(y);
    const float chk = __builtin_fmaf(((y[0] + y[2]) + (y[4] + y[6])) + ((y[1] + y[3]) + (y[5] + y[7])), 0.f, o[8]);
    if (__builtin_expect(__any(__builtin_amdgcn_fmed3f(chk, LMIN, LMAX) != chk), 0)) {
        if (!BR) { tag_wait(a.fl, a.lane * FSTR, kvflag(!C, a.p >> 1, 0, 0)); tag_wait(a.fl, a.lane * FSTR, kvflag(!C, a.p >> 1, 2, 0)); }
        slow_rows(a.rkv, (unsigned)((BR * 32 + (a.head ^ (BR * 2 * PBX))) * 64) * TS, BR ? (a.g >= 8 ? a.g - 8 : 0) : 0, BR ? a.g : 63, a.g, BR, q, a.vk, a.vv0, a.vv1, a.lds, a.lane, IDX, o);
        y = __builtin_shufflevector(o, o, 0, 1, 2, 3, 4, 5, 6, 7) * (__builtin_amdgcn_rcpf(o[8]) * gf);
        PIN(y);
    }
    u32x4 v = (u32x4){cvtpk(y[0], y[1]), cvtpk(y[2], y[3]), cvtpk(y[4], y[5]), cvtpk(y[6], y[7])};
    if (!C && !BR) v = __builtin_elementwise_min(v, (u32x4){~1u, ~1u, ~1u, ~1u});
    BSTORE(v, a.ro, vo, 0);
}
#define OFLAG(BR, C) (WS_OFLAG + ((((BR) * 64 + a.g) * 2 + (C)) * 16 + (a.p ^ ((BR) * PBX))) * 8)
#define OVOFF(BR) (a.vk + (unsigned)((BR) * 64 + a.g) * OS + (a.head ^ ((BR) * 2 * PBX)) * 1024)
#define GATE() (*(const LAS float*)(a.lds + L_G + a.t * 128 + (a.lane & 31) * 4))

struct KV3 { bf16x8 k[3], v0[3], v1[3]; };
#define LDB(K, V0, V1, i) do { const int kt_ = a.g - (i); \
        const __amdgpu_buffer_rsrc_t rb_ = __builtin_amdgcn_make_buffer_rsrc(a.ws + WS_KV, 0, kt_ >= 0 ? KV_BYTES : 0u, 0x00020000); \
        LD3(K, V0, V1, rb_, (unsigned)((32 + (a.head ^ (2 * PBX))) * 64 + (kt_ >= 0 ? kt_ : 0)) * TS); } while (0)
#define LDB_FIRST(f) do { LDB(f.k[0], f.v0[0], f.v1[0], 0); SBAR(); LDB(f.k[1], f.v0[1], f.v1[1], 1); SBAR(); LDB(f.k[2], f.v0[2], f.v1[2], 2); SBAR(); } while (0)
#define LDA_FIRST(f) do { LD3(f.k[0], f.v0[0], f.v1[0], a.rka, so0[0]); SBAR(); LD3(f.k[1], f.v0[1], f.v1[1], a.rka, so0[1]); SBAR(); \
        LD3(f.k[2], f.v0[2], f.v1[2], a.rka, so0[2]); SBAR(); } while (0)
__device__ __forceinline__ unsigned window_flag(const Att& a) {
    const int i = a.lane & 15, kt = a.g - (i < 9 ? i : 0);
    return kvflag(1, a.p >> 1, kt >= 0 ? kt : 0, (a.lane >> 4) & 1);
}

template <int C>
__device__ __forceinline__ void attn_full(const Att& a, f32x16 o0, f32x16 o1, f32x16 o2, f32x16 o3, f32x16 S1, unsigned so,
                                          __amdgpu_buffer_rsrc_t rwo, unsigned vwo, unsigned wi, unsigned v16, unsigned fs2, unsigned vf3, KV3& nxt) {
    const lds_ptr lds = a.lds; const int t = a.t, lane = a.lane;
    const f32x16 zc = f32x16{};
    bf16x8 ka = nxt.k[0], va0 = nxt.v0[0], va1 = nxt.v1[0], kb = nxt.k[1], vb0 = nxt.v0[1], vb1 = nxt.v1[1], kc = nxt.k[2], vc0 = nxt.v0[2], vc1 = nxt.v1[2], kd, vd0, vd1;
    const unsigned jb = a.g >> 2;
    unsigned ki = 3;
    vd1 = lds_frag(a.zp);
    lds_ptr pw = lds + L_PART + (4 * C + t) * PREC + lane * 16;
    lds_cptr pr0 = lds + L_PART + (4 * C + ((t - 1) & 3)) * PREC + lane * 16, pr1 = lds + L_PART + (4 * C + ((t - 2) & 3)) * PREC + lane * 16,
             pr2 = lds + L_PART + (4 * C + ((t - 3) & 3)) * PREC + lane * 16;
    unsigned ovo = OVOFF(0); float gf = 0.f;
    if (C) { BAR(); }
    else { gf = GATE(); PIN(pw); PIN(pr0); PIN(pr1); PIN(pr2); PIN(ovo); PIN(gf);
           tag_raise(a.fl, OFLAG(1, 0)); }
    bf16x8 q0 = lds_frag(a.qp[0]); SBAR();
    bf16x8 q1 = lds_frag(a.qp[1]), q2 = lds_frag(a.qp[2]), q3 = lds_frag(a.qp[3]);
    SBAR();
    f32x16 S0 = MFMA(ka, q0, zc);
    PIN(S0); SBAR();
    PIN(q1); PIN(q2); PIN(q3); PIN(vd1); PIN(o0); PIN(o1); PIN(o2); PIN(o3); PIN(S1);
    __builtin_amdgcn_s_waitcnt(0x0F76);
#pragma unroll
    for (int i = 0; i < 8; ++i) __builtin_amdgcn_s_setprio(C);
    SBAR();
    unsigned wso = 0;
    u64 fv = __builtin_bit_cast(u64, a.fl.vtag);
#define NEXT1() asm volatile("s_add_i32 %0, %0, 1\n\ts_xor_b32 %1, %0, %2" : "+s"(ki), "=s"(so) : "s"(jb) : "scc")
#define NEXT2() asm volatile("s_mulk_i32 %0, %1" : "+s"(so) : "i"(4 * TS) : "scc")
#define WO_OFF() do { if (C) asm volatile("s_lshl_b32 %0, %1, 4" : "=s"(wso) : "s"(wi) : "scc"); } while (0)
#define WO_DMA() do { if (C) __builtin_amdgcn_raw_ptr_buffer_load_lds(rwo, (LAS void*)(lds + L_WO + t * 16384 + wi), 16, vwo, wso, 0, 0); } while (0)
#define WO_NEXT() do { if (C) asm volatile("s_addk_i32 %0, 0x400\n\ts_and_b32 %0, %0, 0x3fff" : "+s"(wi) :: "scc"); } while (0)
#define TILE(K, V0, V1, KN, LK, LV0, LV1, NW, G) do { \
        STEP(S0, S1, o3, LV1, K, q1, zc, o0, V0, LK = BLOAD(a.rka, a.vk, so), WO_OFF(), LV0 = BLOAD(a.rka, a.vv0, so)); \
        STEP(S1, S0, o0, V1, K, q2, zc, o1, V0, LV1 = BLOAD(a.rka, a.vv1, so), , WO_DMA()); \
        STEP(S0, S1, o1, V1, K, q3, zc, o2, V0, NEXT1(), NEXT2(), ); \
        STEP(S1, S0, o2, V1, KN, q0, zc, o3, V0, __builtin_amdgcn_s_waitcnt(0x0F70 | (NW)), G, WO_NEXT()); } while (0)
#define PUTA(O, sl) (*(LAS f32x4*)(pw + (sl) * 2048) = __builtin_shufflevector(O, O, 0, 1, 2, 3))
#define PUTB(O, sl) (*(LAS f32x4*)(pw + (sl) * 2048 + 1024) = __builtin_shufflevector(O, O, 4, 5, 6, 7))
#define PUTL(O, sl) (*(LAS float*)(pw + 6144 + (sl) * 4) = O[8])
    f32x4 x0, y0, x1, y1, x2, y2; float l0, l1, l2;
#define GETP() do { x0 = *(const LAS f32x4*)pr0; x1 = *(const LAS f32x4*)(pr1 + 2048); x2 = *(const LAS f32x4*)(pr2 + 4096); \
        y0 = *(const LAS f32x4*)(pr0 + 1024); y1 = *(const LAS f32x4*)(pr1 + 3072); y2 = *(const LAS f32x4*)(pr2 + 5120); \
        l0 = *(const LAS float*)(pr0 + 6144); l1 = *(const LAS float*)(pr1 + 6148); l2 = *(const LAS float*)(pr2 + 6152); } while (0)
    if (C) {
#pragma nounroll
        do {
            TILE(ka, va0, va1, kb, kd, vd0, vd1, 7, );
            PIN(S0); PIN(S1); PIN(o3); tag_wait(a.fl, fv, v16, fs2);
            TILE(kb, vb0, vb1, kc, ka, va0, va1, 8, );
            TILE(kc, vc0, vc1, kd, kb, vb0, vb1, 9, fv = tag_load(a.fl, v16, fs2));
            TILE(kd, vd0, vd1, ka, kc, vc0, vc1, 10, );
        } while (ki != 19);
        o3 = pv_hi(vd1, S1, o3, IDX);
        const u64 f3 = tag_load(a.fl, vf3, 0);
        cnt_wait(lds + L_CNT + 8);
        PUTA(o0, 0); PUTB(o0, 0); PUTL(o0, 0); PUTA(o1, 1); PUTB(o1, 1); PUTL(o1, 1); PUTA(o2, 2); PUTB(o2, 2); PUTL(o2, 2);
        DRAIN(); cnt_arrive(lds + L_CNT, lane);
        tag_wait(a.fl, f3, vf3, 0);
        LDB_FIRST(nxt);
        cnt_wait(lds + L_CNT);
        GETP(); gf = GATE();
    } else {
        SBAR();
        TILE(ka, va0, va1, kb, kd, vd0, vd1, 6, );
        TILE(kb, vb0, vb1, kc, ka, va0, va1, 6, );
#pragma nounroll
        do {
            TILE(kc, vc0, vc1, kd, kb, vb0, vb1, 6, );
            TILE(kd, vd0, vd1, ka, kc, vc0, vc1, 6, );
            TILE(ka, va0, va1, kb, kd, vd0, vd1, 6, );
            TILE(kb, vb0, vb1, kc, ka, va0, va1, 6, );
        } while (ki != 17);
        STEP(S0, S1, o3, vb1, kc, q1, zc, o0, vc0, , , );
        STEP(S1, S0, o0, vc1, kc, q2, zc, o1, vc0, , , );
        STEP(S0, S1, o1, vc1, kd, q0, zc, o2, vc0, __builtin_amdgcn_s_waitcnt(0x0F73), , );
        STEP(S1, S0, o2, vc1, kd, q1, zc, o0, vd0, , , );
        STEP(S0, S1, o0, vd1, kd, q2, zc, o1, vd0, , , );
        STEP(S1, S0, o1, vd1, kc, q3, zc, o2, vd0, PUTA(o0, 0), PUTL(o0, 0), PUTB(o0, 0));
        STEP(S0, S1, o2, vd1, kd, q3, zc, o3, vc0, PUTA(o1, 1), PUTL(o1, 1), PUTB(o1, 1));
        PUTA(o2, 2); PUTB(o2, 2); asm volatile("" :: "v"(o0), "v"(o1), "v"(o2)); SBAR();
        EX(S1, 0); EX(S1, 1); SBAR(); o3 = pv_hi(vc1, S0, o3, IDX); SBAR(); PUTL(o2, 2); __builtin_amdgcn_s_setprio(2); SBAR();
        EX(S1, 2); EX(S1, 3); EX(S1, 4); EX(S1, 5); EX(S1, 6); EX(S1, 7); SBAR(); EX(S1, 8); EX(S1, 9); SBAR();
        o3 = pv_lo(vd0, S1, o3, IDX); PIN(S1); SBAR();
        CBAR(); GETP(); SBAR();
        EX(S1, 10); EX(S1, 11); EX(S1, 12); EX(S1, 13); EX(S1, 14); EX(S1, 15); SBAR();
        o3 = pv_hi(vd1, S1, o3, IDX); SBAR();
    }
    x0 = (x0 + x1) + x2; y0 = (y0 + y1) + y2; l0 = (l0 + l1) + l2;
    PIN(x0); PIN(y0); PIN(l0); PIN(o3);
#pragma unroll
    for (int r = 0; r < 4; ++r) { o3[r] += x0[r]; o3[4 + r] += y0[r]; }
    o3[8] += l0;
    finish<C, 0>(a, o3, gf, q3, ovo);
}

template <int C>
__device__ __forceinline__ void attn_window(const Att& a, KV3& f, const unsigned (&so0)[4]) {
    const lds_ptr lds = a.lds; const int t = a.t, lane = a.lane;
    const f32x16 zc = f32x16{};
    bf16x8 k[9], v0[9], v1[9];
#pragma unroll
    for (int i = 0; i < 3; ++i) { k[i] = f.k[i]; v0[i] = f.v0[i]; v1[i] = f.v1[i]; }
#define LDW(i) LDB(k[i], v0[i], v1[i], i)
    const bf16x8 qa = lds_frag(lds + L_Q + 8192 + C * 4096 + t * 1024 + lane * 16);
    const f32x16 cd = lds_f32x16(lds + L_MDIAG + lane * 16), cf = lds_f32x16(lds + L_MFAR + lane * 16);
    f32x16 oo = lds_zero16(a.zp);
    const float gt = GATE();
    f32x16 S0 = MFMA(k[0], qa, cd), S1 = MFMA(k[1], qa, zc);
    SBAR(); LDW(3); SBAR();
#pragma unroll
    for (int r = 0; r < 10; ++r) { EX(S0, r); SBAR(); }
    oo = pv_lo(v0[0], S0, oo, IDX); SBAR();
#pragma unroll
    for (int r = 10; r < 16; ++r) EX(S0, r);
    SBAR();
    STEP(S1, S0, oo, v1[0], k[2], qa, zc, oo, v0[1], LDW(4), , );
    STEP(S0, S1, oo, v1[1], k[3], qa, zc, oo, v0[2], LDW(5), , );
    STEP(S1, S0, oo, v1[2], k[4], qa, zc, oo, v0[3], LDW(6), , );
    STEP(S0, S1, oo, v1[3], k[5], qa, zc, oo, v0[4], LDW(7), , );
    STEP(S1, S0, oo, v1[4], k[6], qa, zc, oo, v0[5], LDW(8), , );
    const unsigned fa = kvflag(1, a.p >> 1, t, 0), va = (lane & 31) * FSTR;
    u64 fv = 0;
    STEP(S0, S1, oo, v1[5], k[7], qa, zc, oo, v0[6], if (!C) fv = tag_load(a.fl, va, fa), , );
    STEP(S1, S0, oo, v1[6], k[8], qa, cf, oo, v0[7], , , );
    if (C) { PIN(S0); PIN(S1); PIN(oo); tag_raise(a.fl, OFLAG(0, C)); }
    STEP(S0, S1, oo, v1[7], k[8], qa, cf, oo, v0[8], , , );
    oo = pv_hi(v1[8], S0, oo, IDX);
    if (!C) { PIN(oo); tag_wait(a.fl, fv, va, fa); LDA_FIRST(f); }
    finish<C, 1>(a, oo, 1.f - gt, qa, OVOFF(1));
}

__global__ void __launch_bounds__(512, 2)
gmix_kernel(const char* x, const char* wA, const char* woA, const char* wB, const char* woB, const char* gw, char* out, char* ws) {
    extern __shared__ __attribute__((aligned(16))) char smem[];
    const lds_ptr lds = (lds_ptr)smem;
    const unsigned lds0 = (unsigned)(uintptr_t)lds;
    const int tid = threadIdx.x, lane = tid & 63, r32 = lane & 31, hh = lane >> 5;
    const int w = __builtin_amdgcn_readfirstlane(tid >> 6);
    const int bid = blockIdx.x, xcd = bid & 7, rr = bid >> 3;
    const int p = 4 * (xcd & 3) + (rr & 3), j = 8 * (xcd >> 2) + (rr >> 2);
    const int jo = 2 * xcd + (rr >> 4), po = rr & 15;
    const int t = w & 3, c = w >> 2;
    const int e = p & 1;
    {
        u64 pc; const unsigned ln = (((bid & ~7) | (tid >> 6)) + (bid < CODE_LINES - 256 ? (tid & 32) * 8 : 0)) * 128;
        asm volatile("s_getpc_b64 %0\n\ts_mov_b32 m0, %2\n\ts_nop 0\n\tglobal_load_lds_dword %1, %0" : "=&s"(pc) : "v"(ln), "s"(lds0 + L_SCR) : "memory");
        SBAR();
    }
    unsigned rot = (j + 4 * p) & 7;
    unsigned vx0 = (128 * j + 32 * t + (lane >> 3)) * 1024 + (((lane & 7) ^ (lane >> 4)) * 16), vx1 = (vx0 ^ 64) + 7168, vx2 = vx0 + 14336, vx3 = vx1 + 14336;
    unsigned vw0 = (16 * t + (lane >> 3)) * 3072 + 1024 + 1024 * e + 128 * ((p >> 1) ^ QSWAP) + (((lane & 7) ^ (((lane >> 4) & 1) << 1)) * 16), vw1 = vw0 + 8 * 3072 - 1024;
    unsigned mx = lds0 + t * 4096, mw = lds0 + L_W + t * 2048, ow = rot * (64 * 3072);
    asm volatile("" : "+v"(vx0), "+v"(vx1), "+v"(vx2), "+v"(vx3), "+v"(vw0), "+v"(vw1));
    asm volatile("" : "+s"(mx), "+s"(mw), "+s"(rot), "+s"(ow));
    SBAR();
    asm volatile("" : "+s"(x), "+s"(wA), "+s"(woA), "+s"(wB), "+s"(woB), "+s"(gw), "+s"(out), "+s"(ws));
#define MK_ATT(C) Att a; a.lds = lds; a.ws = ws; a.lane = lane; a.t = t; a.p = p; a.g = 4 * j + t; a.head = 2 * p + C; a.idx = 0xdddddddd; \
    a.rkv = __builtin_amdgcn_make_buffer_rsrc(ws + WS_KV, 0, KV_BYTES, 0x00020000); a.ro = __builtin_amdgcn_make_buffer_rsrc(ws + WS_O, 0, O_BYTES, 0x00020000); \
    a.vk = lane * 16; a.vv0 = r32 < 16 ? 1024 + hh * 256 + r32 * 16 : ((r32 & ~4) == 16 ? 2048 : OOB); a.vv1 = r32 < 16 ? a.vv0 + 512 : a.vv0; a.zp = lds + L_Z + lane * 16; \
    a.rka = __builtin_amdgcn_make_buffer_rsrc(ws + WS_KV + (unsigned)(a.head * 64 + t) * TS, 0, 61 * TS, 0x00020000); \
    _Pragma("unroll") for (int i = 0; i < 4; ++i) a.qp[i] = lds + L_Q + C * 4096 + ((t + 1 + i) & 3) * 1024 + lane * 16
#define MK_TAGS(f) do { f.rs = __builtin_amdgcn_make_buffer_rsrc(ws, 0, WS_KV, 0x00020000); f.v0 = lane ? OOB : 0; PIN(f.v0); } while (0)
#define MK_SO() unsigned so0[4]; _Pragma("unroll") for (int i = 0; i < 4; ++i) so0[i] = (j ^ i) * (4 * TS)
    if (c) {
#define DMA1(s) do { const unsigned ks_ = ((s) + rot) & 7; \
        glds16x4(vx0, vx1, vx2, vx3, x + ks_ * 128, mx + ((s) & 3) * 16384); glds16x2(vw0, vw1, wA + ks_ * (64 * 3072), mw + ((s) & 3) * 8192); } while (0)
#define DMA2(s) do { const unsigned ks_ = (((s) + rot) & 7) * (64 * 3072); \
        glds16x2(vq0, vq1, wq + ks_, mq + ((s) == 14 ? L_Q14 : ((s) & 3) * 16384)); glds16x2(vw0, vw1, wy + ks_, mw + ((s) & 3) * 8192); SBAR(); } while (0)
#define LBAR(N) asm volatile("s_waitcnt vmcnt(" #N ")\n\ts_barrier" ::: "memory")
        glds16x4(vx0, vx1, vx2, vx3, x + rot * 128, mx); glds16x2(vw0, vw1, wA + ow, mw);
        SBAR();
        const unsigned k1 = (1 + rot) & 7;
        glds16x2(vx0, vx1, x + k1 * 128, mx + 16384);
        SBAR();
        if (t == 0)
            asm volatile("s_mov_b32 m0, %2\n\ts_nop 0\n\tglobal_load_lds_dword %0, %1 sc1" :: "v"((lane & 1) * 4), "s"(ws + WS_EPOCH), "s"(lds0 + L_EP) : "memory");
        unsigned vq0 = (32 * (t & 1) + 16 * e + (lane >> 2)) * 3072 + 64 * (p ^ ((t >> 1) * PBX)) + (lane & 3) * 16 + 1024, vq1 = vq0 + (e ? -16 * 3072 : 16 * 3072) - 1024;
        unsigned vwo = ((t & 1) * 256 + (lane >> 2)) * 1024 + 64 * po + (lane & 3) * 16;
        MK_ATT(1);
        MK_TAGS(a.fl);
        int go = 4 * jo + t;
        asm volatile("" : "+s"(go));
        unsigned v16 = (lane & 15) * 8, vf3 = hh ? WS_OFLAG + (64 + go) * 256 + v16 : window_flag(a);
        PIN(vq0); PIN(vq1); PIN(vwo); PIN(a.vk); PIN(a.vv0); PIN(a.vv1); PIN(a.qp[0]); PIN(a.qp[1]); PIN(a.qp[2]); PIN(a.qp[3]); PIN(a.zp); PIN(v16); PIN(vf3); PIN(a.idx);
        SBAR();
        glds16x2(vx0, vx1, x + k1 * 128 + 16384, mx + 16384 + 2048); glds16x2(vw0, vw1, wA + k1 * (64 * 3072), mw + 8192);
        SBAR();
        lds_cptr wo = lds + L_WO + (4 * hh + ((lane & 15) >> 2)) * 64 + ((lane >> 4) & 1) * 32 + (lane & 3) * 8, wg[4];
        unsigned sg[4];
#pragma unroll
        for (int q = 0; q < 4; ++q) { const int gq = ((p >> 2) + 1 + q) & 3; sg[q] = (unsigned)go * OS + gq * 8192; wg[q] = wo + gq * 8192; PIN(wg[q]); asm volatile("" : "+s"(sg[q])); }
        unsigned vout = (32 * go + (lane >> 2)) * 1024 + 64 * po + (lane & 3) * 16;
        lds_ptr otw = lds + L_PART + (4 + t) * PREC + r32 * 80 + hh * 8;
        lds_cptr otr = lds + L_PART + (4 + t) * PREC + (lane >> 2) * 80 + (lane & 3) * 16;
        PIN(wo); PIN(vout); PIN(otw); PIN(otr);
        SBAR();
#pragma nounroll
        for (int s = 2; s < 8; ++s) { LBAR(6); DMA1(s); }
        const unsigned mq = lds0 + t * 2048;
        const char* wq = ((t >> 1) ? wB : wA) - 1024;
        const char* wy = wB + 128 * ((((p >> 1) ^ QSWAP) ^ (PBX >> 1)) - ((p >> 1) ^ QSWAP));
        LBAR(6); DMA2(8);
        LBAR(4); DMA2(9);
        make_tag(a.fl, *(const LAS u32x2*)(lds + L_EP));
        LBAR(4); DMA2(10);
        const unsigned k7 = ((7 + rot) & 7) * (64 * 3072), sx = mq + L_Q15, sw = mw + 32768;
        LBAR(4); glds16(vq0, wq + k7, sx); DMA2(11);
        f32x16 oo = lds_zero16(a.zp), o1 = lds_zero16(a.zp); PIN(oo); PIN(o1);
        LBAR(5); glds16(vw0, wy + k7, sw); DMA2(12);
        f32x16 o2 = lds_zero16(a.zp), o3 = lds_zero16(a.zp); PIN(o2); PIN(o3);
        LBAR(5); glds16(vq1, wq + k7 + 1024, sx + 1024); DMA2(13);
        f32x16 S1 = lds_zero16(a.zp); PIN(S1);
        LBAR(5); glds16(vw1, wy + k7 + 1024, sw + 1024); DMA2(14);
        __amdgpu_buffer_rsrc_t rwo = __builtin_amdgcn_make_buffer_rsrc((void*)((t >> 1) ? woB : woA), 0, 512 * 1024, 0x00020000);
        unsigned wi0 = 4096 * (((rr >> 4) << 1) | (po & 1)) + 1024 * (xcd & 3);
        MK_SO();
        asm volatile("" : "+s"(a.rka), "+s"(rwo), "+s"(wi0), "+s"(so0[0]), "+s"(so0[1]), "+s"(so0[2]), "+s"(so0[3]));
        unsigned fs1 = kvflag(0, p >> 1, (j >> 3) * 32 + t, 0), fs2 = kvflag(0, p >> 1, ((j >> 3) ^ 1) * 32 + t, 0);
        asm volatile("" : "+s"(a.fl.rs), "+s"(fs1), "+s"(fs2));
        __builtin_amdgcn_s_setprio(1);
        LBAR(5);
        const u64 fv = tag_load(a.fl, v16, fs1);
        LBAR(1);
        tag_wait(a.fl, fv, v16, fs1);
        KV3 kw;
        LDA_FIRST(kw);
        attn_full<1>(a, oo, o1, o2, o3, S1, so0[3], rwo, vwo, wi0, v16, fs2, vf3, kw);
        attn_window<1>(a, kw, so0);
#define OBR(n) ((n) >> 4 == 1 ? 0 : 1)
#define OHD(n) (2 * ((n) & 15) + ((n) >= 16))
#define OLD(n) BLOAD(a.ro, a.vk, (unsigned)(OBR(n) * 64 + go) * OS + OHD(n) * 1024)
#define OWF(n) tr_frag(wo + OBR(n) * 32768 + OHD(n) * 1024, wo + OBR(n) * 32768 + OHD(n) * 1024 + 512)
        constexpr int OIN = 6;
        bf16x8 oe[48];
#pragma unroll
        for (int n = 0; n < 48; ++n) {
            if (n == 16) { const u64 v = tag_load(a.fl, v16, WS_OFLAG + go * 256 + 128); tag_raise(a.fl, OFLAG(1, 1)); tag_wait(a.fl, v, v16, WS_OFLAG + go * 256 + 128); }
            if (n == 32) tag_wait(a.fl, v16, WS_OFLAG + (64 + go) * 256 + 128);
            oe[n] = OLD(n); SBAR();
            if (n >= OIN - 1) __builtin_amdgcn_s_waitcnt(0x0F70 | (OIN - 1));
            SBAR();
        }
        bf16x8 wn[4];
        __builtin_amdgcn_s_waitcnt(0x0F70);
        constexpr int NEARLY = 10;
        constexpr int NMID = 34;
        __amdgpu_buffer_rsrc_t rout = __builtin_amdgcn_make_buffer_rsrc(out, 0, 2048 * 1024, 0x00020000);
        asm volatile("" : "+s"(rout));
        f32x16 ya = f32x16{};
        bf16x8 wf[16], of[16];
#define FLD(i) of[i] = BLOAD(a.ro, a.vk, sg[(i) >> 2] + ((i) & 3) * 2048)
#define GRP_BAD(q) ({ unsigned mx_ = 0; _Pragma("unroll") for (int i = 4 * (q); i < 4 * (q) + 4; ++i) { const u32x4 u_ = __builtin_bit_cast(u32x4, of[i]); \
            mx_ = __builtin_elementwise_max(__builtin_elementwise_max(mx_, u_[0]), u_[1]); mx_ = __builtin_elementwise_max(__builtin_elementwise_max(mx_, u_[2]), u_[3]); } \
            __any(mx_ == ~0u); })
#define GRP_LD(q) do { _Pragma("unroll") for (int i = 4 * (q); i < 4 * (q) + 4; ++i) FLD(i); } while (0)
        constexpr int ESTEP = 2, MSTEP = 2, WF0 = 28;
        f32x16 yb, yc, yd;
        unsigned pend = 15;
        BAR();
#pragma unroll
        for (int n = 0; n < 4; ++n) { wn[n] = OWF(n); PIN(wn[n]); }
        BAR();
#pragma unroll
        for (int n = 0; n < 48; ++n) {
            ya = MFMA(wn[n & 3], oe[n], ya); SBAR();
            if (n >= NEARLY && n < NEARLY + 12 * ESTEP && (n - NEARLY) % ESTEP == 0) FLD(((n - NEARLY) / ESTEP) & 15);
            if (n >= NMID && n < NMID + 4 * MSTEP && (n - NMID) % MSTEP == 0) FLD(((n - NMID) / MSTEP + 12) & 15);
            if (n + 4 < 48) wn[n & 3] = OWF(n + 4);
            if (n >= WF0 && n < WF0 + 16) wf[(n - WF0) & 15] = tr_frag(wg[((n - WF0) & 15) >> 2] + ((n - WF0) & 3) * 2048, wg[((n - WF0) & 15) >> 2] + ((n - WF0) & 3) * 2048 + 512);
            if (n == 44) { yb = lds_zero16(a.zp); } if (n == 45) { yc = lds_zero16(a.zp); } if (n == 46) { yd = lds_zero16(a.zp); }
            SBAR();
        }
#define GRP_TRY(q, Y) do { if (pend & (1u << (q))) { if (!GRP_BAD(q)) { \
            _Pragma("unroll") for (int i = 4 * (q); i < 4 * (q) + 4; ++i) Y = MFMA(wf[i], of[i], Y); pend ^= 1u << (q); } } } while (0)
        unsigned spins = SPIN_CAP;
#pragma nounroll
        do {
            GRP_TRY(0, ya); GRP_TRY(1, yb); GRP_TRY(2, yc); GRP_TRY(3, yd);
            asm volatile("s_sub_u32 %1, %1, 1\n\ts_cselect_b32 %0, 0, %0" : "+s"(pend), "+s"(spins) :: "memory", "scc");
            if (pend & 1) GRP_LD(0); if (pend & 2) GRP_LD(1); if (pend & 4) GRP_LD(2); if (pend & 8) GRP_LD(3);
        } while (pend);
        ya = (ya + yb) + (yc + yd);
#pragma unroll
        for (int q = 0; q < 4; ++q) *(LAS u32x2*)(otw + q * 16) = (u32x2){cvtpk(ya[4 * q], ya[4 * q + 1]), cvtpk(ya[4 * q + 2], ya[4 * q + 3])};
        asm volatile("" ::: "memory");
#pragma unroll
        for (int s = 0; s < 2; ++s) __builtin_amdgcn_raw_buffer_store_b128(*(const LAS u32x4*)(otr + s * 1280), rout, vout + s * 16384, 0, SC1);
        if (bid == 0 && tid == 256) __hip_atomic_store((GAS u64*)(ws + WS_EPOCH), a.fl.tag, RLX_AGENT);
    } else {
        glds16((((lane >> 3) + rot) & 7) * 128 + (lane & 7) * 16, gw, lds0 + L_GW + t * 1024);
#pragma unroll
        for (int m = 0; m < 2; ++m) {
            f32x4 mk;
#pragma unroll
            for (int i = 0; i < 4; ++i) { const bool le = (i + 8 * t + 4 * hh) <= r32; mk[i] = (le != (m != 0)) ? 0.f : -__builtin_inff(); }
            *(LAS f32x4*)(lds + L_MDIAG + m * 4096 + t * 1024 + lane * 16) = mk;
        }
        if (tid < 4) *(LAS unsigned*)(lds + L_CNT + tid * 4) = 0;
        *(LAS unsigned*)(lds + L_Z + tid * 4) = 0;
        MK_ATT(0);
        MK_TAGS(a.fl);
        unsigned vwf = window_flag(a);
        PIN(a.vv0); PIN(a.vv1); PIN(vwf);
        if (e) project<1>(lds, a.rkv, a.ro, lane, t, p, j, a.fl); else project<0>(lds, a.rkv, a.ro, lane, t, p, j, a.fl);
        cnt_wait(lds + L_CNT + 8);
        __builtin_amdgcn_s_sleep(36);
        tag_wait(a.fl, vwf, 0);
        KV3 kw;
        LDB_FIRST(kw);
        MK_SO();
        attn_window<0>(a, kw, so0);
        PIN(a.idx);
        attn_full<0>(a, lds_zero16(a.zp), lds_zero16(a.zp), lds_zero16(a.zp), lds_zero16(a.zp), lds_zero16(a.zp), so0[3], a.rkv, 0, 0, 0, 0, 0, kw);
        BAR();
    }
}

extern "C" void kernel_launch(void* const* d_in, const int* in_sizes, int n_in, void* d_out, int out_size, void* d_ws, size_t ws_size,
                              hipStream_t stream) {
    (void)in_sizes; (void)n_in; (void)out_size; (void)ws_size;
    (void)hipFuncSetAttribute((const void*)gmix_kernel, hipFuncAttributeMaxDynamicSharedMemorySize, L_END);
    gmix_kernel<<<256, 512, L_END, stream>>>((const char*)d_in[0], (const char*)d_in[1], (const char*)d_in[2], (const char*)d_in[4],
                                             (const char*)d_in[5], (const char*)d_in[6], (char*)d_out, (char*)d_ws);
}
```

```cpp
#define REVEAL 3
#define RROLE 1
#define RCYC 0
#include <hip/hip_runtime.h>
#include <hip/hip_bf16.h>
#define OUT_LDS

typedef __attribute__((ext_vector_type(8))) __bf16 bf16x8;
typedef __attribute__((ext_vector_type(16))) __bf16 bf16x16;
typedef __attribute__((ext_vector_type(16))) float f32x16;
typedef __attribute__((ext_vector_type(8))) float f32x8;
typedef __attribute__((ext_vector_type(4))) float f32x4;
typedef __attribute__((ext_vector_type(4))) unsigned u32x4;
typedef __attribute__((ext_vector_type(2))) unsigned u32x2;
typedef __attribute__((ext_vector_type(2))) __bf16 bf16x2;
typedef short s16x4 __attribute__((ext_vector_type(4)));
typedef unsigned long long u64;
#define GAS __attribute__((address_space(1)))
#define LAS __attribute__((address_space(3)))
typedef LAS const char* lds_cptr;
typedef LAS char* lds_ptr;

constexpr unsigned WS_EPOCH = 0;
#define FSTR 8
constexpr unsigned WS_OFLAG = 65536;
constexpr unsigned WS_KVFLAG = 131072;
constexpr unsigned WS_KV = 1u << 20;
constexpr unsigned TS = 2176;
constexpr unsigned KV_BYTES = 2u * 32u * 64u * TS;
#define OSTR 32896
constexpr unsigned WS_O = 10u << 20;
constexpr unsigned OS = OSTR;
constexpr unsigned O_BYTES = 128u * OS;
constexpr unsigned WS_STAMP = 16u << 20;
constexpr unsigned OOB = 0x7ff00000u;
constexpr unsigned L_W = 65536;
constexpr unsigned L_WO = 0, L_PART = 65536, PREC = 7168, L_Q14 = 106496, L_Q15 = 114688, L_MDIAG = 122880, L_MFAR = 126976, L_Q = 131072,
                   L_GW = 147456, L_Z = 151552, L_G = 152576, L_CNT = 153088, L_EP = 153344, L_SCR = 153600, L_END = 154112;
constexpr float QSCALE = 0.25f * 1.4426950408889634f;
#define LMAXV 1.2676506e30f
constexpr float LMAX = LMAXV, LMIN = 9.094947e-13f;
constexpr unsigned SPIN_CAP = 1u << 17;

#define SBAR() __builtin_amdgcn_sched_barrier(0)
#define PIN(x) asm volatile("" : "+v"(x))
#define MFMA(a, b, c) __builtin_amdgcn_mfma_f32_32x32x16_bf16(a, b, c, 0, 0, 0)
#define BAR() asm volatile("s_barrier" ::: "memory")
#define CBAR() asm volatile("s_waitcnt lgkmcnt(0)\n\ts_barrier" ::: "memory")
#define DRAIN() asm volatile("s_waitcnt vmcnt(0)" ::: "memory")
#define PACEN 6
#define PACE() asm volatile("s_nop %0" :: "i"(PACEN) : "memory")
#define RLX_AGENT __ATOMIC_RELAXED, __HIP_MEMORY_SCOPE_AGENT
#define FL_SCOPE __HIP_MEMORY_SCOPE_AGENT
#define RLX_ST __ATOMIC_RELAXED, FL_SCOPE
#define STAMP(i) do { if ((i) == 0 || (i) == REVEAL) { u64 a_, b_; SBAR(); asm volatile("s_memrealtime %0\n\ts_memtime %1\n\ts_waitcnt lgkmcnt(0)" : "=s"(a_), "=s"(b_) :: "memory"); \
    if ((i) == 0) { stv.r0 = a_; stv.c0 = b_; stv.d = 0; } else stv.d = RCYC ? (unsigned)(b_ - stv.c0) / 20u : (unsigned)(a_ - stv.r0); SBAR(); } } while (0)
struct Rev { u64 r0, c0; unsigned d; };

__device__ __forceinline__ void glds16(unsigned voff, const void* sbase, unsigned ldsaddr) {
    asm volatile("s_mov_b32 m0, %2\n\ts_nop 0\n\tglobal_load_lds_dwordx4 %0, %1" ::"v"(voff), "s"(sbase), "s"(ldsaddr) : "memory");
}
__device__ __forceinline__ void glds16x2(unsigned v0, unsigned v1, const void* sbase, unsigned ldsaddr) {
    asm volatile("s_mov_b32 m0, %3\n\ts_nop 0\n\tglobal_load_lds_dwordx4 %0, %2\n\tglobal_load_lds_dwordx4 %1, %2 offset:1024"
                 ::"v"(v0), "v"(v1), "s"(sbase), "s"(ldsaddr) : "memory");
}
__device__ __forceinline__ void glds16x4(unsigned v0, unsigned v1, unsigned v2, unsigned v3, const void* sbase, unsigned ldsaddr) {
    asm volatile("s_mov_b32 m0, %5\n\ts_nop 0\n\tglobal_load_lds_dwordx4 %0, %4\n\tglobal_load_lds_dwordx4 %1, %4 offset:1024\n\t"
                 "global_load_lds_dwordx4 %2, %4 offset:2048\n\tglobal_load_lds_dwordx4 %3, %4 offset:3072"
                 ::"v"(v0), "v"(v1), "v"(v2), "v"(v3), "s"(sbase), "s"(ldsaddr) : "memory");
}
__device__ __forceinline__ unsigned cvtpk(float lo, float hi) {
    typedef __attribute__((ext_vector_type(2))) float f32x2;
    return __builtin_bit_cast(unsigned, __builtin_convertvector((f32x2){lo, hi}, bf16x2));
}
__device__ __forceinline__ bf16x8 as_frag(u32x4 v) { return __builtin_bit_cast(bf16x8, v); }
__device__ __forceinline__ bf16x8 lds_frag(lds_cptr p) { return *(const LAS bf16x8*)p; }
__device__ __forceinline__ bf16x8 tr_frag(lds_cptr p0, lds_cptr p1) {
    s16x4 a = __builtin_amdgcn_ds_read_tr16_b64_v4i16((LAS s16x4*)p0);
    s16x4 b = __builtin_amdgcn_ds_read_tr16_b64_v4i16((LAS s16x4*)p1);
    return __builtin_bit_cast(bf16x8, __builtin_shufflevector(a, b, 0, 1, 2, 3, 4, 5, 6, 7));
}
__device__ __forceinline__ f32x16 pv_lo(bf16x8 v, const f32x16& p, f32x16 acc, int idx) {
    f32x8 h = __builtin_shufflevector(p, p, 0, 1, 2, 3, 4, 5, 6, 7);
    return __builtin_amdgcn_smfmac_f32_32x32x32_bf16(v, __builtin_bit_cast(bf16x16, h), acc, idx, 0, 0);
}
__device__ __forceinline__ f32x16 pv_hi(bf16x8 v, const f32x16& p, f32x16 acc, int idx) {
    f32x8 h = __builtin_shufflevector(p, p, 8, 9, 10, 11, 12, 13, 14, 15);
    return __builtin_amdgcn_smfmac_f32_32x32x32_bf16(v, __builtin_bit_cast(bf16x16, h), acc, idx, 0, 0);
}
#define MX3(a, b, c) __builtin_fmaxf(__builtin_fmaxf((a), (b)), (c))
__device__ __forceinline__ float max16(const f32x16& s) {
    float a = MX3(s[0], s[1], s[2]), b = MX3(s[3], s[4], s[5]);
    a = MX3(a, s[6], s[7]); b = MX3(b, s[8], s[9]); a = MX3(a, s[10], s[11]); b = MX3(b, s[12], s[13]);
    return MX3(a, b, __builtin_fmaxf(s[14], s[15]));
}
__device__ __forceinline__ float halfmax(float m) {
    auto r = __builtin_amdgcn_permlane32_swap(__float_as_uint(m), __float_as_uint(m), false, false);
    return __builtin_fmaxf(__uint_as_float(r[0]), __uint_as_float(r[1]));
}
__device__ __forceinline__ f32x16 lds_f32x16(lds_cptr p) {
    f32x4 a = *(const LAS f32x4*)p, b = *(const LAS f32x4*)(p + 1024), c = *(const LAS f32x4*)(p + 2048), d = *(const LAS f32x4*)(p + 3072);
    f32x8 lo = __builtin_shufflevector(a, b, 0, 1, 2, 3, 4, 5, 6, 7), hi = __builtin_shufflevector(c, d, 0, 1, 2, 3, 4, 5, 6, 7);
    return __builtin_shufflevector(lo, hi, 0, 1, 2, 3, 4, 5, 6, 7, 8, 9, 10, 11, 12, 13, 14, 15);
}
#define ST_AUX 16
#define LD_AUX 16
#define BLOAD(rs, vo, so) as_frag(__builtin_amdgcn_raw_buffer_load_b128(rs, vo, so, LD_AUX))
#define BSTORE(v, rs, vo, so) __builtin_amdgcn_raw_buffer_store_b128(v, rs, (vo) + (so), 0, ST_AUX)

#define POLLSLP 8
#define BSLP 36
struct Tags { __amdgpu_buffer_rsrc_t rs; u64 tag; u32x2 vtag; unsigned v0; };
__device__ __forceinline__ u64 tag_load(const Tags& f, unsigned vo, unsigned so) {
    return __builtin_bit_cast(u64, __builtin_amdgcn_raw_buffer_load_b64(f.rs, vo, so, LD_AUX));
}
__device__ __forceinline__ void tag_raise(const Tags& f, unsigned so) {
    DRAIN();
    __builtin_amdgcn_raw_buffer_store_b64(f.vtag, f.rs, f.v0, so, ST_AUX);
}
__device__ __forceinline__ void tag_wait(const Tags& f, u64 v, unsigned vo, unsigned so) {
    if (__builtin_expect(!__all(v == f.tag), 0)) {
#pragma nounroll
        for (unsigned spins = 0; spins < SPIN_CAP; ++spins) {
            __builtin_amdgcn_s_sleep(POLLSLP);
            asm volatile("" ::: "memory");
            if (__all(tag_load(f, vo, so) == f.tag)) break;
        }
    }
    asm volatile("" ::: "memory");
}
__device__ __forceinline__ void tag_wait(const Tags& f, unsigned vo, unsigned so) { tag_wait(f, tag_load(f, vo, so), vo, so); }

__device__ __forceinline__ void cnt_arrive(lds_ptr cnt, int lane) {
    asm volatile("s_waitcnt lgkmcnt(0)" ::: "memory");
    if (lane == 0) __hip_atomic_fetch_add((LAS unsigned*)cnt, 1u, __ATOMIC_RELAXED, __HIP_MEMORY_SCOPE_WORKGROUP);
}
__device__ __forceinline__ void cnt_wait(lds_ptr cnt) {
#pragma nounroll
    for (unsigned spins = 0; spins < (1u << 22); ++spins) {
        if (__builtin_amdgcn_readfirstlane(__hip_atomic_load((LAS unsigned*)cnt, __ATOMIC_RELAXED, __HIP_MEMORY_SCOPE_WORKGROUP)) == 4) break;
        __builtin_amdgcn_s_sleep(1);
    }
    asm volatile("" ::: "memory");
}
__device__ __forceinline__ unsigned kvflag(int br, int quad, int tile, int type) {
    return WS_KVFLAG + (unsigned)(((((br * 8 + quad) * 4 + (tile & 3)) * 2 + (tile >> 5)) * 2 + type) * 8 + ((tile >> 2) & 7)) * FSTR;
}

__device__ __forceinline__ void slow_rows(__amdgpu_buffer_rsrc_t rs, unsigned sbase, int lo, int hi, int g, bool band, bf16x8 q,
                                          unsigned vk, unsigned vv0, unsigned vv1, lds_cptr lds, int lane, int idx, f32x16& acc) {
    float m = -__builtin_inff();
    for (int pass = 0; pass < 2; ++pass) {
        if (pass) { m = halfmax(m); acc = f32x16{}; }
        for (int kt = lo; kt <= hi; ++kt) {
            const unsigned so = sbase + (unsigned)kt * TS;
            const bf16x8 k = BLOAD(rs, vk, so), v0 = BLOAD(rs, vv0, so), v1 = BLOAD(rs, vv1, so);
            f32x16 c = f32x16{};
            if (band && kt == g) c = lds_f32x16(lds + L_MDIAG + lane * 16);
            if (band && kt == g - 8) c = lds_f32x16(lds + L_MFAR + lane * 16);
            f32x16 s = MFMA(k, q, c);
            if (!pass) m = __builtin_fmaxf(m, max16(s));
            else {
#pragma unroll
                for (int r = 0; r < 16; ++r) s[r] = __builtin_amdgcn_exp2f(s[r] - m);
                acc = pv_lo(v0, s, acc, idx); acc = pv_hi(v1, s, acc, idx);
            }
        }
    }
}

#define PK4(A, o) ((u32x4){cvtpk(A[o], A[o + 1]), cvtpk(A[o + 2], A[o + 3]), cvtpk(A[o + 4], A[o + 5]), cvtpk(A[o + 6], A[o + 7])})
#define PROF_ARG , Rev& stv
#define PROF_PASS , stv
__device__ __forceinline__ void make_tag(Tags& f, u32x2 e) {
    f.tag = (((u64)(unsigned)__builtin_amdgcn_readfirstlane(e[1]) << 32) | (unsigned)__builtin_amdgcn_readfirstlane(e[0])) * 0x9E3779B97F4A7C15ull + 0x632BE59BD9B4E019ull;
    asm volatile("" : "+s"(f.tag));
    f.vtag = __builtin_bit_cast(u32x2, f.tag);
    PIN(f.vtag);
}
template <int E>
__device__ __forceinline__ u32x4 pack_kv(int i, const f32x16& a) {
    const int s = i & 1;
    if (E == 0) return PK4(a, 8 * s);
    auto x = __builtin_amdgcn_permlane32_swap(cvtpk(a[8 * s], a[8 * s + 1]), cvtpk(a[8 * s + 4], a[8 * s + 5]), false, false);
    auto y = __builtin_amdgcn_permlane32_swap(cvtpk(a[8 * s + 2], a[8 * s + 3]), cvtpk(a[8 * s + 6], a[8 * s + 7]), false, false);
    return (u32x4){x[0], y[0], x[1], y[1]};
}
#define PUT_KV(i, v, vo) BSTORE(v, rkv, vo, ((i) < 2 ? 64 * TS : 0) + ((i) & 1) * (E ? 512 : 128 * TS))
#define QSWAP 1
#define PBX 4
template <int E>
__device__ __forceinline__ void project(lds_ptr lds, __amdgpu_buffer_rsrc_t rkv, __amdgpu_buffer_rsrc_t ro, int lane, int t, int p, int j, Tags& fl PROF_ARG) {
    const int r32 = lane & 31, hh = lane >> 5, qd4 = (p >> 1) ^ QSWAP;
    const unsigned q4 = (lane & 15) >> 2, wcol = ((lane >> 4) & 1) * 32 + (lane & 3) * 8;
    lds_cptr xa[4];
#pragma unroll
    for (int ks = 0; ks < 4; ++ks) xa[ks] = lds + (32 * t + r32) * 128 + (((hh ^ ((r32 >> 1) & 7)) * 16) ^ (ks * 32));
    unsigned w1o = L_W + (8 * hh + q4) * 128 + ((lane >> 4) & 1) * 64 + (q4 >> 1) * 32 + (lane & 3) * 8, w0o = w1o ^ 32;
    PIN(w0o); PIN(w1o);
    const lds_cptr w0 = lds + w0o, w1 = lds + w1o, wq = lds + (8 * hh + q4) * 64 + wcol;
#define XRD(n) lds_frag(xa[(n) & 3] + (((n) >> 2) & 3) * 16384)
#define WRD(b, n) tr_frag(b + ((n) & 15) * 2048, b + ((n) & 15) * 2048 + 512)
#define YOF(n) ((n) >= 28 ? 32768 + ((n) & 3) * 2048 : ((n) & 15) * 2048)
#define YRD(b, n) tr_frag(b + YOF(n), b + YOF(n) + 512)
#define QOF(br, n) (((n) >= 28 ? L_Q15 - L_Q14 : ((n) >= 24 ? 0 : (((n) >> 2) & 3) * 16384)) + (br) * 4096 + (((n) & 3) ^ E) * 1024)
#define QRD(br, n) tr_frag(((n) >= 24 ? wqh : wq) + QOF(br, n), ((n) >= 24 ? wqh : wq) + QOF(br, n) + 256)
#define MM(W, X, A) (E ? MFMA(X, W, A) : MFMA(W, X, A))
    bf16x8 xf[32], f0[4], f1[4];
    f32x16 a0 = f32x16{}, a1 = f32x16{};
    unsigned gwo = (lane & 15) == ((lane >> 4) & 1) ? L_GW + t * 1024 + hh * 16 : L_Z;
    PIN(gwo);
    f32x4 zg = f32x4{};
    bf16x8 gf[4];
#define RD1(n) do { xf[n] = XRD(n); f0[(n) & 3] = WRD(w0, n); f1[(n) & 3] = WRD(w1, n); SBAR(); } while (0)
#define GRD(n) gf[(n) & 3] = lds_frag(lds + gwo + (n) * 32)
#define GMM(n) zg = __builtin_amdgcn_mfma_f32_16x16x32_bf16(gf[(n) & 3], xf[n], zg, 0, 0, 0)
#define MM1(n) do { a0 = MM(f0[(n) & 3], xf[n], a0); a1 = MM(f1[(n) & 3], xf[n], a1); SBAR(); } while (0)
#pragma unroll
    for (int s = 0; s < 8; ++s) {
        if (s == 0) CBAR(); else BAR();
        SBAR();
        RD1(4 * s); RD1(4 * s + 1);
        if (s) { MM1(4 * s - 1); GMM(4 * s - 4); GMM(4 * s - 3); GMM(4 * s - 2); GMM(4 * s - 1); SBAR(); }
        RD1(4 * s + 2); MM1(4 * s); RD1(4 * s + 3); MM1(4 * s + 1);
        if (s == 0) DRAIN();
        GRD(4 * s); GRD(4 * s + 1); GRD(4 * s + 2); GRD(4 * s + 3); SBAR();
        MM1(4 * s + 2);
        SBAR();
    }
    const unsigned tile = 4 * j + t;
    unsigned vo = (unsigned)(4 * qd4 * 64 + tile) * TS + (E ? (r32 >> 4) * (128 * TS) + 1024 + hh * 256 + (r32 & 15) * 16 : lane * 16);
    unsigned wqo = L_Q14 + (8 * hh + q4) * 64 + wcol;
    PIN(vo); PIN(wqo);
    const lds_cptr wqh = lds + wqo;
    MM1(31); GMM(28); GMM(29); GMM(30); GMM(31); SBAR();
#define PRE8 2
#define PUBN 7
#define LATE0 99
    u32x4 pk[4];
#pragma unroll
    for (int i = 0; i < 4; ++i) { pk[i] = pack_kv<E>(i, i < 2 ? a0 : a1); PIN(pk[i]); if (i < PRE8) PUT_KV(i, pk[i], vo); SBAR(); }
    if (!E) BSTORE(((u32x4){0x3f803f80u, 0x3f803f80u, 0x3f803f80u, 0x3f803f80u}), rkv,
                   (unsigned)((hh * 32 + 4 * (qd4 ^ (hh * (PBX >> 1))) + (r32 >> 3)) * 64 + tile) * TS + 2048 + (lane & 7) * 16, 0);
    { LAS float* gp = (LAS float*)(lds + (lane < 16 ? L_G + t * 128 : L_SCR)) + lane;
      gp[0] = __builtin_amdgcn_rcpf(1.f + __expf(-zg[0])); gp[16] = __builtin_amdgcn_rcpf(1.f + __expf(-zg[1])); }
    SBAR();
    BAR(); SBAR();
    bf16x8 u0 = QRD(0, 0), u1 = QRD(1, 0), y0 = YRD(w0, 0), y1 = YRD(w1, 0);
    const u32x2 tg = *(const LAS u32x2*)(lds + L_EP);
    f32x16 b0 = f32x16{}, b1 = f32x16{}, aQ0 = f32x16{}, aQ1 = f32x16{};
    SBAR();
#pragma unroll
    for (int n = 0; n < 24; ++n) {
        aQ0 = MFMA(u0, xf[n], aQ0); SBAR();
        if (n == 1) { make_tag(fl, tg); SBAR(); }
        if (n == PUBN) { tag_raise(fl, kvflag(0, qd4, tile, E)); SBAR(); }
        if ((n & 3) == 3) {
            if (PRE8 + (n >> 2) < 2) { PUT_KV((PRE8 + (n >> 2)) & 3, pk[(PRE8 + (n >> 2)) & 3], vo); SBAR(); }
            if (n == LATE0) { PUT_KV(2, pk[2], vo); SBAR(); }
            if (n == LATE0 + 4) { PUT_KV(3, pk[3], vo); SBAR(); }
            BAR(); SBAR();
        }
        u0 = QRD(0, n + 1); SBAR();
        aQ1 = MFMA(u1, xf[n], aQ1); SBAR();
        u1 = n < 23 ? QRD(1, n + 1) : QRD(0, 25); SBAR();
        b0 = MM(y0, xf[n], b0); SBAR(); y0 = n < 23 ? YRD(w0, n + 1) : QRD(0, 26); SBAR();
        b1 = MM(y1, xf[n], b1); SBAR(); y1 = n < 23 ? YRD(w1, n + 1) : QRD(0, 27); SBAR();
    }
    aQ0 = MFMA(u0, xf[24], aQ0); SBAR(); u0 = QRD(0, 28); SBAR(); aQ0 = MFMA(u1, xf[25], aQ0); SBAR(); u1 = QRD(0, 29); SBAR();
    aQ0 = MFMA(y0, xf[26], aQ0); SBAR(); y0 = QRD(0, 30); SBAR(); aQ0 = MFMA(y1, xf[27], aQ0); SBAR(); y1 = QRD(0, 31); SBAR();
    aQ0 = MFMA(u0, xf[28], aQ0); SBAR(); aQ0 = MFMA(u1, xf[29], aQ0); SBAR(); u1 = QRD(1, 24); SBAR();
    aQ0 = MFMA(y0, xf[30], aQ0); SBAR(); y0 = YRD(w0, 24); SBAR(); aQ0 = MFMA(y1, xf[31], aQ0); SBAR(); y1 = YRD(w1, 24); SBAR();
    const lds_ptr qd = lds + L_Q + t * 1024 + lane * 16;
#define QBAR 4
#define NFREE 3
    f32x8 qs; u32x4 qk;
#define QEPI(m) do { if ((m) == 1) { PIN(aQ0); qs = __builtin_shufflevector(aQ0, aQ0, 8, 9, 10, 11, 12, 13, 14, 15) * QSCALE; PIN(qs); } \
        if ((m) == 2) { qk = PK4(qs, 0); *(LAS u32x4*)(qd + 4096) = qk; } \
        if ((m) == QBAR) { __builtin_amdgcn_s_setprio(2); __builtin_amdgcn_s_waitcnt(0xC07F | ((2 * (QBAR - 2)) << 8)); BAR(); STAMP(4); } \
        if ((m) > QBAR + NFREE) PACE(); SBAR(); } while (0)
#pragma unroll
    for (int n = 24; n < 32; ++n) {
        aQ1 = MFMA(u1, xf[n], aQ1); SBAR(); if (n < 31) u1 = QRD(1, n + 1); SBAR(); QEPI(3 * (n - 24));
        b0 = MM(y0, xf[n], b0); SBAR(); if (n < 31) y0 = YRD(w0, n + 1); SBAR(); QEPI(3 * (n - 24) + 1);
        b1 = MM(y1, xf[n], b1); SBAR(); if (n < 31) y1 = YRD(w1, n + 1); SBAR(); QEPI(3 * (n - 24) + 2);
    }
#pragma unroll
    for (int r = 0; r < 8; ++r) aQ0[r] *= QSCALE;
#pragma unroll
    for (int r = 0; r < 16; ++r) aQ1[r] *= QSCALE;
    *(LAS u32x4*)qd = PK4(aQ0, 0); *(LAS u32x4*)(qd + 8192) = PK4(aQ1, 0); *(LAS u32x4*)(qd + 12288) = PK4(aQ1, 8);
    cnt_arrive(lds + L_CNT + 8, lane);
    const unsigned vo2 = vo + (32 + 4 * ((qd4 ^ (PBX >> 1)) - qd4)) * 64 * TS;
#define YSLP 8
    u32x4 py[4] = {pack_kv<E>(0, b0), pack_kv<E>(1, b0), pack_kv<E>(2, b1), pack_kv<E>(3, b1)};
    PIN(py[0]); PIN(py[1]); PIN(py[2]); PIN(py[3]);
#define YSTAG 2
    if (t & 1) __builtin_amdgcn_s_sleep(YSTAG);
    if (t & 2) __builtin_amdgcn_s_sleep(2 * YSTAG);
    if (LATE0 > 23) { PUT_KV(2, pk[2], vo); __builtin_amdgcn_s_sleep(YSLP); }
    if (LATE0 > 19) { PUT_KV(3, pk[3], vo); __builtin_amdgcn_s_sleep(YSLP); }
    PUT_KV(0, py[0], vo2); __builtin_amdgcn_s_sleep(YSLP); PUT_KV(1, py[1], vo2); __builtin_amdgcn_s_sleep(YSLP);
    PUT_KV(2, py[2], vo2); __builtin_amdgcn_s_sleep(YSLP); PUT_KV(3, py[3], vo2);
    tag_raise(fl, kvflag(1, qd4, tile, E));
    BSTORE(((u32x4){~0u, ~0u, ~0u, ~0u}), ro, lane * 16, tile * OS + 2 * p * 1024);
    STAMP(19);
}

#define AB_WO (512 * 1024)
#define ROTEXPR j + 4 * p
#define WOX 1
#define WOIN 2
#define CODE_LINES 280

struct Att {
    lds_ptr lds; char* ws; __amdgpu_buffer_rsrc_t rkv, ro, rka;
    Tags fl; int lane, t, p, g, head, idx; unsigned vk, vv0, vv1; lds_cptr zp, qp[4];
};
#define IDX a.idx
#define LD3(K, V0, V1, rs, so) do { K = BLOAD(rs, a.vk, so); V0 = BLOAD(rs, a.vv0, so); V1 = BLOAD(rs, a.vv1, so); } while (0)
#define EX(X, r) X[r] = __builtin_amdgcn_exp2f(X[r])
#define STEP(X, Y, OP, VP, KN, QN, CN, O, V0, F1, F3, F2) do { \
        EX(X, 0); EX(X, 1); SBAR(); OP = pv_hi(VP, Y, OP, IDX); SBAR(); F1; SBAR(); EX(X, 2); EX(X, 3); EX(X, 4); SBAR(); Y = MFMA(KN, QN, CN); SBAR(); F3; SBAR(); \
        EX(X, 5); EX(X, 6); EX(X, 7); SBAR(); EX(X, 8); EX(X, 9); SBAR(); O = pv_lo(V0, X, O, IDX); SBAR(); F2; SBAR(); \
        EX(X, 10); EX(X, 11); EX(X, 12); EX(X, 13); EX(X, 14); EX(X, 15); SBAR(); } while (0)

__device__ __forceinline__ f32x16 lds_zero16(lds_cptr zp) {
    const volatile LAS f32x4* z = (const volatile LAS f32x4*)zp;
    const f32x4 a = *z, b = *z, c = *z, d = *z;
    f32x8 lo = __builtin_shufflevector(a, b, 0, 1, 2, 3, 4, 5, 6, 7), hi = __builtin_shufflevector(c, d, 0, 1, 2, 3, 4, 5, 6, 7);
    return __builtin_shufflevector(lo, hi, 0, 1, 2, 3, 4, 5, 6, 7, 8, 9, 10, 11, 12, 13, 14, 15);
}

template <int C, int BR>
__device__ __forceinline__ void finish(const Att& a, f32x16 o, float gf, bf16x8 q, unsigned vo) {
    f32x8 y = __builtin_shufflevector(o, o, 0, 1, 2, 3, 4, 5, 6, 7) * (__builtin_amdgcn_rcpf(o[8]) * gf);
    PIN(y);
    const float chk = __builtin_fmaf(((y[0] + y[2]) + (y[4] + y[6])) + ((y[1] + y[3]) + (y[5] + y[7])), 0.f, o[8]);
    if (__builtin_expect(__any(__builtin_amdgcn_fmed3f(chk, LMIN, LMAX) != chk), 0)) {
        if (!BR) { tag_wait(a.fl, a.lane * FSTR, kvflag(!C, a.p >> 1, 0, 0)); tag_wait(a.fl, a.lane * FSTR, kvflag(!C, a.p >> 1, 2, 0)); }
        slow_rows(a.rkv, (unsigned)((BR * 32 + (a.head ^ (BR * 2 * PBX))) * 64) * TS, BR ? (a.g >= 8 ? a.g - 8 : 0) : 0, BR ? a.g : 63, a.g, BR, q, a.vk, a.vv0, a.vv1, a.lds, a.lane, IDX, o);
        y = __builtin_shufflevector(o, o, 0, 1, 2, 3, 4, 5, 6, 7) * (__builtin_amdgcn_rcpf(o[8]) * gf);
        PIN(y);
    }
    u32x4 v = (u32x4){cvtpk(y[0], y[1]), cvtpk(y[2], y[3]), cvtpk(y[4], y[5]), cvtpk(y[6], y[7])};
    if (!C && !BR) v = __builtin_elementwise_min(v, (u32x4){~1u, ~1u, ~1u, ~1u});
    BSTORE(v, a.ro, vo, 0);
}
#define OFLAG(BR, C) (WS_OFLAG + ((((BR) * 64 + a.g) * 2 + (C)) * 16 + (a.p ^ ((BR) * PBX))) * 8)
#define OVOFF(BR) (a.vk + (unsigned)((BR) * 64 + a.g) * OS + (a.head ^ ((BR) * 2 * PBX)) * 1024)
#define GATE() (*(const LAS float*)(a.lds + L_G + a.t * 128 + (a.lane & 31) * 4))

struct KV3 { bf16x8 k[3], v0[3], v1[3]; };
#define LDB(K, V0, V1, i) do { const int kt_ = a.g - (i); \
        const __amdgpu_buffer_rsrc_t rb_ = __builtin_amdgcn_make_buffer_rsrc(a.ws + WS_KV, 0, kt_ >= 0 ? KV_BYTES : 0u, 0x00020000); \
        LD3(K, V0, V1, rb_, (unsigned)((32 + (a.head ^ (2 * PBX))) * 64 + (kt_ >= 0 ? kt_ : 0)) * TS); } while (0)
#define LDB_FIRST(f) do { LDB(f.k[0], f.v0[0], f.v1[0], 0); SBAR(); LDB(f.k[1], f.v0[1], f.v1[1], 1); SBAR(); LDB(f.k[2], f.v0[2], f.v1[2], 2); SBAR(); } while (0)
#define LDA_FIRST(f) do { LD3(f.k[0], f.v0[0], f.v1[0], a.rka, so0[0]); SBAR(); LD3(f.k[1], f.v0[1], f.v1[1], a.rka, so0[1]); SBAR(); \
        LD3(f.k[2], f.v0[2], f.v1[2], a.rka, so0[2]); SBAR(); } while (0)
__device__ __forceinline__ unsigned window_flag(const Att& a) {
    const int i = a.lane & 15, kt = a.g - (i < 9 ? i : 0);
    return kvflag(1, a.p >> 1, kt >= 0 ? kt : 0, (a.lane >> 4) & 1);
}

template <int C>
__device__ __forceinline__ void attn_full(const Att& a, f32x16 o0, f32x16 o1, f32x16 o2, f32x16 o3, f32x16 S1, unsigned so,
                                          __amdgpu_buffer_rsrc_t rwo, unsigned vwo, unsigned wi, unsigned v16, unsigned fs2, unsigned vf3, KV3& nxt PROF_ARG) {
    const lds_ptr lds = a.lds; const int t = a.t, lane = a.lane;
    const f32x16 zc = f32x16{};
    bf16x8 ka = nxt.k[0], va0 = nxt.v0[0], va1 = nxt.v1[0], kb = nxt.k[1], vb0 = nxt.v0[1], vb1 = nxt.v1[1], kc = nxt.k[2], vc0 = nxt.v0[2], vc1 = nxt.v1[2], kd, vd0, vd1;
    const unsigned jb = a.g >> 2;
    unsigned ki = 3;
    vd1 = lds_frag(a.zp);
    lds_ptr pw = lds + L_PART + (4 * C + t) * PREC + lane * 16;
    lds_cptr pr0 = lds + L_PART + (4 * C + ((t - 1) & 3)) * PREC + lane * 16, pr1 = lds + L_PART + (4 * C + ((t - 2) & 3)) * PREC + lane * 16,
             pr2 = lds + L_PART + (4 * C + ((t - 3) & 3)) * PREC + lane * 16;
    unsigned ovo = OVOFF(0); float gf = 0.f;
    if (C) { BAR(); STAMP(4); }
    else { gf = GATE(); PIN(pw); PIN(pr0); PIN(pr1); PIN(pr2); PIN(ovo); PIN(gf);
           tag_raise(a.fl, OFLAG(1, 0)); }
    bf16x8 q0 = lds_frag(a.qp[0]); SBAR();
    bf16x8 q1 = lds_frag(a.qp[1]), q2 = lds_frag(a.qp[2]), q3 = lds_frag(a.qp[3]);
    SBAR();
    f32x16 S0 = MFMA(ka, q0, zc);
    PIN(S0); SBAR();
    PIN(q1); PIN(q2); PIN(q3); PIN(vd1); PIN(o0); PIN(o1); PIN(o2); PIN(o3); PIN(S1);
    __builtin_amdgcn_s_waitcnt(0x0F76);
#pragma unroll
    for (int i = 0; i < 8; ++i) __builtin_amdgcn_s_setprio(C);
    STAMP(3);
    SBAR();
    unsigned wso = 0;
    u64 fv = __builtin_bit_cast(u64, a.fl.vtag);
#define NEXT1() asm volatile("s_add_i32 %0, %0, 1\n\ts_xor_b32 %1, %0, %2" : "+s"(ki), "=s"(so) : "s"(jb) : "scc")
#define NEXT2() asm volatile("s_mulk_i32 %0, %1" : "+s"(so) : "i"(4 * TS) : "scc")
#define WOC 1
#define WO_OFF() do { if (C == WOC) asm volatile("s_lshl_b32 %0, %1, 4" : "=s"(wso) : "s"(wi) : "scc"); } while (0)
#define WO_DMA() do { if (C == WOC) __builtin_amdgcn_raw_ptr_buffer_load_lds(rwo, (LAS void*)(lds + L_WO + t * 16384 + wi), 16, vwo, wso, 0, 0); } while (0)
#define WO_NEXT() do { if (C == WOC) asm volatile("s_addk_i32 %0, 0x400\n\ts_and_b32 %0, %0, 0x3fff" : "+s"(wi) :: "scc"); } while (0)
#define TILE(K, V0, V1, KN, LK, LV0, LV1, NW, G) do { \
        STEP(S0, S1, o3, LV1, K, q1, zc, o0, V0, LK = BLOAD(a.rka, a.vk, so), WO_OFF(), LV0 = BLOAD(a.rka, a.vv0, so)); \
        STEP(S1, S0, o0, V1, K, q2, zc, o1, V0, LV1 = BLOAD(a.rka, a.vv1, so), , WO_DMA()); \
        STEP(S0, S1, o1, V1, K, q3, zc, o2, V0, NEXT1(), NEXT2(), ); \
        STEP(S1, S0, o2, V1, KN, q0, zc, o3, V0, __builtin_amdgcn_s_waitcnt(0x0F70 | (NW)), G, WO_NEXT()); } while (0)
#define PUTA(O, sl) (*(LAS f32x4*)(pw + (sl) * 2048) = __builtin_shufflevector(O, O, 0, 1, 2, 3))
#define PUTB(O, sl) (*(LAS f32x4*)(pw + (sl) * 2048 + 1024) = __builtin_shufflevector(O, O, 4, 5, 6, 7))
#define PUTL(O, sl) (*(LAS float*)(pw + 6144 + (sl) * 4) = O[8])
    f32x4 x0, y0, x1, y1, x2, y2; float l0, l1, l2;
#define GETP() do { x0 = *(const LAS f32x4*)pr0; x1 = *(const LAS f32x4*)(pr1 + 2048); x2 = *(const LAS f32x4*)(pr2 + 4096); \
        y0 = *(const LAS f32x4*)(pr0 + 1024); y1 = *(const LAS f32x4*)(pr1 + 3072); y2 = *(const LAS f32x4*)(pr2 + 5120); \
        l0 = *(const LAS float*)(pr0 + 6144); l1 = *(const LAS float*)(pr1 + 6148); l2 = *(const LAS float*)(pr2 + 6152); } while (0)
    if (C) {
#pragma nounroll
        do {
            TILE(ka, va0, va1, kb, kd, vd0, vd1, WOC ? 7 : 6, );
            PIN(S0); PIN(S1); PIN(o3); tag_wait(a.fl, fv, v16, fs2);
            TILE(kb, vb0, vb1, kc, ka, va0, va1, WOC ? 8 : 6, );
            TILE(kc, vc0, vc1, kd, kb, vb0, vb1, WOC ? 9 : 6, fv = tag_load(a.fl, v16, fs2));
            TILE(kd, vd0, vd1, ka, kc, vc0, vc1, WOC ? 10 : 7, );
        } while (ki != 19);
        o3 = pv_hi(vd1, S1, o3, IDX);
        STAMP(6);
        const u64 f3 = tag_load(a.fl, vf3, 0);
        cnt_wait(lds + L_CNT + 8);
        PUTA(o0, 0); PUTB(o0, 0); PUTL(o0, 0); PUTA(o1, 1); PUTB(o1, 1); PUTL(o1, 1); PUTA(o2, 2); PUTB(o2, 2); PUTL(o2, 2);
        DRAIN(); cnt_arrive(lds + L_CNT, lane);
        tag_wait(a.fl, f3, vf3, 0);
        LDB_FIRST(nxt);
        cnt_wait(lds + L_CNT);
        GETP(); gf = GATE();
    } else {
        WO_OFF(); WO_DMA(); WO_NEXT(); WO_OFF(); WO_DMA(); WO_NEXT(); SBAR();
        TILE(ka, va0, va1, kb, kd, vd0, vd1, WOC ? 6 : 9, );
        TILE(kb, vb0, vb1, kc, ka, va0, va1, WOC ? 6 : 9, );
#pragma nounroll
        do {
            TILE(kc, vc0, vc1, kd, kb, vb0, vb1, WOC ? 6 : 9, );
            TILE(kd, vd0, vd1, ka, kc, vc0, vc1, WOC ? 6 : 9, );
            TILE(ka, va0, va1, kb, kd, vd0, vd1, WOC ? 6 : 9, );
            TILE(kb, vb0, vb1, kc, ka, va0, va1, WOC ? 6 : 9, );
        } while (ki != 17);
        STEP(S0, S1, o3, vb1, kc, q1, zc, o0, vc0, , , );
        STEP(S1, S0, o0, vc1, kc, q2, zc, o1, vc0, , , );
        STEP(S0, S1, o1, vc1, kd, q0, zc, o2, vc0, __builtin_amdgcn_s_waitcnt(WOC ? 0x0F73 : 0x0F75), , );
        STEP(S1, S0, o2, vc1, kd, q1, zc, o0, vd0, , , );
        STEP(S0, S1, o0, vd1, kd, q2, zc, o1, vd0, , , );
        STEP(S1, S0, o1, vd1, kc, q3, zc, o2, vd0, PUTA(o0, 0), PUTL(o0, 0), PUTB(o0, 0));
        STEP(S0, S1, o2, vd1, kd, q3, zc, o3, vc0, PUTA(o1, 1), PUTL(o1, 1), PUTB(o1, 1));
        PUTA(o2, 2); PUTB(o2, 2); asm volatile("" :: "v"(o0), "v"(o1), "v"(o2)); SBAR();
        EX(S1, 0); EX(S1, 1); SBAR(); o3 = pv_hi(vc1, S0, o3, IDX); SBAR(); PUTL(o2, 2); __builtin_amdgcn_s_setprio(2); SBAR();
        EX(S1, 2); EX(S1, 3); EX(S1, 4); EX(S1, 5); EX(S1, 6); EX(S1, 7); SBAR(); EX(S1, 8); EX(S1, 9); SBAR();
        o3 = pv_lo(vd0, S1, o3, IDX); PIN(S1); SBAR();
        if (!WOC) DRAIN();
        CBAR(); GETP(); SBAR();
        EX(S1, 10); EX(S1, 11); EX(S1, 12); EX(S1, 13); EX(S1, 14); EX(S1, 15); SBAR();
        o3 = pv_hi(vd1, S1, o3, IDX); SBAR();
        STAMP(6);
    }
    x0 = (x0 + x1) + x2; y0 = (y0 + y1) + y2; l0 = (l0 + l1) + l2;
    PIN(x0); PIN(y0); PIN(l0); PIN(o3);
#pragma unroll
    for (int r = 0; r < 4; ++r) { o3[r] += x0[r]; o3[4 + r] += y0[r]; }
    o3[8] += l0;
    finish<C, 0>(a, o3, gf, q3, ovo);
}

template <int C>
__device__ __forceinline__ void attn_window(const Att& a, KV3& f, const unsigned (&so0)[4] PROF_ARG) {
    const lds_ptr lds = a.lds; const int t = a.t, lane = a.lane;
    const f32x16 zc = f32x16{};
    bf16x8 k[9], v0[9], v1[9];
#pragma unroll
    for (int i = 0; i < 3; ++i) { k[i] = f.k[i]; v0[i] = f.v0[i]; v1[i] = f.v1[i]; }
#define LDW(i) LDB(k[i], v0[i], v1[i], i)
    const bf16x8 qa = lds_frag(lds + L_Q + 8192 + C * 4096 + t * 1024 + lane * 16);
    const f32x16 cd = lds_f32x16(lds + L_MDIAG + lane * 16), cf = lds_f32x16(lds + L_MFAR + lane * 16);
    f32x16 oo = lds_zero16(a.zp);
    const float gt = GATE();
    f32x16 S0 = MFMA(k[0], qa, cd), S1 = MFMA(k[1], qa, zc);
    SBAR(); LDW(3); SBAR();
#pragma unroll
    for (int r = 0; r < 10; ++r) { EX(S0, r); SBAR(); }
    oo = pv_lo(v0[0], S0, oo, IDX); SBAR();
#pragma unroll
    for (int r = 10; r < 16; ++r) EX(S0, r);
    SBAR();
    STEP(S1, S0, oo, v1[0], k[2], qa, zc, oo, v0[1], LDW(4), , );
    STEP(S0, S1, oo, v1[1], k[3], qa, zc, oo, v0[2], LDW(5), , );
    STEP(S1, S0, oo, v1[2], k[4], qa, zc, oo, v0[3], LDW(6), , );
    STEP(S0, S1, oo, v1[3], k[5], qa, zc, oo, v0[4], LDW(7), , );
    STEP(S1, S0, oo, v1[4], k[6], qa, zc, oo, v0[5], LDW(8), , );
    const unsigned fa = kvflag(1, a.p >> 1, t, 0), va = (lane & 31) * FSTR;
    u64 fv = 0;
    STEP(S0, S1, oo, v1[5], k[7], qa, zc, oo, v0[6], if (!C) fv = tag_load(a.fl, va, fa), , );
    STEP(S1, S0, oo, v1[6], k[8], qa, cf, oo, v0[7], , , );
    if (C) { PIN(S0); PIN(S1); PIN(oo); tag_raise(a.fl, OFLAG(0, C)); }
    STEP(S0, S1, oo, v1[7], k[8], qa, cf, oo, v0[8], , , );
    oo = pv_hi(v1[8], S0, oo, IDX);
    STAMP(7);
    if (!C) { PIN(oo); tag_wait(a.fl, fv, va, fa); LDA_FIRST(f); }
    finish<C, 1>(a, oo, 1.f - gt, qa, OVOFF(1));
}

__global__ void __launch_bounds__(512, 2)
gmix_kernel(const char* x, const char* wA, const char* woA, const char* wB, const char* woB, const char* gw, char* out, char* ws) {
    extern __shared__ __attribute__((aligned(16))) char smem[];
    const lds_ptr lds = (lds_ptr)smem;
    const unsigned lds0 = (unsigned)(uintptr_t)lds;
    const int tid = threadIdx.x, lane = tid & 63, r32 = lane & 31, hh = lane >> 5;
    const int w = __builtin_amdgcn_readfirstlane(tid >> 6);
    const int bid = blockIdx.x, xcd = bid & 7, rr = bid >> 3;
    const int p = 4 * (xcd & 3) + (rr & 3), j = 8 * (xcd >> 2) + (rr >> 2);
    const int jo = 2 * xcd + (rr >> 4), po = rr & 15;
    const int t = w & 3, c = w >> 2;
    const int e = p & 1;
    Rev stv;
    STAMP(0);
    {
        u64 pc; const unsigned ln = (((bid & ~7) | (tid >> 6)) + (bid < CODE_LINES - 256 ? (tid & 32) * 8 : 0)) * 128;
        asm volatile("s_getpc_b64 %0\n\ts_mov_b32 m0, %2\n\ts_nop 0\n\tglobal_load_lds_dword %1, %0" : "=&s"(pc) : "v"(ln), "s"(lds0 + L_SCR) : "memory");
        SBAR();
    }
    unsigned rot = (ROTEXPR) & 7;
    unsigned vx0 = (128 * j + 32 * t + (lane >> 3)) * 1024 + (((lane & 7) ^ (lane >> 4)) * 16), vx1 = (vx0 ^ 64) + 7168, vx2 = vx0 + 14336, vx3 = vx1 + 14336;
    unsigned vw0 = (16 * t + (lane >> 3)) * 3072 + 1024 + 1024 * e + 128 * ((p >> 1) ^ QSWAP) + (((lane & 7) ^ (((lane >> 4) & 1) << 1)) * 16), vw1 = vw0 + 8 * 3072 - 1024;
    unsigned mx = lds0 + t * 4096, mw = lds0 + L_W + t * 2048, ow = rot * (64 * 3072);
    asm volatile("" : "+v"(vx0), "+v"(vx1), "+v"(vx2), "+v"(vx3), "+v"(vw0), "+v"(vw1));
    asm volatile("" : "+s"(mx), "+s"(mw), "+s"(rot), "+s"(ow));
    SBAR();
    asm volatile("" : "+s"(x), "+s"(wA), "+s"(woA), "+s"(wB), "+s"(woB), "+s"(gw), "+s"(out), "+s"(ws));
#define MK_ATT(C) Att a; a.lds = lds; a.ws = ws; a.lane = lane; a.t = t; a.p = p; a.g = 4 * j + t; a.head = 2 * p + C; a.idx = 0xdddddddd; \
    a.rkv = __builtin_amdgcn_make_buffer_rsrc(ws + WS_KV, 0, KV_BYTES, 0x00020000); a.ro = __builtin_amdgcn_make_buffer_rsrc(ws + WS_O, 0, O_BYTES, 0x00020000); \
    a.vk = lane * 16; a.vv0 = r32 < 16 ? 1024 + hh * 256 + r32 * 16 : ((r32 & ~4) == 16 ? 2048 : OOB); a.vv1 = r32 < 16 ? a.vv0 + 512 : a.vv0; a.zp = lds + L_Z + lane * 16; \
    a.rka = __builtin_amdgcn_make_buffer_rsrc(ws + WS_KV + (unsigned)(a.head * 64 + t) * TS, 0, 61 * TS, 0x00020000); \
    _Pragma("unroll") for (int i = 0; i < 4; ++i) a.qp[i] = lds + L_Q + C * 4096 + ((t + 1 + i) & 3) * 1024 + lane * 16
#define RWO __builtin_amdgcn_make_buffer_rsrc((void*)((t >> 1) ? woB : woA), 0, AB_WO, 0x00020000)
#define VWO (((t & 1) * 256 + (lane >> 2)) * 1024 + 64 * po + (lane & 3) * 16)
#define WI0 (4096 * (((rr >> 4) << 1) | (po & 1)) + WOX * 1024 * (xcd & 3))
#define MK_TAGS(f) do { f.rs = __builtin_amdgcn_make_buffer_rsrc(ws, 0, WS_KV, 0x00020000); f.v0 = lane ? OOB : 0; PIN(f.v0); } while (0)
#define MK_SO() unsigned so0[4]; _Pragma("unroll") for (int i = 0; i < 4; ++i) so0[i] = (j ^ i) * (4 * TS)
    if (c) {
#define DMA1(s) do { const unsigned ks_ = ((s) + rot) & 7; \
        glds16x4(vx0, vx1, vx2, vx3, x + ks_ * 128, mx + ((s) & 3) * 16384); glds16x2(vw0, vw1, wA + ks_ * (64 * 3072), mw + ((s) & 3) * 8192); } while (0)
#define DMA2(s) do { const unsigned ks_ = (((s) + rot) & 7) * (64 * 3072); \
        glds16x2(vq0, vq1, wq + ks_, mq + ((s) == 14 ? L_Q14 : ((s) & 3) * 16384)); glds16x2(vw0, vw1, wy + ks_, mw + ((s) & 3) * 8192); SBAR(); } while (0)
#define LBAR(N) asm volatile("s_waitcnt vmcnt(" #N ")\n\ts_barrier" ::: "memory")
        STAMP(20);
        glds16x4(vx0, vx1, vx2, vx3, x + rot * 128, mx); glds16x2(vw0, vw1, wA + ow, mw);
        SBAR();
        const unsigned k1 = (1 + rot) & 7;
        glds16x2(vx0, vx1, x + k1 * 128, mx + 16384);
        SBAR();
        if (t == 0) asm volatile("s_mov_b32 m0, %2\n\ts_nop 0\n\tglobal_load_lds_dword %0, %1 sc1" :: "v"((lane & 1) * 4), "s"(ws + WS_EPOCH), "s"(lds0 + L_EP) : "memory");
        unsigned vq0 = (32 * (t & 1) + 16 * e + (lane >> 2)) * 3072 + 64 * (p ^ ((t >> 1) * PBX)) + (lane & 3) * 16 + 1024, vq1 = vq0 + (e ? -16 * 3072 : 16 * 3072) - 1024;
        unsigned vwo = VWO;
        MK_ATT(1);
        MK_TAGS(a.fl);
        int go = 4 * jo + t;
        asm volatile("" : "+s"(go));
        unsigned v16 = (lane & 15) * 8, vf3 = hh ? WS_OFLAG + (64 + go) * 256 + v16 : window_flag(a);
        PIN(vq0); PIN(vq1); PIN(vwo); PIN(a.vk); PIN(a.vv0); PIN(a.vv1); PIN(a.qp[0]); PIN(a.qp[1]); PIN(a.qp[2]); PIN(a.qp[3]); PIN(a.zp); PIN(v16); PIN(vf3); PIN(a.idx);
        SBAR();
        STAMP(30);
        glds16x2(vx0, vx1, x + k1 * 128 + 16384, mx + 16384 + 2048); glds16x2(vw0, vw1, wA + k1 * (64 * 3072), mw + 8192);
        STAMP(22);
        SBAR();
        lds_cptr wo = lds + L_WO + (4 * hh + ((lane & 15) >> 2)) * 64 + ((lane >> 4) & 1) * 32 + (lane & 3) * 8, wg[4];
        unsigned sg[4];
#pragma unroll
        for (int q = 0; q < 4; ++q) { const int gq = ((p >> 2) + 1 + q) & 3; sg[q] = (unsigned)go * OS + gq * 8192; wg[q] = wo + gq * 8192; PIN(wg[q]); asm volatile("" : "+s"(sg[q])); }
        unsigned vout = (32 * go + (lane >> 2)) * 1024 + 64 * po + (lane & 3) * 16;
        lds_ptr otw = lds + L_PART + (4 + t) * PREC + r32 * 80 + hh * 8;
        lds_cptr otr = lds + L_PART + (4 + t) * PREC + (lane >> 2) * 80 + (lane & 3) * 16;
        PIN(wo); PIN(vout); PIN(otw); PIN(otr);
        SBAR();
#pragma nounroll
        for (int s = 2; s < 8; ++s) { LBAR(6); DMA1(s);
            if (s == 2) STAMP(1); if (s == 3) STAMP(2);
        }
        const unsigned mq = lds0 + t * 2048;
        const char* wq = ((t >> 1) ? wB : wA) - 1024;
        const char* wy = wB + 128 * ((((p >> 1) ^ QSWAP) ^ (PBX >> 1)) - ((p >> 1) ^ QSWAP));
        LBAR(6); STAMP(14); DMA2(8);
        LBAR(4); STAMP(15); DMA2(9);
        make_tag(a.fl, *(const LAS u32x2*)(lds + L_EP));
        LBAR(4); STAMP(16); DMA2(10);
        const unsigned k7 = ((7 + rot) & 7) * (64 * 3072), sx = mq + L_Q15, sw = mw + 32768;
        LBAR(4); STAMP(17); glds16(vq0, wq + k7, sx); DMA2(11);
        f32x16 oo = lds_zero16(a.zp), o1 = lds_zero16(a.zp); PIN(oo); PIN(o1);
        LBAR(5); STAMP(18); glds16(vw0, wy + k7, sw); DMA2(12);
        f32x16 o2 = lds_zero16(a.zp), o3 = lds_zero16(a.zp); PIN(o2); PIN(o3);
        LBAR(5); STAMP(19); glds16(vq1, wq + k7 + 1024, sx + 1024); DMA2(13);
        f32x16 S1 = lds_zero16(a.zp); PIN(S1);
        LBAR(5); STAMP(21); glds16(vw1, wy + k7 + 1024, sw + 1024); DMA2(14);
        __amdgpu_buffer_rsrc_t rwo = RWO; unsigned wi0 = WI0;
        MK_SO();
        asm volatile("" : "+s"(a.rka), "+s"(rwo), "+s"(wi0), "+s"(so0[0]), "+s"(so0[1]), "+s"(so0[2]), "+s"(so0[3]));
        unsigned fs1 = kvflag(0, p >> 1, (j >> 3) * 32 + t, 0), fs2 = kvflag(0, p >> 1, ((j >> 3) ^ 1) * 32 + t, 0);
        asm volatile("" : "+s"(a.fl.rs), "+s"(fs1), "+s"(fs2));
        __builtin_amdgcn_s_setprio(1);
        LBAR(5); STAMP(24);
        const u64 fv = tag_load(a.fl, v16, fs1);
        LBAR(1); STAMP(29);
        tag_wait(a.fl, fv, v16, fs1);
        STAMP(5);
        KV3 kw;
        LDA_FIRST(kw);
        attn_full<1>(a, oo, o1, o2, o3, S1, so0[3], rwo, vwo, wi0, v16, fs2, vf3, kw PROF_PASS);
        STAMP(23);
        attn_window<1>(a, kw, so0 PROF_PASS);
        STAMP(8);
#define OBR(n) ((n) >> 4 == 1 ? 0 : 1)
#define OHD(n) (2 * ((n) & 15) + ((n) >= 16))
        const __amdgpu_buffer_rsrc_t roe = a.ro;
#define OLD(n) BLOAD(roe, a.vk, (unsigned)(OBR(n) * 64 + go) * OS + OHD(n) * 1024)
#define OWF(n) tr_frag(wo + OBR(n) * 32768 + OHD(n) * 1024, wo + OBR(n) * 32768 + OHD(n) * 1024 + 512)
#define OIN 6
        bf16x8 oe[48];
#pragma unroll
        for (int n = 0; n < 48; ++n) {
            if (n == 16) { const u64 v = tag_load(a.fl, v16, WS_OFLAG + go * 256 + 128); tag_raise(a.fl, OFLAG(1, 1)); tag_wait(a.fl, v, v16, WS_OFLAG + go * 256 + 128); STAMP(9); }
            if (n == 32) tag_wait(a.fl, v16, WS_OFLAG + (64 + go) * 256 + 128);
            oe[n] = OLD(n); SBAR();
            if (n >= OIN - 1) __builtin_amdgcn_s_waitcnt(0x0F70 | (OIN - 1));
            SBAR();
        }
        bf16x8 wn[4];
        __builtin_amdgcn_s_waitcnt(0x0F70);
#define NEARLY 10
#define NMID 34
        __amdgpu_buffer_rsrc_t rout = __builtin_amdgcn_make_buffer_rsrc(out, 0, 2048 * 1024, 0x00020000);
        asm volatile("" : "+s"(rout));
        f32x16 ya = f32x16{};
        bf16x8 wf[16], of[16];
#define FLD(i) of[i] = BLOAD(a.ro, a.vk, sg[(i) >> 2] + ((i) & 3) * 2048)
#define GRP_BAD(q) ({ unsigned mx_ = 0; _Pragma("unroll") for (int i = 4 * (q); i < 4 * (q) + 4; ++i) { const u32x4 u_ = __builtin_bit_cast(u32x4, of[i]); \
            mx_ = __builtin_elementwise_max(__builtin_elementwise_max(mx_, u_[0]), u_[1]); mx_ = __builtin_elementwise_max(__builtin_elementwise_max(mx_, u_[2]), u_[3]); } \
            __any(mx_ == ~0u); })
#define GRP_LD(q) do { _Pragma("unroll") for (int i = 4 * (q); i < 4 * (q) + 4; ++i) FLD(i); } while (0)
#define ESTEP 2
#define MSTEP 2
#define WF0 28
        f32x16 yb, yc, yd;
        unsigned pend = 15, mx3 = 0;
        STAMP(10);
        BAR();
#pragma unroll
        for (int n = 0; n < 4; ++n) { wn[n] = OWF(n); PIN(wn[n]); }
        BAR();
#pragma unroll
        for (int n = 0; n < 48; ++n) {
            ya = MFMA(wn[n & 3], oe[n], ya); SBAR();
            if (n >= NEARLY && n < NEARLY + 12 * ESTEP && (n - NEARLY) % ESTEP == 0) FLD(((n - NEARLY) / ESTEP) & 15);
            if (n >= NMID && n < NMID + 4 * MSTEP && (n - NMID) % MSTEP == 0) FLD(((n - NMID) / MSTEP + 12) & 15);
            if (n + 4 < 48) wn[n & 3] = OWF(n + 4);
            if (n >= WF0 && n < WF0 + 16) wf[(n - WF0) & 15] = tr_frag(wg[((n - WF0) & 15) >> 2] + ((n - WF0) & 3) * 2048, wg[((n - WF0) & 15) >> 2] + ((n - WF0) & 3) * 2048 + 512);
            if (n == 44) { yb = lds_zero16(a.zp); } if (n == 45) { yc = lds_zero16(a.zp); } if (n == 46) { yd = lds_zero16(a.zp); }
            SBAR();
        }
        STAMP(31);
#define GRP_TRY(q, Y) do { if (pend & (1u << (q))) { if (!GRP_BAD(q)) { \
            _Pragma("unroll") for (int i = 4 * (q); i < 4 * (q) + 4; ++i) Y = MFMA(wf[i], of[i], Y); pend ^= 1u << (q); } } } while (0)
        unsigned spins = SPIN_CAP;
#pragma nounroll
        do {
            GRP_TRY(0, ya); GRP_TRY(1, yb); GRP_TRY(2, yc); GRP_TRY(3, yd);
            asm volatile("s_sub_u32 %1, %1, 1\n\ts_cselect_b32 %0, 0, %0" : "+s"(pend), "+s"(spins) :: "memory", "scc");
            if (pend & 1) GRP_LD(0); if (pend & 2) GRP_LD(1); if (pend & 4) GRP_LD(2); if (pend & 8) GRP_LD(3);
        } while (pend);
        ya = (ya + yb) + (yc + yd);
#define OUT_AUX 16
#pragma unroll
        for (int q = 0; q < 4; ++q) *(LAS u32x2*)(otw + q * 16) = (u32x2){cvtpk(ya[4 * q], ya[4 * q + 1]), cvtpk(ya[4 * q + 2], ya[4 * q + 3])};
        asm volatile("" ::: "memory");
#pragma unroll
        for (int s = 0; s < 2; ++s) __builtin_amdgcn_raw_buffer_store_b128(*(const LAS u32x4*)(otr + s * 1280), rout, vout + s * 16384, 0, OUT_AUX);
        if (bid == 0 && tid == 256) __hip_atomic_store((GAS u64*)(ws + WS_EPOCH), a.fl.tag, RLX_ST);
        STAMP(12);
    } else {
        glds16((((lane >> 3) + rot) & 7) * 128 + (lane & 7) * 16, gw, lds0 + L_GW + t * 1024);
#pragma unroll
        for (int m = 0; m < 2; ++m) {
            f32x4 mk;
#pragma unroll
            for (int i = 0; i < 4; ++i) { const bool le = (i + 8 * t + 4 * hh) <= r32; mk[i] = (le != (m != 0)) ? 0.f : -__builtin_inff(); }
            *(LAS f32x4*)(lds + L_MDIAG + m * 4096 + t * 1024 + lane * 16) = mk;
        }
        if (tid < 4) *(LAS unsigned*)(lds + L_CNT + tid * 4) = 0;
        *(LAS unsigned*)(lds + L_Z + tid * 4) = 0;
        MK_ATT(0);
        MK_TAGS(a.fl);
        unsigned vwf = window_flag(a);
        PIN(a.vv0); PIN(a.vv1); PIN(vwf);
        STAMP(21);
        if (e) project<1>(lds, a.rkv, a.ro, lane, t, p, j, a.fl PROF_PASS); else project<0>(lds, a.rkv, a.ro, lane, t, p, j, a.fl PROF_PASS);
        cnt_wait(lds + L_CNT + 8);
        __builtin_amdgcn_s_sleep(BSLP);
        STAMP(24);
        tag_wait(a.fl, vwf, 0);
        STAMP(25);
        KV3 kw;
        LDB_FIRST(kw);
        MK_SO();
        attn_window<0>(a, kw, so0 PROF_PASS);
        STAMP(23);
        PIN(a.idx);
        attn_full<0>(a, lds_zero16(a.zp), lds_zero16(a.zp), lds_zero16(a.zp), lds_zero16(a.zp), lds_zero16(a.zp), so0[3], RWO, VWO, WI0, 0, 0, 0, kw PROF_PASS);
        BAR();
        STAMP(8);
    }
    DRAIN();
    STAMP(13);
    if (c == RROLE && (bid & 1) == 1) while (__builtin_amdgcn_s_memrealtime() < stv.r0 + 6000 + stv.d) __builtin_amdgcn_s_sleep(1);
}

extern "C" void kernel_launch(void* const* d_in, const int* in_sizes, int n_in, void* d_out, int out_size, void* d_ws, size_t ws_size,
                              hipStream_t stream) {
    (void)in_sizes; (void)n_in; (void)out_size; (void)ws_size;
    (void)hipFuncSetAttribute((const void*)gmix_kernel, hipFuncAttributeMaxDynamicSharedMemorySize, L_END);
    gmix_kernel<<<256, 512, L_END, stream>>>((const char*)d_in[0], (const char*)d_in[1], (const char*)d_in[2], (const char*)d_in[4],
                                             (const char*)d_in[5], (const char*)d_in[6], (char*)d_out, (char*)d_ws);
}
```

```cpp
#include <hip/hip_runtime.h>
#include <hip/hip_bf16.h>
#define OUT_LDS

typedef __attribute__((ext_vector_type(8))) __bf16 bf16x8;
typedef __attribute__((ext_vector_type(16))) __bf16 bf16x16;
typedef __attribute__((ext_vector_type(16))) float f32x16;
typedef __attribute__((ext_vector_type(8))) float f32x8;
typedef __attribute__((ext_vector_type(4))) float f32x4;
typedef __attribute__((ext_vector_type(4))) unsigned u32x4;
typedef __attribute__((ext_vector_type(2))) unsigned u32x2;
typedef __attribute__((ext_vector_type(2))) __bf16 bf16x2;
typedef short s16x4 __attribute__((ext_vector_type(4)));
typedef unsigned long long u64;
#define GAS __attribute__((address_space(1)))
#define LAS __attribute__((address_space(3)))
typedef LAS const char* lds_cptr;
typedef LAS char* lds_ptr;

constexpr unsigned WS_EPOCH = 0;
#define FSTR 8
constexpr unsigned WS_OFLAG = 65536;
constexpr unsigned WS_KVFLAG = 131072;
constexpr unsigned WS_G = 524288;
constexpr unsigned WS_KV = 1u << 20;
constexpr unsigned TS = 2176;
constexpr unsigned KV_BYTES = 2u * 32u * 64u * TS;
#define OSTR 32896
constexpr unsigned WS_O = 10u << 20;
constexpr unsigned OS = OSTR;
constexpr unsigned O_BYTES = 128u * OS;
constexpr unsigned WS_STAMP = 16u << 20;
constexpr unsigned OOB = 0x7ff00000u;
constexpr unsigned L_W = 65536;
constexpr unsigned L_WO = 0, L_PART = 65536, PREC = 7168, L_Q14 = 106496, L_Q15 = 114688, L_MDIAG = 122880, L_MFAR = 126976, L_Q = 131072,
                   L_GW = 147456, L_Z = 151552, L_G = 152576, L_CNT = 153088, L_EP = 153344, L_SCR = 153600, L_END = 154112;
constexpr float QSCALE = 0.25f * 1.4426950408889634f;
#define LMAXV 1.2676506e30f
constexpr float LMAX = LMAXV, LMIN = 9.094947e-13f;
constexpr unsigned SPIN_CAP = 1u << 17;

#define SBAR() __builtin_amdgcn_sched_barrier(0)
#define PIN(x) asm volatile("" : "+v"(x))
#define MFMA(a, b, c) __builtin_amdgcn_mfma_f32_32x32x16_bf16(a, b, c, 0, 0, 0)
#define BAR() asm volatile("s_barrier" ::: "memory")
#define CBAR() asm volatile("s_waitcnt lgkmcnt(0)\n\ts_barrier" ::: "memory")
#define DRAIN() asm volatile("s_waitcnt vmcnt(0)" ::: "memory")
#define PACEN 6
#define PACE() asm volatile("s_nop %0" :: "i"(PACEN) : "memory")
#define RLX_AGENT __ATOMIC_RELAXED, __HIP_MEMORY_SCOPE_AGENT
#define FL_SCOPE __HIP_MEMORY_SCOPE_AGENT
#define RLX_ST __ATOMIC_RELAXED, FL_SCOPE
#define STAMP(i)

__device__ __forceinline__ void glds16(unsigned voff, const void* sbase, unsigned ldsaddr) {
    asm volatile("s_mov_b32 m0, %2\n\ts_nop 0\n\tglobal_load_lds_dwordx4 %0, %1" ::"v"(voff), "s"(sbase), "s"(ldsaddr) : "memory");
}
__device__ __forceinline__ void glds16x2(unsigned v0, unsigned v1, const void* sbase, unsigned ldsaddr) {
    asm volatile("s_mov_b32 m0, %3\n\ts_nop 0\n\tglobal_load_lds_dwordx4 %0, %2\n\tglobal_load_lds_dwordx4 %1, %2 offset:1024"
                 ::"v"(v0), "v"(v1), "s"(sbase), "s"(ldsaddr) : "memory");
}
__device__ __forceinline__ void glds16x4(unsigned v0, unsigned v1, unsigned v2, unsigned v3, const void* sbase, unsigned ldsaddr) {
    asm volatile("s_mov_b32 m0, %5\n\ts_nop 0\n\tglobal_load_lds_dwordx4 %0, %4\n\tglobal_load_lds_dwordx4 %1, %4 offset:1024\n\t"
                 "global_load_lds_dwordx4 %2, %4 offset:2048\n\tglobal_load_lds_dwordx4 %3, %4 offset:3072"
                 ::"v"(v0), "v"(v1), "v"(v2), "v"(v3), "s"(sbase), "s"(ldsaddr) : "memory");
}
__device__ __forceinline__ unsigned cvtpk(float lo, float hi) {
    typedef __attribute__((ext_vector_type(2))) float f32x2;
    return __builtin_bit_cast(unsigned, __builtin_convertvector((f32x2){lo, hi}, bf16x2));
}
__device__ __forceinline__ bf16x8 as_frag(u32x4 v) { return __builtin_bit_cast(bf16x8, v); }
__device__ __forceinline__ bf16x8 lds_frag(lds_cptr p) { return *(const LAS bf16x8*)p; }
__device__ __forceinline__ bf16x8 tr_frag(lds_cptr p0, lds_cptr p1) {
    s16x4 a = __builtin_amdgcn_ds_read_tr16_b64_v4i16((LAS s16x4*)p0);
    s16x4 b = __builtin_amdgcn_ds_read_tr16_b64_v4i16((LAS s16x4*)p1);
    return __builtin_bit_cast(bf16x8, __builtin_shufflevector(a, b, 0, 1, 2, 3, 4, 5, 6, 7));
}
__device__ __forceinline__ f32x16 pv_lo(bf16x8 v, const f32x16& p, f32x16 acc, int idx) {
    f32x8 h = __builtin_shufflevector(p, p, 0, 1, 2, 3, 4, 5, 6, 7);
    return __builtin_amdgcn_smfmac_f32_32x32x32_bf16(v, __builtin_bit_cast(bf16x16, h), acc, idx, 0, 0);
}
__device__ __forceinline__ f32x16 pv_hi(bf16x8 v, const f32x16& p, f32x16 acc, int idx) {
    f32x8 h = __builtin_shufflevector(p, p, 8, 9, 10, 11, 12, 13, 14, 15);
    return __builtin_amdgcn_smfmac_f32_32x32x32_bf16(v, __builtin_bit_cast(bf16x16, h), acc, idx, 0, 0);
}
#define MX3(a, b, c) __builtin_fmaxf(__builtin_fmaxf((a), (b)), (c))
__device__ __forceinline__ float max16(const f32x16& s) {
    float a = MX3(s[0], s[1], s[2]), b = MX3(s[3], s[4], s[5]);
    a = MX3(a, s[6], s[7]); b = MX3(b, s[8], s[9]); a = MX3(a, s[10], s[11]); b = MX3(b, s[12], s[13]);
    return MX3(a, b, __builtin_fmaxf(s[14], s[15]));
}
__device__ __forceinline__ float halfmax(float m) {
    auto r = __builtin_amdgcn_permlane32_swap(__float_as_uint(m), __float_as_uint(m), false, false);
    return __builtin_fmaxf(__uint_as_float(r[0]), __uint_as_float(r[1]));
}
__device__ __forceinline__ f32x16 lds_f32x16(lds_cptr p) {
    f32x4 a = *(const LAS f32x4*)p, b = *(const LAS f32x4*)(p + 1024), c = *(const LAS f32x4*)(p + 2048), d = *(const LAS f32x4*)(p + 3072);
    f32x8 lo = __builtin_shufflevector(a, b, 0, 1, 2, 3, 4, 5, 6, 7), hi = __builtin_shufflevector(c, d, 0, 1, 2, 3, 4, 5, 6, 7);
    return __builtin_shufflevector(lo, hi, 0, 1, 2, 3, 4, 5, 6, 7, 8, 9, 10, 11, 12, 13, 14, 15);
}
#define ST_AUX 16
#define LD_AUX 16
#define BLOAD(rs, vo, so) as_frag(__builtin_amdgcn_raw_buffer_load_b128(rs, vo, so, LD_AUX))
#define BSTORE(v, rs, vo, so) __builtin_amdgcn_raw_buffer_store_b128(v, rs, (vo) + (so), 0, ST_AUX)

#define POLLSLP 8
#define BSLP 36
struct Tags { __amdgpu_buffer_rsrc_t rs; u64 tag; u32x2 vtag; unsigned v0; };
__device__ __forceinline__ u64 tag_load(const Tags& f, unsigned vo, unsigned so) {
    return __builtin_bit_cast(u64, __builtin_amdgcn_raw_buffer_load_b64(f.rs, vo, so, LD_AUX));
}
__device__ __forceinline__ void tag_raise(const Tags& f, unsigned so) {
    DRAIN();
    __builtin_amdgcn_raw_buffer_store_b64(f.vtag, f.rs, f.v0, so, ST_AUX);
}
__device__ __forceinline__ void tag_wait(const Tags& f, u64 v, unsigned vo, unsigned so) {
    if (__builtin_expect(!__all(v == f.tag), 0)) {
#pragma nounroll
        for (unsigned spins = 0; spins < SPIN_CAP; ++spins) {
            __builtin_amdgcn_s_sleep(POLLSLP);
            asm volatile("" ::: "memory");
            if (__all(tag_load(f, vo, so) == f.tag)) break;
        }
    }
    asm volatile("" ::: "memory");
}
__device__ __forceinline__ void tag_wait(const Tags& f, unsigned vo, unsigned so) { tag_wait(f, tag_load(f, vo, so), vo, so); }

__device__ __forceinline__ void cnt_arrive(lds_ptr cnt, int lane) {
    asm volatile("s_waitcnt lgkmcnt(0)" ::: "memory");
    if (lane == 0) __hip_atomic_fetch_add((LAS unsigned*)cnt, 1u, __ATOMIC_RELAXED, __HIP_MEMORY_SCOPE_WORKGROUP);
}
__device__ __forceinline__ void cnt_wait(lds_ptr cnt) {
#pragma nounroll
    for (unsigned spins = 0; spins < (1u << 22); ++spins) {
        if (__builtin_amdgcn_readfirstlane(__hip_atomic_load((LAS unsigned*)cnt, __ATOMIC_RELAXED, __HIP_MEMORY_SCOPE_WORKGROUP)) == 4) break;
        __builtin_amdgcn_s_sleep(1);
    }
    asm volatile("" ::: "memory");
}
__device__ __forceinline__ unsigned kvflag(int br, int quad, int tile, int type) {
    return WS_KVFLAG + (unsigned)(((((br * 8 + quad) * 4 + (tile & 3)) * 2 + (tile >> 5)) * 2 + type) * 8 + ((tile >> 2) & 7)) * FSTR;
}

__device__ __forceinline__ void slow_rows(__amdgpu_buffer_rsrc_t rs, unsigned sbase, int lo, int hi, int g, bool band, bf16x8 q,
                                          unsigned vk, unsigned vv0, unsigned vv1, lds_cptr lds, int lane, int idx, f32x16& acc) {
    float m = -__builtin_inff();
    for (int pass = 0; pass < 2; ++pass) {
        if (pass) { m = halfmax(m); acc = f32x16{}; }
        for (int kt = lo; kt <= hi; ++kt) {
            const unsigned so = sbase + (unsigned)kt * TS;
            const bf16x8 k = BLOAD(rs, vk, so), v0 = BLOAD(rs, vv0, so), v1 = BLOAD(rs, vv1, so);
            f32x16 c = f32x16{};
            if (band && kt == g) c = lds_f32x16(lds + L_MDIAG + lane * 16);
            if (band && kt == g - 8) c = lds_f32x16(lds + L_MFAR + lane * 16);
            f32x16 s = MFMA(k, q, c);
            if (!pass) m = __builtin_fmaxf(m, max16(s));
            else {
#pragma unroll
                for (int r = 0; r < 16; ++r) s[r] = __builtin_amdgcn_exp2f(s[r] - m);
                acc = pv_lo(v0, s, acc, idx); acc = pv_hi(v1, s, acc, idx);
            }
        }
    }
}

#define PK4(A, o) ((u32x4){cvtpk(A[o], A[o + 1]), cvtpk(A[o + 2], A[o + 3]), cvtpk(A[o + 4], A[o + 5]), cvtpk(A[o + 6], A[o + 7])})
#define PROF_ARG
#define PROF_PASS
__device__ __forceinline__ void make_tag(Tags& f, u32x2 e) {
    f.tag = (((u64)(unsigned)__builtin_amdgcn_readfirstlane(e[1]) << 32) | (unsigned)__builtin_amdgcn_readfirstlane(e[0])) * 0x9E3779B97F4A7C15ull + 0x632BE59BD9B4E019ull;
    asm volatile("" : "+s"(f.tag));
    f.vtag = __builtin_bit_cast(u32x2, f.tag);
    PIN(f.vtag);
}
template <int E>
__device__ __forceinline__ u32x4 pack_kv(int i, const f32x16& a) {
    const int s = i & 1;
    if (E == 0) return PK4(a, 8 * s);
    auto x = __builtin_amdgcn_permlane32_swap(cvtpk(a[8 * s], a[8 * s + 1]), cvtpk(a[8 * s + 4], a[8 * s + 5]), false, false);
    auto y = __builtin_amdgcn_permlane32_swap(cvtpk(a[8 * s + 2], a[8 * s + 3]), cvtpk(a[8 * s + 6], a[8 * s + 7]), false, false);
    return (u32x4){x[0], y[0], x[1], y[1]};
}
#define PUT_KV(i, v, vo) BSTORE(v, rkv, vo, ((i) < 2 ? 64 * TS : 0) + ((i) & 1) * (E ? 512 : 128 * TS))
#define PBX 8
#define GE 1
#define XQ(p) (((p) >> 3) * 4 + (((p) + 1 + (((p) >> 2) & 1) * 2) & 3))
template <int E>
__device__ __forceinline__ void project(lds_ptr lds, __amdgpu_buffer_rsrc_t rkv, __amdgpu_buffer_rsrc_t ro, int lane, int t, int p, int j, Tags& fl PROF_ARG) {
    const int r32 = lane & 31, hh = lane >> 5, qd4 = XQ(p);
    const unsigned q4 = (lane & 15) >> 2, wcol = ((lane >> 4) & 1) * 32 + (lane & 3) * 8;
    lds_cptr xa[4];
#pragma unroll
    for (int ks = 0; ks < 4; ++ks) xa[ks] = lds + (32 * t + r32) * 128 + (((hh ^ ((r32 >> 1) & 7)) * 16) ^ (ks * 32));
    unsigned w1o = L_W + (8 * hh + q4) * 128 + ((lane >> 4) & 1) * 64 + (q4 >> 1) * 32 + (lane & 3) * 8, w0o = w1o ^ 32;
    PIN(w0o); PIN(w1o);
    const lds_cptr w0 = lds + w0o, w1 = lds + w1o, wq = lds + (8 * hh + q4) * 64 + wcol;
#define XRD(n) lds_frag(xa[(n) & 3] + (((n) >> 2) & 3) * 16384)
#define WRD(b, n) tr_frag(b + ((n) & 15) * 2048, b + ((n) & 15) * 2048 + 512)
#define YOF(n) ((n) >= 28 ? 32768 + ((n) & 3) * 2048 : ((n) & 15) * 2048)
#define YRD(b, n) tr_frag(b + YOF(n), b + YOF(n) + 512)
#define QOF(br, n) (((n) >= 28 ? L_Q15 - L_Q14 : ((n) >= 24 ? 0 : (((n) >> 2) & 3) * 16384)) + (br) * 4096 + ((n) & 3) * 1024)
#define QRD(br, n) tr_frag(((n) >= 24 ? wqh : wq) + QOF(br, n), ((n) >= 24 ? wqh : wq) + QOF(br, n) + 256)
#define MM(W, X, A) (E ? MFMA(X, W, A) : MFMA(W, X, A))
    bf16x8 xf[32], f0[4], f1[4];
    f32x16 a0 = f32x16{}, a1 = f32x16{};
    unsigned gwo = (lane & 15) == ((lane >> 4) & 1) ? L_GW + t * 1024 + hh * 16 : L_Z;
    if (E == GE) PIN(gwo);
    f32x4 zg = f32x4{};
    bf16x8 gf[4];
#define RD1(n) do { xf[n] = XRD(n); f0[(n) & 3] = WRD(w0, n); f1[(n) & 3] = WRD(w1, n); SBAR(); } while (0)
#define GRD(n) do { if (E == GE) gf[(n) & 3] = lds_frag(lds + gwo + (n) * 32); } while (0)
#define GMM(n) do { if (E == GE) zg = __builtin_amdgcn_mfma_f32_16x16x32_bf16(gf[(n) & 3], xf[n], zg, 0, 0, 0); } while (0)
#define MM1(n) do { a0 = MM(f0[(n) & 3], xf[n], a0); a1 = MM(f1[(n) & 3], xf[n], a1); SBAR(); } while (0)
#pragma unroll
    for (int s = 0; s < 8; ++s) {
        if (s == 0) CBAR(); else BAR();
        SBAR();
        RD1(4 * s); RD1(4 * s + 1);
        if (s) { MM1(4 * s - 1); GMM(4 * s - 4); GMM(4 * s - 3); GMM(4 * s - 2); GMM(4 * s - 1); SBAR(); }
        RD1(4 * s + 2); MM1(4 * s); RD1(4 * s + 3); MM1(4 * s + 1);
        if (s == 0 && E == GE) DRAIN();
        GRD(4 * s); GRD(4 * s + 1); GRD(4 * s + 2); GRD(4 * s + 3); SBAR();
        MM1(4 * s + 2);
        SBAR();
    }
    const unsigned tile = 4 * j + t;
    unsigned vo = (unsigned)(4 * qd4 * 64 + tile) * TS + (E ? (r32 >> 4) * (128 * TS) + 1024 + hh * 256 + (r32 & 15) * 16 : lane * 16);
    unsigned wqo = L_Q14 + (8 * hh + q4) * 64 + wcol;
    PIN(vo); PIN(wqo);
    const lds_cptr wqh = lds + wqo;
    MM1(31); GMM(28); GMM(29); GMM(30); GMM(31); SBAR();
#define PRE8 2
#define PUBN 7
#define LATE0 99
    u32x4 pk[4];
#pragma unroll
    for (int i = 0; i < 4; ++i) { pk[i] = pack_kv<E>(i, i < 2 ? a0 : a1); PIN(pk[i]); if (i < PRE8) PUT_KV(i, pk[i], vo); SBAR(); }
    if (E == GE) BSTORE(((u32x4){0x3f803f80u, 0x3f803f80u, 0x3f803f80u, 0x3f803f80u}), rkv,
                   (unsigned)((hh * 32 + 4 * (qd4 ^ (hh * (PBX >> 1))) + (r32 >> 3)) * 64 + tile) * TS + 2048 + (lane & 7) * 16, 0);
    if (E == GE) __builtin_amdgcn_raw_buffer_store_b64((u32x2){__float_as_uint(__builtin_amdgcn_rcpf(1.f + __expf(-zg[0]))), __float_as_uint(__builtin_amdgcn_rcpf(1.f + __expf(-zg[1])))},
                                                  fl.rs, lane < 16 ? lane * 8 : OOB, WS_G + (qd4 * 64 + tile) * 128, ST_AUX);
    SBAR();
    BAR(); SBAR();
    bf16x8 u0 = QRD(0, 0), u1 = QRD(1, 0), y0 = YRD(w0, 0), y1 = YRD(w1, 0);
    const u32x2 tg = *(const LAS u32x2*)(lds + L_EP);
    f32x16 b0 = f32x16{}, b1 = f32x16{}, aQ0 = f32x16{}, aQ1 = f32x16{};
    SBAR();
#pragma unroll
    for (int n = 0; n < 24; ++n) {
        aQ0 = MFMA(u0, xf[n], aQ0); SBAR();
        if (n == 1) { make_tag(fl, tg); SBAR(); }
        if (n == PUBN) { tag_raise(fl, kvflag(0, qd4, tile, E)); SBAR(); }
        if ((n & 3) == 3) {
            if (PRE8 + (n >> 2) < 2) { PUT_KV((PRE8 + (n >> 2)) & 3, pk[(PRE8 + (n >> 2)) & 3], vo); SBAR(); }
            if (n == LATE0) { PUT_KV(2, pk[2], vo); SBAR(); }
            if (n == LATE0 + 4) { PUT_KV(3, pk[3], vo); SBAR(); }
            BAR(); SBAR();
        }
        u0 = QRD(0, n + 1); SBAR();
        aQ1 = MFMA(u1, xf[n], aQ1); SBAR();
        u1 = n < 23 ? QRD(1, n + 1) : QRD(0, 25); SBAR();
        b0 = MM(y0, xf[n], b0); SBAR(); y0 = n < 23 ? YRD(w0, n + 1) : QRD(0, 26); SBAR();
        b1 = MM(y1, xf[n], b1); SBAR(); y1 = n < 23 ? YRD(w1, n + 1) : QRD(0, 27); SBAR();
    }
    aQ0 = MFMA(u0, xf[24], aQ0); SBAR(); u0 = QRD(0, 28); SBAR(); aQ0 = MFMA(u1, xf[25], aQ0); SBAR(); u1 = QRD(0, 29); SBAR();
    aQ0 = MFMA(y0, xf[26], aQ0); SBAR(); y0 = QRD(0, 30); SBAR(); aQ0 = MFMA(y1, xf[27], aQ0); SBAR(); y1 = QRD(0, 31); SBAR();
    aQ0 = MFMA(u0, xf[28], aQ0); SBAR(); aQ0 = MFMA(u1, xf[29], aQ0); SBAR(); u1 = QRD(1, 24); SBAR();
    aQ0 = MFMA(y0, xf[30], aQ0); SBAR(); y0 = YRD(w0, 24); SBAR(); aQ0 = MFMA(y1, xf[31], aQ0); SBAR(); y1 = YRD(w1, 24); SBAR();
    const lds_ptr qd = lds + L_Q + t * 1024 + lane * 16;
#define QBAR 4
#define NFREE 3
    f32x8 qs; u32x4 qk;
#define QEPI(m) do { if ((m) == 1) { PIN(aQ0); qs = __builtin_shufflevector(aQ0, aQ0, 8, 9, 10, 11, 12, 13, 14, 15) * QSCALE; PIN(qs); } \
        if ((m) == 2) { qk = PK4(qs, 0); *(LAS u32x4*)(qd + 4096) = qk; } \
        if ((m) == QBAR) { __builtin_amdgcn_s_setprio(2); __builtin_amdgcn_s_waitcnt(0xC07F | ((2 * (QBAR - 2)) << 8)); BAR(); STAMP(4); } \
        if ((m) > QBAR + NFREE) PACE(); SBAR(); } while (0)
#pragma unroll
    for (int n = 24; n < 32; ++n) {
        aQ1 = MFMA(u1, xf[n], aQ1); SBAR(); if (n < 31) u1 = QRD(1, n + 1); SBAR(); QEPI(3 * (n - 24));
        b0 = MM(y0, xf[n], b0); SBAR(); if (n < 31) y0 = YRD(w0, n + 1); SBAR(); QEPI(3 * (n - 24) + 1);
        b1 = MM(y1, xf[n], b1); SBAR(); if (n < 31) y1 = YRD(w1, n + 1); SBAR(); QEPI(3 * (n - 24) + 2);
    }
#pragma unroll
    for (int r = 0; r < 8; ++r) aQ0[r] *= QSCALE;
#pragma unroll
    for (int r = 0; r < 16; ++r) aQ1[r] *= QSCALE;
    *(LAS u32x4*)qd = PK4(aQ0, 0); *(LAS u32x4*)(qd + 8192) = PK4(aQ1, 0); *(LAS u32x4*)(qd + 12288) = PK4(aQ1, 8);
    cnt_arrive(lds + L_CNT + 8, lane);
    const unsigned vo2 = vo + (32 + 4 * ((qd4 ^ (PBX >> 1)) - qd4)) * 64 * TS;
#define YSLP 8
    u32x4 py[4] = {pack_kv<E>(0, b0), pack_kv<E>(1, b0), pack_kv<E>(2, b1), pack_kv<E>(3, b1)};
    PIN(py[0]); PIN(py[1]); PIN(py[2]); PIN(py[3]);
#define YSTAG 2
    if (t & 1) __builtin_amdgcn_s_sleep(YSTAG);
    if (t & 2) __builtin_amdgcn_s_sleep(2 * YSTAG);
    if (LATE0 > 23) { PUT_KV(2, pk[2], vo); __builtin_amdgcn_s_sleep(YSLP); }
    if (LATE0 > 19) { PUT_KV(3, pk[3], vo); __builtin_amdgcn_s_sleep(YSLP); }
    PUT_KV(0, py[0], vo2); __builtin_amdgcn_s_sleep(YSLP); PUT_KV(1, py[1], vo2); __builtin_amdgcn_s_sleep(YSLP);
    PUT_KV(2, py[2], vo2); __builtin_amdgcn_s_sleep(YSLP); PUT_KV(3, py[3], vo2);
    tag_raise(fl, kvflag(1, qd4, tile, E));
    BSTORE(((u32x4){~0u, ~0u, ~0u, ~0u}), ro, lane * 16, tile * OS + 2 * p * 1024);
    STAMP(19);
}

#define AB_WO (512 * 1024)
#define ROTEXPR j + 4 * p
#define WOX 1
#define WOIN 2
#define CODE_LINES 296

struct Att {
    lds_ptr lds; char* ws; __amdgpu_buffer_rsrc_t rkv, ro, rka;
    Tags fl; int lane, t, p, g, head, idx; float gt; unsigned vk, vv0, vv1; lds_cptr zp, qp[4];
};
#define IDX a.idx
#define LD3(K, V0, V1, rs, so) do { K = BLOAD(rs, a.vk, so); V0 = BLOAD(rs, a.vv0, so); V1 = BLOAD(rs, a.vv1, so); } while (0)
#define EX(X, r) X[r] = __builtin_amdgcn_exp2f(X[r])
#define STEP(X, Y, OP, VP, KN, QN, CN, O, V0, F1, F3, F2) do { \
        EX(X, 0); EX(X, 1); SBAR(); OP = pv_hi(VP, Y, OP, IDX); SBAR(); F1; SBAR(); EX(X, 2); EX(X, 3); EX(X, 4); SBAR(); Y = MFMA(KN, QN, CN); SBAR(); F3; SBAR(); \
        EX(X, 5); EX(X, 6); EX(X, 7); SBAR(); EX(X, 8); EX(X, 9); SBAR(); O = pv_lo(V0, X, O, IDX); SBAR(); F2; SBAR(); \
        EX(X, 10); EX(X, 11); EX(X, 12); EX(X, 13); EX(X, 14); EX(X, 15); SBAR(); } while (0)

__device__ __forceinline__ f32x16 lds_zero16(lds_cptr zp) {
    const volatile LAS f32x4* z = (const volatile LAS f32x4*)zp;
    const f32x4 a = *z, b = *z, c = *z, d = *z;
    f32x8 lo = __builtin_shufflevector(a, b, 0, 1, 2, 3, 4, 5, 6, 7), hi = __builtin_shufflevector(c, d, 0, 1, 2, 3, 4, 5, 6, 7);
    return __builtin_shufflevector(lo, hi, 0, 1, 2, 3, 4, 5, 6, 7, 8, 9, 10, 11, 12, 13, 14, 15);
}

template <int C, int BR>
__device__ __forceinline__ void finish(const Att& a, f32x16 o, float gf, bf16x8 q, unsigned vo) {
    f32x8 y = __builtin_shufflevector(o, o, 0, 1, 2, 3, 4, 5, 6, 7) * (__builtin_amdgcn_rcpf(o[8]) * gf);
    PIN(y);
    const float chk = __builtin_fmaf(((y[0] + y[2]) + (y[4] + y[6])) + ((y[1] + y[3]) + (y[5] + y[7])), 0.f, o[8]);
    if (__builtin_expect(__any(__builtin_amdgcn_fmed3f(chk, LMIN, LMAX) != chk), 0)) {
        if (!BR) { tag_wait(a.fl, a.lane * FSTR, kvflag(!C, a.p >> 1, 0, 0)); tag_wait(a.fl, a.lane * FSTR, kvflag(!C, a.p >> 1, 2, 0)); }
        slow_rows(a.rkv, (unsigned)((BR * 32 + (a.head ^ (BR * 2 * PBX))) * 64) * TS, BR ? (a.g >= 8 ? a.g - 8 : 0) : 0, BR ? a.g : 63, a.g, BR, q, a.vk, a.vv0, a.vv1, a.lds, a.lane, IDX, o);
        y = __builtin_shufflevector(o, o, 0, 1, 2, 3, 4, 5, 6, 7) * (__builtin_amdgcn_rcpf(o[8]) * gf);
        PIN(y);
    }
    u32x4 v = (u32x4){cvtpk(y[0], y[1]), cvtpk(y[2], y[3]), cvtpk(y[4], y[5]), cvtpk(y[6], y[7])};
    if (!C && !BR) v = __builtin_elementwise_min(v, (u32x4){~1u, ~1u, ~1u, ~1u});
    BSTORE(v, a.ro, vo, 0);
}
#define OFLAG(BR, C) (WS_OFLAG + ((((BR) * 64 + a.g) * 2 + (C)) * 16 + (a.p ^ ((BR) * PBX))) * 8)
#define OVOFF(BR) (a.vk + (unsigned)((BR) * 64 + a.g) * OS + (a.head ^ ((BR) * 2 * PBX)) * 1024)
#define GATE() __uint_as_float(__builtin_amdgcn_raw_buffer_load_b32(a.fl.rs, (a.lane & 15) * 8 + (a.lane & 16) / 4, WS_G + ((a.p >> 1) * 64 + a.g) * 128, LD_AUX))

struct KV3 { bf16x8 k[3], v0[3], v1[3]; };
#define LDB(K, V0, V1, i) do { const int kt_ = a.g - (i); \
        const __amdgpu_buffer_rsrc_t rb_ = __builtin_amdgcn_make_buffer_rsrc(a.ws + WS_KV, 0, kt_ >= 0 ? KV_BYTES : 0u, 0x00020000); \
        LD3(K, V0, V1, rb_, (unsigned)((32 + (a.head ^ (2 * PBX))) * 64 + (kt_ >= 0 ? kt_ : 0)) * TS); } while (0)
#define LDB_FIRST(f) do { LDB(f.k[0], f.v0[0], f.v1[0], 0); SBAR(); LDB(f.k[1], f.v0[1], f.v1[1], 1); SBAR(); LDB(f.k[2], f.v0[2], f.v1[2], 2); SBAR(); } while (0)
#define LDA_FIRST(f) do { LD3(f.k[0], f.v0[0], f.v1[0], a.rka, so0[0]); SBAR(); LD3(f.k[1], f.v0[1], f.v1[1], a.rka, so0[1]); SBAR(); \
        LD3(f.k[2], f.v0[2], f.v1[2], a.rka, so0[2]); SBAR(); } while (0)
__device__ __forceinline__ unsigned window_flag(const Att& a) {
    const int i = a.lane & 15, kt = a.g - (i < 9 ? i : 0);
    return kvflag(1, a.p >> 1, kt >= 0 ? kt : 0, (a.lane >> 4) & 1);
}

template <int C>
__device__ __forceinline__ void attn_full(Att& a, f32x16 o0, f32x16 o1, f32x16 o2, f32x16 o3, f32x16 S1, unsigned so,
                                          __amdgpu_buffer_rsrc_t rwo, unsigned vwo, unsigned wi, unsigned v16, unsigned fs2, unsigned vf3, KV3& nxt PROF_ARG) {
    const lds_ptr lds = a.lds; const int t = a.t, lane = a.lane;
    const f32x16 zc = f32x16{};
    bf16x8 ka = nxt.k[0], va0 = nxt.v0[0], va1 = nxt.v1[0], kb = nxt.k[1], vb0 = nxt.v0[1], vb1 = nxt.v1[1], kc = nxt.k[2], vc0 = nxt.v0[2], vc1 = nxt.v1[2], kd, vd0, vd1;
    const unsigned jb = a.g >> 2;
    unsigned ki = 3;
    vd1 = lds_frag(a.zp);
    lds_ptr pw = lds + L_PART + (4 * C + t) * PREC + lane * 16;
    lds_cptr pr0 = lds + L_PART + (4 * C + ((t - 1) & 3)) * PREC + lane * 16, pr1 = lds + L_PART + (4 * C + ((t - 2) & 3)) * PREC + lane * 16,
             pr2 = lds + L_PART + (4 * C + ((t - 3) & 3)) * PREC + lane * 16;
    unsigned ovo = OVOFF(0);
    if (C) { BAR(); STAMP(4); }
    else { PIN(pw); PIN(pr0); PIN(pr1); PIN(pr2); PIN(ovo); PIN(a.gt);
           tag_raise(a.fl, OFLAG(1, 0)); }
    bf16x8 q0 = lds_frag(a.qp[0]); SBAR();
    bf16x8 q1 = lds_frag(a.qp[1]), q2 = lds_frag(a.qp[2]), q3 = lds_frag(a.qp[3]);
    SBAR();
    f32x16 S0 = MFMA(ka, q0, zc);
    PIN(S0); SBAR();
    PIN(q1); PIN(q2); PIN(q3); PIN(vd1); PIN(o0); PIN(o1); PIN(o2); PIN(o3); PIN(S1);
    __builtin_amdgcn_s_waitcnt(0x0F76);
#pragma unroll
    for (int i = 0; i < 8; ++i) __builtin_amdgcn_s_setprio(C);
    STAMP(3);
    SBAR();
    unsigned wso = 0;
    u64 fv = __builtin_bit_cast(u64, a.fl.vtag);
#define NEXT1() asm volatile("s_add_i32 %0, %0, 1\n\ts_xor_b32 %1, %0, %2" : "+s"(ki), "=s"(so) : "s"(jb) : "scc")
#define NEXT2() asm volatile("s_mulk_i32 %0, %1" : "+s"(so) : "i"(4 * TS) : "scc")
#define WOC 1
#define WO_OFF() do { if (C == WOC) asm volatile("s_lshl_b32 %0, %1, 4" : "=s"(wso) : "s"(wi) : "scc"); } while (0)
#define WO_DMA() do { if (C == WOC) __builtin_amdgcn_raw_ptr_buffer_load_lds(rwo, (LAS void*)(lds + L_WO + t * 16384 + wi), 16, vwo, wso, 0, 0); } while (0)
#define WO_NEXT() do { if (C == WOC) asm volatile("s_addk_i32 %0, 0x400\n\ts_and_b32 %0, %0, 0x3fff" : "+s"(wi) :: "scc"); } while (0)
#define TILE(K, V0, V1, KN, LK, LV0, LV1, NW, G) do { \
        STEP(S0, S1, o3, LV1, K, q1, zc, o0, V0, LK = BLOAD(a.rka, a.vk, so), WO_OFF(), LV0 = BLOAD(a.rka, a.vv0, so)); \
        STEP(S1, S0, o0, V1, K, q2, zc, o1, V0, LV1 = BLOAD(a.rka, a.vv1, so), , WO_DMA()); \
        STEP(S0, S1, o1, V1, K, q3, zc, o2, V0, NEXT1(), NEXT2(), ); \
        STEP(S1, S0, o2, V1, KN, q0, zc, o3, V0, __builtin_amdgcn_s_waitcnt(0x0F70 | (NW)), G, WO_NEXT()); } while (0)
#define PUTA(O, sl) (*(LAS f32x4*)(pw + (sl) * 2048) = __builtin_shufflevector(O, O, 0, 1, 2, 3))
#define PUTB(O, sl) (*(LAS f32x4*)(pw + (sl) * 2048 + 1024) = __builtin_shufflevector(O, O, 4, 5, 6, 7))
#define PUTL(O, sl) (*(LAS float*)(pw + 6144 + (sl) * 4) = O[8])
    f32x4 x0, y0, x1, y1, x2, y2; float l0, l1, l2;
#define GETP() do { x0 = *(const LAS f32x4*)pr0; x1 = *(const LAS f32x4*)(pr1 + 2048); x2 = *(const LAS f32x4*)(pr2 + 4096); \
        y0 = *(const LAS f32x4*)(pr0 + 1024); y1 = *(const LAS f32x4*)(pr1 + 3072); y2 = *(const LAS f32x4*)(pr2 + 5120); \
        l0 = *(const LAS float*)(pr0 + 6144); l1 = *(const LAS float*)(pr1 + 6148); l2 = *(const LAS float*)(pr2 + 6152); } while (0)
    if (C) {
#pragma nounroll
        do {
            TILE(ka, va0, va1, kb, kd, vd0, vd1, WOC ? 7 : 6, );
            PIN(S0); PIN(S1); PIN(o3); tag_wait(a.fl, fv, v16, fs2);
            TILE(kb, vb0, vb1, kc, ka, va0, va1, WOC ? 8 : 6, );
            TILE(kc, vc0, vc1, kd, kb, vb0, vb1, WOC ? 9 : 6, fv = tag_load(a.fl, v16, fs2));
            TILE(kd, vd0, vd1, ka, kc, vc0, vc1, WOC ? 10 : 7, );
        } while (ki != 19);
        o3 = pv_hi(vd1, S1, o3, IDX);
        STAMP(6);
        const u64 f3 = tag_load(a.fl, vf3, 0);
        a.gt = GATE();
        cnt_wait(lds + L_CNT + 8);
        PUTA(o0, 0); PUTB(o0, 0); PUTL(o0, 0); PUTA(o1, 1); PUTB(o1, 1); PUTL(o1, 1); PUTA(o2, 2); PUTB(o2, 2); PUTL(o2, 2);
        DRAIN(); cnt_arrive(lds + L_CNT, lane);
        tag_wait(a.fl, f3, vf3, 0);
        LDB_FIRST(nxt);
        cnt_wait(lds + L_CNT);
        GETP();
    } else {
        WO_OFF(); WO_DMA(); WO_NEXT(); WO_OFF(); WO_DMA(); WO_NEXT(); SBAR();
        TILE(ka, va0, va1, kb, kd, vd0, vd1, WOC ? 6 : 9, );
        TILE(kb, vb0, vb1, kc, ka, va0, va1, WOC ? 6 : 9, );
#pragma nounroll
        do {
            TILE(kc, vc0, vc1, kd, kb, vb0, vb1, WOC ? 6 : 9, );
            TILE(kd, vd0, vd1, ka, kc, vc0, vc1, WOC ? 6 : 9, );
            TILE(ka, va0, va1, kb, kd, vd0, vd1, WOC ? 6 : 9, );
            TILE(kb, vb0, vb1, kc, ka, va0, va1, WOC ? 6 : 9, );
        } while (ki != 17);
        STEP(S0, S1, o3, vb1, kc, q1, zc, o0, vc0, , , );
        STEP(S1, S0, o0, vc1, kc, q2, zc, o1, vc0, , , );
        STEP(S0, S1, o1, vc1, kd, q0, zc, o2, vc0, __builtin_amdgcn_s_waitcnt(WOC ? 0x0F73 : 0x0F75), , );
        STEP(S1, S0, o2, vc1, kd, q1, zc, o0, vd0, , , );
        STEP(S0, S1, o0, vd1, kd, q2, zc, o1, vd0, , , );
        STEP(S1, S0, o1, vd1, kc, q3, zc, o2, vd0, PUTA(o0, 0), PUTL(o0, 0), PUTB(o0, 0));
        STEP(S0, S1, o2, vd1, kd, q3, zc, o3, vc0, PUTA(o1, 1), PUTL(o1, 1), PUTB(o1, 1));
        PUTA(o2, 2); PUTB(o2, 2); asm volatile("" :: "v"(o0), "v"(o1), "v"(o2)); SBAR();
        EX(S1, 0); EX(S1, 1); SBAR(); o3 = pv_hi(vc1, S0, o3, IDX); SBAR(); PUTL(o2, 2); __builtin_amdgcn_s_setprio(2); SBAR();
        EX(S1, 2); EX(S1, 3); EX(S1, 4); EX(S1, 5); EX(S1, 6); EX(S1, 7); SBAR(); EX(S1, 8); EX(S1, 9); SBAR();
        o3 = pv_lo(vd0, S1, o3, IDX); PIN(S1); SBAR();
        if (!WOC) DRAIN();
        CBAR(); GETP(); SBAR();
        EX(S1, 10); EX(S1, 11); EX(S1, 12); EX(S1, 13); EX(S1, 14); EX(S1, 15); SBAR();
        o3 = pv_hi(vd1, S1, o3, IDX); SBAR();
        STAMP(6);
    }
    x0 = (x0 + x1) + x2; y0 = (y0 + y1) + y2; l0 = (l0 + l1) + l2;
    PIN(x0); PIN(y0); PIN(l0); PIN(o3);
#pragma unroll
    for (int r = 0; r < 4; ++r) { o3[r] += x0[r]; o3[4 + r] += y0[r]; }
    o3[8] += l0;
    finish<C, 0>(a, o3, a.gt, q3, ovo);
}

template <int C>
__device__ __forceinline__ void attn_window(const Att& a, KV3& f, const unsigned (&so0)[4] PROF_ARG) {
    const lds_ptr lds = a.lds; const int t = a.t, lane = a.lane;
    const f32x16 zc = f32x16{};
    bf16x8 k[9], v0[9], v1[9];
#pragma unroll
    for (int i = 0; i < 3; ++i) { k[i] = f.k[i]; v0[i] = f.v0[i]; v1[i] = f.v1[i]; }
#define LDW(i) LDB(k[i], v0[i], v1[i], i)
    const bf16x8 qa = lds_frag(lds + L_Q + 8192 + C * 4096 + t * 1024 + lane * 16);
    const f32x16 cd = lds_f32x16(lds + L_MDIAG + lane * 16), cf = lds_f32x16(lds + L_MFAR + lane * 16);
    f32x16 oo = lds_zero16(a.zp);
    f32x16 S0 = MFMA(k[0], qa, cd), S1 = MFMA(k[1], qa, zc);
    SBAR(); LDW(3); SBAR();
#pragma unroll
    for (int r = 0; r < 10; ++r) { EX(S0, r); SBAR(); }
    oo = pv_lo(v0[0], S0, oo, IDX); SBAR();
#pragma unroll
    for (int r = 10; r < 16; ++r) EX(S0, r);
    SBAR();
    STEP(S1, S0, oo, v1[0], k[2], qa, zc, oo, v0[1], LDW(4), , );
    STEP(S0, S1, oo, v1[1], k[3], qa, zc, oo, v0[2], LDW(5), , );
    STEP(S1, S0, oo, v1[2], k[4], qa, zc, oo, v0[3], LDW(6), , );
    STEP(S0, S1, oo, v1[3], k[5], qa, zc, oo, v0[4], LDW(7), , );
    STEP(S1, S0, oo, v1[4], k[6], qa, zc, oo, v0[5], LDW(8), , );
    const unsigned fa = kvflag(1, a.p >> 1, t, 0), va = (lane & 31) * FSTR;
    u64 fv = 0;
    STEP(S0, S1, oo, v1[5], k[7], qa, zc, oo, v0[6], if (!C) fv = tag_load(a.fl, va, fa), , );
    STEP(S1, S0, oo, v1[6], k[8], qa, cf, oo, v0[7], , , );
    if (C) { PIN(S0); PIN(S1); PIN(oo); tag_raise(a.fl, OFLAG(0, C)); }
    STEP(S0, S1, oo, v1[7], k[8], qa, cf, oo, v0[8], , , );
    oo = pv_hi(v1[8], S0, oo, IDX);
    STAMP(7);
    if (!C) { PIN(oo); tag_wait(a.fl, fv, va, fa); LDA_FIRST(f); }
    finish<C, 1>(a, oo, 1.f - a.gt, qa, OVOFF(1));
}

__global__ void __launch_bounds__(512, 2)
gmix_kernel(const char* x, const char* wA, const char* woA, const char* wB, const char* woB, const char* gw, char* out, char* ws) {
    extern __shared__ __attribute__((aligned(16))) char smem[];
    const lds_ptr lds = (lds_ptr)smem;
    const unsigned lds0 = (unsigned)(uintptr_t)lds;
    const int tid = threadIdx.x, lane = tid & 63, r32 = lane & 31, hh = lane >> 5;
    const int w = __builtin_amdgcn_readfirstlane(tid >> 6);
    const int bid = blockIdx.x, xcd = bid & 7, rr = bid >> 3;
    const int p = 4 * (xcd & 3) + (rr & 3), j = 8 * (xcd >> 2) + (rr >> 2);
    const int jo = 2 * xcd + (rr >> 4), po = rr & 15;
    const int t = w & 3, c = w >> 2;
    const int e = (xcd & 1) ^ GE;
    STAMP(0);
    {
        u64 pc; const unsigned ln = (((bid & ~7) | (tid >> 6)) + (bid < CODE_LINES - 256 ? (tid & 32) * 8 : 0)) * 128;
        asm volatile("s_getpc_b64 %0\n\ts_mov_b32 m0, %2\n\ts_nop 0\n\tglobal_load_lds_dword %1, %0" : "=&s"(pc) : "v"(ln), "s"(lds0 + L_SCR) : "memory");
        SBAR();
    }
    unsigned rot = (ROTEXPR) & 7;
    unsigned vx0 = (128 * j + 32 * t + (lane >> 3)) * 1024 + (((lane & 7) ^ (lane >> 4)) * 16), vx1 = (vx0 ^ 64) + 7168, vx2 = vx0 + 14336, vx3 = vx1 + 14336;
    unsigned vw0 = (16 * t + (lane >> 3)) * 3072 + 1024 + 1024 * e + 128 * XQ(p) + (((lane & 7) ^ (((lane >> 4) & 1) << 1)) * 16), vw1 = vw0 + 8 * 3072 - 1024;
    unsigned mx = lds0 + t * 4096, mw = lds0 + L_W + t * 2048, ow = rot * (64 * 3072);
    asm volatile("" : "+v"(vx0), "+v"(vx1), "+v"(vx2), "+v"(vx3), "+v"(vw0), "+v"(vw1));
    asm volatile("" : "+s"(mx), "+s"(mw), "+s"(rot), "+s"(ow));
    SBAR();
    asm volatile("" : "+s"(x), "+s"(wA), "+s"(woA), "+s"(wB), "+s"(woB), "+s"(gw), "+s"(out), "+s"(ws));
#define MK_ATT(C) Att a; a.lds = lds; a.ws = ws; a.lane = lane; a.t = t; a.p = p; a.g = 4 * j + t; a.head = 2 * p + C; a.idx = 0xdddddddd; \
    a.rkv = __builtin_amdgcn_make_buffer_rsrc(ws + WS_KV, 0, KV_BYTES, 0x00020000); a.ro = __builtin_amdgcn_make_buffer_rsrc(ws + WS_O, 0, O_BYTES, 0x00020000); \
    a.vk = lane * 16; a.vv0 = r32 < 16 ? 1024 + hh * 256 + r32 * 16 : ((r32 & ~4) == 16 ? 2048 : OOB); a.vv1 = r32 < 16 ? a.vv0 + 512 : a.vv0; a.zp = lds + L_Z + lane * 16; \
    a.rka = __builtin_amdgcn_make_buffer_rsrc(ws + WS_KV + (unsigned)(a.head * 64 + t) * TS, 0, 61 * TS, 0x00020000); \
    _Pragma("unroll") for (int i = 0; i < 4; ++i) a.qp[i] = lds + L_Q + C * 4096 + ((t + 1 + i) & 3) * 1024 + lane * 16
#define RWO __builtin_amdgcn_make_buffer_rsrc((void*)((t >> 1) ? woB : woA), 0, AB_WO, 0x00020000)
#define VWO (((t & 1) * 256 + (lane >> 2)) * 1024 + 64 * po + (lane & 3) * 16)
#define WI0 (4096 * (((rr >> 4) << 1) | (po & 1)) + WOX * 1024 * (xcd & 3))
#define MK_TAGS(f) do { f.rs = __builtin_amdgcn_make_buffer_rsrc(ws, 0, WS_KV, 0x00020000); f.v0 = lane ? OOB : 0; PIN(f.v0); } while (0)
#define MK_SO() unsigned so0[4]; _Pragma("unroll") for (int i = 0; i < 4; ++i) so0[i] = (j ^ i) * (4 * TS)
    if (__builtin_expect(c, 1)) {
#define DMA1(s) do { const unsigned ks_ = ((s) + rot) & 7; \
        glds16x4(vx0, vx1, vx2, vx3, x + ks_ * 128, mx + ((s) & 3) * 16384); glds16x2(vw0, vw1, wA + ks_ * (64 * 3072), mw + ((s) & 3) * 8192); } while (0)
#define DMA2(s) do { const unsigned ks_ = (((s) + rot) & 7) * (64 * 3072); \
        glds16x2(vq0, vq1, wq + ks_, mq + ((s) == 14 ? L_Q14 : ((s) & 3) * 16384)); glds16x2(vw0, vw1, wy + ks_, mw + ((s) & 3) * 8192); SBAR(); } while (0)
#define LBAR(N) asm volatile("s_waitcnt vmcnt(" #N ")\n\ts_barrier" ::: "memory")
        STAMP(20);
        glds16x4(vx0, vx1, vx2, vx3, x + rot * 128, mx); glds16x2(vw0, vw1, wA + ow, mw);
        SBAR();
        const unsigned k1 = (1 + rot) & 7;
        glds16x2(vx0, vx1, x + k1 * 128, mx + 16384);
        SBAR();
        if (t == 0) asm volatile("s_mov_b32 m0, %2\n\ts_nop 0\n\tglobal_load_lds_dword %0, %1 sc1" :: "v"((lane & 1) * 4), "s"(ws + WS_EPOCH), "s"(lds0 + L_EP) : "memory");
        unsigned vq0 = (32 * (t & 1) + (lane >> 2)) * 3072 + 64 * (p ^ ((t >> 1) * PBX)) + (lane & 3) * 16, vq1 = vq0 + 16 * 3072 - 1024;
        unsigned vwo = VWO;
        MK_ATT(1);
        MK_TAGS(a.fl);
        int go = 4 * jo + t;
        asm volatile("" : "+s"(go));
        unsigned v16 = (lane & 15) * 8, vf3 = hh ? WS_OFLAG + (64 + go) * 256 + v16 : window_flag(a);
        PIN(vq0); PIN(vq1); PIN(vwo); PIN(a.vk); PIN(a.vv0); PIN(a.vv1); PIN(a.qp[0]); PIN(a.qp[1]); PIN(a.qp[2]); PIN(a.qp[3]); PIN(a.zp); PIN(v16); PIN(vf3); PIN(a.idx);
        SBAR();
        STAMP(30);
        glds16x2(vx0, vx1, x + k1 * 128 + 16384, mx + 16384 + 2048); glds16x2(vw0, vw1, wA + k1 * (64 * 3072), mw + 8192);
        STAMP(22);
        SBAR();
        lds_cptr wo = lds + L_WO + (4 * hh + ((lane & 15) >> 2)) * 64 + ((lane >> 4) & 1) * 32 + (lane & 3) * 8, wg[4];
        unsigned sg[4];
#pragma unroll
        for (int q = 0; q < 4; ++q) { const int gq = ((p >> 2) + 1 + q) & 3; sg[q] = (unsigned)go * OS + gq * 8192; wg[q] = wo + gq * 8192; PIN(wg[q]); asm volatile("" : "+s"(sg[q])); }
        unsigned vout = (32 * go + (lane >> 2)) * 1024 + 64 * po + (lane & 3) * 16;
        lds_ptr otw = lds + L_PART + (4 + t) * PREC + r32 * 80 + hh * 8;
        lds_cptr otr = lds + L_PART + (4 + t) * PREC + (lane >> 2) * 80 + (lane & 3) * 16;
        PIN(wo); PIN(vout); PIN(otw); PIN(otr);
        SBAR();
#pragma nounroll
        for (int s = 2; s < 8; ++s) { LBAR(6); DMA1(s);
        }
        const unsigned mq = lds0 + t * 2048;
        const char* wq = (t >> 1) ? wB : wA;
        const char* wy = wB + 128 * ((XQ(p) ^ (PBX >> 1)) - XQ(p));
        LBAR(6); STAMP(14); DMA2(8);
        LBAR(4); STAMP(15); DMA2(9);
        make_tag(a.fl, *(const LAS u32x2*)(lds + L_EP));
        LBAR(4); STAMP(16); DMA2(10);
        const unsigned k7 = ((7 + rot) & 7) * (64 * 3072), sx = mq + L_Q15, sw = mw + 32768;
        LBAR(4); STAMP(17); glds16(vq0, wq + k7, sx); DMA2(11);
        f32x16 oo = lds_zero16(a.zp), o1 = lds_zero16(a.zp); PIN(oo); PIN(o1);
        LBAR(5); STAMP(18); glds16(vw0, wy + k7, sw); DMA2(12);
        f32x16 o2 = lds_zero16(a.zp), o3 = lds_zero16(a.zp); PIN(o2); PIN(o3);
        LBAR(5); STAMP(19); glds16(vq1, wq + k7 + 1024, sx + 1024); DMA2(13);
        f32x16 S1 = lds_zero16(a.zp); PIN(S1);
        LBAR(5); STAMP(21); glds16(vw1, wy + k7 + 1024, sw + 1024); DMA2(14);
        __amdgpu_buffer_rsrc_t rwo = RWO; unsigned wi0 = WI0;
        MK_SO();
        asm volatile("" : "+s"(a.rka), "+s"(rwo), "+s"(wi0), "+s"(so0[0]), "+s"(so0[1]), "+s"(so0[2]), "+s"(so0[3]));
        unsigned fs1 = kvflag(0, p >> 1, (j >> 3) * 32 + t, 0), fs2 = kvflag(0, p >> 1, ((j >> 3) ^ 1) * 32 + t, 0);
        asm volatile("" : "+s"(a.fl.rs), "+s"(fs1), "+s"(fs2));
        __builtin_amdgcn_s_setprio(1);
        LBAR(5); STAMP(24);
        const u64 fv = tag_load(a.fl, v16, fs1);
        LBAR(1); STAMP(29);
        tag_wait(a.fl, fv, v16, fs1);
        STAMP(5);
        KV3 kw;
        LDA_FIRST(kw);
        attn_full<1>(a, oo, o1, o2, o3, S1, so0[3], rwo, vwo, wi0, v16, fs2, vf3, kw PROF_PASS);
        STAMP(23);
        attn_window<1>(a, kw, so0 PROF_PASS);
        STAMP(8);
#define OBR(n) ((n) >> 4 == 1 ? 0 : 1)
#define OHD(n) (2 * ((n) & 15) + ((n) >= 16))
        const __amdgpu_buffer_rsrc_t roe = a.ro;
#define OLD(n) BLOAD(roe, a.vk, (unsigned)(OBR(n) * 64 + go) * OS + OHD(n) * 1024)
#define OWF(n) tr_frag(wo + OBR(n) * 32768 + OHD(n) * 1024, wo + OBR(n) * 32768 + OHD(n) * 1024 + 512)
#define OIN 6
        bf16x8 oe[48];
#pragma unroll
        for (int n = 0; n < 48; ++n) {
            if (n == 16) { const u64 v = tag_load(a.fl, v16, WS_OFLAG + go * 256 + 128); tag_raise(a.fl, OFLAG(1, 1)); tag_wait(a.fl, v, v16, WS_OFLAG + go * 256 + 128); STAMP(9); }
            if (n == 32) tag_wait(a.fl, v16, WS_OFLAG + (64 + go) * 256 + 128);
            oe[n] = OLD(n); SBAR();
            if (n >= OIN - 1) __builtin_amdgcn_s_waitcnt(0x0F70 | (OIN - 1));
            SBAR();
        }
        bf16x8 wn[4];
        __builtin_amdgcn_s_waitcnt(0x0F70);
#define NEARLY 10
#define NMID 34
        __amdgpu_buffer_rsrc_t rout = __builtin_amdgcn_make_buffer_rsrc(out, 0, 2048 * 1024, 0x00020000);
        asm volatile("" : "+s"(rout));
        f32x16 ya = f32x16{};
        bf16x8 wf[16], of[16];
#define FLD(i) of[i] = BLOAD(a.ro, a.vk, sg[(i) >> 2] + ((i) & 3) * 2048)
#define GRP_BAD(q) ({ unsigned mx_ = 0; _Pragma("unroll") for (int i = 4 * (q); i < 4 * (q) + 4; ++i) { const u32x4 u_ = __builtin_bit_cast(u32x4, of[i]); \
            mx_ = __builtin_elementwise_max(__builtin_elementwise_max(mx_, u_[0]), u_[1]); mx_ = __builtin_elementwise_max(__builtin_elementwise_max(mx_, u_[2]), u_[3]); } \
            __any(mx_ == ~0u); })
#define GRP_LD(q) asm volatile("buffer_load_dwordx4 %0, %4, %5, %6 offen sc1\n\tbuffer_load_dwordx4 %1, %4, %5, %6 offen offset:2048 sc1\n\t" \
            "buffer_load_dwordx4 %2, %4, %5, %7 offen sc1\n\tbuffer_load_dwordx4 %3, %4, %5, %7 offen offset:2048 sc1" \
            : "+v"(of[4 * (q)]), "+v"(of[4 * (q) + 1]), "+v"(of[4 * (q) + 2]), "+v"(of[4 * (q) + 3]) : "v"(a.vk), "s"(a.ro), "s"(sg[q]), "s"(sg[q] + 4096) : "memory")
#define ESTEP 2
#define MSTEP 2
#define CK0 36
        f32x16 yb, yc, yd; s16x4 wh;
        unsigned mxg[4] = {0, 0, 0, 0};
        STAMP(10);
        BAR();
#pragma unroll
        for (int n = 0; n < 4; ++n) { wn[n] = OWF(n); PIN(wn[n]); }
#define NB 0
#pragma unroll
        for (int n = 0; n < 48; ++n) {
            if (n == NB) { BAR(); SBAR(); }
            ya = MFMA(wn[n & 3], oe[n], ya); SBAR();
            if (n >= NEARLY && n < NEARLY + 12 * ESTEP && (n - NEARLY) % ESTEP == 0) FLD(((n - NEARLY) / ESTEP) & 15);
            if (n >= NMID && n < NMID + 4 * MSTEP && (n - NMID) % MSTEP == 0) FLD(((n - NMID) / MSTEP + 12) & 15);
            if (n + 4 < 48) wn[n & 3] = OWF(n + 4);
            if (n < NB) PACE();
            if (n >= 16) { const int i = ((n - 16) >> 1) & 15;
                const s16x4 h = __builtin_amdgcn_ds_read_tr16_b64_v4i16((LAS s16x4*)(wg[i >> 2] + (i & 3) * 2048 + (n & 1) * 512));
                if (n & 1) wf[i] = __builtin_bit_cast(bf16x8, __builtin_shufflevector(wh, h, 0, 1, 2, 3, 4, 5, 6, 7)); else wh = h; }
            if (n >= CK0 && n < CK0 + 12) { const u32x4 u = __builtin_bit_cast(u32x4, of[(n - CK0) & 15]); unsigned& m = mxg[((n - CK0) >> 2) & 3];
                m = __builtin_elementwise_max(__builtin_elementwise_max(m, u[0]), u[1]); m = __builtin_elementwise_max(__builtin_elementwise_max(m, u[2]), u[3]); PIN(m); }
            SBAR();
        }
        STAMP(31);
#define GRP_MM(q, Y) do { if (__builtin_expect(good & (1u << (q)), 1)) { Y = MFMA(wf[4 * (q)], of[4 * (q)], (q) ? f32x16{} : Y); \
            _Pragma("unroll") for (int i = 4 * (q) + 1; i < 4 * (q) + 4; ++i) Y = MFMA(wf[i], of[i], Y); } } while (0)
        asm volatile("" : "=v"(yb), "=v"(yc), "=v"(yd));
        unsigned good = 15;
        asm volatile("" : "+s"(good));
        if (__any(mxg[0] == ~0u)) { GRP_LD(0); good ^= 1; }
        if (__any(mxg[1] == ~0u)) { GRP_LD(1); good ^= 2; }
        if (__any(mxg[2] == ~0u)) { GRP_LD(2); good ^= 4; }
        GRP_MM(0, ya); GRP_MM(1, yb); GRP_MM(2, yc);
        if (GRP_BAD(3)) { GRP_LD(3); good ^= 8; }
        GRP_MM(3, yd);
        if (__builtin_expect(good != 15, 0)) {
            unsigned pend = good ^ 15, spins = SPIN_CAP;
#pragma nounroll
            do {
                __builtin_amdgcn_s_waitcnt(0x0F70);
                asm volatile("" : "+v"(of[0]), "+v"(of[1]), "+v"(of[2]), "+v"(of[3]), "+v"(of[4]), "+v"(of[5]), "+v"(of[6]), "+v"(of[7]),
                                  "+v"(of[8]), "+v"(of[9]), "+v"(of[10]), "+v"(of[11]), "+v"(of[12]), "+v"(of[13]), "+v"(of[14]), "+v"(of[15]) :: "memory");
                good = pend;
                if (pend & 1) { if (GRP_BAD(0)) { GRP_LD(0); good ^= 1; } }
                if (pend & 2) { if (GRP_BAD(1)) { GRP_LD(1); good ^= 2; } }
                if (pend & 4) { if (GRP_BAD(2)) { GRP_LD(2); good ^= 4; } }
                if (pend & 8) { if (GRP_BAD(3)) { GRP_LD(3); good ^= 8; } }
                pend ^= good;
                GRP_MM(0, ya); GRP_MM(1, yb); GRP_MM(2, yc); GRP_MM(3, yd);
                asm volatile("s_sub_u32 %1, %1, 1\n\ts_cselect_b32 %0, 0, %0" : "+s"(pend), "+s"(spins) :: "memory", "scc");
            } while (pend);
        }
        asm volatile("s_waitcnt vmcnt(0)\n\ts_nop 4" : "+v"(ya), "+v"(yb), "+v"(yc), "+v"(yd) :: "memory");
        STAMP(26);
        ya = ((ya + yb) + yc) + yd;
#define OUT_AUX 16
#pragma unroll
        for (int q = 0; q < 4; ++q) *(LAS u32x2*)(otw + q * 16) = (u32x2){cvtpk(ya[4 * q], ya[4 * q + 1]), cvtpk(ya[4 * q + 2], ya[4 * q + 3])};
        asm volatile("" ::: "memory");
#pragma unroll
        for (int s = 0; s < 2; ++s) __builtin_amdgcn_raw_buffer_store_b128(*(const LAS u32x4*)(otr + s * 1280), rout, vout + s * 16384, 0, OUT_AUX);
        if (bid == 0 && tid == 256) __hip_atomic_store((GAS u64*)(ws + WS_EPOCH), a.fl.tag, RLX_ST);
        STAMP(12);
    } else {
        if (e == GE) glds16((((lane >> 3) + rot) & 7) * 128 + (lane & 7) * 16, gw, lds0 + L_GW + t * 1024);
#pragma unroll
        for (int m = 0; m < 2; ++m) {
            f32x4 mk;
#pragma unroll
            for (int i = 0; i < 4; ++i) { const bool le = (i + 8 * t + 4 * hh) <= r32; mk[i] = (le != (m != 0)) ? 0.f : -__builtin_inff(); }
            *(LAS f32x4*)(lds + L_MDIAG + m * 4096 + t * 1024 + lane * 16) = mk;
        }
        if (tid < 4) *(LAS unsigned*)(lds + L_CNT + tid * 4) = 0;
        *(LAS unsigned*)(lds + L_Z + tid * 4) = 0;
        MK_ATT(0);
        MK_TAGS(a.fl);
        unsigned vwf = window_flag(a);
        PIN(a.vv0); PIN(a.vv1); PIN(vwf);
        STAMP(21);
        if (e) project<1>(lds, a.rkv, a.ro, lane, t, p, j, a.fl PROF_PASS); else project<0>(lds, a.rkv, a.ro, lane, t, p, j, a.fl PROF_PASS);
        cnt_wait(lds + L_CNT + 8);
        __builtin_amdgcn_s_sleep(BSLP);
        STAMP(24);
        tag_wait(a.fl, vwf, 0);
        STAMP(25);
        KV3 kw;
        a.gt = GATE();
        LDB_FIRST(kw);
        MK_SO();
        attn_window<0>(a, kw, so0 PROF_PASS);
        STAMP(23);
        PIN(a.idx);
        attn_full<0>(a, lds_zero16(a.zp), lds_zero16(a.zp), lds_zero16(a.zp), lds_zero16(a.zp), lds_zero16(a.zp), so0[3], RWO, VWO, WI0, 0, 0, 0, kw PROF_PASS);
        BAR();
        STAMP(8);
    }
}

extern "C" void kernel_launch(void* const* d_in, const int* in_sizes, int n_in, void* d_out, int out_size, void* d_ws, size_t ws_size,
                              hipStream_t stream) {
    (void)in_sizes; (void)n_in; (void)out_size; (void)ws_size;
    (void)hipFuncSetAttribute((const void*)gmix_kernel, hipFuncAttributeMaxDynamicSharedMemorySize, L_END);
    gmix_kernel<<<256, 512, L_END, stream>>>((const char*)d_in[0], (const char*)d_in[1], (const char*)d_in[2], (const char*)d_in[4],
                                             (const char*)d_in[5], (const char*)d_in[6], (char*)d_out, (char*)d_ws);
}
```

```cpp
#include <hip/hip_runtime.h>
#include <hip/hip_bf16.h>

typedef __attribute__((ext_vector_type(8))) __bf16 bf16x8;
typedef __attribute__((ext_vector_type(16))) __bf16 bf16x16;
typedef __attribute__((ext_vector_type(16))) float f32x16;
typedef __attribute__((ext_vector_type(8))) float f32x8;
typedef __attribute__((ext_vector_type(4))) float f32x4;
typedef __attribute__((ext_vector_type(4))) unsigned u32x4;
typedef __attribute__((ext_vector_type(2))) unsigned u32x2;
typedef __attribute__((ext_vector_type(2))) __bf16 bf16x2;
typedef short s16x4 __attribute__((ext_vector_type(4)));
typedef unsigned long long u64;
#define GAS __attribute__((address_space(1)))
#define LAS __attribute__((address_space(3)))
typedef LAS const char* lds_cptr;
typedef LAS char* lds_ptr;

constexpr unsigned WS_EPOCH = 0;
constexpr unsigned FSTR = 8;
constexpr unsigned WS_OFLAG = 65536;
constexpr unsigned WS_KVFLAG = 131072;
constexpr unsigned WS_G = 524288;
constexpr unsigned WS_KV = 1u << 20;
constexpr unsigned TS = 2176;
constexpr unsigned KV_BYTES = 2u * 32u * 64u * TS;
constexpr unsigned WS_O = 10u << 20;
constexpr unsigned OS = 32896;
constexpr unsigned O_BYTES = 128u * OS;
constexpr unsigned OOB = 0x7ff00000u;
constexpr unsigned L_W = 65536;
constexpr unsigned L_WO = 0, L_PART = 65536, PREC = 7168, L_Q14 = 106496, L_Q15 = 114688, L_MDIAG = 122880, L_MFAR = 126976, L_Q = 131072,
                   L_GW = 147456, L_Z = 151552, L_CNT = 153088, L_EP = 153344, L_SCR = 153600, L_END = 154112;
constexpr float QSCALE = 0.25f * 1.4426950408889634f;
constexpr float LMAX = 1.2676506e30f, LMIN = 9.094947e-13f;
constexpr unsigned SPIN_CAP = 1u << 17;

#define SBAR() __builtin_amdgcn_sched_barrier(0)
#define PIN(x) asm volatile("" : "+v"(x))
#define MFMA(a, b, c) __builtin_amdgcn_mfma_f32_32x32x16_bf16(a, b, c, 0, 0, 0)
#define BAR() asm volatile("s_barrier" ::: "memory")
#define CBAR() asm volatile("s_waitcnt lgkmcnt(0)\n\ts_barrier" ::: "memory")
#define DRAIN() asm volatile("s_waitcnt vmcnt(0)" ::: "memory")
#define PACE() asm volatile("s_nop 6" ::: "memory")
#define RLX_AGENT __ATOMIC_RELAXED, __HIP_MEMORY_SCOPE_AGENT

__device__ __forceinline__ void glds16(unsigned voff, const void* sbase, unsigned ldsaddr) {
    asm volatile("s_mov_b32 m0, %2\n\ts_nop 0\n\tglobal_load_lds_dwordx4 %0, %1" ::"v"(voff), "s"(sbase), "s"(ldsaddr) : "memory");
}
__device__ __forceinline__ void glds16x2(unsigned v0, unsigned v1, const void* sbase, unsigned ldsaddr) {
    asm volatile("s_mov_b32 m0, %3\n\ts_nop 0\n\tglobal_load_lds_dwordx4 %0, %2\n\tglobal_load_lds_dwordx4 %1, %2 offset:1024"
                 ::"v"(v0), "v"(v1), "s"(sbase), "s"(ldsaddr) : "memory");
}
__device__ __forceinline__ void glds16x4(unsigned v0, unsigned v1, unsigned v2, unsigned v3, const void* sbase, unsigned ldsaddr) {
    asm volatile("s_mov_b32 m0, %5\n\ts_nop 0\n\tglobal_load_lds_dwordx4 %0, %4\n\tglobal_load_lds_dwordx4 %1, %4 offset:1024\n\t"
                 "global_load_lds_dwordx4 %2, %4 offset:2048\n\tglobal_load_lds_dwordx4 %3, %4 offset:3072"
                 ::"v"(v0), "v"(v1), "v"(v2), "v"(v3), "s"(sbase), "s"(ldsaddr) : "memory");
}
__device__ __forceinline__ unsigned cvtpk(float lo, float hi) {
    typedef __attribute__((ext_vector_type(2))) float f32x2;
    return __builtin_bit_cast(unsigned, __builtin_convertvector((f32x2){lo, hi}, bf16x2));
}
__device__ __forceinline__ bf16x8 as_frag(u32x4 v) { return __builtin_bit_cast(bf16x8, v); }
__device__ __forceinline__ bf16x8 lds_frag(lds_cptr p) { return *(const LAS bf16x8*)p; }
__device__ __forceinline__ bf16x8 tr_frag(lds_cptr p0, lds_cptr p1) {
    s16x4 a = __builtin_amdgcn_ds_read_tr16_b64_v4i16((LAS s16x4*)p0);
    s16x4 b = __builtin_amdgcn_ds_read_tr16_b64_v4i16((LAS s16x4*)p1);
    return __builtin_bit_cast(bf16x8, __builtin_shufflevector(a, b, 0, 1, 2, 3, 4, 5, 6, 7));
}
__device__ __forceinline__ f32x16 pv_lo(bf16x8 v, const f32x16& p, f32x16 acc, int idx) {
    f32x8 h = __builtin_shufflevector(p, p, 0, 1, 2, 3, 4, 5, 6, 7);
    return __builtin_amdgcn_smfmac_f32_32x32x32_bf16(v, __builtin_bit_cast(bf16x16, h), acc, idx, 0, 0);
}
__device__ __forceinline__ f32x16 pv_hi(bf16x8 v, const f32x16& p, f32x16 acc, int idx) {
    f32x8 h = __builtin_shufflevector(p, p, 8, 9, 10, 11, 12, 13, 14, 15);
    return __builtin_amdgcn_smfmac_f32_32x32x32_bf16(v, __builtin_bit_cast(bf16x16, h), acc, idx, 0, 0);
}
#define MX3(a, b, c) __builtin_fmaxf(__builtin_fmaxf((a), (b)), (c))
__device__ __forceinline__ float max16(const f32x16& s) {
    float a = MX3(s[0], s[1], s[2]), b = MX3(s[3], s[4], s[5]);
    a = MX3(a, s[6], s[7]); b = MX3(b, s[8], s[9]); a = MX3(a, s[10], s[11]); b = MX3(b, s[12], s[13]);
    return MX3(a, b, __builtin_fmaxf(s[14], s[15]));
}
__device__ __forceinline__ float halfmax(float m) {
    auto r = __builtin_amdgcn_permlane32_swap(__float_as_uint(m), __float_as_uint(m), false, false);
    return __builtin_fmaxf(__uint_as_float(r[0]), __uint_as_float(r[1]));
}
__device__ __forceinline__ f32x16 lds_f32x16(lds_cptr p) {
    f32x4 a = *(const LAS f32x4*)p, b = *(const LAS f32x4*)(p + 1024), c = *(const LAS f32x4*)(p + 2048), d = *(const LAS f32x4*)(p + 3072);
    f32x8 lo = __builtin_shufflevector(a, b, 0, 1, 2, 3, 4, 5, 6, 7), hi = __builtin_shufflevector(c, d, 0, 1, 2, 3, 4, 5, 6, 7);
    return __builtin_shufflevector(lo, hi, 0, 1, 2, 3, 4, 5, 6, 7, 8, 9, 10, 11, 12, 13, 14, 15);
}
constexpr int SC1 = 16;
#define BLOAD(rs, vo, so) as_frag(__builtin_amdgcn_raw_buffer_load_b128(rs, vo, so, SC1))
#define BSTORE(v, rs, vo, so) __builtin_amdgcn_raw_buffer_store_b128(v, rs, (vo) + (so), 0, SC1)

struct Tags { __amdgpu_buffer_rsrc_t rs; u64 tag; u32x2 vtag; unsigned v0; };
__device__ __forceinline__ u64 tag_load(const Tags& f, unsigned vo, unsigned so) {
    return __builtin_bit_cast(u64, __builtin_amdgcn_raw_buffer_load_b64(f.rs, vo, so, SC1));
}
__device__ __forceinline__ void tag_raise(const Tags& f, unsigned so) {
    DRAIN();
    __builtin_amdgcn_raw_buffer_store_b64(f.vtag, f.rs, f.v0, so, SC1);
}
__device__ __forceinline__ void tag_wait(const Tags& f, u64 v, unsigned vo, unsigned so) {
    if (__builtin_expect(!__all(v == f.tag), 0)) {
#pragma nounroll
        for (unsigned spins = 0; spins < SPIN_CAP; ++spins) {
            __builtin_amdgcn_s_sleep(8);
            asm volatile("" ::: "memory");
            if (__all(tag_load(f, vo, so) == f.tag)) break;
        }
    }
    asm volatile("" ::: "memory");
}
__device__ __forceinline__ void tag_wait(const Tags& f, unsigned vo, unsigned so) { tag_wait(f, tag_load(f, vo, so), vo, so); }

__device__ __forceinline__ void cnt_arrive(lds_ptr cnt, int lane) {
    asm volatile("s_waitcnt lgkmcnt(0)" ::: "memory");
    if (lane == 0) __hip_atomic_fetch_add((LAS unsigned*)cnt, 1u, __ATOMIC_RELAXED, __HIP_MEMORY_SCOPE_WORKGROUP);
}
__device__ __forceinline__ void cnt_wait(lds_ptr cnt) {
#pragma nounroll
    for (unsigned spins = 0; spins < (1u << 22); ++spins) {
        if (__builtin_amdgcn_readfirstlane(__hip_atomic_load((LAS unsigned*)cnt, __ATOMIC_RELAXED, __HIP_MEMORY_SCOPE_WORKGROUP)) == 4) break;
        __builtin_amdgcn_s_sleep(1);
    }
    asm volatile("" ::: "memory");
}
__device__ __forceinline__ unsigned kvflag(int br, int quad, int tile, int type) {
    return WS_KVFLAG + (unsigned)(((((br * 8 + quad) * 4 + (tile & 3)) * 2 + (tile >> 5)) * 2 + type) * 8 + ((tile >> 2) & 7)) * FSTR;
}

__device__ __forceinline__ void slow_rows(__amdgpu_buffer_rsrc_t rs, unsigned sbase, int lo, int hi, int g, bool band, bf16x8 q,
                                          unsigned vk, unsigned vv0, unsigned vv1, lds_cptr lds, int lane, int idx, f32x16& acc) {
    float m = -__builtin_inff();
    for (int pass = 0; pass < 2; ++pass) {
        if (pass) { m = halfmax(m); acc = f32x16{}; }
        for (int kt = lo; kt <= hi; ++kt) {
            const unsigned so = sbase + (unsigned)kt * TS;
            const bf16x8 k = BLOAD(rs, vk, so), v0 = BLOAD(rs, vv0, so), v1 = BLOAD(rs, vv1, so);
            f32x16 c = f32x16{};
            if (band && kt == g) c = lds_f32x16(lds + L_MDIAG + lane * 16);
            if (band && kt == g - 8) c = lds_f32x16(lds + L_MFAR + lane * 16);
            f32x16 s = MFMA(k, q, c);
            if (!pass) m = __builtin_fmaxf(m, max16(s));
            else {
#pragma unroll
                for (int r = 0; r < 16; ++r) s[r] = __builtin_amdgcn_exp2f(s[r] - m);
                acc = pv_lo(v0, s, acc, idx); acc = pv_hi(v1, s, acc, idx);
            }
        }
    }
}

#define PK4(A, o) ((u32x4){cvtpk(A[o], A[o + 1]), cvtpk(A[o + 2], A[o + 3]), cvtpk(A[o + 4], A[o + 5]), cvtpk(A[o + 6], A[o + 7])})
__device__ __forceinline__ void make_tag(Tags& f, u32x2 e) {
    f.tag = (((u64)(unsigned)__builtin_amdgcn_readfirstlane(e[1]) << 32) | (unsigned)__builtin_amdgcn_readfirstlane(e[0])) * 0x9E3779B97F4A7C15ull + 0x632BE59BD9B4E019ull;
    asm volatile("" : "+s"(f.tag));
    f.vtag = __builtin_bit_cast(u32x2, f.tag);
    PIN(f.vtag);
}
template <int E>
__device__ __forceinline__ u32x4 pack_kv(int i, const f32x16& a) {
    const int s = i & 1;
    if (E == 0) return PK4(a, 8 * s);
    auto x = __builtin_amdgcn_permlane32_swap(cvtpk(a[8 * s], a[8 * s + 1]), cvtpk(a[8 * s + 4], a[8 * s + 5]), false, false);
    auto y = __builtin_amdgcn_permlane32_swap(cvtpk(a[8 * s + 2], a[8 * s + 3]), cvtpk(a[8 * s + 6], a[8 * s + 7]), false, false);
    return (u32x4){x[0], y[0], x[1], y[1]};
}
#define PUT_KV(i, v, vo) BSTORE(v, rkv, vo, ((i) < 2 ? 64 * TS : 0) + ((i) & 1) * (E ? 512 : 128 * TS))
constexpr int PBX = 8;
constexpr int GE = 1;
#define XQ(p) (((p) >> 3) * 4 + (((p) + 1 + (((p) >> 2) & 1) * 2) & 3))
template <int E>
__device__ __forceinline__ void project(lds_ptr lds, __amdgpu_buffer_rsrc_t rkv, __amdgpu_buffer_rsrc_t ro, int lane, int t, int p, int j, Tags& fl) {
    const int r32 = lane & 31, hh = lane >> 5, qd4 = XQ(p);
    const unsigned q4 = (lane & 15) >> 2, wcol = ((lane >> 4) & 1) * 32 + (lane & 3) * 8;
    lds_cptr xa[4];
#pragma unroll
    for (int ks = 0; ks < 4; ++ks) xa[ks] = lds + (32 * t + r32) * 128 + (((hh ^ ((r32 >> 1) & 7)) * 16) ^ (ks * 32));
    unsigned w1o = L_W + (8 * hh + q4) * 128 + ((lane >> 4) & 1) * 64 + (q4 >> 1) * 32 + (lane & 3) * 8, w0o = w1o ^ 32;
    PIN(w0o); PIN(w1o);
    const lds_cptr w0 = lds + w0o, w1 = lds + w1o, wq = lds + (8 * hh + q4) * 64 + wcol;
#define XRD(n) lds_frag(xa[(n) & 3] + (((n) >> 2) & 3) * 16384)
#define WRD(b, n) tr_frag(b + ((n) & 15) * 2048, b + ((n) & 15) * 2048 + 512)
#define YOF(n) ((n) >= 28 ? 32768 + ((n) & 3) * 2048 : ((n) & 15) * 2048)
#define YRD(b, n) tr_frag(b + YOF(n), b + YOF(n) + 512)
#define QOF(br, n) (((n) >= 28 ? L_Q15 - L_Q14 : ((n) >= 24 ? 0 : (((n) >> 2) & 3) * 16384)) + (br) * 4096 + ((n) & 3) * 1024)
#define QRD(br, n) tr_frag(((n) >= 24 ? wqh : wq) + QOF(br, n), ((n) >= 24 ? wqh : wq) + QOF(br, n) + 256)
#define MM(W, X, A) (E ? MFMA(X, W, A) : MFMA(W, X, A))
    bf16x8 xf[32], f0[4], f1[4];
    f32x16 a0 = f32x16{}, a1 = f32x16{};
    unsigned gwo = (lane & 15) == ((lane >> 4) & 1) ? L_GW + t * 1024 + hh * 16 : L_Z;
    if (E == GE) PIN(gwo);
    f32x4 zg = f32x4{};
    bf16x8 gf[4];
#define RD1(n) do { xf[n] = XRD(n); f0[(n) & 3] = WRD(w0, n); f1[(n) & 3] = WRD(w1, n); SBAR(); } while (0)
#define GRD(n) do { if (E == GE) gf[(n) & 3] = lds_frag(lds + gwo + (n) * 32); } while (0)
#define GMM(n) do { if (E == GE) zg = __builtin_amdgcn_mfma_f32_16x16x32_bf16(gf[(n) & 3], xf[n], zg, 0, 0, 0); } while (0)
#define MM1(n) do { a0 = MM(f0[(n) & 3], xf[n], a0); a1 = MM(f1[(n) & 3], xf[n], a1); SBAR(); } while (0)
#pragma unroll
    for (int s = 0; s < 8; ++s) {
        if (s == 0) CBAR(); else BAR();
        SBAR();
        RD1(4 * s); RD1(4 * s + 1);
        if (s) { MM1(4 * s - 1); GMM(4 * s - 4); GMM(4 * s - 3); GMM(4 * s - 2); GMM(4 * s - 1); SBAR(); }
        RD1(4 * s + 2); MM1(4 * s); RD1(4 * s + 3); MM1(4 * s + 1);
        if (s == 0 && E == GE) DRAIN();
        GRD(4 * s); GRD(4 * s + 1); GRD(4 * s + 2); GRD(4 * s + 3); SBAR();
        MM1(4 * s + 2);
        SBAR();
    }
    const unsigned tile = 4 * j + t;
    unsigned vo = (unsigned)(4 * qd4 * 64 + tile) * TS + (E ? (r32 >> 4) * (128 * TS) + 1024 + hh * 256 + (r32 & 15) * 16 : lane * 16);
    unsigned wqo = L_Q14 + (8 * hh + q4) * 64 + wcol;
    PIN(vo); PIN(wqo);
    const lds_cptr wqh = lds + wqo;
    MM1(31); GMM(28); GMM(29); GMM(30); GMM(31); SBAR();
    u32x4 pk[4];
#pragma unroll
    for (int i = 0; i < 4; ++i) { pk[i] = pack_kv<E>(i, i < 2 ? a0 : a1); PIN(pk[i]); if (i < 2) PUT_KV(i, pk[i], vo); SBAR(); }
    if (E == GE) BSTORE(((u32x4){0x3f803f80u, 0x3f803f80u, 0x3f803f80u, 0x3f803f80u}), rkv,
                   (unsigned)((hh * 32 + 4 * (qd4 ^ (hh * (PBX >> 1))) + (r32 >> 3)) * 64 + tile) * TS + 2048 + (lane & 7) * 16, 0);
    if (E == GE) __builtin_amdgcn_raw_buffer_store_b64((u32x2){__float_as_uint(__builtin_amdgcn_rcpf(1.f + __expf(-zg[0]))), __float_as_uint(__builtin_amdgcn_rcpf(1.f + __expf(-zg[1])))},
                                                  fl.rs, lane < 16 ? lane * 8 : OOB, WS_G + (qd4 * 64 + tile) * 128, SC1);
    SBAR();
    BAR(); SBAR();
    bf16x8 u0 = QRD(0, 0), u1 = QRD(1, 0), y0 = YRD(w0, 0), y1 = YRD(w1, 0);
    const u32x2 tg = *(const LAS u32x2*)(lds + L_EP);
    f32x16 b0 = f32x16{}, b1 = f32x16{}, aQ0 = f32x16{}, aQ1 = f32x16{};
    SBAR();
#pragma unroll
    for (int n = 0; n < 24; ++n) {
        aQ0 = MFMA(u0, xf[n], aQ0); SBAR();
        if (n == 1) { make_tag(fl, tg); SBAR(); }
        if (n == 7) { tag_raise(fl, kvflag(0, qd4, tile, E)); SBAR(); }
        if ((n & 3) == 3) { BAR(); SBAR(); }
        u0 = QRD(0, n + 1); SBAR();
        aQ1 = MFMA(u1, xf[n], aQ1); SBAR();
        u1 = n < 23 ? QRD(1, n + 1) : QRD(0, 25); SBAR();
        b0 = MM(y0, xf[n], b0); SBAR(); y0 = n < 23 ? YRD(w0, n + 1) : QRD(0, 26); SBAR();
        b1 = MM(y1, xf[n], b1); SBAR(); y1 = n < 23 ? YRD(w1, n + 1) : QRD(0, 27); SBAR();
    }
    aQ0 = MFMA(u0, xf[24], aQ0); SBAR(); u0 = QRD(0, 28); SBAR(); aQ0 = MFMA(u1, xf[25], aQ0); SBAR(); u1 = QRD(0, 29); SBAR();
    aQ0 = MFMA(y0, xf[26], aQ0); SBAR(); y0 = QRD(0, 30); SBAR(); aQ0 = MFMA(y1, xf[27], aQ0); SBAR(); y1 = QRD(0, 31); SBAR();
    aQ0 = MFMA(u0, xf[28], aQ0); SBAR(); aQ0 = MFMA(u1, xf[29], aQ0); SBAR(); u1 = QRD(1, 24); SBAR();
    aQ0 = MFMA(y0, xf[30], aQ0); SBAR(); y0 = YRD(w0, 24); SBAR(); aQ0 = MFMA(y1, xf[31], aQ0); SBAR(); y1 = YRD(w1, 24); SBAR();
    const lds_ptr qd = lds + L_Q + t * 1024 + lane * 16;
    constexpr int QBAR = 4;
    constexpr int NFREE = 3;
    f32x8 qs; u32x4 qk;
#define QEPI(m) do { if ((m) == 1) { PIN(aQ0); qs = __builtin_shufflevector(aQ0, aQ0, 8, 9, 10, 11, 12, 13, 14, 15) * QSCALE; PIN(qs); } \
        if ((m) == 2) { qk = PK4(qs, 0); *(LAS u32x4*)(qd + 4096) = qk; } \
        if ((m) == QBAR) { __builtin_amdgcn_s_setprio(2); __builtin_amdgcn_s_waitcnt(0xC07F | ((2 * (QBAR - 2)) << 8)); BAR(); } \
        if ((m) > QBAR + NFREE) PACE(); SBAR(); } while (0)
#pragma unroll
    for (int n = 24; n < 32; ++n) {
        aQ1 = MFMA(u1, xf[n], aQ1); SBAR(); if (n < 31) u1 = QRD(1, n + 1); SBAR(); QEPI(3 * (n - 24));
        b0 = MM(y0, xf[n], b0); SBAR(); if (n < 31) y0 = YRD(w0, n + 1); SBAR(); QEPI(3 * (n - 24) + 1);
        b1 = MM(y1, xf[n], b1); SBAR(); if (n < 31) y1 = YRD(w1, n + 1); SBAR(); QEPI(3 * (n - 24) + 2);
    }
#pragma unroll
    for (int r = 0; r < 8; ++r) aQ0[r] *= QSCALE;
#pragma unroll
    for (int r = 0; r < 16; ++r) aQ1[r] *= QSCALE;
    *(LAS u32x4*)qd = PK4(aQ0, 0); *(LAS u32x4*)(qd + 8192) = PK4(aQ1, 0); *(LAS u32x4*)(qd + 12288) = PK4(aQ1, 8);
    cnt_arrive(lds + L_CNT + 8, lane);
    const unsigned vo2 = vo + (32 + 4 * ((qd4 ^ (PBX >> 1)) - qd4)) * 64 * TS;
    u32x4 py[4] = {pack_kv<E>(0, b0), pack_kv<E>(1, b0), pack_kv<E>(2, b1), pack_kv<E>(3, b1)};
    PIN(py[0]); PIN(py[1]); PIN(py[2]); PIN(py[3]);
    constexpr int YSLP = 8, YSTAG = 2;
    if (t & 1) __builtin_amdgcn_s_sleep(YSTAG);
    if (t & 2) __builtin_amdgcn_s_sleep(2 * YSTAG);
    PUT_KV(2, pk[2], vo); __builtin_amdgcn_s_sleep(YSLP); PUT_KV(3, pk[3], vo); __builtin_amdgcn_s_sleep(YSLP);
    PUT_KV(0, py[0], vo2); __builtin_amdgcn_s_sleep(YSLP); PUT_KV(1, py[1], vo2); __builtin_amdgcn_s_sleep(YSLP);
    PUT_KV(2, py[2], vo2); __builtin_amdgcn_s_sleep(YSLP); PUT_KV(3, py[3], vo2);
    tag_raise(fl, kvflag(1, qd4, tile, E));
    BSTORE(((u32x4){~0u, ~0u, ~0u, ~0u}), ro, lane * 16, tile * OS + 2 * p * 1024);
}

constexpr int CODE_LINES = 296;

struct Att {
    lds_ptr lds; char* ws; __amdgpu_buffer_rsrc_t rkv, ro, rka;
    Tags fl; int lane, t, p, g, head, idx; float gt; unsigned vk, vv0, vv1; lds_cptr zp, qp[4];
};
#define IDX a.idx
#define LD3(K, V0, V1, rs, so) do { K = BLOAD(rs, a.vk, so); V0 = BLOAD(rs, a.vv0, so); V1 = BLOAD(rs, a.vv1, so); } while (0)
#define EX(X, r) X[r] = __builtin_amdgcn_exp2f(X[r])
#define STEP(X, Y, OP, VP, KN, QN, CN, O, V0, F1, F3, F2) do { \
        EX(X, 0); EX(X, 1); SBAR(); OP = pv_hi(VP, Y, OP, IDX); SBAR(); F1; SBAR(); EX(X, 2); EX(X, 3); EX(X, 4); SBAR(); Y = MFMA(KN, QN, CN); SBAR(); F3; SBAR(); \
        EX(X, 5); EX(X, 6); EX(X, 7); SBAR(); EX(X, 8); EX(X, 9); SBAR(); O = pv_lo(V0, X, O, IDX); SBAR(); F2; SBAR(); \
        EX(X, 10); EX(X, 11); EX(X, 12); EX(X, 13); EX(X, 14); EX(X, 15); SBAR(); } while (0)

__device__ __forceinline__ f32x16 lds_zero16(lds_cptr zp) {
    const volatile LAS f32x4* z = (const volatile LAS f32x4*)zp;
    const f32x4 a = *z, b = *z, c = *z, d = *z;
    f32x8 lo = __builtin_shufflevector(a, b, 0, 1, 2, 3, 4, 5, 6, 7), hi = __builtin_shufflevector(c, d, 0, 1, 2, 3, 4, 5, 6, 7);
    return __builtin_shufflevector(lo, hi, 0, 1, 2, 3, 4, 5, 6, 7, 8, 9, 10, 11, 12, 13, 14, 15);
}

template <int C, int BR>
__device__ __forceinline__ void finish(const Att& a, f32x16 o, float gf, bf16x8 q, unsigned vo) {
    f32x8 y = __builtin_shufflevector(o, o, 0, 1, 2, 3, 4, 5, 6, 7) * (__builtin_amdgcn_rcpf(o[8]) * gf);
    PIN(y);
    const float chk = __builtin_fmaf(((y[0] + y[2]) + (y[4] + y[6])) + ((y[1] + y[3]) + (y[5] + y[7])), 0.f, o[8]);
    if (__builtin_expect(__any(__builtin_amdgcn_fmed3f(chk, LMIN, LMAX) != chk), 0)) {
        if (!BR) { tag_wait(a.fl, a.lane * FSTR, kvflag(!C, a.p >> 1, 0, 0)); tag_wait(a.fl, a.lane * FSTR, kvflag(!C, a.p >> 1, 2, 0)); }
        slow_rows(a.rkv, (unsigned)((BR * 32 + (a.head ^ (BR * 2 * PBX))) * 64) * TS, BR ? (a.g >= 8 ? a.g - 8 : 0) : 0, BR ? a.g : 63, a.g, BR, q, a.vk, a.vv0, a.vv1, a.lds, a.lane, IDX, o);
        y = __builtin_shufflevector(o, o, 0, 1, 2, 3, 4, 5, 6, 7) * (__builtin_amdgcn_rcpf(o[8]) * gf);
        PIN(y);
    }
    u32x4 v = (u32x4){cvtpk(y[0], y[1]), cvtpk(y[2], y[3]), cvtpk(y[4], y[5]), cvtpk(y[6], y[7])};
    if (!C && !BR) v = __builtin_elementwise_min(v, (u32x4){~1u, ~1u, ~1u, ~1u});
    BSTORE(v, a.ro, vo, 0);
}
#define OFLAG(BR, C) (WS_OFLAG + ((((BR) * 64 + a.g) * 2 + (C)) * 16 + (a.p ^ ((BR) * PBX))) * 8)
#define OVOFF(BR) (a.vk + (unsigned)((BR) * 64 + a.g) * OS + (a.head ^ ((BR) * 2 * PBX)) * 1024)
#define GATE() __uint_as_float(__builtin_amdgcn_raw_buffer_load_b32(a.fl.rs, (a.lane & 15) * 8 + (a.lane & 16) / 4, WS_G + ((a.p >> 1) * 64 + a.g) * 128, SC1))

struct KV3 { bf16x8 k[3], v0[3], v1[3]; };
#define LDB(K, V0, V1, i) do { const int kt_ = a.g - (i); \
        const __amdgpu_buffer_rsrc_t rb_ = __builtin_amdgcn_make_buffer_rsrc(a.ws + WS_KV, 0, kt_ >= 0 ? KV_BYTES : 0u, 0x00020000); \
        LD3(K, V0, V1, rb_, (unsigned)((32 + (a.head ^ (2 * PBX))) * 64 + (kt_ >= 0 ? kt_ : 0)) * TS); } while (0)
#define LDB_FIRST(f) do { LDB(f.k[0], f.v0[0], f.v1[0], 0); SBAR(); LDB(f.k[1], f.v0[1], f.v1[1], 1); SBAR(); LDB(f.k[2], f.v0[2], f.v1[2], 2); SBAR(); } while (0)
#define LDA_FIRST(f) do { LD3(f.k[0], f.v0[0], f.v1[0], a.rka, so0[0]); SBAR(); LD3(f.k[1], f.v0[1], f.v1[1], a.rka, so0[1]); SBAR(); \
        LD3(f.k[2], f.v0[2], f.v1[2], a.rka, so0[2]); SBAR(); } while (0)
__device__ __forceinline__ unsigned window_flag(const Att& a) {
    const int i = a.lane & 15, kt = a.g - (i < 9 ? i : 0);
    return kvflag(1, a.p >> 1, kt >= 0 ? kt : 0, (a.lane >> 4) & 1);
}

template <int C>
__device__ __forceinline__ void attn_full(Att& a, f32x16 o0, f32x16 o1, f32x16 o2, f32x16 o3, f32x16 S1, unsigned so,
                                          __amdgpu_buffer_rsrc_t rwo, unsigned vwo, unsigned wi, unsigned v16, unsigned fs2, unsigned vf3, KV3& nxt) {
    const lds_ptr lds = a.lds; const int t = a.t, lane = a.lane;
    const f32x16 zc = f32x16{};
    bf16x8 ka = nxt.k[0], va0 = nxt.v0[0], va1 = nxt.v1[0], kb = nxt.k[1], vb0 = nxt.v0[1], vb1 = nxt.v1[1], kc = nxt.k[2], vc0 = nxt.v0[2], vc1 = nxt.v1[2], kd, vd0, vd1;
    const unsigned jb = a.g >> 2;
    unsigned ki = 3;
    vd1 = lds_frag(a.zp);
    lds_ptr pw = lds + L_PART + (4 * C + t) * PREC + lane * 16;
    lds_cptr pr0 = lds + L_PART + (4 * C + ((t - 1) & 3)) * PREC + lane * 16, pr1 = lds + L_PART + (4 * C + ((t - 2) & 3)) * PREC + lane * 16,
             pr2 = lds + L_PART + (4 * C + ((t - 3) & 3)) * PREC + lane * 16;
    unsigned ovo = OVOFF(0);
    if (C) { BAR(); }
    else { PIN(pw); PIN(pr0); PIN(pr1); PIN(pr2); PIN(ovo); PIN(a.gt);
           tag_raise(a.fl, OFLAG(1, 0)); }
    bf16x8 q0 = lds_frag(a.qp[0]); SBAR();
    bf16x8 q1 = lds_frag(a.qp[1]), q2 = lds_frag(a.qp[2]), q3 = lds_frag(a.qp[3]);
    SBAR();
    f32x16 S0 = MFMA(ka, q0, zc);
    PIN(S0); SBAR();
    PIN(q1); PIN(q2); PIN(q3); PIN(vd1); PIN(o0); PIN(o1); PIN(o2); PIN(o3); PIN(S1);
    __builtin_amdgcn_s_waitcnt(0x0F76);
#pragma unroll
    for (int i = 0; i < 8; ++i) __builtin_amdgcn_s_setprio(C);
    SBAR();
    unsigned wso = 0;
    u64 fv = __builtin_bit_cast(u64, a.fl.vtag);
#define NEXT1() asm volatile("s_add_i32 %0, %0, 1\n\ts_xor_b32 %1, %0, %2" : "+s"(ki), "=s"(so) : "s"(jb) : "scc")
#define NEXT2() asm volatile("s_mulk_i32 %0, %1" : "+s"(so) : "i"(4 * TS) : "scc")
#define WO_OFF() do { if (C) asm volatile("s_lshl_b32 %0, %1, 4" : "=s"(wso) : "s"(wi) : "scc"); } while (0)
#define WO_DMA() do { if (C) __builtin_amdgcn_raw_ptr_buffer_load_lds(rwo, (LAS void*)(lds + L_WO + t * 16384 + wi), 16, vwo, wso, 0, 0); } while (0)
#define WO_NEXT() do { if (C) asm volatile("s_addk_i32 %0, 0x400\n\ts_and_b32 %0, %0, 0x3fff" : "+s"(wi) :: "scc"); } while (0)
#define TILE(K, V0, V1, KN, LK, LV0, LV1, NW, G) do { \
        STEP(S0, S1, o3, LV1, K, q1, zc, o0, V0, LK = BLOAD(a.rka, a.vk, so), WO_OFF(), LV0 = BLOAD(a.rka, a.vv0, so)); \
        STEP(S1, S0, o0, V1, K, q2, zc, o1, V0, LV1 = BLOAD(a.rka, a.vv1, so), , WO_DMA()); \
        STEP(S0, S1, o1, V1, K, q3, zc, o2, V0, NEXT1(), NEXT2(), ); \
        STEP(S1, S0, o2, V1, KN, q0, zc, o3, V0, __builtin_amdgcn_s_waitcnt(0x0F70 | (NW)), G, WO_NEXT()); } while (0)
#define PUTA(O, sl) (*(LAS f32x4*)(pw + (sl) * 2048) = __builtin_shufflevector(O, O, 0, 1, 2, 3))
#define PUTB(O, sl) (*(LAS f32x4*)(pw + (sl) * 2048 + 1024) = __builtin_shufflevector(O, O, 4, 5, 6, 7))
#define PUTL(O, sl) (*(LAS float*)(pw + 6144 + (sl) * 4) = O[8])
    f32x4 x0, y0, x1, y1, x2, y2; float l0, l1, l2;
#define GETP() do { x0 = *(const LAS f32x4*)pr0; x1 = *(const LAS f32x4*)(pr1 + 2048); x2 = *(const LAS f32x4*)(pr2 + 4096); \
        y0 = *(const LAS f32x4*)(pr0 + 1024); y1 = *(const LAS f32x4*)(pr1 + 3072); y2 = *(const LAS f32x4*)(pr2 + 5120); \
        l0 = *(const LAS float*)(pr0 + 6144); l1 = *(const LAS float*)(pr1 + 6148); l2 = *(const LAS float*)(pr2 + 6152); } while (0)
    if (C) {
#pragma nounroll
        do {
            TILE(ka, va0, va1, kb, kd, vd0, vd1, 7, );
            PIN(S0); PIN(S1); PIN(o3); tag_wait(a.fl, fv, v16, fs2);
            TILE(kb, vb0, vb1, kc, ka, va0, va1, 8, );
            TILE(kc, vc0, vc1, kd, kb, vb0, vb1, 9, fv = tag_load(a.fl, v16, fs2));
            TILE(kd, vd0, vd1, ka, kc, vc0, vc1, 10, );
        } while (ki != 19);
        o3 = pv_hi(vd1, S1, o3, IDX);
        const u64 f3 = tag_load(a.fl, vf3, 0);
        a.gt = GATE();
        cnt_wait(lds + L_CNT + 8);
        PUTA(o0, 0); PUTB(o0, 0); PUTL(o0, 0); PUTA(o1, 1); PUTB(o1, 1); PUTL(o1, 1); PUTA(o2, 2); PUTB(o2, 2); PUTL(o2, 2);
        DRAIN(); cnt_arrive(lds + L_CNT, lane);
        tag_wait(a.fl, f3, vf3, 0);
        LDB_FIRST(nxt);
        cnt_wait(lds + L_CNT);
        GETP();
    } else {
        SBAR();
        TILE(ka, va0, va1, kb, kd, vd0, vd1, 6, );
        TILE(kb, vb0, vb1, kc, ka, va0, va1, 6, );
#pragma nounroll
        do {
            TILE(kc, vc0, vc1, kd, kb, vb0, vb1, 6, );
            TILE(kd, vd0, vd1, ka, kc, vc0, vc1, 6, );
            TILE(ka, va0, va1, kb, kd, vd0, vd1, 6, );
            TILE(kb, vb0, vb1, kc, ka, va0, va1, 6, );
        } while (ki != 17);
        STEP(S0, S1, o3, vb1, kc, q1, zc, o0, vc0, , , );
        STEP(S1, S0, o0, vc1, kc, q2, zc, o1, vc0, , , );
        STEP(S0, S1, o1, vc1, kd, q0, zc, o2, vc0, __builtin_amdgcn_s_waitcnt(0x0F73), , );
        STEP(S1, S0, o2, vc1, kd, q1, zc, o0, vd0, , , );
        STEP(S0, S1, o0, vd1, kd, q2, zc, o1, vd0, , , );
        STEP(S1, S0, o1, vd1, kc, q3, zc, o2, vd0, PUTA(o0, 0), PUTL(o0, 0), PUTB(o0, 0));
        STEP(S0, S1, o2, vd1, kd, q3, zc, o3, vc0, PUTA(o1, 1), PUTL(o1, 1), PUTB(o1, 1));
        PUTA(o2, 2); PUTB(o2, 2); asm volatile("" :: "v"(o0), "v"(o1), "v"(o2)); SBAR();
        EX(S1, 0); EX(S1, 1); SBAR(); o3 = pv_hi(vc1, S0, o3, IDX); SBAR(); PUTL(o2, 2); __builtin_amdgcn_s_setprio(2); SBAR();
        EX(S1, 2); EX(S1, 3); EX(S1, 4); EX(S1, 5); EX(S1, 6); EX(S1, 7); SBAR(); EX(S1, 8); EX(S1, 9); SBAR();
        o3 = pv_lo(vd0, S1, o3, IDX); PIN(S1); SBAR();
        CBAR(); GETP(); SBAR();
        EX(S1, 10); EX(S1, 11); EX(S1, 12); EX(S1, 13); EX(S1, 14); EX(S1, 15); SBAR();
        o3 = pv_hi(vd1, S1, o3, IDX); SBAR();
    }
    x0 = (x0 + x1) + x2; y0 = (y0 + y1) + y2; l0 = (l0 + l1) + l2;
    PIN(x0); PIN(y0); PIN(l0); PIN(o3);
#pragma unroll
    for (int r = 0; r < 4; ++r) { o3[r] += x0[r]; o3[4 + r] += y0[r]; }
    o3[8] += l0;
    finish<C, 0>(a, o3, a.gt, q3, ovo);
}

template <int C>
__device__ __forceinline__ void attn_window(const Att& a, KV3& f, const unsigned (&so0)[4]) {
    const lds_ptr lds = a.lds; const int t = a.t, lane = a.lane;
    const f32x16 zc = f32x16{};
    bf16x8 k[9], v0[9], v1[9];
#pragma unroll
    for (int i = 0; i < 3; ++i) { k[i] = f.k[i]; v0[i] = f.v0[i]; v1[i] = f.v1[i]; }
#define LDW(i) LDB(k[i], v0[i], v1[i], i)
    const bf16x8 qa = lds_frag(lds + L_Q + 8192 + C * 4096 + t * 1024 + lane * 16);
    const f32x16 cd = lds_f32x16(lds + L_MDIAG + lane * 16), cf = lds_f32x16(lds + L_MFAR + lane * 16);
    f32x16 oo = lds_zero16(a.zp);
    f32x16 S0 = MFMA(k[0], qa, cd), S1 = MFMA(k[1], qa, zc);
    SBAR(); LDW(3); SBAR();
#pragma unroll
    for (int r = 0; r < 10; ++r) { EX(S0, r); SBAR(); }
    oo = pv_lo(v0[0], S0, oo, IDX); SBAR();
#pragma unroll
    for (int r = 10; r < 16; ++r) EX(S0, r);
    SBAR();
    STEP(S1, S0, oo, v1[0], k[2], qa, zc, oo, v0[1], LDW(4), , );
    STEP(S0, S1, oo, v1[1], k[3], qa, zc, oo, v0[2], LDW(5), , );
    STEP(S1, S0, oo, v1[2], k[4], qa, zc, oo, v0[3], LDW(6), , );
    STEP(S0, S1, oo, v1[3], k[5], qa, zc, oo, v0[4], LDW(7), , );
    STEP(S1, S0, oo, v1[4], k[6], qa, zc, oo, v0[5], LDW(8), , );
    const unsigned fa = kvflag(1, a.p >> 1, t, 0), va = (lane & 31) * FSTR;
    u64 fv = 0;
    STEP(S0, S1, oo, v1[5], k[7], qa, zc, oo, v0[6], if (!C) fv = tag_load(a.fl, va, fa), , );
    STEP(S1, S0, oo, v1[6], k[8], qa, cf, oo, v0[7], , , );
    if (C) { PIN(S0); PIN(S1); PIN(oo); tag_raise(a.fl, OFLAG(0, C)); }
    STEP(S0, S1, oo, v1[7], k[8], qa, cf, oo, v0[8], , , );
    oo = pv_hi(v1[8], S0, oo, IDX);
    if (!C) { PIN(oo); tag_wait(a.fl, fv, va, fa); LDA_FIRST(f); }
    finish<C, 1>(a, oo, 1.f - a.gt, qa, OVOFF(1));
}

__global__ void __launch_bounds__(512, 2)
gmix_kernel(const char* x, const char* wA, const char* woA, const char* wB, const char* woB, const char* gw, char* out, char* ws) {
    extern __shared__ __attribute__((aligned(16))) char smem[];
    const lds_ptr lds = (lds_ptr)smem;
    const unsigned lds0 = (unsigned)(uintptr_t)lds;
    const int tid = threadIdx.x, lane = tid & 63, r32 = lane & 31, hh = lane >> 5;
    const int w = __builtin_amdgcn_readfirstlane(tid >> 6);
    const int bid = blockIdx.x, xcd = bid & 7, rr = bid >> 3;
    const int p = 4 * (xcd & 3) + (rr & 3), j = 8 * (xcd >> 2) + (rr >> 2);
    const int jo = 2 * xcd + (rr >> 4), po = rr & 15;
    const int t = w & 3, c = w >> 2;
    const int e = (xcd & 1) ^ GE;
    {
        u64 pc; const unsigned ln = (((bid & ~7) | (tid >> 6)) + (bid < CODE_LINES - 256 ? (tid & 32) * 8 : 0)) * 128;
        asm volatile("s_getpc_b64 %0\n\ts_mov_b32 m0, %2\n\ts_nop 0\n\tglobal_load_lds_dword %1, %0" : "=&s"(pc) : "v"(ln), "s"(lds0 + L_SCR) : "memory");
        SBAR();
    }
    unsigned rot = (j + 4 * p) & 7;
    unsigned vx0 = (128 * j + 32 * t + (lane >> 3)) * 1024 + (((lane & 7) ^ (lane >> 4)) * 16), vx1 = (vx0 ^ 64) + 7168, vx2 = vx0 + 14336, vx3 = vx1 + 14336;
    unsigned vw0 = (16 * t + (lane >> 3)) * 3072 + 1024 + 1024 * e + 128 * XQ(p) + (((lane & 7) ^ (((lane >> 4) & 1) << 1)) * 16), vw1 = vw0 + 8 * 3072 - 1024;
    unsigned mx = lds0 + t * 4096, mw = lds0 + L_W + t * 2048, ow = rot * (64 * 3072);
    asm volatile("" : "+v"(vx0), "+v"(vx1), "+v"(vx2), "+v"(vx3), "+v"(vw0), "+v"(vw1));
    asm volatile("" : "+s"(mx), "+s"(mw), "+s"(rot), "+s"(ow));
    SBAR();
    asm volatile("" : "+s"(x), "+s"(wA), "+s"(woA), "+s"(wB), "+s"(woB), "+s"(gw), "+s"(out), "+s"(ws));
#define MK_ATT(C) Att a; a.lds = lds; a.ws = ws; a.lane = lane; a.t = t; a.p = p; a.g = 4 * j + t; a.head = 2 * p + C; a.idx = 0xdddddddd; \
    a.rkv = __builtin_amdgcn_make_buffer_rsrc(ws + WS_KV, 0, KV_BYTES, 0x00020000); a.ro = __builtin_amdgcn_make_buffer_rsrc(ws + WS_O, 0, O_BYTES, 0x00020000); \
    a.vk = lane * 16; a.vv0 = r32 < 16 ? 1024 + hh * 256 + r32 * 16 : ((r32 & ~4) == 16 ? 2048 : OOB); a.vv1 = r32 < 16 ? a.vv0 + 512 : a.vv0; a.zp = lds + L_Z + lane * 16; \
    a.rka = __builtin_amdgcn_make_buffer_rsrc(ws + WS_KV + (unsigned)(a.head * 64 + t) * TS, 0, 61 * TS, 0x00020000); \
    _Pragma("unroll") for (int i = 0; i < 4; ++i) a.qp[i] = lds + L_Q + C * 4096 + ((t + 1 + i) & 3) * 1024 + lane * 16
#define MK_TAGS(f) do { f.rs = __builtin_amdgcn_make_buffer_rsrc(ws, 0, WS_KV, 0x00020000); f.v0 = lane ? OOB : 0; PIN(f.v0); } while (0)
#define MK_SO() unsigned so0[4]; _Pragma("unroll") for (int i = 0; i < 4; ++i) so0[i] = (j ^ i) * (4 * TS)
    if (__builtin_expect(c, 1)) {
#define DMA1(s) do { const unsigned ks_ = ((s) + rot) & 7; \
        glds16x4(vx0, vx1, vx2, vx3, x + ks_ * 128, mx + ((s) & 3) * 16384); glds16x2(vw0, vw1, wA + ks_ * (64 * 3072), mw + ((s) & 3) * 8192); } while (0)
#define DMA2(s) do { const unsigned ks_ = (((s) + rot) & 7) * (64 * 3072); \
        glds16x2(vq0, vq1, wq + ks_, mq + ((s) == 14 ? L_Q14 : ((s) & 3) * 16384)); glds16x2(vw0, vw1, wy + ks_, mw + ((s) & 3) * 8192); SBAR(); } while (0)
#define LBAR(N) asm volatile("s_waitcnt vmcnt(" #N ")\n\ts_barrier" ::: "memory")
        glds16x4(vx0, vx1, vx2, vx3, x + rot * 128, mx); glds16x2(vw0, vw1, wA + ow, mw);
        SBAR();
        const unsigned k1 = (1 + rot) & 7;
        glds16x2(vx0, vx1, x + k1 * 128, mx + 16384);
        SBAR();
        if (t == 0)
            asm volatile("s_mov_b32 m0, %2\n\ts_nop 0\n\tglobal_load_lds_dword %0, %1 sc1" :: "v"((lane & 1) * 4), "s"(ws + WS_EPOCH), "s"(lds0 + L_EP) : "memory");
        unsigned vq0 = (32 * (t & 1) + (lane >> 2)) * 3072 + 64 * (p ^ ((t >> 1) * PBX)) + (lane & 3) * 16, vq1 = vq0 + 16 * 3072 - 1024;
        unsigned vwo = ((t & 1) * 256 + (lane >> 2)) * 1024 + 64 * po + (lane & 3) * 16;
        MK_ATT(1);
        MK_TAGS(a.fl);
        int go = 4 * jo + t;
        asm volatile("" : "+s"(go));
        unsigned v16 = (lane & 15) * 8, vf3 = hh ? WS_OFLAG + (64 + go) * 256 + v16 : window_flag(a);
        PIN(vq0); PIN(vq1); PIN(vwo); PIN(a.vk); PIN(a.vv0); PIN(a.vv1); PIN(a.qp[0]); PIN(a.qp[1]); PIN(a.qp[2]); PIN(a.qp[3]); PIN(a.zp); PIN(v16); PIN(vf3); PIN(a.idx);
        SBAR();
        glds16x2(vx0, vx1, x + k1 * 128 + 16384, mx + 16384 + 2048); glds16x2(vw0, vw1, wA + k1 * (64 * 3072), mw + 8192);
        SBAR();
        lds_cptr wo = lds + L_WO + (4 * hh + ((lane & 15) >> 2)) * 64 + ((lane >> 4) & 1) * 32 + (lane & 3) * 8, wg[4];
        unsigned sg[4];
#pragma unroll
        for (int q = 0; q < 4; ++q) { const int gq = ((p >> 2) + 1 + q) & 3; sg[q] = (unsigned)go * OS + gq * 8192; wg[q] = wo + gq * 8192; PIN(wg[q]); asm volatile("" : "+s"(sg[q])); }
        unsigned vout = (32 * go + (lane >> 2)) * 1024 + 64 * po + (lane & 3) * 16;
        lds_ptr otw = lds + L_PART + (4 + t) * PREC + r32 * 80 + hh * 8;
        lds_cptr otr = lds + L_PART + (4 + t) * PREC + (lane >> 2) * 80 + (lane & 3) * 16;
        PIN(wo); PIN(vout); PIN(otw); PIN(otr);
        SBAR();
#pragma nounroll
        for (int s = 2; s < 8; ++s) { LBAR(6); DMA1(s); }
        const unsigned mq = lds0 + t * 2048;
        const char* wq = (t >> 1) ? wB : wA;
        const char* wy = wB + 128 * ((XQ(p) ^ (PBX >> 1)) - XQ(p));
        LBAR(6); DMA2(8);
        LBAR(4); DMA2(9);
        make_tag(a.fl, *(const LAS u32x2*)(lds + L_EP));
        LBAR(4); DMA2(10);
        const unsigned k7 = ((7 + rot) & 7) * (64 * 3072), sx = mq + L_Q15, sw = mw + 32768;
        LBAR(4); glds16(vq0, wq + k7, sx); DMA2(11);
        f32x16 oo = lds_zero16(a.zp), o1 = lds_zero16(a.zp); PIN(oo); PIN(o1);
        LBAR(5); glds16(vw0, wy + k7, sw); DMA2(12);
        f32x16 o2 = lds_zero16(a.zp), o3 = lds_zero16(a.zp); PIN(o2); PIN(o3);
        LBAR(5); glds16(vq1, wq + k7 + 1024, sx + 1024); DMA2(13);
        f32x16 S1 = lds_zero16(a.zp); PIN(S1);
        LBAR(5); glds16(vw1, wy + k7 + 1024, sw + 1024); DMA2(14);
        __amdgpu_buffer_rsrc_t rwo = __builtin_amdgcn_make_buffer_rsrc((void*)((t >> 1) ? woB : woA), 0, 512 * 1024, 0x00020000);
        unsigned wi0 = 4096 * (((rr >> 4) << 1) | (po & 1)) + 1024 * (xcd & 3);
        MK_SO();
        asm volatile("" : "+s"(a.rka), "+s"(rwo), "+s"(wi0), "+s"(so0[0]), "+s"(so0[1]), "+s"(so0[2]), "+s"(so0[3]));
        unsigned fs1 = kvflag(0, p >> 1, (j >> 3) * 32 + t, 0), fs2 = kvflag(0, p >> 1, ((j >> 3) ^ 1) * 32 + t, 0);
        asm volatile("" : "+s"(a.fl.rs), "+s"(fs1), "+s"(fs2));
        __builtin_amdgcn_s_setprio(1);
        LBAR(5);
        const u64 fv = tag_load(a.fl, v16, fs1);
        LBAR(1);
        tag_wait(a.fl, fv, v16, fs1);
        KV3 kw;
        LDA_FIRST(kw);
        attn_full<1>(a, oo, o1, o2, o3, S1, so0[3], rwo, vwo, wi0, v16, fs2, vf3, kw);
        attn_window<1>(a, kw, so0);
#define OBR(n) ((n) >> 4 == 1 ? 0 : 1)
#define OHD(n) (2 * ((n) & 15) + ((n) >= 16))
#define OLD(n) BLOAD(a.ro, a.vk, (unsigned)(OBR(n) * 64 + go) * OS + OHD(n) * 1024)
#define OWF(n) tr_frag(wo + OBR(n) * 32768 + OHD(n) * 1024, wo + OBR(n) * 32768 + OHD(n) * 1024 + 512)
        constexpr int OIN = 6;
        bf16x8 oe[48];
#pragma unroll
        for (int n = 0; n < 48; ++n) {
            if (n == 16) { const u64 v = tag_load(a.fl, v16, WS_OFLAG + go * 256 + 128); tag_raise(a.fl, OFLAG(1, 1)); tag_wait(a.fl, v, v16, WS_OFLAG + go * 256 + 128); }
            if (n == 32) tag_wait(a.fl, v16, WS_OFLAG + (64 + go) * 256 + 128);
            oe[n] = OLD(n); SBAR();
            if (n >= OIN - 1) __builtin_amdgcn_s_waitcnt(0x0F70 | (OIN - 1));
            SBAR();
        }
        bf16x8 wn[4];
        __builtin_amdgcn_s_waitcnt(0x0F70);
        constexpr int NEARLY = 10;
        constexpr int NMID = 34;
        __amdgpu_buffer_rsrc_t rout = __builtin_amdgcn_make_buffer_rsrc(out, 0, 2048 * 1024, 0x00020000);
        asm volatile("" : "+s"(rout));
        f32x16 ya = f32x16{};
        bf16x8 wf[16], of[16];
#define FLD(i) of[i] = BLOAD(a.ro, a.vk, sg[(i) >> 2] + ((i) & 3) * 2048)
#define GRP_BAD(q) ({ unsigned mx_ = 0; _Pragma("unroll") for (int i = 4 * (q); i < 4 * (q) + 4; ++i) { const u32x4 u_ = __builtin_bit_cast(u32x4, of[i]); \
            mx_ = __builtin_elementwise_max(__builtin_elementwise_max(mx_, u_[0]), u_[1]); mx_ = __builtin_elementwise_max(__builtin_elementwise_max(mx_, u_[2]), u_[3]); } \
            __any(mx_ == ~0u); })
#define GRP_LD(q) asm volatile("buffer_load_dwordx4 %0, %4, %5, %6 offen sc1\n\tbuffer_load_dwordx4 %1, %4, %5, %6 offen offset:2048 sc1\n\t" \
            "buffer_load_dwordx4 %2, %4, %5, %7 offen sc1\n\tbuffer_load_dwordx4 %3, %4, %5, %7 offen offset:2048 sc1" \
            : "+v"(of[4 * (q)]), "+v"(of[4 * (q) + 1]), "+v"(of[4 * (q) + 2]), "+v"(of[4 * (q) + 3]) : "v"(a.vk), "s"(a.ro), "s"(sg[q]), "s"(sg[q] + 4096) : "memory")
        constexpr int ESTEP = 2, MSTEP = 2, CK0 = 36;
        f32x16 yb, yc, yd; s16x4 wh;
        unsigned mxg[4] = {0, 0, 0, 0};
        BAR();
#pragma unroll
        for (int n = 0; n < 4; ++n) { wn[n] = OWF(n); PIN(wn[n]); }
#pragma unroll
        for (int n = 0; n < 48; ++n) {
            if (n == 0) { BAR(); SBAR(); }
            ya = MFMA(wn[n & 3], oe[n], ya); SBAR();
            if (n >= NEARLY && n < NEARLY + 12 * ESTEP && (n - NEARLY) % ESTEP == 0) FLD(((n - NEARLY) / ESTEP) & 15);
            if (n >= NMID && n < NMID + 4 * MSTEP && (n - NMID) % MSTEP == 0) FLD(((n - NMID) / MSTEP + 12) & 15);
            if (n + 4 < 48) wn[n & 3] = OWF(n + 4);
            if (n >= 16) { const int i = ((n - 16) >> 1) & 15;
                const s16x4 h = __builtin_amdgcn_ds_read_tr16_b64_v4i16((LAS s16x4*)(wg[i >> 2] + (i & 3) * 2048 + (n & 1) * 512));
                if (n & 1) wf[i] = __builtin_bit_cast(bf16x8, __builtin_shufflevector(wh, h, 0, 1, 2, 3, 4, 5, 6, 7)); else wh = h; }
            if (n >= CK0 && n < CK0 + 12) { const u32x4 u = __builtin_bit_cast(u32x4, of[(n - CK0) & 15]); unsigned& m = mxg[((n - CK0) >> 2) & 3];
                m = __builtin_elementwise_max(__builtin_elementwise_max(m, u[0]), u[1]); m = __builtin_elementwise_max(__builtin_elementwise_max(m, u[2]), u[3]); PIN(m); }
            SBAR();
        }
#define GRP_MM(q, Y) do { if (__builtin_expect(good & (1u << (q)), 1)) { Y = MFMA(wf[4 * (q)], of[4 * (q)], (q) ? f32x16{} : Y); \
            _Pragma("unroll") for (int i = 4 * (q) + 1; i < 4 * (q) + 4; ++i) Y = MFMA(wf[i], of[i], Y); } } while (0)
        asm volatile("" : "=v"(yb), "=v"(yc), "=v"(yd));
        unsigned good = 15;
        asm volatile("" : "+s"(good));
        if (__any(mxg[0] == ~0u)) { GRP_LD(0); good ^= 1; }
        if (__any(mxg[1] == ~0u)) { GRP_LD(1); good ^= 2; }
        if (__any(mxg[2] == ~0u)) { GRP_LD(2); good ^= 4; }
        GRP_MM(0, ya); GRP_MM(1, yb); GRP_MM(2, yc);
        if (GRP_BAD(3)) { GRP_LD(3); good ^= 8; }
        GRP_MM(3, yd);
        if (__builtin_expect(good != 15, 0)) {
            unsigned pend = good ^ 15, spins = SPIN_CAP;
#pragma nounroll
            do {
                __builtin_amdgcn_s_waitcnt(0x0F70);
                asm volatile("" : "+v"(of[0]), "+v"(of[1]), "+v"(of[2]), "+v"(of[3]), "+v"(of[4]), "+v"(of[5]), "+v"(of[6]), "+v"(of[7]),
                                  "+v"(of[8]), "+v"(of[9]), "+v"(of[10]), "+v"(of[11]), "+v"(of[12]), "+v"(of[13]), "+v"(of[14]), "+v"(of[15]) :: "memory");
                good = pend;
                if (pend & 1) { if (GRP_BAD(0)) { GRP_LD(0); good ^= 1; } }
                if (pend & 2) { if (GRP_BAD(1)) { GRP_LD(1); good ^= 2; } }
                if (pend & 4) { if (GRP_BAD(2)) { GRP_LD(2); good ^= 4; } }
                if (pend & 8) { if (GRP_BAD(3)) { GRP_LD(3); good ^= 8; } }
                pend ^= good;
                GRP_MM(0, ya); GRP_MM(1, yb); GRP_MM(2, yc); GRP_MM(3, yd);
                asm volatile("s_sub_u32 %1, %1, 1\n\ts_cselect_b32 %0, 0, %0" : "+s"(pend), "+s"(spins) :: "memory", "scc");
            } while (pend);
        }
        asm volatile("s_waitcnt vmcnt(0)\n\ts_nop 4" : "+v"(ya), "+v"(yb), "+v"(yc), "+v"(yd) :: "memory");
        ya = ((ya + yb) + yc) + yd;
#pragma unroll
        for (int q = 0; q < 4; ++q) *(LAS u32x2*)(otw + q * 16) = (u32x2){cvtpk(ya[4 * q], ya[4 * q + 1]), cvtpk(ya[4 * q + 2], ya[4 * q + 3])};
        asm volatile("" ::: "memory");
#pragma unroll
        for (int s = 0; s < 2; ++s) __builtin_amdgcn_raw_buffer_store_b128(*(const LAS u32x4*)(otr + s * 1280), rout, vout + s * 16384, 0, SC1);
        if (bid == 0 && tid == 256) __hip_atomic_store((GAS u64*)(ws + WS_EPOCH), a.fl.tag, RLX_AGENT);
    } else {
        if (e == GE) glds16((((lane >> 3) + rot) & 7) * 128 + (lane & 7) * 16, gw, lds0 + L_GW + t * 1024);
#pragma unroll
        for (int m = 0; m < 2; ++m) {
            f32x4 mk;
#pragma unroll
            for (int i = 0; i < 4; ++i) { const bool le = (i + 8 * t + 4 * hh) <= r32; mk[i] = (le != (m != 0)) ? 0.f : -__builtin_inff(); }
            *(LAS f32x4*)(lds + L_MDIAG + m * 4096 + t * 1024 + lane * 16) = mk;
        }
        if (tid < 4) *(LAS unsigned*)(lds + L_CNT + tid * 4) = 0;
        *(LAS unsigned*)(lds + L_Z + tid * 4) = 0;
        MK_ATT(0);
        MK_TAGS(a.fl);
        unsigned vwf = window_flag(a);
        PIN(a.vv0); PIN(a.vv1); PIN(vwf);
        if (e) project<1>(lds, a.rkv, a.ro, lane, t, p, j, a.fl); else project<0>(lds, a.rkv, a.ro, lane, t, p, j, a.fl);
        cnt_wait(lds + L_CNT + 8);
        __builtin_amdgcn_s_sleep(36);
        tag_wait(a.fl, vwf, 0);
        KV3 kw;
        a.gt = GATE();
        LDB_FIRST(kw);
        MK_SO();
        attn_window<0>(a, kw, so0);
        PIN(a.idx);
        attn_full<0>(a, lds_zero16(a.zp), lds_zero16(a.zp), lds_zero16(a.zp), lds_zero16(a.zp), lds_zero16(a.zp), so0[3], a.rkv, 0, 0, 0, 0, 0, kw);
        BAR();
    }
}

extern "C" void kernel_launch(void* const* d_in, const int* in_sizes, int n_in, void* d_out, int out_size, void* d_ws, size_t ws_size,
                              hipStream_t stream) {
    (void)in_sizes; (void)n_in; (void)out_size; (void)ws_size;
    (void)hipFuncSetAttribute((const void*)gmix_kernel, hipFuncAttributeMaxDynamicSharedMemorySize, L_END);
    gmix_kernel<<<256, 512, L_END, stream>>>((const char*)d_in[0], (const char*)d_in[1], (const char*)d_in[2], (const char*)d_in[4],
                                             (const char*)d_in[5], (const char*)d_in[6], (char*)d_out, (char*)d_ws);
}
```

```cpp
#include <hip/hip_runtime.h>
#include <hip/hip_bf16.h>
#define OUT_LDS

typedef __attribute__((ext_vector_type(8))) __bf16 bf16x8;
typedef __attribute__((ext_vector_type(16))) __bf16 bf16x16;
typedef __attribute__((ext_vector_type(16))) float f32x16;
typedef __attribute__((ext_vector_type(8))) float f32x8;
typedef __attribute__((ext_vector_type(4))) float f32x4;
typedef __attribute__((ext_vector_type(4))) unsigned u32x4;
typedef __attribute__((ext_vector_type(2))) unsigned u32x2;
typedef __attribute__((ext_vector_type(2))) __bf16 bf16x2;
typedef short s16x4 __attribute__((ext_vector_type(4)));
typedef unsigned long long u64;
#define GAS __attribute__((address_space(1)))
#define LAS __attribute__((address_space(3)))
typedef LAS const char* lds_cptr;
typedef LAS char* lds_ptr;

constexpr unsigned WS_EPOCH = 0;
#define FSTR 8
constexpr unsigned WS_OFLAG = 65536;
constexpr unsigned WS_KVFLAG = 131072;
constexpr unsigned WS_G = 524288;
constexpr unsigned WS_KV = 1u << 20;
constexpr unsigned TS = 2176;
constexpr unsigned KV_BYTES = 2u * 32u * 64u * TS;
#define OSTR 32896
constexpr unsigned WS_O = 10u << 20;
constexpr unsigned OS = OSTR;
constexpr unsigned O_BYTES = 128u * OS;
constexpr unsigned WS_UFLAG = 49152;
constexpr unsigned WS_U = 15u << 20;
constexpr unsigned U_BYTES = 4u << 20;
constexpr unsigned WS_STAMP = 19u << 20;
constexpr unsigned OOB = 0x7ff00000u;
constexpr unsigned L_W = 65536;
constexpr unsigned L_WO = 0, L_PART = 65536, PREC = 7168, L_Q14 = 106496, L_Q15 = 114688, L_MDIAG = 122880, L_MFAR = 126976, L_Q = 131072,
                   L_GW = 147456, L_Z = 151552, L_G = 152576, L_CNT = 153088, L_EP = 153344, L_SCR = 153600, L_U = 154112, L_END = 163328;
constexpr float QSCALE = 0.25f * 1.4426950408889634f;
#define LMAXV 1.2676506e30f
constexpr float LMAX = LMAXV, LMIN = 9.094947e-13f;
constexpr unsigned SPIN_CAP = 1u << 17;

#define SBAR() __builtin_amdgcn_sched_barrier(0)
#define PIN(x) asm volatile("" : "+v"(x))
#define MFMA(a, b, c) __builtin_amdgcn_mfma_f32_32x32x16_bf16(a, b, c, 0, 0, 0)
#define BAR() asm volatile("s_barrier" ::: "memory")
#define CBAR() asm volatile("s_waitcnt lgkmcnt(0)\n\ts_barrier" ::: "memory")
#define DRAIN() asm volatile("s_waitcnt vmcnt(0)" ::: "memory")
#define PACEN 6
#define PACE() asm volatile("s_nop %0" :: "i"(PACEN) : "memory")
#define RLX_AGENT __ATOMIC_RELAXED, __HIP_MEMORY_SCOPE_AGENT
#define FL_SCOPE __HIP_MEMORY_SCOPE_AGENT
#define RLX_ST __ATOMIC_RELAXED, FL_SCOPE
#define STAMP(i)

__device__ __forceinline__ void glds16(unsigned voff, const void* sbase, unsigned ldsaddr) {
    asm volatile("s_mov_b32 m0, %2\n\ts_nop 0\n\tglobal_load_lds_dwordx4 %0, %1" ::"v"(voff), "s"(sbase), "s"(ldsaddr) : "memory");
}
__device__ __forceinline__ void glds16x2(unsigned v0, unsigned v1, const void* sbase, unsigned ldsaddr) {
    asm volatile("s_mov_b32 m0, %3\n\ts_nop 0\n\tglobal_load_lds_dwordx4 %0, %2\n\tglobal_load_lds_dwordx4 %1, %2 offset:1024"
                 ::"v"(v0), "v"(v1), "s"(sbase), "s"(ldsaddr) : "memory");
}
__device__ __forceinline__ void glds16x4(unsigned v0, unsigned v1, unsigned v2, unsigned v3, const void* sbase, unsigned ldsaddr) {
    asm volatile("s_mov_b32 m0, %5\n\ts_nop 0\n\tglobal_load_lds_dwordx4 %0, %4\n\tglobal_load_lds_dwordx4 %1, %4 offset:1024\n\t"
                 "global_load_lds_dwordx4 %2, %4 offset:2048\n\tglobal_load_lds_dwordx4 %3, %4 offset:3072"
                 ::"v"(v0), "v"(v1), "v"(v2), "v"(v3), "s"(sbase), "s"(ldsaddr) : "memory");
}
__device__ __forceinline__ unsigned cvtpk(float lo, float hi) {
    typedef __attribute__((ext_vector_type(2))) float f32x2;
    return __builtin_bit_cast(unsigned, __builtin_convertvector((f32x2){lo, hi}, bf16x2));
}
__device__ __forceinline__ bf16x8 as_frag(u32x4 v) { return __builtin_bit_cast(bf16x8, v); }
__device__ __forceinline__ bf16x8 lds_frag(lds_cptr p) { return *(const LAS bf16x8*)p; }
__device__ __forceinline__ bf16x8 tr_frag(lds_cptr p0, lds_cptr p1) {
    s16x4 a = __builtin_amdgcn_ds_read_tr16_b64_v4i16((LAS s16x4*)p0);
    s16x4 b = __builtin_amdgcn_ds_read_tr16_b64_v4i16((LAS s16x4*)p1);
    return __builtin_bit_cast(bf16x8, __builtin_shufflevector(a, b, 0, 1, 2, 3, 4, 5, 6, 7));
}
__device__ __forceinline__ f32x16 pv_lo(bf16x8 v, const f32x16& p, f32x16 acc, int idx) {
    f32x8 h = __builtin_shufflevector(p, p, 0, 1, 2, 3, 4, 5, 6, 7);
    return __builtin_amdgcn_smfmac_f32_32x32x32_bf16(v, __builtin_bit_cast(bf16x16, h), acc, idx, 0, 0);
}
__device__ __forceinline__ f32x16 pv_hi(bf16x8 v, const f32x16& p, f32x16 acc, int idx) {
    f32x8 h = __builtin_shufflevector(p, p, 8, 9, 10, 11, 12, 13, 14, 15);
    return __builtin_amdgcn_smfmac_f32_32x32x32_bf16(v, __builtin_bit_cast(bf16x16, h), acc, idx, 0, 0);
}
#define MX3(a, b, c) __builtin_fmaxf(__builtin_fmaxf((a), (b)), (c))
__device__ __forceinline__ float max16(const f32x16& s) {
    float a = MX3(s[0], s[1], s[2]), b = MX3(s[3], s[4], s[5]);
    a = MX3(a, s[6], s[7]); b = MX3(b, s[8], s[9]); a = MX3(a, s[10], s[11]); b = MX3(b, s[12], s[13]);
    return MX3(a, b, __builtin_fmaxf(s[14], s[15]));
}
__device__ __forceinline__ float halfmax(float m) {
    auto r = __builtin_amdgcn_permlane32_swap(__float_as_uint(m), __float_as_uint(m), false, false);
    return __builtin_fmaxf(__uint_as_float(r[0]), __uint_as_float(r[1]));
}
__device__ __forceinline__ f32x16 lds_f32x16(lds_cptr p) {
    f32x4 a = *(const LAS f32x4*)p, b = *(const LAS f32x4*)(p + 1024), c = *(const LAS f32x4*)(p + 2048), d = *(const LAS f32x4*)(p + 3072);
    f32x8 lo = __builtin_shufflevector(a, b, 0, 1, 2, 3, 4, 5, 6, 7), hi = __builtin_shufflevector(c, d, 0, 1, 2, 3, 4, 5, 6, 7);
    return __builtin_shufflevector(lo, hi, 0, 1, 2, 3, 4, 5, 6, 7, 8, 9, 10, 11, 12, 13, 14, 15);
}
#define ST_AUX 16
#define LD_AUX 16
#define BLOAD(rs, vo, so) as_frag(__builtin_amdgcn_raw_buffer_load_b128(rs, vo, so, LD_AUX))
#define BSTORE(v, rs, vo, so) __builtin_amdgcn_raw_buffer_store_b128(v, rs, (vo) + (so), 0, ST_AUX)

#define POLLSLP 8
#define BSLP 34
struct Tags { __amdgpu_buffer_rsrc_t rs; u64 tag; u32x2 vtag; unsigned v0; };
__device__ __forceinline__ u64 tag_load(const Tags& f, unsigned vo, unsigned so) {
    return __builtin_bit_cast(u64, __builtin_amdgcn_raw_buffer_load_b64(f.rs, vo, so, LD_AUX));
}
__device__ __forceinline__ void tag_raise(const Tags& f, unsigned so) {
    DRAIN();
    __builtin_amdgcn_raw_buffer_store_b64(f.vtag, f.rs, f.v0, so, ST_AUX);
}
__device__ __forceinline__ void tag_wait(const Tags& f, u64 v, unsigned vo, unsigned so) {
    if (__builtin_expect(!__all(v == f.tag), 0)) {
#pragma nounroll
        for (unsigned spins = 0; spins < SPIN_CAP; ++spins) {
            __builtin_amdgcn_s_sleep(POLLSLP);
            asm volatile("" ::: "memory");
            if (__all(tag_load(f, vo, so) == f.tag)) break;
        }
    }
    asm volatile("" ::: "memory");
}
__device__ __forceinline__ void tag_wait(const Tags& f, unsigned vo, unsigned so) { tag_wait(f, tag_load(f, vo, so), vo, so); }

__device__ __forceinline__ void cnt_arrive(lds_ptr cnt, int lane) {
    asm volatile("s_waitcnt lgkmcnt(0)" ::: "memory");
    if (lane == 0) __hip_atomic_fetch_add((LAS unsigned*)cnt, 1u, __ATOMIC_RELAXED, __HIP_MEMORY_SCOPE_WORKGROUP);
}
__device__ __forceinline__ void cnt_wait(lds_ptr cnt) {
#pragma nounroll
    for (unsigned spins = 0; spins < (1u << 22); ++spins) {
        if (__builtin_amdgcn_readfirstlane(__hip_atomic_load((LAS unsigned*)cnt, __ATOMIC_RELAXED, __HIP_MEMORY_SCOPE_WORKGROUP)) == 4) break;
        __builtin_amdgcn_s_sleep(1);
    }
    asm volatile("" ::: "memory");
}
__device__ __forceinline__ unsigned kvflag(int br, int quad, int tile, int type) {
    return WS_KVFLAG + (unsigned)(((((br * 8 + quad) * 4 + (tile & 3)) * 2 + (tile >> 5)) * 2 + type) * 8 + ((tile >> 2) & 7)) * FSTR;
}

__device__ __forceinline__ void slow_rows(__amdgpu_buffer_rsrc_t rs, unsigned sbase, int lo, int hi, int g, bool band, bf16x8 q,
                                          unsigned vk, unsigned vv0, unsigned vv1, lds_cptr lds, int lane, int idx, f32x16& acc) {
    float m = -__builtin_inff();
    for (int pass = 0; pass < 2; ++pass) {
        if (pass) { m = halfmax(m); acc = f32x16{}; }
        for (int kt = lo; kt <= hi; ++kt) {
            const unsigned so = sbase + (unsigned)kt * TS;
            const bf16x8 k = BLOAD(rs, vk, so), v0 = BLOAD(rs, vv0, so), v1 = BLOAD(rs, vv1, so);
            f32x16 c = f32x16{};
            if (band && kt == g) c = lds_f32x16(lds + L_MDIAG + lane * 16);
            if (band && kt == g - 8) c = lds_f32x16(lds + L_MFAR + lane * 16);
            f32x16 s = MFMA(k, q, c);
            if (!pass) m = __builtin_fmaxf(m, max16(s));
            else {
#pragma unroll
                for (int r = 0; r < 16; ++r) s[r] = __builtin_amdgcn_exp2f(s[r] - m);
                acc = pv_lo(v0, s, acc, idx); acc = pv_hi(v1, s, acc, idx);
            }
        }
    }
}

#define PK4(A, o) ((u32x4){cvtpk(A[o], A[o + 1]), cvtpk(A[o + 2], A[o + 3]), cvtpk(A[o + 4], A[o + 5]), cvtpk(A[o + 6], A[o + 7])})
#define PROF_ARG
#define PROF_PASS
__device__ __forceinline__ void make_tag(Tags& f, u32x2 e) {
    f.tag = (((u64)(unsigned)__builtin_amdgcn_readfirstlane(e[1]) << 32) | (unsigned)__builtin_amdgcn_readfirstlane(e[0])) * 0x9E3779B97F4A7C15ull + 0x632BE59BD9B4E019ull;
    asm volatile("" : "+s"(f.tag));
    f.vtag = __builtin_bit_cast(u32x2, f.tag);
    PIN(f.vtag);
}
template <int E>
__device__ __forceinline__ u32x4 pack_kv(int i, const f32x16& a) {
    const int s = i & 1;
    if (E == 0) return PK4(a, 8 * s);
    auto x = __builtin_amdgcn_permlane32_swap(cvtpk(a[8 * s], a[8 * s + 1]), cvtpk(a[8 * s + 4], a[8 * s + 5]), false, false);
    auto y = __builtin_amdgcn_permlane32_swap(cvtpk(a[8 * s + 2], a[8 * s + 3]), cvtpk(a[8 * s + 6], a[8 * s + 7]), false, false);
    return (u32x4){x[0], y[0], x[1], y[1]};
}
#define PUT_KV(i, v, vo) BSTORE(v, rkv, vo, ((i) < 2 ? 64 * TS : 0) + ((i) & 1) * (E ? 512 : 128 * TS))
#define PBX 8
#define GE 1
#define XQ(p) (((p) >> 3) * 4 + (((p) + 1 + (((p) >> 2) & 1) * 2) & 3))
template <int E>
__device__ __forceinline__ void project(lds_ptr lds, __amdgpu_buffer_rsrc_t rkv, __amdgpu_buffer_rsrc_t ro, int lane, int t, int p, int j, Tags& fl, __amdgpu_buffer_rsrc_t ru, unsigned su PROF_ARG) {
    const int r32 = lane & 31, hh = lane >> 5, qd4 = XQ(p);
    const unsigned q4 = (lane & 15) >> 2, wcol = ((lane >> 4) & 1) * 32 + (lane & 3) * 8;
    lds_cptr xa[4];
#pragma unroll
    for (int ks = 0; ks < 4; ++ks) xa[ks] = lds + (32 * t + r32) * 128 + (((hh ^ ((r32 >> 1) & 7)) * 16) ^ (ks * 32));
    unsigned w1o = L_W + (8 * hh + q4) * 128 + ((lane >> 4) & 1) * 64 + (q4 >> 1) * 32 + (lane & 3) * 8, w0o = w1o ^ 32;
    PIN(w0o); PIN(w1o);
    const lds_cptr w0 = lds + w0o, w1 = lds + w1o, wq = lds + (8 * hh + q4) * 64 + wcol;
#define XRD(n) lds_frag(xa[(n) & 3] + (((n) >> 2) & 3) * 16384)
#define WRD(b, n) tr_frag(b + ((n) & 15) * 2048, b + ((n) & 15) * 2048 + 512)
#define YOF(n) ((n) >= 28 ? 32768 + ((n) & 3) * 2048 : ((n) & 15) * 2048)
#define YRD(b, n) tr_frag(b + YOF(n), b + YOF(n) + 512)
#define QOF(br, n) (((n) >= 28 ? L_Q15 - L_Q14 : ((n) >= 24 ? 0 : (((n) >> 2) & 3) * 16384)) + (br) * 4096 + ((n) & 3) * 1024)
#define QRD(br, n) tr_frag(((n) >= 24 ? wqh : wq) + QOF(br, n), ((n) >= 24 ? wqh : wq) + QOF(br, n) + 256)
#define MM(W, X, A) (E ? MFMA(X, W, A) : MFMA(W, X, A))
    bf16x8 xf[32], f0[4], f1[4];
    f32x16 a0 = f32x16{}, a1 = f32x16{};
    unsigned gwo = (lane & 15) == ((lane >> 4) & 1) ? L_GW + t * 1024 + hh * 16 : L_Z;
    if (E == GE) PIN(gwo);
    f32x4 zg = f32x4{};
    bf16x8 gf[4];
#define RD1(n) do { xf[n] = XRD(n); f0[(n) & 3] = WRD(w0, n); f1[(n) & 3] = WRD(w1, n); SBAR(); } while (0)
#define GRD(n) do { if (E == GE) gf[(n) & 3] = lds_frag(lds + gwo + (n) * 32); } while (0)
#define GMM(n) do { if (E == GE) zg = __builtin_amdgcn_mfma_f32_16x16x32_bf16(gf[(n) & 3], xf[n], zg, 0, 0, 0); } while (0)
#define MM1(n) do { a0 = MM(f0[(n) & 3], xf[n], a0); a1 = MM(f1[(n) & 3], xf[n], a1); SBAR(); } while (0)
#pragma unroll
    for (int s = 0; s < 8; ++s) {
        if (s == 0) CBAR(); else BAR();
        SBAR();
        RD1(4 * s); RD1(4 * s + 1);
        if (s) { MM1(4 * s - 1); GMM(4 * s - 4); GMM(4 * s - 3); GMM(4 * s - 2); GMM(4 * s - 1); SBAR(); }
        RD1(4 * s + 2); MM1(4 * s); RD1(4 * s + 3); MM1(4 * s + 1);
        if (s == 0 && E == GE) DRAIN();
        GRD(4 * s); GRD(4 * s + 1); GRD(4 * s + 2); GRD(4 * s + 3); SBAR();
        MM1(4 * s + 2);
        SBAR();
    }
    const unsigned tile = 4 * j + t;
    unsigned vo = (unsigned)(4 * qd4 * 64 + tile) * TS + (E ? (r32 >> 4) * (128 * TS) + 1024 + hh * 256 + (r32 & 15) * 16 : lane * 16);
    unsigned wqo = L_Q14 + (8 * hh + q4) * 64 + wcol;
    PIN(vo); PIN(wqo);
    const lds_cptr wqh = lds + wqo;
    MM1(31); GMM(28); GMM(29); GMM(30); GMM(31); SBAR();
#define PRE8 2
#define PUBN 7
#define LATE0 99
    u32x4 pk[4];
#pragma unroll
    for (int i = 0; i < 4; ++i) { pk[i] = pack_kv<E>(i, i < 2 ? a0 : a1); PIN(pk[i]); if (i < PRE8) PUT_KV(i, pk[i], vo); SBAR(); }
    if (E == GE) BSTORE(((u32x4){0x3f803f80u, 0x3f803f80u, 0x3f803f80u, 0x3f803f80u}), rkv,
                   (unsigned)((hh * 32 + 4 * (qd4 ^ (hh * (PBX >> 1))) + (r32 >> 3)) * 64 + tile) * TS + 2048 + (lane & 7) * 16, 0);
    if (E == GE) __builtin_amdgcn_raw_buffer_store_b64((u32x2){__float_as_uint(__builtin_amdgcn_rcpf(1.f + __expf(-zg[0]))), __float_as_uint(__builtin_amdgcn_rcpf(1.f + __expf(-zg[1])))},
                                                  fl.rs, lane < 16 ? lane * 8 : OOB, WS_G + (qd4 * 64 + tile) * 128, ST_AUX);
    SBAR();
    BAR(); SBAR();
    bf16x8 u0 = QRD(0, 0), u1 = QRD(1, 0), y0 = YRD(w0, 0), y1 = YRD(w1, 0);
    const u32x2 tg = *(const LAS u32x2*)(lds + L_EP);
    f32x16 b0 = f32x16{}, b1 = f32x16{}, aQ0 = f32x16{}, aQ1 = f32x16{};
    SBAR();
#pragma unroll
    for (int n = 0; n < 24; ++n) {
        aQ0 = MFMA(u0, xf[n], aQ0); SBAR();
        if (n == 1) { make_tag(fl, tg); SBAR(); }
        if (n == PUBN) { tag_raise(fl, kvflag(0, qd4, tile, E)); SBAR(); }
        if ((n & 3) == 3) {
            if (PRE8 + (n >> 2) < 2) { PUT_KV((PRE8 + (n >> 2)) & 3, pk[(PRE8 + (n >> 2)) & 3], vo); SBAR(); }
            if (n == LATE0) { PUT_KV(2, pk[2], vo); SBAR(); }
            if (n == LATE0 + 4) { PUT_KV(3, pk[3], vo); SBAR(); }
            BAR(); SBAR();
        }
        u0 = QRD(0, n + 1); SBAR();
        aQ1 = MFMA(u1, xf[n], aQ1); SBAR();
        u1 = n < 23 ? QRD(1, n + 1) : QRD(0, 25); SBAR();
        b0 = MM(y0, xf[n], b0); SBAR(); y0 = n < 23 ? YRD(w0, n + 1) : QRD(0, 26); SBAR();
        b1 = MM(y1, xf[n], b1); SBAR(); y1 = n < 23 ? YRD(w1, n + 1) : QRD(0, 27); SBAR();
    }
    aQ0 = MFMA(u0, xf[24], aQ0); SBAR(); u0 = QRD(0, 28); SBAR(); aQ0 = MFMA(u1, xf[25], aQ0); SBAR(); u1 = QRD(0, 29); SBAR();
    aQ0 = MFMA(y0, xf[26], aQ0); SBAR(); y0 = QRD(0, 30); SBAR(); aQ0 = MFMA(y1, xf[27], aQ0); SBAR(); y1 = QRD(0, 31); SBAR();
    aQ0 = MFMA(u0, xf[28], aQ0); SBAR(); aQ0 = MFMA(u1, xf[29], aQ0); SBAR(); u1 = QRD(1, 24); SBAR();
    aQ0 = MFMA(y0, xf[30], aQ0); SBAR(); y0 = YRD(w0, 24); SBAR(); aQ0 = MFMA(y1, xf[31], aQ0); SBAR(); y1 = YRD(w1, 24); SBAR();
    const lds_ptr qd = lds + L_Q + t * 1024 + lane * 16;
#define QBAR 4
#define NFREE 3
    f32x8 qs; u32x4 qk;
#define QEPI(m) do { if ((m) == 1) { PIN(aQ0); qs = __builtin_shufflevector(aQ0, aQ0, 8, 9, 10, 11, 12, 13, 14, 15) * QSCALE; PIN(qs); } \
        if ((m) == 2) { qk = PK4(qs, 0); *(LAS u32x4*)(qd + 4096) = qk; } \
        if ((m) == QBAR) { __builtin_amdgcn_s_setprio(2); __builtin_amdgcn_s_waitcnt(0xC07F | ((2 * (QBAR - 2)) << 8)); BAR(); STAMP(4); } \
        if ((m) > QBAR + NFREE) PACE(); SBAR(); } while (0)
#pragma unroll
    for (int n = 24; n < 32; ++n) {
        aQ1 = MFMA(u1, xf[n], aQ1); SBAR(); if (n < 31) u1 = QRD(1, n + 1); SBAR(); QEPI(3 * (n - 24));
        b0 = MM(y0, xf[n], b0); SBAR(); if (n < 31) y0 = YRD(w0, n + 1); SBAR(); QEPI(3 * (n - 24) + 1);
        b1 = MM(y1, xf[n], b1); SBAR(); if (n < 31) y1 = YRD(w1, n + 1); SBAR(); QEPI(3 * (n - 24) + 2);
    }
#pragma unroll
    for (int r = 0; r < 8; ++r) aQ0[r] *= QSCALE;
#pragma unroll
    for (int r = 0; r < 16; ++r) aQ1[r] *= QSCALE;
    const u32x4 qe = PK4(aQ0, 0);
    *(LAS u32x4*)qd = qe; *(LAS u32x4*)(qd + 8192) = PK4(aQ1, 0); *(LAS u32x4*)(qd + 12288) = PK4(aQ1, 8);
    cnt_arrive(lds + L_CNT + 8, lane);
    const unsigned vo2 = vo + (32 + 4 * ((qd4 ^ (PBX >> 1)) - qd4)) * 64 * TS;
#define YSLP 10
    u32x4 py[4] = {pack_kv<E>(0, b0), pack_kv<E>(1, b0), pack_kv<E>(2, b1), pack_kv<E>(3, b1)};
    PIN(py[0]); PIN(py[1]); PIN(py[2]); PIN(py[3]);
#define YSTAG 2
    if (t & 1) __builtin_amdgcn_s_sleep(YSTAG);
    if (t & 2) __builtin_amdgcn_s_sleep(2 * YSTAG);
    BSTORE(qe, ru, lane * 16, su);
    if (LATE0 > 23) { PUT_KV(2, pk[2], vo); __builtin_amdgcn_s_sleep(YSLP); }
    if (LATE0 > 19) { PUT_KV(3, pk[3], vo); __builtin_amdgcn_s_sleep(YSLP); }
    PUT_KV(0, py[0], vo2); __builtin_amdgcn_s_sleep(YSLP); PUT_KV(1, py[1], vo2); __builtin_amdgcn_s_sleep(YSLP);
    PUT_KV(2, py[2], vo2); __builtin_amdgcn_s_sleep(YSLP); PUT_KV(3, py[3], vo2);
    tag_raise(fl, kvflag(1, qd4, tile, E));
    BSTORE(((u32x4){~0u, ~0u, ~0u, ~0u}), ro, lane * 16, tile * OS + 2 * p * 1024);
    STAMP(19);
}

#define AB_WO (512 * 1024)
#define ROTEXPR j + 4 * p
#define WOX 1
#define WOIN 2
#define CODE_LINES 304

struct Att {
    lds_ptr lds; char* ws; __amdgpu_buffer_rsrc_t rkv, ro, rka;
    Tags fl; int lane, t, p, g, head, idx; float gt; unsigned vk, vv0, vv1; lds_cptr zp, qp[4];
};
#define IDX a.idx
#define LD3(K, V0, V1, rs, so) do { K = BLOAD(rs, a.vk, so); V0 = BLOAD(rs, a.vv0, so); V1 = BLOAD(rs, a.vv1, so); } while (0)
#define EX(X, r) X[r] = __builtin_amdgcn_exp2f(X[r])
#define STEP(X, Y, OP, VP, KN, QN, CN, O, V0, F1, F3, F2) do { \
        EX(X, 0); EX(X, 1); SBAR(); OP = pv_hi(VP, Y, OP, IDX); SBAR(); F1; SBAR(); EX(X, 2); EX(X, 3); EX(X, 4); SBAR(); Y = MFMA(KN, QN, CN); SBAR(); F3; SBAR(); \
        EX(X, 5); EX(X, 6); EX(X, 7); SBAR(); EX(X, 8); EX(X, 9); SBAR(); O = pv_lo(V0, X, O, IDX); SBAR(); F2; SBAR(); \
        EX(X, 10); EX(X, 11); EX(X, 12); EX(X, 13); EX(X, 14); EX(X, 15); SBAR(); } while (0)

__device__ __forceinline__ f32x16 lds_zero16(lds_cptr zp) {
    const volatile LAS f32x4* z = (const volatile LAS f32x4*)zp;
    const f32x4 a = *z, b = *z, c = *z, d = *z;
    f32x8 lo = __builtin_shufflevector(a, b, 0, 1, 2, 3, 4, 5, 6, 7), hi = __builtin_shufflevector(c, d, 0, 1, 2, 3, 4, 5, 6, 7);
    return __builtin_shufflevector(lo, hi, 0, 1, 2, 3, 4, 5, 6, 7, 8, 9, 10, 11, 12, 13, 14, 15);
}

template <int C, int BR>
__device__ __forceinline__ void finish(const Att& a, f32x16 o, float gf, bf16x8 q, unsigned vo) {
    f32x8 y = __builtin_shufflevector(o, o, 0, 1, 2, 3, 4, 5, 6, 7) * (__builtin_amdgcn_rcpf(o[8]) * gf);
    PIN(y);
    const float chk = __builtin_fmaf(((y[0] + y[2]) + (y[4] + y[6])) + ((y[1] + y[3]) + (y[5] + y[7])), 0.f, o[8]);
    if (__builtin_expect(__any(__builtin_amdgcn_fmed3f(chk, LMIN, LMAX) != chk), 0)) {
        if (!BR) { tag_wait(a.fl, a.lane * FSTR, kvflag(!C, a.p >> 1, 0, 0)); tag_wait(a.fl, a.lane * FSTR, kvflag(!C, a.p >> 1, 2, 0)); }
        slow_rows(a.rkv, (unsigned)((BR * 32 + (a.head ^ (BR * 2 * PBX))) * 64) * TS, BR ? (a.g >= 8 ? a.g - 8 : 0) : 0, BR ? a.g : 63, a.g, BR, q, a.vk, a.vv0, a.vv1, a.lds, a.lane, IDX, o);
        y = __builtin_shufflevector(o, o, 0, 1, 2, 3, 4, 5, 6, 7) * (__builtin_amdgcn_rcpf(o[8]) * gf);
        PIN(y);
    }
    u32x4 v = (u32x4){cvtpk(y[0], y[1]), cvtpk(y[2], y[3]), cvtpk(y[4], y[5]), cvtpk(y[6], y[7])};
    if (!C && !BR) v = __builtin_elementwise_min(v, (u32x4){~1u, ~1u, ~1u, ~1u});
    BSTORE(v, a.ro, vo, 0);
}
#define OFLAG(BR, C) (WS_OFLAG + ((((BR) * 64 + a.g) * 2 + (C)) * 16 + (a.p ^ ((BR) * PBX))) * 8)
#define OVOFF(BR) (a.vk + (unsigned)((BR) * 64 + a.g) * OS + (a.head ^ ((BR) * 2 * PBX)) * 1024)
#define GATE() __uint_as_float(__builtin_amdgcn_raw_buffer_load_b32(a.fl.rs, (a.lane & 15) * 8 + (a.lane & 16) / 4, WS_G + ((a.p >> 1) * 64 + a.g) * 128, LD_AUX))

struct KV3 { bf16x8 k[3], v0[3], v1[3]; };
#define LDB(K, V0, V1, i) do { const int kt_ = a.g - (i); \
        const __amdgpu_buffer_rsrc_t rb_ = __builtin_amdgcn_make_buffer_rsrc(a.ws + WS_KV, 0, kt_ >= 0 ? KV_BYTES : 0u, 0x00020000); \
        LD3(K, V0, V1, rb_, (unsigned)((32 + (a.head ^ (2 * PBX))) * 64 + (kt_ >= 0 ? kt_ : 0)) * TS); } while (0)
#define LDB_FIRST(f) do { LDB(f.k[0], f.v0[0], f.v1[0], 0); SBAR(); LDB(f.k[1], f.v0[1], f.v1[1], 1); SBAR(); LDB(f.k[2], f.v0[2], f.v1[2], 2); SBAR(); } while (0)
#define LDA_FIRST(f) do { LD3(f.k[0], f.v0[0], f.v1[0], a.rka, so0[0]); SBAR(); LD3(f.k[1], f.v0[1], f.v1[1], a.rka, so0[1]); SBAR(); \
        LD3(f.k[2], f.v0[2], f.v1[2], a.rka, so0[2]); SBAR(); } while (0)
__device__ __forceinline__ unsigned window_flag(const Att& a) {
    const int i = a.lane & 15, kt = a.g - (i < 9 ? i : 0);
    return kvflag(1, a.p >> 1, kt >= 0 ? kt : 0, (a.lane >> 4) & 1);
}

template <int C>
__device__ __forceinline__ void attn_full(Att& a, f32x16 o0, f32x16 o1, f32x16 o2, f32x16 o3, f32x16 S1, unsigned so,
                                          __amdgpu_buffer_rsrc_t rwo, unsigned vwo, unsigned wi, unsigned v16, unsigned fs2, unsigned vf3, KV3& nxt, bool giver PROF_ARG) {
    const lds_ptr lds = a.lds; const int t = a.t, lane = a.lane;
    const f32x16 zc = f32x16{};
    bf16x8 ka = nxt.k[0], va0 = nxt.v0[0], va1 = nxt.v1[0], kb = nxt.k[1], vb0 = nxt.v0[1], vb1 = nxt.v1[1], kc = nxt.k[2], vc0 = nxt.v0[2], vc1 = nxt.v1[2], kd, vd0, vd1;
    const unsigned jb = a.g >> 2;
    unsigned ki = 3;
    vd1 = lds_frag(a.zp);
    lds_ptr pw = lds + L_PART + (4 * C + t) * PREC + lane * 16;
    lds_cptr pr0 = lds + L_PART + (4 * C + ((t - 1) & 3)) * PREC + lane * 16, pr1 = lds + L_PART + (4 * C + ((t - 2) & 3)) * PREC + lane * 16,
             pr2 = lds + L_PART + (4 * C + ((t - 3) & 3)) * PREC + lane * 16;
    lds_cptr pr3 = lds + L_U + t * 2304 + lane * 16, pl3 = lds + L_U + t * 2304 + 2048 + lane * 4;
    unsigned ovo = OVOFF(0);
    if (C) { BAR(); STAMP(4); }
    else { PIN(pw); PIN(pr0); PIN(pr1); PIN(pr2); PIN(pr3); PIN(pl3); PIN(ovo); PIN(a.gt);
           tag_raise(a.fl, OFLAG(1, 0)); }
    bf16x8 q0 = lds_frag(a.qp[0]); SBAR();
    bf16x8 q1 = lds_frag(a.qp[1]), q2 = lds_frag(a.qp[2]), q3 = lds_frag(a.qp[3]);
    SBAR();
    f32x16 S0 = MFMA(ka, q0, zc);
    PIN(S0); SBAR();
    PIN(q1); PIN(q2); PIN(q3); PIN(vd1); PIN(o0); PIN(o1); PIN(o2); PIN(o3); PIN(S1);
    __builtin_amdgcn_s_waitcnt(0x0F76);
#pragma unroll
    for (int i = 0; i < 8; ++i) __builtin_amdgcn_s_setprio(C);
    STAMP(3);
    SBAR();
    unsigned wso = 0;
    u64 fv = __builtin_bit_cast(u64, a.fl.vtag);
#define NEXT1() asm volatile("s_add_i32 %0, %0, 1\n\ts_xor_b32 %1, %0, %2" : "+s"(ki), "=s"(so) : "s"(jb) : "scc")
#define NEXT2() asm volatile("s_mulk_i32 %0, %1" : "+s"(so) : "i"(4 * TS) : "scc")
#define WOC 1
#define WO_OFF() do { if (C == WOC) asm volatile("s_lshl_b32 %0, %1, 4" : "=s"(wso) : "s"(wi) : "scc"); } while (0)
#define WO_DMA() do { if (C == WOC) __builtin_amdgcn_raw_ptr_buffer_load_lds(rwo, (LAS void*)(lds + L_WO + t * 16384 + wi), 16, vwo, wso, 0, 0); } while (0)
#define WO_NEXT() do { if (C == WOC) asm volatile("s_addk_i32 %0, 0x400\n\ts_and_b32 %0, %0, 0x3fff" : "+s"(wi) :: "scc"); } while (0)
#define TILE(K, V0, V1, KN, LK, LV0, LV1, NW, G) do { \
        STEP(S0, S1, o3, LV1, K, q1, zc, o0, V0, LK = BLOAD(a.rka, a.vk, so), WO_OFF(), LV0 = BLOAD(a.rka, a.vv0, so)); \
        STEP(S1, S0, o0, V1, K, q2, zc, o1, V0, LV1 = BLOAD(a.rka, a.vv1, so), , WO_DMA()); \
        STEP(S0, S1, o1, V1, K, q3, zc, o2, V0, NEXT1(), NEXT2(), ); \
        STEP(S1, S0, o2, V1, KN, q0, zc, o3, V0, __builtin_amdgcn_s_waitcnt(0x0F70 | (NW)), G, WO_NEXT()); } while (0)
#define PUTA(O, sl) (*(LAS f32x4*)(pw + (sl) * 2048) = __builtin_shufflevector(O, O, 0, 1, 2, 3))
#define PUTB(O, sl) (*(LAS f32x4*)(pw + (sl) * 2048 + 1024) = __builtin_shufflevector(O, O, 4, 5, 6, 7))
#define PUTL(O, sl) (*(LAS float*)(pw + 6144 + (sl) * 4) = O[8])
    f32x4 x0, y0, x1, y1, x2, y2, x3, y3; float l0, l1, l2, l3;
#define GETP() do { x0 = *(const LAS f32x4*)pr0; x1 = *(const LAS f32x4*)(pr1 + 2048); x2 = *(const LAS f32x4*)(pr2 + 4096); \
        y0 = *(const LAS f32x4*)(pr0 + 1024); y1 = *(const LAS f32x4*)(pr1 + 3072); y2 = *(const LAS f32x4*)(pr2 + 5120); \
        l0 = *(const LAS float*)(pr0 + 6144); l1 = *(const LAS float*)(pr1 + 6148); l2 = *(const LAS float*)(pr2 + 6152); } while (0)
    if (C) {
#pragma nounroll
        do {
            TILE(ka, va0, va1, kb, kd, vd0, vd1, WOC ? 7 : 6, );
            PIN(S0); PIN(S1); PIN(o3); tag_wait(a.fl, fv, v16, fs2);
            TILE(kb, vb0, vb1, kc, ka, va0, va1, WOC ? 8 : 6, );
            TILE(kc, vc0, vc1, kd, kb, vb0, vb1, WOC ? 9 : 6, fv = tag_load(a.fl, v16, fs2));
            TILE(kd, vd0, vd1, ka, kc, vc0, vc1, WOC ? 10 : 7, );
        } while (ki != 19);
        o3 = pv_hi(vd1, S1, o3, IDX);
        STAMP(6);
        const u64 f3 = tag_load(a.fl, vf3, 0);
        a.gt = GATE();
        cnt_wait(lds + L_CNT + 8);
        PUTA(o0, 0); PUTB(o0, 0); PUTL(o0, 0); PUTA(o1, 1); PUTB(o1, 1); PUTL(o1, 1); PUTA(o2, 2); PUTB(o2, 2); PUTL(o2, 2);
        DRAIN(); cnt_arrive(lds + L_CNT, lane);
        tag_wait(a.fl, f3, vf3, 0);
        LDB_FIRST(nxt);
        cnt_wait(lds + L_CNT);
        GETP();
    } else {
        WO_OFF(); WO_DMA(); WO_NEXT(); WO_OFF(); WO_DMA(); WO_NEXT(); SBAR();
        if (giver) { kd = kc; vd0 = vc0; const bf16x8 z = vd1; vd1 = vc1; kc = kb; vc0 = vb0; vc1 = vb1; kb = ka; vb0 = va0; vb1 = va1; va1 = z; }
        else { TILE(ka, va0, va1, kb, kd, vd0, vd1, WOC ? 6 : 9, ); PIN(S0); PIN(S1); PIN(o3); }
        const unsigned kend = giver ? 16 : 17;
        TILE(kb, vb0, vb1, kc, ka, va0, va1, WOC ? 6 : 9, );
#pragma nounroll
        do {
            TILE(kc, vc0, vc1, kd, kb, vb0, vb1, WOC ? 6 : 9, );
            TILE(kd, vd0, vd1, ka, kc, vc0, vc1, WOC ? 6 : 9, );
            TILE(ka, va0, va1, kb, kd, vd0, vd1, WOC ? 6 : 9, );
            TILE(kb, vb0, vb1, kc, ka, va0, va1, WOC ? 6 : 9, );
        } while (ki != kend);
        STEP(S0, S1, o3, vb1, kc, q1, zc, o0, vc0, , , );
        STEP(S1, S0, o0, vc1, kc, q2, zc, o1, vc0, , , );
        STEP(S0, S1, o1, vc1, kd, q0, zc, o2, vc0, __builtin_amdgcn_s_waitcnt(WOC ? 0x0F73 : 0x0F75), , );
        STEP(S1, S0, o2, vc1, kd, q1, zc, o0, vd0, , , );
        STEP(S0, S1, o0, vd1, kd, q2, zc, o1, vd0, , , );
        STEP(S1, S0, o1, vd1, kc, q3, zc, o2, vd0, PUTA(o0, 0), PUTL(o0, 0), PUTB(o0, 0));
        STEP(S0, S1, o2, vd1, kd, q3, zc, o3, vc0, PUTA(o1, 1), PUTL(o1, 1), PUTB(o1, 1));
        PUTA(o2, 2); PUTB(o2, 2); asm volatile("" :: "v"(o0), "v"(o1), "v"(o2)); SBAR();
        EX(S1, 0); EX(S1, 1); SBAR(); o3 = pv_hi(vc1, S0, o3, IDX); SBAR(); PUTL(o2, 2); __builtin_amdgcn_s_setprio(2); SBAR();
        EX(S1, 2); EX(S1, 3); EX(S1, 4); EX(S1, 5); EX(S1, 6); EX(S1, 7); SBAR(); EX(S1, 8); EX(S1, 9); SBAR();
        o3 = pv_lo(vd0, S1, o3, IDX); PIN(S1); SBAR();
        if (!WOC) DRAIN();
        CBAR(); GETP(); x3 = *(const LAS f32x4*)pr3; y3 = *(const LAS f32x4*)(pr3 + 1024); l3 = *(const LAS float*)pl3; SBAR();
        EX(S1, 10); EX(S1, 11); EX(S1, 12); EX(S1, 13); EX(S1, 14); EX(S1, 15); SBAR();
        o3 = pv_hi(vd1, S1, o3, IDX); SBAR();
        STAMP(6);
    }
    x0 = (x0 + x1) + x2; y0 = (y0 + y1) + y2; l0 = (l0 + l1) + l2;
    if (!C) { x0 += x3; y0 += y3; l0 += l3; }
    PIN(x0); PIN(y0); PIN(l0); PIN(o3);
#pragma unroll
    for (int r = 0; r < 4; ++r) { o3[r] += x0[r]; o3[4 + r] += y0[r]; }
    o3[8] += l0;
    finish<C, 0>(a, o3, a.gt, q3, ovo);
}

template <int C>
__device__ __forceinline__ void attn_window(const Att& a, KV3& f, const unsigned (&so0)[4] PROF_ARG) {
    const lds_ptr lds = a.lds; const int t = a.t, lane = a.lane;
    const f32x16 zc = f32x16{};
    bf16x8 k[9], v0[9], v1[9];
#pragma unroll
    for (int i = 0; i < 3; ++i) { k[i] = f.k[i]; v0[i] = f.v0[i]; v1[i] = f.v1[i]; }
#define LDW(i) LDB(k[i], v0[i], v1[i], i)
    const bf16x8 qa = lds_frag(lds + L_Q + 8192 + C * 4096 + t * 1024 + lane * 16);
    const f32x16 cd = lds_f32x16(lds + L_MDIAG + lane * 16), cf = lds_f32x16(lds + L_MFAR + lane * 16);
    f32x16 oo = lds_zero16(a.zp);
    f32x16 S0 = MFMA(k[0], qa, cd), S1 = MFMA(k[1], qa, zc);
    SBAR(); LDW(3); SBAR();
#pragma unroll
    for (int r = 0; r < 10; ++r) { EX(S0, r); SBAR(); }
    oo = pv_lo(v0[0], S0, oo, IDX); SBAR();
#pragma unroll
    for (int r = 10; r < 16; ++r) EX(S0, r);
    SBAR();
    STEP(S1, S0, oo, v1[0], k[2], qa, zc, oo, v0[1], LDW(4), , );
    STEP(S0, S1, oo, v1[1], k[3], qa, zc, oo, v0[2], LDW(5), , );
    STEP(S1, S0, oo, v1[2], k[4], qa, zc, oo, v0[3], LDW(6), , );
    STEP(S0, S1, oo, v1[3], k[5], qa, zc, oo, v0[4], LDW(7), , );
    STEP(S1, S0, oo, v1[4], k[6], qa, zc, oo, v0[5], LDW(8), , );
    const unsigned fa = kvflag(1, a.p >> 1, t, 0), va = (lane & 31) * FSTR;
    u64 fv = 0;
    STEP(S0, S1, oo, v1[5], k[7], qa, zc, oo, v0[6], if (!C) fv = tag_load(a.fl, va, fa), , );
    STEP(S1, S0, oo, v1[6], k[8], qa, cf, oo, v0[7], , , );
    if (C) { PIN(S0); PIN(S1); PIN(oo); tag_raise(a.fl, OFLAG(0, C)); }
    STEP(S0, S1, oo, v1[7], k[8], qa, cf, oo, v0[8], , , );
    oo = pv_hi(v1[8], S0, oo, IDX);
    STAMP(7);
    if (!C) { PIN(oo); tag_wait(a.fl, fv, va, fa); LDA_FIRST(f); }
    finish<C, 1>(a, oo, 1.f - a.gt, qa, OVOFF(1));
}

__global__ void __launch_bounds__(512, 2)
gmix_kernel(const char* x, const char* wA, const char* woA, const char* wB, const char* woB, const char* gw, char* out, char* ws) {
    extern __shared__ __attribute__((aligned(16))) char smem[];
    const lds_ptr lds = (lds_ptr)smem;
    const unsigned lds0 = (unsigned)(uintptr_t)lds;
    const int tid = threadIdx.x, lane = tid & 63, r32 = lane & 31, hh = lane >> 5;
    const int w = __builtin_amdgcn_readfirstlane(tid >> 6);
    const int bid = blockIdx.x, xcd = bid & 7, rr = bid >> 3;
    const int p = 4 * (xcd & 3) + (rr & 3), j = 8 * (xcd >> 2) + (rr >> 2);
    const int jo = 2 * xcd + (rr >> 4), po = rr & 15;
    const int t = w & 3, c = w >> 2;
    const int e = (xcd & 1) ^ GE;
    STAMP(0);
    {
        u64 pc; const unsigned ln = (((bid & ~7) | (tid >> 6)) + (bid < CODE_LINES - 256 ? (tid & 32) * 8 : 0)) * 128;
        asm volatile("s_getpc_b64 %0\n\ts_mov_b32 m0, %2\n\ts_nop 0\n\tglobal_load_lds_dword %1, %0" : "=&s"(pc) : "v"(ln), "s"(lds0 + L_SCR) : "memory");
        SBAR();
    }
    unsigned rot = (ROTEXPR) & 7;
    unsigned vx0 = (128 * j + 32 * t + (lane >> 3)) * 1024 + (((lane & 7) ^ (lane >> 4)) * 16), vx1 = (vx0 ^ 64) + 7168, vx2 = vx0 + 14336, vx3 = vx1 + 14336;
    unsigned vw0 = (16 * t + (lane >> 3)) * 3072 + 1024 + 1024 * e + 128 * XQ(p) + (((lane & 7) ^ (((lane >> 4) & 1) << 1)) * 16), vw1 = vw0 + 8 * 3072 - 1024;
    unsigned mx = lds0 + t * 4096, mw = lds0 + L_W + t * 2048, ow = rot * (64 * 3072);
    asm volatile("" : "+v"(vx0), "+v"(vx1), "+v"(vx2), "+v"(vx3), "+v"(vw0), "+v"(vw1));
    asm volatile("" : "+s"(mx), "+s"(mw), "+s"(rot), "+s"(ow));
    SBAR();
    asm volatile("" : "+s"(x), "+s"(wA), "+s"(woA), "+s"(wB), "+s"(woB), "+s"(gw), "+s"(out), "+s"(ws));
#define MK_ATT(C) Att a; a.lds = lds; a.ws = ws; a.lane = lane; a.t = t; a.p = p; a.g = 4 * j + t; a.head = 2 * p + C; a.idx = 0xdddddddd; \
    a.rkv = __builtin_amdgcn_make_buffer_rsrc(ws + WS_KV, 0, KV_BYTES, 0x00020000); a.ro = __builtin_amdgcn_make_buffer_rsrc(ws + WS_O, 0, O_BYTES, 0x00020000); \
    a.vk = lane * 16; a.vv0 = r32 < 16 ? 1024 + hh * 256 + r32 * 16 : ((r32 & ~4) == 16 ? 2048 : OOB); a.vv1 = r32 < 16 ? a.vv0 + 512 : a.vv0; a.zp = lds + L_Z + lane * 16; \
    a.rka = __builtin_amdgcn_make_buffer_rsrc(ws + WS_KV + (unsigned)(a.head * 64 + t) * TS, 0, 61 * TS, 0x00020000); \
    _Pragma("unroll") for (int i = 0; i < 4; ++i) a.qp[i] = lds + L_Q + C * 4096 + ((t + 1 + i) & 3) * 1024 + lane * 16
#define RWO __builtin_amdgcn_make_buffer_rsrc((void*)((t >> 1) ? woB : woA), 0, AB_WO, 0x00020000)
#define VWO (((t & 1) * 256 + (lane >> 2)) * 1024 + 64 * po + (lane & 3) * 16)
#define WI0 (4096 * (((rr >> 4) << 1) | (po & 1)) + WOX * 1024 * (xcd & 3))
#define MK_TAGS(f) do { f.rs = __builtin_amdgcn_make_buffer_rsrc(ws, 0, WS_KV, 0x00020000); f.v0 = lane ? OOB : 0; PIN(f.v0); } while (0)
#define GIVERS 0xa8
    const bool giver = (GIVERS >> xcd) & 1;
    const int px = xcd == 0 ? 7 : xcd + 1;
    const bool taker = ((xcd & 1) == 0) && xcd != 6 && ((GIVERS >> px) & 1);
#define RUG __builtin_amdgcn_make_buffer_rsrc(ws + WS_U, 0, giver ? U_BYTES : 0u, 0x00020000)
#define SUG ((unsigned)(bid * 4 + t) * 4096)
#define MK_SO() unsigned so0[4]; _Pragma("unroll") for (int i = 0; i < 4; ++i) so0[i] = (j ^ i) * (4 * TS)
    if (__builtin_expect(c, 1)) {
#define DMA1(s) do { const unsigned ks_ = ((s) + rot) & 7; \
        glds16x4(vx0, vx1, vx2, vx3, x + ks_ * 128, mx + ((s) & 3) * 16384); glds16x2(vw0, vw1, wA + ks_ * (64 * 3072), mw + ((s) & 3) * 8192); } while (0)
#define DMA2(s) do { const unsigned ks_ = (((s) + rot) & 7) * (64 * 3072); \
        glds16x2(vq0, vq1, wq + ks_, mq + ((s) == 14 ? L_Q14 : ((s) & 3) * 16384)); glds16x2(vw0, vw1, wy + ks_, mw + ((s) & 3) * 8192); SBAR(); } while (0)
#define LBAR(N) asm volatile("s_waitcnt vmcnt(" #N ")\n\ts_barrier" ::: "memory")
        STAMP(20);
        glds16x4(vx0, vx1, vx2, vx3, x + rot * 128, mx); glds16x2(vw0, vw1, wA + ow, mw);
        SBAR();
        const unsigned k1 = (1 + rot) & 7;
        glds16x2(vx0, vx1, x + k1 * 128, mx + 16384);
        SBAR();
        if (t == 0) asm volatile("s_mov_b32 m0, %2\n\ts_nop 0\n\tglobal_load_lds_dword %0, %1 sc1" :: "v"((lane & 1) * 4), "s"(ws + WS_EPOCH), "s"(lds0 + L_EP) : "memory");
        unsigned vq0 = (32 * (t & 1) + (lane >> 2)) * 3072 + 64 * (p ^ ((t >> 1) * PBX)) + (lane & 3) * 16, vq1 = vq0 + 16 * 3072 - 1024;
        unsigned vwo = VWO;
        MK_ATT(1);
        MK_TAGS(a.fl);
        int go = 4 * jo + t;
        asm volatile("" : "+s"(go));
        unsigned v16 = (lane & 15) * 8, vf3 = hh ? WS_OFLAG + (64 + go) * 256 + v16 : window_flag(a);
        const int pp = 4 * (px & 3) + (rr & 3), pj = 8 * (px >> 2) + (rr >> 2), pb = (bid & ~7) | px;
        if (taker && lane >= 48)
            vf3 = lane < 56 ? kvflag(1, pp >> 1, 4 * (pj ^ 15) + (lane & 3), (lane >> 2) & 1) : kvflag(1, XQ(pp), 4 * pj + t, (px & 1) ^ GE);
        PIN(vq0); PIN(vq1); PIN(vwo); PIN(a.vk); PIN(a.vv0); PIN(a.vv1); PIN(a.qp[0]); PIN(a.qp[1]); PIN(a.qp[2]); PIN(a.qp[3]); PIN(a.zp); PIN(v16); PIN(vf3); PIN(a.idx);
        SBAR();
        STAMP(30);
        glds16x2(vx0, vx1, x + k1 * 128 + 16384, mx + 16384 + 2048); glds16x2(vw0, vw1, wA + k1 * (64 * 3072), mw + 8192);
        STAMP(22);
        SBAR();
        lds_cptr wo = lds + L_WO + (4 * hh + ((lane & 15) >> 2)) * 64 + ((lane >> 4) & 1) * 32 + (lane & 3) * 8, wg[4];
        unsigned sg[4];
#pragma unroll
        for (int q = 0; q < 4; ++q) { const int gq = ((p >> 2) + 1 + q) & 3; sg[q] = (unsigned)go * OS + gq * 8192; wg[q] = wo + gq * 8192; PIN(wg[q]); asm volatile("" : "+s"(sg[q])); }
        unsigned vout = (32 * go + (lane >> 2)) * 1024 + 64 * po + (lane & 3) * 16;
        lds_ptr otw = lds + L_PART + (4 + t) * PREC + r32 * 80 + hh * 8;
        lds_cptr otr = lds + L_PART + (4 + t) * PREC + (lane >> 2) * 80 + (lane & 3) * 16;
        unsigned vf4 = giver && hh ? WS_UFLAG + (bid * 4 + t) * 8 : WS_OFLAG + (64 + go) * 256 + 128 + v16;
        PIN(wo); PIN(vout); PIN(otw); PIN(otr); PIN(vf4);
        SBAR();
#pragma nounroll
        for (int s = 2; s < 8; ++s) { LBAR(6); DMA1(s);
        }
        const unsigned mq = lds0 + t * 2048;
        const char* wq = (t >> 1) ? wB : wA;
        const char* wy = wB + 128 * ((XQ(p) ^ (PBX >> 1)) - XQ(p));
        LBAR(6); STAMP(14); DMA2(8);
        LBAR(4); STAMP(15); DMA2(9);
        make_tag(a.fl, *(const LAS u32x2*)(lds + L_EP));
        LBAR(4); STAMP(16); DMA2(10);
        const unsigned k7 = ((7 + rot) & 7) * (64 * 3072), sx = mq + L_Q15, sw = mw + 32768;
        LBAR(4); STAMP(17); glds16(vq0, wq + k7, sx); DMA2(11);
        f32x16 oo = lds_zero16(a.zp), o1 = lds_zero16(a.zp); PIN(oo); PIN(o1);
        LBAR(5); STAMP(18); glds16(vw0, wy + k7, sw); DMA2(12);
        f32x16 o2 = lds_zero16(a.zp), o3 = lds_zero16(a.zp); PIN(o2); PIN(o3);
        LBAR(5); STAMP(19); glds16(vq1, wq + k7 + 1024, sx + 1024); DMA2(13);
        f32x16 S1 = lds_zero16(a.zp); PIN(S1);
        LBAR(5); STAMP(21); glds16(vw1, wy + k7 + 1024, sw + 1024); DMA2(14);
        __amdgpu_buffer_rsrc_t rwo = RWO; unsigned wi0 = WI0;
        MK_SO();
        asm volatile("" : "+s"(a.rka), "+s"(rwo), "+s"(wi0), "+s"(so0[0]), "+s"(so0[1]), "+s"(so0[2]), "+s"(so0[3]));
        unsigned fs1 = kvflag(0, p >> 1, (j >> 3) * 32 + t, 0), fs2 = kvflag(0, p >> 1, ((j >> 3) ^ 1) * 32 + t, 0);
        asm volatile("" : "+s"(a.fl.rs), "+s"(fs1), "+s"(fs2));
        __builtin_amdgcn_s_setprio(1);
        LBAR(5); STAMP(24);
        const u64 fv = tag_load(a.fl, v16, fs1);
        LBAR(1); STAMP(29);
        tag_wait(a.fl, fv, v16, fs1);
        STAMP(5);
        KV3 kw;
        LDA_FIRST(kw);
        attn_full<1>(a, oo, o1, o2, o3, S1, so0[3], rwo, vwo, wi0, v16, fs2, vf3, kw, false PROF_PASS);
        STAMP(23);
        attn_window<1>(a, kw, so0 PROF_PASS);
        STAMP(8);
        if (taker) {
            const __amdgpu_buffer_rsrc_t ru = __builtin_amdgcn_make_buffer_rsrc(ws + WS_U, 0, U_BYTES, 0x00020000);
            const unsigned su = (unsigned)(pb * 4 + t) * 4096, sk = (unsigned)(2 * pp * 64 + 4 * (pj ^ 15)) * TS;
            const f32x16 zc = f32x16{};
            const bf16x8 q = BLOAD(ru, a.vk, su);
            bf16x8 k[4], v0[4], v1[4];
            LD3(k[0], v0[0], v1[0], a.rkv, sk); SBAR(); LD3(k[1], v0[1], v1[1], a.rkv, sk + TS); SBAR(); LD3(k[2], v0[2], v1[2], a.rkv, sk + 2 * TS); SBAR();
            f32x16 ou = lds_zero16(a.zp);
            tag_raise(a.fl, OFLAG(1, 1));
            LD3(k[3], v0[3], v1[3], a.rkv, sk + 3 * TS); SBAR();
            f32x16 U0 = MFMA(k[0], q, zc), U1 = MFMA(k[1], q, zc);
            SBAR();
#pragma unroll
            for (int r = 0; r < 10; ++r) { EX(U0, r); SBAR(); }
            ou = pv_lo(v0[0], U0, ou, IDX); SBAR();
#pragma unroll
            for (int r = 10; r < 16; ++r) EX(U0, r);
            SBAR();
            STEP(U1, U0, ou, v1[0], k[2], q, zc, ou, v0[1], , , );
            STEP(U0, U1, ou, v1[1], k[3], q, zc, ou, v0[2], , , );
            STEP(U1, U0, ou, v1[2], k[3], q, zc, ou, v0[3], , , );
            ou = pv_hi(v1[3], U1, ou, IDX);
            BSTORE(__builtin_bit_cast(u32x4, __builtin_shufflevector(ou, ou, 0, 1, 2, 3)), ru, a.vk, su + 1024);
            BSTORE(__builtin_bit_cast(u32x4, __builtin_shufflevector(ou, ou, 4, 5, 6, 7)), ru, a.vk, su + 2048);
            __builtin_amdgcn_raw_buffer_store_b32(__float_as_uint(ou[8]), ru, a.vk / 4, su + 3072, ST_AUX);
        }
#define OBR(n) ((n) >> 4 == 1 ? 0 : 1)
#define OHD(n) (2 * ((n) & 15) + ((n) >= 16))
        const __amdgpu_buffer_rsrc_t roe = a.ro;
#define OLD(n) BLOAD(roe, a.vk, (unsigned)(OBR(n) * 64 + go) * OS + OHD(n) * 1024)
#define OWF(n) tr_frag(wo + OBR(n) * 32768 + OHD(n) * 1024, wo + OBR(n) * 32768 + OHD(n) * 1024 + 512)
#define OIN 6
        bf16x8 oe[48], ux, uy; unsigned ul;
#pragma unroll
        for (int n = 0; n < 48; ++n) {
            if (n == 16) { const u64 v = tag_load(a.fl, v16, WS_OFLAG + go * 256 + 128); if (!taker) tag_raise(a.fl, OFLAG(1, 1)); tag_wait(a.fl, v, v16, WS_OFLAG + go * 256 + 128); STAMP(9); }
            if (n == OIN && taker) tag_raise(a.fl, WS_UFLAG + (pb * 4 + t) * 8);
            if (n == 32) {
                tag_wait(a.fl, vf4, 0);
                const __amdgpu_buffer_rsrc_t ru = RUG;
                ux = BLOAD(ru, a.vk, SUG + 1024); uy = BLOAD(ru, a.vk, SUG + 2048);
                ul = __builtin_amdgcn_raw_buffer_load_b32(ru, a.vk / 4, SUG + 3072, LD_AUX);
                SBAR();
            }
            if (n == 32 + OIN) {
                *(LAS bf16x8*)(lds + L_U + t * 2304 + a.vk) = ux; *(LAS bf16x8*)(lds + L_U + t * 2304 + 1024 + a.vk) = uy;
                *(LAS unsigned*)(lds + L_U + t * 2304 + 2048 + a.vk / 4) = ul;
                SBAR();
            }
            oe[n] = OLD(n); SBAR();
            if (n >= OIN - 1) __builtin_amdgcn_s_waitcnt(0x0F70 | (OIN - 1));
            SBAR();
        }
        bf16x8 wn[4];
        __builtin_amdgcn_s_waitcnt(0x0F70);
#define NEARLY 10
#define NMID 34
        __amdgpu_buffer_rsrc_t rout = __builtin_amdgcn_make_buffer_rsrc(out, 0, 2048 * 1024, 0x00020000);
        asm volatile("" : "+s"(rout));
        f32x16 ya = f32x16{};
        bf16x8 wf[16], of[16];
#define FLD(i) of[i] = BLOAD(a.ro, a.vk, sg[(i) >> 2] + ((i) & 3) * 2048)
#define GRP_BAD(q) ({ unsigned mx_ = 0; _Pragma("unroll") for (int i = 4 * (q); i < 4 * (q) + 4; ++i) { const u32x4 u_ = __builtin_bit_cast(u32x4, of[i]); \
            mx_ = __builtin_elementwise_max(__builtin_elementwise_max(mx_, u_[0]), u_[1]); mx_ = __builtin_elementwise_max(__builtin_elementwise_max(mx_, u_[2]), u_[3]); } \
            __any(mx_ == ~0u); })
#define GRP_LD(q) asm volatile("buffer_load_dwordx4 %0, %4, %5, %6 offen sc1\n\tbuffer_load_dwordx4 %1, %4, %5, %6 offen offset:2048 sc1\n\t" \
            "buffer_load_dwordx4 %2, %4, %5, %7 offen sc1\n\tbuffer_load_dwordx4 %3, %4, %5, %7 offen offset:2048 sc1" \
            : "+v"(of[4 * (q)]), "+v"(of[4 * (q) + 1]), "+v"(of[4 * (q) + 2]), "+v"(of[4 * (q) + 3]) : "v"(a.vk), "s"(a.ro), "s"(sg[q]), "s"(sg[q] + 4096) : "memory")
#define ESTEP 2
#define MSTEP 2
#define CK0 36
        f32x16 yb, yc, yd; s16x4 wh;
        unsigned mxg[4] = {0, 0, 0, 0};
        STAMP(10);
        CBAR();
#pragma unroll
        for (int n = 0; n < 4; ++n) { wn[n] = OWF(n); PIN(wn[n]); }
#define NB 0
#pragma unroll
        for (int n = 0; n < 48; ++n) {
            if (n == NB) { BAR(); SBAR(); }
            ya = MFMA(wn[n & 3], oe[n], ya); SBAR();
            if (n >= NEARLY && n < NEARLY + 12 * ESTEP && (n - NEARLY) % ESTEP == 0) FLD(((n - NEARLY) / ESTEP) & 15);
            if (n >= NMID && n < NMID + 4 * MSTEP && (n - NMID) % MSTEP == 0) FLD(((n - NMID) / MSTEP + 12) & 15);
            if (n + 4 < 48) wn[n & 3] = OWF(n + 4);
            if (n < NB) PACE();
            if (n >= 16) { const int i = ((n - 16) >> 1) & 15;
                const s16x4 h = __builtin_amdgcn_ds_read_tr16_b64_v4i16((LAS s16x4*)(wg[i >> 2] + (i & 3) * 2048 + (n & 1) * 512));
                if (n & 1) wf[i] = __builtin_bit_cast(bf16x8, __builtin_shufflevector(wh, h, 0, 1, 2, 3, 4, 5, 6, 7)); else wh = h; }
            if (n >= CK0 && n < CK0 + 12) { const u32x4 u = __builtin_bit_cast(u32x4, of[(n - CK0) & 15]); unsigned& m = mxg[((n - CK0) >> 2) & 3];
                m = __builtin_elementwise_max(__builtin_elementwise_max(m, u[0]), u[1]); m = __builtin_elementwise_max(__builtin_elementwise_max(m, u[2]), u[3]); PIN(m); }
            SBAR();
        }
        STAMP(31);
#define GRP_MM(q, Y) do { if (__builtin_expect(good & (1u << (q)), 1)) { Y = MFMA(wf[4 * (q)], of[4 * (q)], (q) ? f32x16{} : Y); \
            _Pragma("unroll") for (int i = 4 * (q) + 1; i < 4 * (q) + 4; ++i) Y = MFMA(wf[i], of[i], Y); } } while (0)
        asm volatile("" : "=v"(yb), "=v"(yc), "=v"(yd));
        unsigned good = 15;
        asm volatile("" : "+s"(good));
        if (__any(mxg[0] == ~0u)) { GRP_LD(0); good ^= 1; }
        if (__any(mxg[1] == ~0u)) { GRP_LD(1); good ^= 2; }
        if (__any(mxg[2] == ~0u)) { GRP_LD(2); good ^= 4; }
        GRP_MM(0, ya); GRP_MM(1, yb); GRP_MM(2, yc);
        if (GRP_BAD(3)) { GRP_LD(3); good ^= 8; }
        GRP_MM(3, yd);
        if (__builtin_expect(good != 15, 0)) {
            unsigned pend = good ^ 15, spins = SPIN_CAP;
#pragma nounroll
            do {
                __builtin_amdgcn_s_waitcnt(0x0F70);
                asm volatile("" : "+v"(of[0]), "+v"(of[1]), "+v"(of[2]), "+v"(of[3]), "+v"(of[4]), "+v"(of[5]), "+v"(of[6]), "+v"(of[7]),
                                  "+v"(of[8]), "+v"(of[9]), "+v"(of[10]), "+v"(of[11]), "+v"(of[12]), "+v"(of[13]), "+v"(of[14]), "+v"(of[15]) :: "memory");
                good = pend;
                if (pend & 1) { if (GRP_BAD(0)) { GRP_LD(0); good ^= 1; } }
                if (pend & 2) { if (GRP_BAD(1)) { GRP_LD(1); good ^= 2; } }
                if (pend & 4) { if (GRP_BAD(2)) { GRP_LD(2); good ^= 4; } }
                if (pend & 8) { if (GRP_BAD(3)) { GRP_LD(3); good ^= 8; } }
                pend ^= good;
                GRP_MM(0, ya); GRP_MM(1, yb); GRP_MM(2, yc); GRP_MM(3, yd);
                asm volatile("s_sub_u32 %1, %1, 1\n\ts_cselect_b32 %0, 0, %0" : "+s"(pend), "+s"(spins) :: "memory", "scc");
            } while (pend);
        }
        asm volatile("s_waitcnt vmcnt(0)\n\ts_nop 4" : "+v"(ya), "+v"(yb), "+v"(yc), "+v"(yd) :: "memory");
        STAMP(26);
        ya = ((ya + yb) + yc) + yd;
#define OUT_AUX 16
#pragma unroll
        for (int q = 0; q < 4; ++q) *(LAS u32x2*)(otw + q * 16) = (u32x2){cvtpk(ya[4 * q], ya[4 * q + 1]), cvtpk(ya[4 * q + 2], ya[4 * q + 3])};
        asm volatile("" ::: "memory");
#pragma unroll
        for (int s = 0; s < 2; ++s) __builtin_amdgcn_raw_buffer_store_b128(*(const LAS u32x4*)(otr + s * 1280), rout, vout + s * 16384, 0, OUT_AUX);
        if (bid == 0 && tid == 256) __hip_atomic_store((GAS u64*)(ws + WS_EPOCH), a.fl.tag, RLX_ST);
        STAMP(12);
    } else {
        if (e == GE) glds16((((lane >> 3) + rot) & 7) * 128 + (lane & 7) * 16, gw, lds0 + L_GW + t * 1024);
#pragma unroll
        for (int m = 0; m < 2; ++m) {
            f32x4 mk;
#pragma unroll
            for (int i = 0; i < 4; ++i) { const bool le = (i + 8 * t + 4 * hh) <= r32; mk[i] = (le != (m != 0)) ? 0.f : -__builtin_inff(); }
            *(LAS f32x4*)(lds + L_MDIAG + m * 4096 + t * 1024 + lane * 16) = mk;
        }
        if (tid < 4) *(LAS unsigned*)(lds + L_CNT + tid * 4) = 0;
        *(LAS unsigned*)(lds + L_Z + tid * 4) = 0;
        MK_ATT(0);
        MK_TAGS(a.fl);
        unsigned vwf = window_flag(a);
        PIN(a.vv0); PIN(a.vv1); PIN(vwf);
        STAMP(21);
        if (e) project<1>(lds, a.rkv, a.ro, lane, t, p, j, a.fl, RUG, SUG PROF_PASS); else project<0>(lds, a.rkv, a.ro, lane, t, p, j, a.fl, RUG, SUG PROF_PASS);
        cnt_wait(lds + L_CNT + 8);
        __builtin_amdgcn_s_sleep(BSLP);
        STAMP(24);
        tag_wait(a.fl, vwf, 0);
        STAMP(25);
        KV3 kw;
        a.gt = GATE();
        LDB_FIRST(kw);
        MK_SO();
        attn_window<0>(a, kw, so0 PROF_PASS);
        STAMP(23);
        PIN(a.idx);
        attn_full<0>(a, lds_zero16(a.zp), lds_zero16(a.zp), lds_zero16(a.zp), lds_zero16(a.zp), lds_zero16(a.zp), so0[3], RWO, VWO, WI0, 0, 0, 0, kw, giver PROF_PASS);
        BAR();
        STAMP(8);
    }
}

extern "C" void kernel_launch(void* const* d_in, const int* in_sizes, int n_in, void* d_out, int out_size, void* d_ws, size_t ws_size,
                              hipStream_t stream) {
    (void)in_sizes; (void)n_in; (void)out_size; (void)ws_size;
    (void)hipFuncSetAttribute((const void*)gmix_kernel, hipFuncAttributeMaxDynamicSharedMemorySize, L_END);
    gmix_kernel<<<256, 512, L_END, stream>>>((const char*)d_in[0], (const char*)d_in[1], (const char*)d_in[2], (const char*)d_in[4],
                                             (const char*)d_in[5], (const char*)d_in[6], (char*)d_out, (char*)d_ws);
}
```

```cpp
#include <hip/hip_runtime.h>
#include <hip/hip_bf16.h>

typedef __attribute__((ext_vector_type(8))) __bf16 bf16x8;
typedef __attribute__((ext_vector_type(16))) __bf16 bf16x16;
typedef __attribute__((ext_vector_type(16))) float f32x16;
typedef __attribute__((ext_vector_type(8))) float f32x8;
typedef __attribute__((ext_vector_type(4))) float f32x4;
typedef __attribute__((ext_vector_type(4))) unsigned u32x4;
typedef __attribute__((ext_vector_type(2))) unsigned u32x2;
typedef __attribute__((ext_vector_type(2))) __bf16 bf16x2;
typedef short s16x4 __attribute__((ext_vector_type(4)));
typedef unsigned long long u64;
#define GAS __attribute__((address_space(1)))
#define LAS __attribute__((address_space(3)))
typedef LAS const char* lds_cptr;
typedef LAS char* lds_ptr;

constexpr unsigned WS_EPOCH = 0;
constexpr unsigned FSTR = 8;
constexpr unsigned WS_OFLAG = 65536;
constexpr unsigned WS_KVFLAG = 131072;
constexpr unsigned WS_G = 524288;
constexpr unsigned WS_KV = 1u << 20;
constexpr unsigned TS = 2176;
constexpr unsigned KV_BYTES = 2u * 32u * 64u * TS;
constexpr unsigned WS_O = 10u << 20;
constexpr unsigned OS = 32896;
constexpr unsigned O_BYTES = 128u * OS;
constexpr unsigned OOB = 0x7ff00000u;
constexpr unsigned L_W = 65536;
constexpr unsigned L_WO = 0, L_PART = 65536, PREC = 7168, L_Q14 = 106496, L_Q15 = 114688, L_MDIAG = 122880, L_MFAR = 126976, L_Q = 131072,
                   L_GW = 147456, L_Z = 151552, L_CNT = 153088, L_EP = 153344, L_SCR = 153600, L_END = 154112;
constexpr float QSCALE = 0.25f * 1.4426950408889634f;
constexpr float LMAX = 1.2676506e30f, LMIN = 9.094947e-13f;
constexpr unsigned SPIN_CAP = 1u << 17;

#define SBAR() __builtin_amdgcn_sched_barrier(0)
#define PIN(x) asm volatile("" : "+v"(x))
#define MFMA(a, b, c) __builtin_amdgcn_mfma_f32_32x32x16_bf16(a, b, c, 0, 0, 0)
#define BAR() asm volatile("s_barrier" ::: "memory")
#define CBAR() asm volatile("s_waitcnt lgkmcnt(0)\n\ts_barrier" ::: "memory")
#define DRAIN() asm volatile("s_waitcnt vmcnt(0)" ::: "memory")
#define PACE() asm volatile("s_nop 6" ::: "memory")
#define RLX_AGENT __ATOMIC_RELAXED, __HIP_MEMORY_SCOPE_AGENT

__device__ __forceinline__ void glds16(unsigned voff, const void* sbase, unsigned ldsaddr) {
    asm volatile("s_mov_b32 m0, %2\n\ts_nop 0\n\tglobal_load_lds_dwordx4 %0, %1" ::"v"(voff), "s"(sbase), "s"(ldsaddr) : "memory");
}
__device__ __forceinline__ void glds16x2(unsigned v0, unsigned v1, const void* sbase, unsigned ldsaddr) {
    asm volatile("s_mov_b32 m0, %3\n\ts_nop 0\n\tglobal_load_lds_dwordx4 %0, %2\n\tglobal_load_lds_dwordx4 %1, %2 offset:1024"
                 ::"v"(v0), "v"(v1), "s"(sbase), "s"(ldsaddr) : "memory");
}
__device__ __forceinline__ void glds16x4(unsigned v0, unsigned v1, unsigned v2, unsigned v3, const void* sbase, unsigned ldsaddr) {
    asm volatile("s_mov_b32 m0, %5\n\ts_nop 0\n\tglobal_load_lds_dwordx4 %0, %4\n\tglobal_load_lds_dwordx4 %1, %4 offset:1024\n\t"
                 "global_load_lds_dwordx4 %2, %4 offset:2048\n\tglobal_load_lds_dwordx4 %3, %4 offset:3072"
                 ::"v"(v0), "v"(v1), "v"(v2), "v"(v3), "s"(sbase), "s"(ldsaddr) : "memory");
}
__device__ __forceinline__ unsigned cvtpk(float lo, float hi) {
    typedef __attribute__((ext_vector_type(2))) float f32x2;
    return __builtin_bit_cast(unsigned, __builtin_convertvector((f32x2){lo, hi}, bf16x2));
}
__device__ __forceinline__ bf16x8 as_frag(u32x4 v) { return __builtin_bit_cast(bf16x8, v); }
__device__ __forceinline__ bf16x8 lds_frag(lds_cptr p) { return *(const LAS bf16x8*)p; }
__device__ __forceinline__ bf16x8 tr_frag(lds_cptr p0, lds_cptr p1) {
    s16x4 a = __builtin_amdgcn_ds_read_tr16_b64_v4i16((LAS s16x4*)p0);
    s16x4 b = __builtin_amdgcn_ds_read_tr16_b64_v4i16((LAS s16x4*)p1);
    return __builtin_bit_cast(bf16x8, __builtin_shufflevector(a, b, 0, 1, 2, 3, 4, 5, 6, 7));
}
__device__ __forceinline__ f32x16 pv_lo(bf16x8 v, const f32x16& p, f32x16 acc, int idx) {
    f32x8 h = __builtin_shufflevector(p, p, 0, 1, 2, 3, 4, 5, 6, 7);
    return __builtin_amdgcn_smfmac_f32_32x32x32_bf16(v, __builtin_bit_cast(bf16x16, h), acc, idx, 0, 0);
}
__device__ __forceinline__ f32x16 pv_hi(bf16x8 v, const f32x16& p, f32x16 acc, int idx) {
    f32x8 h = __builtin_shufflevector(p, p, 8, 9, 10, 11, 12, 13, 14, 15);
    return __builtin_amdgcn_smfmac_f32_32x32x32_bf16(v, __builtin_bit_cast(bf16x16, h), acc, idx, 0, 0);
}
#define MX3(a, b, c) __builtin_fmaxf(__builtin_fmaxf((a), (b)), (c))
__device__ __forceinline__ float max16(const f32x16& s) {
    float a = MX3(s[0], s[1], s[2]), b = MX3(s[3], s[4], s[5]);
    a = MX3(a, s[6], s[7]); b = MX3(b, s[8], s[9]); a = MX3(a, s[10], s[11]); b = MX3(b, s[12], s[13]);
    return MX3(a, b, __builtin_fmaxf(s[14], s[15]));
}
__device__ __forceinline__ float halfmax(float m) {
    auto r = __builtin_amdgcn_permlane32_swap(__float_as_uint(m), __float_as_uint(m), false, false);
    return __builtin_fmaxf(__uint_as_float(r[0]), __uint_as_float(r[1]));
}
__device__ __forceinline__ f32x16 lds_f32x16(lds_cptr p) {
    f32x4 a = *(const LAS f32x4*)p, b = *(const LAS f32x4*)(p + 1024), c = *(const LAS f32x4*)(p + 2048), d = *(const LAS f32x4*)(p + 3072);
    f32x8 lo = __builtin_shufflevector(a, b, 0, 1, 2, 3, 4, 5, 6, 7), hi = __builtin_shufflevector(c, d, 0, 1, 2, 3, 4, 5, 6, 7);
    return __builtin_shufflevector(lo, hi, 0, 1, 2, 3, 4, 5, 6, 7, 8, 9, 10, 11, 12, 13, 14, 15);
}
constexpr int SC1 = 16;
#define BLOAD(rs, vo, so) as_frag(__builtin_amdgcn_raw_buffer_load_b128(rs, vo, so, SC1))
#define BSTORE(v, rs, vo, so) __builtin_amdgcn_raw_buffer_store_b128(v, rs, (vo) + (so), 0, SC1)

struct Tags { __amdgpu_buffer_rsrc_t rs; u64 tag; u32x2 vtag; unsigned v0; };
__device__ __forceinline__ u64 tag_load(const Tags& f, unsigned vo, unsigned so) {
    return __builtin_bit_cast(u64, __builtin_amdgcn_raw_buffer_load_b64(f.rs, vo, so, SC1));
}
__device__ __forceinline__ void tag_raise(const Tags& f, unsigned so) {
    DRAIN();
    __builtin_amdgcn_raw_buffer_store_b64(f.vtag, f.rs, f.v0, so, SC1);
}
__device__ __forceinline__ void tag_wait(const Tags& f, u64 v, unsigned vo, unsigned so) {
    if (__builtin_expect(!__all(v == f.tag), 0)) {
#pragma nounroll
        for (unsigned spins = 0; spins < SPIN_CAP; ++spins) {
            __builtin_amdgcn_s_sleep(8);
            asm volatile("" ::: "memory");
            if (__all(tag_load(f, vo, so) == f.tag)) break;
        }
    }
    asm volatile("" ::: "memory");
}
__device__ __forceinline__ void tag_wait(const Tags& f, unsigned vo, unsigned so) { tag_wait(f, tag_load(f, vo, so), vo, so); }

__device__ __forceinline__ void cnt_arrive(lds_ptr cnt, int lane) {
    asm volatile("s_waitcnt lgkmcnt(0)" ::: "memory");
    if (lane == 0) __hip_atomic_fetch_add((LAS unsigned*)cnt, 1u, __ATOMIC_RELAXED, __HIP_MEMORY_SCOPE_WORKGROUP);
}
__device__ __forceinline__ void cnt_wait(lds_ptr cnt) {
#pragma nounroll
    for (unsigned spins = 0; spins < (1u << 22); ++spins) {
        if (__builtin_amdgcn_readfirstlane(__hip_atomic_load((LAS unsigned*)cnt, __ATOMIC_RELAXED, __HIP_MEMORY_SCOPE_WORKGROUP)) == 4) break;
        __builtin_amdgcn_s_sleep(1);
    }
    asm volatile("" ::: "memory");
}
__device__ __forceinline__ unsigned kvflag(int br, int quad, int tile, int type) {
    return WS_KVFLAG + (unsigned)(((((br * 8 + quad) * 4 + (tile & 3)) * 2 + (tile >> 5)) * 2 + type) * 8 + ((tile >> 2) & 7)) * FSTR;
}

__device__ __forceinline__ void slow_rows(__amdgpu_buffer_rsrc_t rs, unsigned sbase, int lo, int hi, int g, bool band, bf16x8 q,
                                          unsigned vk, unsigned vv0, unsigned vv1, lds_cptr lds, int lane, int idx, f32x16& acc) {
    float m = -__builtin_inff();
    for (int pass = 0; pass < 2; ++pass) {
        if (pass) { m = halfmax(m); acc = f32x16{}; }
        for (int kt = lo; kt <= hi; ++kt) {
            const unsigned so = sbase + (unsigned)kt * TS;
            const bf16x8 k = BLOAD(rs, vk, so), v0 = BLOAD(rs, vv0, so), v1 = BLOAD(rs, vv1, so);
            f32x16 c = f32x16{};
            if (band && kt == g) c = lds_f32x16(lds + L_MDIAG + lane * 16);
            if (band && kt == g - 8) c = lds_f32x16(lds + L_MFAR + lane * 16);
            f32x16 s = MFMA(k, q, c);
            if (!pass) m = __builtin_fmaxf(m, max16(s));
            else {
#pragma unroll
                for (int r = 0; r < 16; ++r) s[r] = __builtin_amdgcn_exp2f(s[r] - m);
                acc = pv_lo(v0, s, acc, idx); acc = pv_hi(v1, s, acc, idx);
            }
        }
    }
}

#define PK4(A, o) ((u32x4){cvtpk(A[o], A[o + 1]), cvtpk(A[o + 2], A[o + 3]), cvtpk(A[o + 4], A[o + 5]), cvtpk(A[o + 6], A[o + 7])})
__device__ __forceinline__ void make_tag(Tags& f, u32x2 e) {
    f.tag = (((u64)(unsigned)__builtin_amdgcn_readfirstlane(e[1]) << 32) | (unsigned)__builtin_amdgcn_readfirstlane(e[0])) * 0x9E3779B97F4A7C15ull + 0x632BE59BD9B4E019ull;
    asm volatile("" : "+s"(f.tag));
    f.vtag = __builtin_bit_cast(u32x2, f.tag);
    PIN(f.vtag);
}
template <int E>
__device__ __forceinline__ u32x4 pack_kv(int i, const f32x16& a) {
    const int s = i & 1;
    if (E == 0) return PK4(a, 8 * s);
    auto x = __builtin_amdgcn_permlane32_swap(cvtpk(a[8 * s], a[8 * s + 1]), cvtpk(a[8 * s + 4], a[8 * s + 5]), false, false);
    auto y = __builtin_amdgcn_permlane32_swap(cvtpk(a[8 * s + 2], a[8 * s + 3]), cvtpk(a[8 * s + 6], a[8 * s + 7]), false, false);
    return (u32x4){x[0], y[0], x[1], y[1]};
}
#define PUT_KV(i, v, vo) BSTORE(v, rkv, vo, ((i) < 2 ? 64 * TS : 0) + ((i) & 1) * (E ? 512 : 128 * TS))
constexpr int PBX = 8;
constexpr int GE = 1;
#define XQ(p) (((p) >> 3) * 4 + (((p) + 1 + (((p) >> 2) & 1) * 2) & 3))
template <int E>
__device__ __forceinline__ void project(lds_ptr lds, __amdgpu_buffer_rsrc_t rkv, __amdgpu_buffer_rsrc_t ro, int lane, int t, int p, int j, Tags& fl) {
    const int r32 = lane & 31, hh = lane >> 5, qd4 = XQ(p);
    const unsigned q4 = (lane & 15) >> 2, wcol = ((lane >> 4) & 1) * 32 + (lane & 3) * 8;
    lds_cptr xa[4];
#pragma unroll
    for (int ks = 0; ks < 4; ++ks) xa[ks] = lds + (32 * t + r32) * 128 + (((hh ^ ((r32 >> 1) & 7)) * 16) ^ (ks * 32));
    unsigned w1o = L_W + (8 * hh + q4) * 128 + ((lane >> 4) & 1) * 64 + (q4 >> 1) * 32 + (lane & 3) * 8, w0o = w1o ^ 32;
    PIN(w0o); PIN(w1o);
    const lds_cptr w0 = lds + w0o, w1 = lds + w1o, wq = lds + (8 * hh + q4) * 64 + wcol;
#define XRD(n) lds_frag(xa[(n) & 3] + (((n) >> 2) & 3) * 16384)
#define WRD(b, n) tr_frag(b + ((n) & 15) * 2048, b + ((n) & 15) * 2048 + 512)
#define YOF(n) ((n) >= 28 ? 32768 + ((n) & 3) * 2048 : ((n) & 15) * 2048)
#define YRD(b, n) tr_frag(b + YOF(n), b + YOF(n) + 512)
#define QOF(br, n) (((n) >= 28 ? L_Q15 - L_Q14 : ((n) >= 24 ? 0 : (((n) >> 2) & 3) * 16384)) + (br) * 4096 + ((n) & 3) * 1024)
#define QRD(br, n) tr_frag(((n) >= 24 ? wqh : wq) + QOF(br, n), ((n) >= 24 ? wqh : wq) + QOF(br, n) + 256)
#define MM(W, X, A) (E ? MFMA(X, W, A) : MFMA(W, X, A))
    bf16x8 xf[32], f0[4], f1[4];
    f32x16 a0 = f32x16{}, a1 = f32x16{};
    unsigned gwo = (lane & 15) == ((lane >> 4) & 1) ? L_GW + t * 1024 + hh * 16 : L_Z;
    if (E == GE) PIN(gwo);
    f32x4 zg = f32x4{};
    bf16x8 gf[4];
#define RD1(n) do { xf[n] = XRD(n); f0[(n) & 3] = WRD(w0, n); f1[(n) & 3] = WRD(w1, n); SBAR(); } while (0)
#define GRD(n) do { if (E == GE) gf[(n) & 3] = lds_frag(lds + gwo + (n) * 32); } while (0)
#define GMM(n) do { if (E == GE) zg = __builtin_amdgcn_mfma_f32_16x16x32_bf16(gf[(n) & 3], xf[n], zg, 0, 0, 0); } while (0)
#define MM1(n) do { a0 = MM(f0[(n) & 3], xf[n], a0); a1 = MM(f1[(n) & 3], xf[n], a1); SBAR(); } while (0)
#pragma unroll
    for (int s = 0; s < 8; ++s) {
        if (s == 0) CBAR(); else BAR();
        SBAR();
        RD1(4 * s); RD1(4 * s + 1);
        if (s) { MM1(4 * s - 1); GMM(4 * s - 4); GMM(4 * s - 3); GMM(4 * s - 2); GMM(4 * s - 1); SBAR(); }
        RD1(4 * s + 2); MM1(4 * s); RD1(4 * s + 3); MM1(4 * s + 1);
        if (s == 0 && E == GE) DRAIN();
        GRD(4 * s); GRD(4 * s + 1); GRD(4 * s + 2); GRD(4 * s + 3); SBAR();
        MM1(4 * s + 2);
        SBAR();
    }
    const unsigned tile = 4 * j + t;
    unsigned vo = (unsigned)(4 * qd4 * 64 + tile) * TS + (E ? (r32 >> 4) * (128 * TS) + 1024 + hh * 256 + (r32 & 15) * 16 : lane * 16);
    unsigned wqo = L_Q14 + (8 * hh + q4) * 64 + wcol;
    PIN(vo); PIN(wqo);
    const lds_cptr wqh = lds + wqo;
    MM1(31); GMM(28); GMM(29); GMM(30); GMM(31); SBAR();
    u32x4 pk[4];
#pragma unroll
    for (int i = 0; i < 4; ++i) { pk[i] = pack_kv<E>(i, i < 2 ? a0 : a1); PIN(pk[i]); if (i < 2) PUT_KV(i, pk[i], vo); SBAR(); }
    if (E == GE) BSTORE(((u32x4){0x3f803f80u, 0x3f803f80u, 0x3f803f80u, 0x3f803f80u}), rkv,
                   (unsigned)((hh * 32 + 4 * (qd4 ^ (hh * (PBX >> 1))) + (r32 >> 3)) * 64 + tile) * TS + 2048 + (lane & 7) * 16, 0);
    if (E == GE) __builtin_amdgcn_raw_buffer_store_b64((u32x2){__float_as_uint(__builtin_amdgcn_rcpf(1.f + __expf(-zg[0]))), __float_as_uint(__builtin_amdgcn_rcpf(1.f + __expf(-zg[1])))},
                                                  fl.rs, lane < 16 ? lane * 8 : OOB, WS_G + (qd4 * 64 + tile) * 128, SC1);
    SBAR();
    BAR(); SBAR();
    bf16x8 u0 = QRD(0, 0), u1 = QRD(1, 0), y0 = YRD(w0, 0), y1 = YRD(w1, 0);
    const u32x2 tg = *(const LAS u32x2*)(lds + L_EP);
    f32x16 b0 = f32x16{}, b1 = f32x16{}, aQ0 = f32x16{}, aQ1 = f32x16{};
    SBAR();
#pragma unroll
    for (int n = 0; n < 24; ++n) {
        aQ0 = MFMA(u0, xf[n], aQ0); SBAR();
        if (n == 1) { make_tag(fl, tg); SBAR(); }
        if (n == 7) { tag_raise(fl, kvflag(0, qd4, tile, E)); SBAR(); }
        if ((n & 3) == 3) { BAR(); SBAR(); }
        u0 = QRD(0, n + 1); SBAR();
        aQ1 = MFMA(u1, xf[n], aQ1); SBAR();
        u1 = n < 23 ? QRD(1, n + 1) : QRD(0, 25); SBAR();
        b0 = MM(y0, xf[n], b0); SBAR(); y0 = n < 23 ? YRD(w0, n + 1) : QRD(0, 26); SBAR();
        b1 = MM(y1, xf[n], b1); SBAR(); y1 = n < 23 ? YRD(w1, n + 1) : QRD(0, 27); SBAR();
    }
    aQ0 = MFMA(u0, xf[24], aQ0); SBAR(); u0 = QRD(0, 28); SBAR(); aQ0 = MFMA(u1, xf[25], aQ0); SBAR(); u1 = QRD(0, 29); SBAR();
    aQ0 = MFMA(y0, xf[26], aQ0); SBAR(); y0 = QRD(0, 30); SBAR(); aQ0 = MFMA(y1, xf[27], aQ0); SBAR(); y1 = QRD(0, 31); SBAR();
    aQ0 = MFMA(u0, xf[28], aQ0); SBAR(); aQ0 = MFMA(u1, xf[29], aQ0); SBAR(); u1 = QRD(1, 24); SBAR();
    aQ0 = MFMA(y0, xf[30], aQ0); SBAR(); y0 = YRD(w0, 24); SBAR(); aQ0 = MFMA(y1, xf[31], aQ0); SBAR(); y1 = YRD(w1, 24); SBAR();
    const lds_ptr qd = lds + L_Q + t * 1024 + lane * 16;
    constexpr int QBAR = 4;
    constexpr int NFREE = 3;
    f32x8 qs; u32x4 qk;
#define QEPI(m) do { if ((m) == 1) { PIN(aQ0); qs = __builtin_shufflevector(aQ0, aQ0, 8, 9, 10, 11, 12, 13, 14, 15) * QSCALE; PIN(qs); } \
        if ((m) == 2) { qk = PK4(qs, 0); *(LAS u32x4*)(qd + 4096) = qk; } \
        if ((m) == QBAR) { __builtin_amdgcn_s_setprio(2); __builtin_amdgcn_s_waitcnt(0xC07F | ((2 * (QBAR - 2)) << 8)); BAR(); } \
        if ((m) > QBAR + NFREE) PACE(); SBAR(); } while (0)
#pragma unroll
    for (int n = 24; n < 32; ++n) {
        aQ1 = MFMA(u1, xf[n], aQ1); SBAR(); if (n < 31) u1 = QRD(1, n + 1); SBAR(); QEPI(3 * (n - 24));
        b0 = MM(y0, xf[n], b0); SBAR(); if (n < 31) y0 = YRD(w0, n + 1); SBAR(); QEPI(3 * (n - 24) + 1);
        b1 = MM(y1, xf[n], b1); SBAR(); if (n < 31) y1 = YRD(w1, n + 1); SBAR(); QEPI(3 * (n - 24) + 2);
    }
#pragma unroll
    for (int r = 0; r < 8; ++r) aQ0[r] *= QSCALE;
#pragma unroll
    for (int r = 0; r < 16; ++r) aQ1[r] *= QSCALE;
    *(LAS u32x4*)qd = PK4(aQ0, 0); *(LAS u32x4*)(qd + 8192) = PK4(aQ1, 0); *(LAS u32x4*)(qd + 12288) = PK4(aQ1, 8);
    cnt_arrive(lds + L_CNT + 8, lane);
    const unsigned vo2 = vo + (32 + 4 * ((qd4 ^ (PBX >> 1)) - qd4)) * 64 * TS;
    u32x4 py[4] = {pack_kv<E>(0, b0), pack_kv<E>(1, b0), pack_kv<E>(2, b1), pack_kv<E>(3, b1)};
    PIN(py[0]); PIN(py[1]); PIN(py[2]); PIN(py[3]);
    constexpr int YSLP = 10, YSTAG = 2;
    if (t & 1) __builtin_amdgcn_s_sleep(YSTAG);
    if (t & 2) __builtin_amdgcn_s_sleep(2 * YSTAG);
    PUT_KV(2, pk[2], vo); __builtin_amdgcn_s_sleep(YSLP); PUT_KV(3, pk[3], vo); __builtin_amdgcn_s_sleep(YSLP);
    PUT_KV(0, py[0], vo2); __builtin_amdgcn_s_sleep(YSLP); PUT_KV(1, py[1], vo2); __builtin_amdgcn_s_sleep(YSLP);
    PUT_KV(2, py[2], vo2); __builtin_amdgcn_s_sleep(YSLP); PUT_KV(3, py[3], vo2);
    tag_raise(fl, kvflag(1, qd4, tile, E));
    BSTORE(((u32x4){~0u, ~0u, ~0u, ~0u}), ro, lane * 16, tile * OS + 2 * p * 1024);
}

constexpr int CODE_LINES = 296;

struct Att {
    lds_ptr lds; char* ws; __amdgpu_buffer_rsrc_t rkv, ro, rka;
    Tags fl; int lane, t, p, g, head, idx; float gt; unsigned vk, vv0, vv1; lds_cptr zp, qp[4];
};
#define IDX a.idx
#define LD3(K, V0, V1, rs, so) do { K = BLOAD(rs, a.vk, so); V0 = BLOAD(rs, a.vv0, so); V1 = BLOAD(rs, a.vv1, so); } while (0)
#define EX(X, r) X[r] = __builtin_amdgcn_exp2f(X[r])
#define STEP(X, Y, OP, VP, KN, QN, CN, O, V0, F1, F3, F2) do { \
        EX(X, 0); EX(X, 1); SBAR(); OP = pv_hi(VP, Y, OP, IDX); SBAR(); F1; SBAR(); EX(X, 2); EX(X, 3); EX(X, 4); SBAR(); Y = MFMA(KN, QN, CN); SBAR(); F3; SBAR(); \
        EX(X, 5); EX(X, 6); EX(X, 7); SBAR(); EX(X, 8); EX(X, 9); SBAR(); O = pv_lo(V0, X, O, IDX); SBAR(); F2; SBAR(); \
        EX(X, 10); EX(X, 11); EX(X, 12); EX(X, 13); EX(X, 14); EX(X, 15); SBAR(); } while (0)

__device__ __forceinline__ f32x16 lds_zero16(lds_cptr zp) {
    const volatile LAS f32x4* z = (const volatile LAS f32x4*)zp;
    const f32x4 a = *z, b = *z, c = *z, d = *z;
    f32x8 lo = __builtin_shufflevector(a, b, 0, 1, 2, 3, 4, 5, 6, 7), hi = __builtin_shufflevector(c, d, 0, 1, 2, 3, 4, 5, 6, 7);
    return __builtin_shufflevector(lo, hi, 0, 1, 2, 3, 4, 5, 6, 7, 8, 9, 10, 11, 12, 13, 14, 15);
}

template <int C, int BR>
__device__ __forceinline__ void finish(const Att& a, f32x16 o, float gf, bf16x8 q, unsigned vo) {
    f32x8 y = __builtin_shufflevector(o, o, 0, 1, 2, 3, 4, 5, 6, 7) * (__builtin_amdgcn_rcpf(o[8]) * gf);
    PIN(y);
    const float chk = __builtin_fmaf(((y[0] + y[2]) + (y[4] + y[6])) + ((y[1] + y[3]) + (y[5] + y[7])), 0.f, o[8]);
    if (__builtin_expect(__any(__builtin_amdgcn_fmed3f(chk, LMIN, LMAX) != chk), 0)) {
        if (!BR) { tag_wait(a.fl, a.lane * FSTR, kvflag(!C, a.p >> 1, 0, 0)); tag_wait(a.fl, a.lane * FSTR, kvflag(!C, a.p >> 1, 2, 0)); }
        slow_rows(a.rkv, (unsigned)((BR * 32 + (a.head ^ (BR * 2 * PBX))) * 64) * TS, BR ? (a.g >= 8 ? a.g - 8 : 0) : 0, BR ? a.g : 63, a.g, BR, q, a.vk, a.vv0, a.vv1, a.lds, a.lane, IDX, o);
        y = __builtin_shufflevector(o, o, 0, 1, 2, 3, 4, 5, 6, 7) * (__builtin_amdgcn_rcpf(o[8]) * gf);
        PIN(y);
    }
    u32x4 v = (u32x4){cvtpk(y[0], y[1]), cvtpk(y[2], y[3]), cvtpk(y[4], y[5]), cvtpk(y[6], y[7])};
    if (!C && !BR) v = __builtin_elementwise_min(v, (u32x4){~1u, ~1u, ~1u, ~1u});
    BSTORE(v, a.ro, vo, 0);
}
#define OFLAG(BR, C) (WS_OFLAG + ((((BR) * 64 + a.g) * 2 + (C)) * 16 + (a.p ^ ((BR) * PBX))) * 8)
#define OVOFF(BR) (a.vk + (unsigned)((BR) * 64 + a.g) * OS + (a.head ^ ((BR) * 2 * PBX)) * 1024)
#define GATE() __uint_as_float(__builtin_amdgcn_raw_buffer_load_b32(a.fl.rs, (a.lane & 15) * 8 + (a.lane & 16) / 4, WS_G + ((a.p >> 1) * 64 + a.g) * 128, SC1))

struct KV3 { bf16x8 k[3], v0[3], v1[3]; };
#define LDB(K, V0, V1, i) do { const int kt_ = a.g - (i); \
        const __amdgpu_buffer_rsrc_t rb_ = __builtin_amdgcn_make_buffer_rsrc(a.ws + WS_KV, 0, kt_ >= 0 ? KV_BYTES : 0u, 0x00020000); \
        LD3(K, V0, V1, rb_, (unsigned)((32 + (a.head ^ (2 * PBX))) * 64 + (kt_ >= 0 ? kt_ : 0)) * TS); } while (0)
#define LDB_FIRST(f) do { LDB(f.k[0], f.v0[0], f.v1[0], 0); SBAR(); LDB(f.k[1], f.v0[1], f.v1[1], 1); SBAR(); LDB(f.k[2], f.v0[2], f.v1[2], 2); SBAR(); } while (0)
#define LDA_FIRST(f) do { LD3(f.k[0], f.v0[0], f.v1[0], a.rka, so0[0]); SBAR(); LD3(f.k[1], f.v0[1], f.v1[1], a.rka, so0[1]); SBAR(); \
        LD3(f.k[2], f.v0[2], f.v1[2], a.rka, so0[2]); SBAR(); } while (0)
__device__ __forceinline__ unsigned window_flag(const Att& a) {
    const int i = a.lane & 15, kt = a.g - (i < 9 ? i : 0);
    return kvflag(1, a.p >> 1, kt >= 0 ? kt : 0, (a.lane >> 4) & 1);
}

template <int C>
__device__ __forceinline__ void attn_full(Att& a, f32x16 o0, f32x16 o1, f32x16 o2, f32x16 o3, f32x16 S1, unsigned so,
                                          __amdgpu_buffer_rsrc_t rwo, unsigned vwo, unsigned wi, unsigned v16, unsigned fs2, unsigned vf3, KV3& nxt) {
    const lds_ptr lds = a.lds; const int t = a.t, lane = a.lane;
    const f32x16 zc = f32x16{};
    bf16x8 ka = nxt.k[0], va0 = nxt.v0[0], va1 = nxt.v1[0], kb = nxt.k[1], vb0 = nxt.v0[1], vb1 = nxt.v1[1], kc = nxt.k[2], vc0 = nxt.v0[2], vc1 = nxt.v1[2], kd, vd0, vd1;
    const unsigned jb = a.g >> 2;
    unsigned ki = 3;
    vd1 = lds_frag(a.zp);
    lds_ptr pw = lds + L_PART + (4 * C + t) * PREC + lane * 16;
    lds_cptr pr0 = lds + L_PART + (4 * C + ((t - 1) & 3)) * PREC + lane * 16, pr1 = lds + L_PART + (4 * C + ((t - 2) & 3)) * PREC + lane * 16,
             pr2 = lds + L_PART + (4 * C + ((t - 3) & 3)) * PREC + lane * 16;
    unsigned ovo = OVOFF(0);
    if (C) { BAR(); }
    else { PIN(pw); PIN(pr0); PIN(pr1); PIN(pr2); PIN(ovo); PIN(a.gt);
           tag_raise(a.fl, OFLAG(1, 0)); }
    bf16x8 q0 = lds_frag(a.qp[0]); SBAR();
    bf16x8 q1 = lds_frag(a.qp[1]), q2 = lds_frag(a.qp[2]), q3 = lds_frag(a.qp[3]);
    SBAR();
    f32x16 S0 = MFMA(ka, q0, zc);
    PIN(S0); SBAR();
    PIN(q1); PIN(q2); PIN(q3); PIN(vd1); PIN(o0); PIN(o1); PIN(o2); PIN(o3); PIN(S1);
    __builtin_amdgcn_s_waitcnt(0x0F76);
#pragma unroll
    for (int i = 0; i < 8; ++i) __builtin_amdgcn_s_setprio(C);
    SBAR();
    unsigned wso = 0;
    u64 fv = __builtin_bit_cast(u64, a.fl.vtag);
#define NEXT1() asm volatile("s_add_i32 %0, %0, 1\n\ts_xor_b32 %1, %0, %2" : "+s"(ki), "=s"(so) : "s"(jb) : "scc")
#define NEXT2() asm volatile("s_mulk_i32 %0, %1" : "+s"(so) : "i"(4 * TS) : "scc")
#define WO_OFF() do { if (C) asm volatile("s_lshl_b32 %0, %1, 4" : "=s"(wso) : "s"(wi) : "scc"); } while (0)
#define WO_DMA() do { if (C) __builtin_amdgcn_raw_ptr_buffer_load_lds(rwo, (LAS void*)(lds + L_WO + t * 16384 + wi), 16, vwo, wso, 0, 0); } while (0)
#define WO_NEXT() do { if (C) asm volatile("s_addk_i32 %0, 0x400\n\ts_and_b32 %0, %0, 0x3fff" : "+s"(wi) :: "scc"); } while (0)
#define TILE(K, V0, V1, KN, LK, LV0, LV1, NW, G) do { \
        STEP(S0, S1, o3, LV1, K, q1, zc, o0, V0, LK = BLOAD(a.rka, a.vk, so), WO_OFF(), LV0 = BLOAD(a.rka, a.vv0, so)); \
        STEP(S1, S0, o0, V1, K, q2, zc, o1, V0, LV1 = BLOAD(a.rka, a.vv1, so), , WO_DMA()); \
        STEP(S0, S1, o1, V1, K, q3, zc, o2, V0, NEXT1(), NEXT2(), ); \
        STEP(S1, S0, o2, V1, KN, q0, zc, o3, V0, __builtin_amdgcn_s_waitcnt(0x0F70 | (NW)), G, WO_NEXT()); } while (0)
#define PUTA(O, sl) (*(LAS f32x4*)(pw + (sl) * 2048) = __builtin_shufflevector(O, O, 0, 1, 2, 3))
#define PUTB(O, sl) (*(LAS f32x4*)(pw + (sl) * 2048 + 1024) = __builtin_shufflevector(O, O, 4, 5, 6, 7))
#define PUTL(O, sl) (*(LAS float*)(pw + 6144 + (sl) * 4) = O[8])
    f32x4 x0, y0, x1, y1, x2, y2; float l0, l1, l2;
#define GETP() do { x0 = *(const LAS f32x4*)pr0; x1 = *(const LAS f32x4*)(pr1 + 2048); x2 = *(const LAS f32x4*)(pr2 + 4096); \
        y0 = *(const LAS f32x4*)(pr0 + 1024); y1 = *(const LAS f32x4*)(pr1 + 3072); y2 = *(const LAS f32x4*)(pr2 + 5120); \
        l0 = *(const LAS float*)(pr0 + 6144); l1 = *(const LAS float*)(pr1 + 6148); l2 = *(const LAS float*)(pr2 + 6152); } while (0)
    if (C) {
#pragma nounroll
        do {
            TILE(ka, va0, va1, kb, kd, vd0, vd1, 7, );
            PIN(S0); PIN(S1); PIN(o3); tag_wait(a.fl, fv, v16, fs2);
            TILE(kb, vb0, vb1, kc, ka, va0, va1, 8, );
            TILE(kc, vc0, vc1, kd, kb, vb0, vb1, 9, fv = tag_load(a.fl, v16, fs2));
            TILE(kd, vd0, vd1, ka, kc, vc0, vc1, 10, );
        } while (ki != 19);
        o3 = pv_hi(vd1, S1, o3, IDX);
        const u64 f3 = tag_load(a.fl, vf3, 0);
        a.gt = GATE();
        cnt_wait(lds + L_CNT + 8);
        PUTA(o0, 0); PUTB(o0, 0); PUTL(o0, 0); PUTA(o1, 1); PUTB(o1, 1); PUTL(o1, 1); PUTA(o2, 2); PUTB(o2, 2); PUTL(o2, 2);
        DRAIN(); cnt_arrive(lds + L_CNT, lane);
        tag_wait(a.fl, f3, vf3, 0);
        LDB_FIRST(nxt);
        cnt_wait(lds + L_CNT);
        GETP();
    } else {
        SBAR();
        TILE(ka, va0, va1, kb, kd, vd0, vd1, 6, );
        TILE(kb, vb0, vb1, kc, ka, va0, va1, 6, );
#pragma nounroll
        do {
            TILE(kc, vc0, vc1, kd, kb, vb0, vb1, 6, );
            TILE(kd, vd0, vd1, ka, kc, vc0, vc1, 6, );
            TILE(ka, va0, va1, kb, kd, vd0, vd1, 6, );
            TILE(kb, vb0, vb1, kc, ka, va0, va1, 6, );
        } while (ki != 17);
        STEP(S0, S1, o3, vb1, kc, q1, zc, o0, vc0, , , );
        STEP(S1, S0, o0, vc1, kc, q2, zc, o1, vc0, , , );
        STEP(S0, S1, o1, vc1, kd, q0, zc, o2, vc0, __builtin_amdgcn_s_waitcnt(0x0F73), , );
        STEP(S1, S0, o2, vc1, kd, q1, zc, o0, vd0, , , );
        STEP(S0, S1, o0, vd1, kd, q2, zc, o1, vd0, , , );
        STEP(S1, S0, o1, vd1, kc, q3, zc, o2, vd0, PUTA(o0, 0), PUTL(o0, 0), PUTB(o0, 0));
        STEP(S0, S1, o2, vd1, kd, q3, zc, o3, vc0, PUTA(o1, 1), PUTL(o1, 1), PUTB(o1, 1));
        PUTA(o2, 2); PUTB(o2, 2); asm volatile("" :: "v"(o0), "v"(o1), "v"(o2)); SBAR();
        EX(S1, 0); EX(S1, 1); SBAR(); o3 = pv_hi(vc1, S0, o3, IDX); SBAR(); PUTL(o2, 2); __builtin_amdgcn_s_setprio(2); SBAR();
        EX(S1, 2); EX(S1, 3); EX(S1, 4); EX(S1, 5); EX(S1, 6); EX(S1, 7); SBAR(); EX(S1, 8); EX(S1, 9); SBAR();
        o3 = pv_lo(vd0, S1, o3, IDX); PIN(S1); SBAR();
        CBAR(); GETP(); SBAR();
        EX(S1, 10); EX(S1, 11); EX(S1, 12); EX(S1, 13); EX(S1, 14); EX(S1, 15); SBAR();
        o3 = pv_hi(vd1, S1, o3, IDX); SBAR();
    }
    x0 = (x0 + x1) + x2; y0 = (y0 + y1) + y2; l0 = (l0 + l1) + l2;
    PIN(x0); PIN(y0); PIN(l0); PIN(o3);
#pragma unroll
    for (int r = 0; r < 4; ++r) { o3[r] += x0[r]; o3[4 + r] += y0[r]; }
    o3[8] += l0;
    finish<C, 0>(a, o3, a.gt, q3, ovo);
}

template <int C>
__device__ __forceinline__ void attn_window(const Att& a, KV3& f, const unsigned (&so0)[4]) {
    const lds_ptr lds = a.lds; const int t = a.t, lane = a.lane;
    const f32x16 zc = f32x16{};
    bf16x8 k[9], v0[9], v1[9];
#pragma unroll
    for (int i = 0; i < 3; ++i) { k[i] = f.k[i]; v0[i] = f.v0[i]; v1[i] = f.v1[i]; }
#define LDW(i) LDB(k[i], v0[i], v1[i], i)
    const bf16x8 qa = lds_frag(lds + L_Q + 8192 + C * 4096 + t * 1024 + lane * 16);
    const f32x16 cd = lds_f32x16(lds + L_MDIAG + lane * 16), cf = lds_f32x16(lds + L_MFAR + lane * 16);
    f32x16 oo = lds_zero16(a.zp);
    f32x16 S0 = MFMA(k[0], qa, cd), S1 = MFMA(k[1], qa, zc);
    SBAR(); LDW(3); SBAR();
#pragma unroll
    for (int r = 0; r < 10; ++r) { EX(S0, r); SBAR(); }
    oo = pv_lo(v0[0], S0, oo, IDX); SBAR();
#pragma unroll
    for (int r = 10; r < 16; ++r) EX(S0, r);
    SBAR();
    STEP(S1, S0, oo, v1[0], k[2], qa, zc, oo, v0[1], LDW(4), , );
    STEP(S0, S1, oo, v1[1], k[3], qa, zc, oo, v0[2], LDW(5), , );
    STEP(S1, S0, oo, v1[2], k[4], qa, zc, oo, v0[3], LDW(6), , );
    STEP(S0, S1, oo, v1[3], k[5], qa, zc, oo, v0[4], LDW(7), , );
    STEP(S1, S0, oo, v1[4], k[6], qa, zc, oo, v0[5], LDW(8), , );
    const unsigned fa = kvflag(1, a.p >> 1, t, 0), va = (lane & 31) * FSTR;
    u64 fv = 0;
    STEP(S0, S1, oo, v1[5], k[7], qa, zc, oo, v0[6], if (!C) fv = tag_load(a.fl, va, fa), , );
    STEP(S1, S0, oo, v1[6], k[8], qa, cf, oo, v0[7], , , );
    if (C) { PIN(S0); PIN(S1); PIN(oo); tag_raise(a.fl, OFLAG(0, C)); }
    STEP(S0, S1, oo, v1[7], k[8], qa, cf, oo, v0[8], , , );
    oo = pv_hi(v1[8], S0, oo, IDX);
    if (!C) { PIN(oo); tag_wait(a.fl, fv, va, fa); LDA_FIRST(f); }
    finish<C, 1>(a, oo, 1.f - a.gt, qa, OVOFF(1));
}

__global__ void __launch_bounds__(512, 2)
gmix_kernel(const char* x, const char* wA, const char* woA, const char* wB, const char* woB, const char* gw, char* out, char* ws) {
    extern __shared__ __attribute__((aligned(16))) char smem[];
    const lds_ptr lds = (lds_ptr)smem;
    const unsigned lds0 = (unsigned)(uintptr_t)lds;
    const int tid = threadIdx.x, lane = tid & 63, r32 = lane & 31, hh = lane >> 5;
    const int w = __builtin_amdgcn_readfirstlane(tid >> 6);
    const int bid = blockIdx.x, xcd = bid & 7, rr = bid >> 3;
    const int p = 4 * (xcd & 3) + (rr & 3), j = 8 * (xcd >> 2) + (rr >> 2);
    const int jo = 2 * xcd + (rr >> 4), po = rr & 15;
    const int t = w & 3, c = w >> 2;
    const int e = (xcd & 1) ^ GE;
    {
        u64 pc; const unsigned ln = (((bid & ~7) | (tid >> 6)) + (bid < CODE_LINES - 256 ? (tid & 32) * 8 : 0)) * 128;
        asm volatile("s_getpc_b64 %0\n\ts_mov_b32 m0, %2\n\ts_nop 0\n\tglobal_load_lds_dword %1, %0" : "=&s"(pc) : "v"(ln), "s"(lds0 + L_SCR) : "memory");
        SBAR();
    }
    unsigned rot = (j + 4 * p) & 7;
    unsigned vx0 = (128 * j + 32 * t + (lane >> 3)) * 1024 + (((lane & 7) ^ (lane >> 4)) * 16), vx1 = (vx0 ^ 64) + 7168, vx2 = vx0 + 14336, vx3 = vx1 + 14336;
    unsigned vw0 = (16 * t + (lane >> 3)) * 3072 + 1024 + 1024 * e + 128 * XQ(p) + (((lane & 7) ^ (((lane >> 4) & 1) << 1)) * 16), vw1 = vw0 + 8 * 3072 - 1024;
    unsigned mx = lds0 + t * 4096, mw = lds0 + L_W + t * 2048, ow = rot * (64 * 3072);
    asm volatile("" : "+v"(vx0), "+v"(vx1), "+v"(vx2), "+v"(vx3), "+v"(vw0), "+v"(vw1));
    asm volatile("" : "+s"(mx), "+s"(mw), "+s"(rot), "+s"(ow));
    SBAR();
    asm volatile("" : "+s"(x), "+s"(wA), "+s"(woA), "+s"(wB), "+s"(woB), "+s"(gw), "+s"(out), "+s"(ws));
#define MK_ATT(C) Att a; a.lds = lds; a.ws = ws; a.lane = lane; a.t = t; a.p = p; a.g = 4 * j + t; a.head = 2 * p + C; a.idx = 0xdddddddd; \
    a.rkv = __builtin_amdgcn_make_buffer_rsrc(ws + WS_KV, 0, KV_BYTES, 0x00020000); a.ro = __builtin_amdgcn_make_buffer_rsrc(ws + WS_O, 0, O_BYTES, 0x00020000); \
    a.vk = lane * 16; a.vv0 = r32 < 16 ? 1024 + hh * 256 + r32 * 16 : ((r32 & ~4) == 16 ? 2048 : OOB); a.vv1 = r32 < 16 ? a.vv0 + 512 : a.vv0; a.zp = lds + L_Z + lane * 16; \
    a.rka = __builtin_amdgcn_make_buffer_rsrc(ws + WS_KV + (unsigned)(a.head * 64 + t) * TS, 0, 61 * TS, 0x00020000); \
    _Pragma("unroll") for (int i = 0; i < 4; ++i) a.qp[i] = lds + L_Q + C * 4096 + ((t + 1 + i) & 3) * 1024 + lane * 16
#define MK_TAGS(f) do { f.rs = __builtin_amdgcn_make_buffer_rsrc(ws, 0, WS_KV, 0x00020000); f.v0 = lane ? OOB : 0; PIN(f.v0); } while (0)
#define MK_SO() unsigned so0[4]; _Pragma("unroll") for (int i = 0; i < 4; ++i) so0[i] = (j ^ i) * (4 * TS)
    if (__builtin_expect(c, 1)) {
#define DMA1(s) do { const unsigned ks_ = ((s) + rot) & 7; \
        glds16x4(vx0, vx1, vx2, vx3, x + ks_ * 128, mx + ((s) & 3) * 16384); glds16x2(vw0, vw1, wA + ks_ * (64 * 3072), mw + ((s) & 3) * 8192); } while (0)
#define DMA2(s) do { const unsigned ks_ = (((s) + rot) & 7) * (64 * 3072); \
        glds16x2(vq0, vq1, wq + ks_, mq + ((s) == 14 ? L_Q14 : ((s) & 3) * 16384)); glds16x2(vw0, vw1, wy + ks_, mw + ((s) & 3) * 8192); SBAR(); } while (0)
#define LBAR(N) asm volatile("s_waitcnt vmcnt(" #N ")\n\ts_barrier" ::: "memory")
        glds16x4(vx0, vx1, vx2, vx3, x + rot * 128, mx); glds16x2(vw0, vw1, wA + ow, mw);
        SBAR();
        const unsigned k1 = (1 + rot) & 7;
        glds16x2(vx0, vx1, x + k1 * 128, mx + 16384);
        SBAR();
        if (t == 0)
            asm volatile("s_mov_b32 m0, %2\n\ts_nop 0\n\tglobal_load_lds_dword %0, %1 sc1" :: "v"((lane & 1) * 4), "s"(ws + WS_EPOCH), "s"(lds0 + L_EP) : "memory");
        unsigned vq0 = (32 * (t & 1) + (lane >> 2)) * 3072 + 64 * (p ^ ((t >> 1) * PBX)) + (lane & 3) * 16, vq1 = vq0 + 16 * 3072 - 1024;
        unsigned vwo = ((t & 1) * 256 + (lane >> 2)) * 1024 + 64 * po + (lane & 3) * 16;
        MK_ATT(1);
        MK_TAGS(a.fl);
        int go = 4 * jo + t;
        asm volatile("" : "+s"(go));
        unsigned v16 = (lane & 15) * 8, vf3 = hh ? WS_OFLAG + (64 + go) * 256 + v16 : window_flag(a);
        PIN(vq0); PIN(vq1); PIN(vwo); PIN(a.vk); PIN(a.vv0); PIN(a.vv1); PIN(a.qp[0]); PIN(a.qp[1]); PIN(a.qp[2]); PIN(a.qp[3]); PIN(a.zp); PIN(v16); PIN(vf3); PIN(a.idx);
        SBAR();
        glds16x2(vx0, vx1, x + k1 * 128 + 16384, mx + 16384 + 2048); glds16x2(vw0, vw1, wA + k1 * (64 * 3072), mw + 8192);
        SBAR();
        lds_cptr wo = lds + L_WO + (4 * hh + ((lane & 15) >> 2)) * 64 + ((lane >> 4) & 1) * 32 + (lane & 3) * 8, wg[4];
        unsigned sg[4];
#pragma unroll
        for (int q = 0; q < 4; ++q) { const int gq = ((p >> 2) + 1 + q) & 3; sg[q] = (unsigned)go * OS + gq * 8192; wg[q] = wo + gq * 8192; PIN(wg[q]); asm volatile("" : "+s"(sg[q])); }
        unsigned vout = (32 * go + (lane >> 2)) * 1024 + 64 * po + (lane & 3) * 16;
        lds_ptr otw = lds + L_PART + (4 + t) * PREC + r32 * 80 + hh * 8;
        lds_cptr otr = lds + L_PART + (4 + t) * PREC + (lane >> 2) * 80 + (lane & 3) * 16;
        PIN(wo); PIN(vout); PIN(otw); PIN(otr);
        SBAR();
#pragma nounroll
        for (int s = 2; s < 8; ++s) { LBAR(6); DMA1(s); }
        const unsigned mq = lds0 + t * 2048;
        const char* wq = (t >> 1) ? wB : wA;
        const char* wy = wB + 128 * ((XQ(p) ^ (PBX >> 1)) - XQ(p));
        LBAR(6); DMA2(8);
        LBAR(4); DMA2(9);
        make_tag(a.fl, *(const LAS u32x2*)(lds + L_EP));
        LBAR(4); DMA2(10);
        const unsigned k7 = ((7 + rot) & 7) * (64 * 3072), sx = mq + L_Q15, sw = mw + 32768;
        LBAR(4); glds16(vq0, wq + k7, sx); DMA2(11);
        f32x16 oo = lds_zero16(a.zp), o1 = lds_zero16(a.zp); PIN(oo); PIN(o1);
        LBAR(5); glds16(vw0, wy + k7, sw); DMA2(12);
        f32x16 o2 = lds_zero16(a.zp), o3 = lds_zero16(a.zp); PIN(o2); PIN(o3);
        LBAR(5); glds16(vq1, wq + k7 + 1024, sx + 1024); DMA2(13);
        f32x16 S1 = lds_zero16(a.zp); PIN(S1);
        LBAR(5); glds16(vw1, wy + k7 + 1024, sw + 1024); DMA2(14);
        __amdgpu_buffer_rsrc_t rwo = __builtin_amdgcn_make_buffer_rsrc((void*)((t >> 1) ? woB : woA), 0, 512 * 1024, 0x00020000);
        unsigned wi0 = 4096 * (((rr >> 4) << 1) | (po & 1)) + 1024 * (xcd & 3);
        MK_SO();
        asm volatile("" : "+s"(a.rka), "+s"(rwo), "+s"(wi0), "+s"(so0[0]), "+s"(so0[1]), "+s"(so0[2]), "+s"(so0[3]));
        unsigned fs1 = kvflag(0, p >> 1, (j >> 3) * 32 + t, 0), fs2 = kvflag(0, p >> 1, ((j >> 3) ^ 1) * 32 + t, 0);
        asm volatile("" : "+s"(a.fl.rs), "+s"(fs1), "+s"(fs2));
        __builtin_amdgcn_s_setprio(1);
        LBAR(5);
        const u64 fv = tag_load(a.fl, v16, fs1);
        LBAR(1);
        tag_wait(a.fl, fv, v16, fs1);
        KV3 kw;
        LDA_FIRST(kw);
        attn_full<1>(a, oo, o1, o2, o3, S1, so0[3], rwo, vwo, wi0, v16, fs2, vf3, kw);
        attn_window<1>(a, kw, so0);
#define OBR(n) ((n) >> 4 == 1 ? 0 : 1)
#define OHD(n) (2 * ((n) & 15) + ((n) >= 16))
#define OLD(n) BLOAD(a.ro, a.vk, (unsigned)(OBR(n) * 64 + go) * OS + OHD(n) * 1024)
#define OWF(n) tr_frag(wo + OBR(n) * 32768 + OHD(n) * 1024, wo + OBR(n) * 32768 + OHD(n) * 1024 + 512)
        constexpr int OIN = 6;
        bf16x8 oe[48];
#pragma unroll
        for (int n = 0; n < 48; ++n) {
            if (n == 16) { const u64 v = tag_load(a.fl, v16, WS_OFLAG + go * 256 + 128); tag_raise(a.fl, OFLAG(1, 1)); tag_wait(a.fl, v, v16, WS_OFLAG + go * 256 + 128); }
            if (n == 32) tag_wait(a.fl, v16, WS_OFLAG + (64 + go) * 256 + 128);
            oe[n] = OLD(n); SBAR();
            if (n >= OIN - 1) __builtin_amdgcn_s_waitcnt(0x0F70 | (OIN - 1));
            SBAR();
        }
        bf16x8 wn[4];
        __builtin_amdgcn_s_waitcnt(0x0F70);
        constexpr int NEARLY = 10;
        constexpr int NMID = 34;
        __amdgpu_buffer_rsrc_t rout = __builtin_amdgcn_make_buffer_rsrc(out, 0, 2048 * 1024, 0x00020000);
        asm volatile("" : "+s"(rout));
        f32x16 ya = f32x16{};
        bf16x8 wf[16], of[16];
#define FLD(i) of[i] = BLOAD(a.ro, a.vk, sg[(i) >> 2] + ((i) & 3) * 2048)
#define GRP_BAD(q) ({ unsigned mx_ = 0; _Pragma("unroll") for (int i = 4 * (q); i < 4 * (q) + 4; ++i) { const u32x4 u_ = __builtin_bit_cast(u32x4, of[i]); \
            mx_ = __builtin_elementwise_max(__builtin_elementwise_max(mx_, u_[0]), u_[1]); mx_ = __builtin_elementwise_max(__builtin_elementwise_max(mx_, u_[2]), u_[3]); } \
            __any(mx_ == ~0u); })
#define GRP_LD(q) asm volatile("buffer_load_dwordx4 %0, %4, %5, %6 offen sc1\n\tbuffer_load_dwordx4 %1, %4, %5, %6 offen offset:2048 sc1\n\t" \
            "buffer_load_dwordx4 %2, %4, %5, %7 offen sc1\n\tbuffer_load_dwordx4 %3, %4, %5, %7 offen offset:2048 sc1" \
            : "+v"(of[4 * (q)]), "+v"(of[4 * (q) + 1]), "+v"(of[4 * (q) + 2]), "+v"(of[4 * (q) + 3]) : "v"(a.vk), "s"(a.ro), "s"(sg[q]), "s"(sg[q] + 4096) : "memory")
        constexpr int ESTEP = 2, MSTEP = 2, CK0 = 36;
        f32x16 yb, yc, yd; s16x4 wh;
        unsigned mxg[4] = {0, 0, 0, 0};
        BAR();
#pragma unroll
        for (int n = 0; n < 4; ++n) { wn[n] = OWF(n); PIN(wn[n]); }
#pragma unroll
        for (int n = 0; n < 48; ++n) {
            if (n == 0) { BAR(); SBAR(); }
            ya = MFMA(wn[n & 3], oe[n], ya); SBAR();
            if (n >= NEARLY && n < NEARLY + 12 * ESTEP && (n - NEARLY) % ESTEP == 0) FLD(((n - NEARLY) / ESTEP) & 15);
            if (n >= NMID && n < NMID + 4 * MSTEP && (n - NMID) % MSTEP == 0) FLD(((n - NMID) / MSTEP + 12) & 15);
            if (n + 4 < 48) wn[n & 3] = OWF(n + 4);
            if (n >= 16) { const int i = ((n - 16) >> 1) & 15;
                const s16x4 h = __builtin_amdgcn_ds_read_tr16_b64_v4i16((LAS s16x4*)(wg[i >> 2] + (i & 3) * 2048 + (n & 1) * 512));
                if (n & 1) wf[i] = __builtin_bit_cast(bf16x8, __builtin_shufflevector(wh, h, 0, 1, 2, 3, 4, 5, 6, 7)); else wh = h; }
            if (n >= CK0 && n < CK0 + 12) { const u32x4 u = __builtin_bit_cast(u32x4, of[(n - CK0) & 15]); unsigned& m = mxg[((n - CK0) >> 2) & 3];
                m = __builtin_elementwise_max(__builtin_elementwise_max(m, u[0]), u[1]); m = __builtin_elementwise_max(__builtin_elementwise_max(m, u[2]), u[3]); PIN(m); }
            SBAR();
        }
#define GRP_MM(q, Y) do { if (__builtin_expect(good & (1u << (q)), 1)) { Y = MFMA(wf[4 * (q)], of[4 * (q)], (q) ? f32x16{} : Y); \
            _Pragma("unroll") for (int i = 4 * (q) + 1; i < 4 * (q) + 4; ++i) Y = MFMA(wf[i], of[i], Y); } } while (0)
        asm volatile("" : "=v"(yb), "=v"(yc), "=v"(yd));
        unsigned good = 15;
        asm volatile("" : "+s"(good));
        if (__any(mxg[0] == ~0u)) { GRP_LD(0); good ^= 1; }
        if (__any(mxg[1] == ~0u)) { GRP_LD(1); good ^= 2; }
        if (__any(mxg[2] == ~0u)) { GRP_LD(2); good ^= 4; }
        GRP_MM(0, ya); GRP_MM(1, yb); GRP_MM(2, yc);
        if (GRP_BAD(3)) { GRP_LD(3); good ^= 8; }
        GRP_MM(3, yd);
        if (__builtin_expect(good != 15, 0)) {
            unsigned pend = good ^ 15, spins = SPIN_CAP;
#pragma nounroll
            do {
                __builtin_amdgcn_s_waitcnt(0x0F70);
                asm volatile("" : "+v"(of[0]), "+v"(of[1]), "+v"(of[2]), "+v"(of[3]), "+v"(of[4]), "+v"(of[5]), "+v"(of[6]), "+v"(of[7]),
                                  "+v"(of[8]), "+v"(of[9]), "+v"(of[10]), "+v"(of[11]), "+v"(of[12]), "+v"(of[13]), "+v"(of[14]), "+v"(of[15]) :: "memory");
                good = pend;
                if (pend & 1) { if (GRP_BAD(0)) { GRP_LD(0); good ^= 1; } }
                if (pend & 2) { if (GRP_BAD(1)) { GRP_LD(1); good ^= 2; } }
                if (pend & 4) { if (GRP_BAD(2)) { GRP_LD(2); good ^= 4; } }
                if (pend & 8) { if (GRP_BAD(3)) { GRP_LD(3); good ^= 8; } }
                pend ^= good;
                GRP_MM(0, ya); GRP_MM(1, yb); GRP_MM(2, yc); GRP_MM(3, yd);
                asm volatile("s_sub_u32 %1, %1, 1\n\ts_cselect_b32 %0, 0, %0" : "+s"(pend), "+s"(spins) :: "memory", "scc");
            } while (pend);
        }
        asm volatile("s_waitcnt vmcnt(0)\n\ts_nop 4" : "+v"(ya), "+v"(yb), "+v"(yc), "+v"(yd) :: "memory");
        ya = ((ya + yb) + yc) + yd;
#pragma unroll
        for (int q = 0; q < 4; ++q) *(LAS u32x2*)(otw + q * 16) = (u32x2){cvtpk(ya[4 * q], ya[4 * q + 1]), cvtpk(ya[4 * q + 2], ya[4 * q + 3])};
        asm volatile("" ::: "memory");
#pragma unroll
        for (int s = 0; s < 2; ++s) __builtin_amdgcn_raw_buffer_store_b128(*(const LAS u32x4*)(otr + s * 1280), rout, vout + s * 16384, 0, SC1);
        if (bid == 0 && tid == 256) __hip_atomic_store((GAS u64*)(ws + WS_EPOCH), a.fl.tag, RLX_AGENT);
    } else {
        if (e == GE) glds16((((lane >> 3) + rot) & 7) * 128 + (lane & 7) * 16, gw, lds0 + L_GW + t * 1024);
#pragma unroll
        for (int m = 0; m < 2; ++m) {
            f32x4 mk;
#pragma unroll
            for (int i = 0; i < 4; ++i) { const bool le = (i + 8 * t + 4 * hh) <= r32; mk[i] = (le != (m != 0)) ? 0.f : -__builtin_inff(); }
            *(LAS f32x4*)(lds + L_MDIAG + m * 4096 + t * 1024 + lane * 16) = mk;
        }
        if (tid < 4) *(LAS unsigned*)(lds + L_CNT + tid * 4) = 0;
        *(LAS unsigned*)(lds + L_Z + tid * 4) = 0;
        MK_ATT(0);
        MK_TAGS(a.fl);
        unsigned vwf = window_flag(a);
        PIN(a.vv0); PIN(a.vv1); PIN(vwf);
        if (e) project<1>(lds, a.rkv, a.ro, lane, t, p, j, a.fl); else project<0>(lds, a.rkv, a.ro, lane, t, p, j, a.fl);
        cnt_wait(lds + L_CNT + 8);
        __builtin_amdgcn_s_sleep(34);
        tag_wait(a.fl, vwf, 0);
        KV3 kw;
        a.gt = GATE();
        LDB_FIRST(kw);
        MK_SO();
        attn_window<0>(a, kw, so0);
        PIN(a.idx);
        attn_full<0>(a, lds_zero16(a.zp), lds_zero16(a.zp), lds_zero16(a.zp), lds_zero16(a.zp), lds_zero16(a.zp), so0[3], a.rkv, 0, 0, 0, 0, 0, kw);
        BAR();
    }
}

extern "C" void kernel_launch(void* const* d_in, const int* in_sizes, int n_in, void* d_out, int out_size, void* d_ws, size_t ws_size,
                              hipStream_t stream) {
    (void)in_sizes; (void)n_in; (void)out_size; (void)ws_size;
    (void)hipFuncSetAttribute((const void*)gmix_kernel, hipFuncAttributeMaxDynamicSharedMemorySize, L_END);
    gmix_kernel<<<256, 512, L_END, stream>>>((const char*)d_in[0], (const char*)d_in[1], (const char*)d_in[2], (const char*)d_in[4],
                                             (const char*)d_in[5], (const char*)d_in[6], (char*)d_out, (char*)d_ws);
}
```
